```python
import torch

def _softmax(x, axis=-1):
    return torch.softmax(x, dim=axis)

def _gelu(x):
    return torch.nn.functional.gelu(x, approximate='tanh')

def _silu(x):
    return torch.nn.functional.silu(x)

def _sigmoid(x):
    return torch.sigmoid(x)

def _layernorm(x, axis=-1, eps=1e-05):
    m = x.mean(dim=axis, keepdim=True)
    v = x.var(dim=axis, keepdim=True, unbiased=False)
    return (x - m) / torch.sqrt(v + eps)

def _scatter_add(tgt, idx, src, axis=0):
    return tgt.clone().index_add_(axis, idx, src)

def _segment_sum(src, seg, n):
    out = torch.zeros((n,) + src.shape[1:], dtype=src.dtype)
    return out.index_add_(0, seg, src)

def _pad(x, pw):
    flat = tuple((v for pair in reversed(pw) for v in pair))
    return torch.nn.functional.pad(x, flat)
kernel_name = 'adaLN_cond__grouped_kv_mqa__dense_activation_silu__prompt'

def setup_inputs():

    _w = lambda a: (lambda t: t.to(torch.bfloat16) if t.is_floating_point() else t)(torch.as_tensor(a))

    def _A_setup():
        B, S, D, H = (1, 2048, 512, 32)
        Dh = D // H
        return {'x': _w((lambda *s: torch.randn(*s, dtype=torch.bfloat16) * 0.05)(B, S, D)), 'wq': _w((lambda *s: torch.randn(*s, dtype=torch.bfloat16) * 0.05)(D, H * Dh)), 'wk': _w((lambda *s: torch.randn(*s, dtype=torch.bfloat16) * 0.05)(D, Dh)), 'wv': _w((lambda *s: torch.randn(*s, dtype=torch.bfloat16) * 0.05)(D, Dh)), 'wo': _w((lambda *s: torch.randn(*s, dtype=torch.bfloat16) * 0.05)(H * Dh, D))}
    _A_in = _A_setup()

    def _B_setup():
        B, S, D, F = (1, 2048, 512, 1376)
        return {'x': _w((lambda *s: torch.randn(*s, dtype=torch.bfloat16) * 0.05)(B, S, D)), 'w1': _w((lambda *s: torch.randn(*s, dtype=torch.bfloat16) * 3.2)(D, F) * D ** (-0.5)), 'w2': _w((lambda *s: torch.randn(*s, dtype=torch.bfloat16) * 1.6)(F, D) * F ** (-0.5))}
    _B_in = _B_setup()
    return {'A_x': _A_in['x'], 'A_wq': _A_in['wq'], 'A_wk': _A_in['wk'], 'A_wv': _A_in['wv'], 'A_wo': _A_in['wo'], 'B_x': _B_in['x'], 'B_w1': _B_in['w1'], 'B_w2': _B_in['w2'], 'c': _w((lambda *s: torch.randn(*s, dtype=torch.bfloat16) * 0.1)(1, 512) * 8.0), 'w_mod': _w((lambda *s: torch.randn(*s, dtype=torch.bfloat16) * 0.1)(512, 6 * 512) * 512 ** (-0.5) * 8.0)}

def reference(A_x, A_wq, A_wk, A_wv, A_wo, B_x, B_w1, B_w2, c, w_mod):

    def _A_ref(x, wq, wk, wv, wo):
        B, S, D = x.shape
        H = 32
        Dh = D // H
        q = (lambda x, *d: x.permute(*d))((x @ wq).reshape(B, S, H, Dh), 0, 2, 1, 3)
        k = x @ wk
        v = x @ wv
        scores = torch.einsum('bhqd,bkd->bhqk', q, k) * Dh ** (-0.5)
        p = _softmax(scores, axis=-1)
        o = torch.einsum('bhqk,bkd->bhqd', p, v)
        return (lambda x, *d: x.permute(*d))(o, 0, 2, 1, 3).reshape(B, S, H * Dh) @ wo

    def _B_ref(x, w1, w2):
        h = torch.einsum('bsd,df->bsf', x, w1)
        h = _silu(h)
        out = torch.einsum('bsf,fd->bsd', h, w2)
        return out
    x = A_x
    mod = (_silu(c) @ w_mod).reshape(c.shape[0], 6, 1, -1)
    sa, ca, ga, sb, cb, gb = (mod[:, 0].clone(), mod[:, 1].clone(), mod[:, 2].clone(), mod[:, 3].clone(), mod[:, 4].clone(), mod[:, 5].clone())
    h = _layernorm(x, axis=-1) * (1 + ca) + sa
    x = x + ga * _A_ref(h, A_wq, A_wk, A_wv, A_wo)
    h = _layernorm(x, axis=-1) * (1 + cb) + sb
    x = x + gb * _B_ref(h, B_w1, B_w2)
    return x
```

```cpp
#include <hip/hip_runtime.h>
#include <hip/hip_bf16.h>
typedef unsigned short u16;
typedef __bf16 bf8 __attribute__((ext_vector_type(8)));
typedef __bf16 bf16v __attribute__((ext_vector_type(16)));
typedef float f16v __attribute__((ext_vector_type(16)));
typedef float f8v __attribute__((ext_vector_type(8)));
typedef float f4 __attribute__((ext_vector_type(4)));
typedef unsigned u4 __attribute__((ext_vector_type(4)));
constexpr int S = 2048, D = 512, F = 1376, H = 32, DH = 16;
__device__ __forceinline__ float bf2f(u16 x) { return __uint_as_float((unsigned)x << 16); }
__device__ __forceinline__ u16 f2bf(float f) { return __builtin_bit_cast(u16, (__bf16)f); }
__device__ __forceinline__ float silu(float x) { return x / (1.f + __expf(-x)); }

__global__ void __launch_bounds__(256) k_mod(const u16* c, const u16* wm, float* mod) {
  __shared__ float red[64][33];
  int t = threadIdx.x, r = t >> 2, cg = t & 3, col0 = blockIdx.x * 32 + cg * 8;
  float acc[8] = {};
  for (int i = 0; i < 8; ++i) {
    int row = r + 64 * i;
    float s = bf2f(f2bf(silu(bf2f(c[row]))));
    u4 w = *(const u4*)(wm + row * 3072 + col0);
    for (int j = 0; j < 4; ++j) { acc[2*j] += s * __uint_as_float(w[j] << 16); acc[2*j+1] += s * __uint_as_float(w[j] & 0xffff0000u); }
  }
  for (int j = 0; j < 8; ++j) red[r][cg * 8 + j] = acc[j];
  __syncthreads();
  if (t < 32) { float s = 0; for (int i = 0; i < 64; ++i) s += red[i][t]; mod[blockIdx.x * 32 + t] = s; }
}

template <bool F32IN>
__global__ void __launch_bounds__(256) k_ln(const void* xin, const float* sh, const float* sc, u16* out) {
  int row = blockIdx.x * 4 + (threadIdx.x >> 6), l = threadIdx.x & 63;
  float v[8];
  if (F32IN) { const f4* p = (const f4*)((const float*)xin + row * D + l * 8); f4 a = p[0], b = p[1]; for (int j = 0; j < 4; ++j) { v[j] = a[j]; v[4+j] = b[j]; } }
  else { u4 w = *(const u4*)((const u16*)xin + row * D + l * 8); for (int j = 0; j < 4; ++j) { v[2*j] = __uint_as_float(w[j] << 16); v[2*j+1] = __uint_as_float(w[j] & 0xffff0000u); } }
  float s = 0; for (int j = 0; j < 8; ++j) s += v[j];
  for (int o = 32; o; o >>= 1) s += __shfl_xor(s, o);
  float m = s * (1.f / D), q = 0;
  for (int j = 0; j < 8; ++j) { v[j] -= m; q += v[j] * v[j]; }
  for (int o = 32; o; o >>= 1) q += __shfl_xor(q, o);
  float r = rsqrtf(q * (1.f / D) + 1e-5f);
  u4 w;
  for (int j = 0; j < 4; ++j) {
    int c0 = l * 8 + 2 * j;
    float a = v[2*j] * r * (1.f + sc[c0]) + sh[c0], b = v[2*j+1] * r * (1.f + sc[c0+1]) + sh[c0+1];
    w[j] = f2bf(a) | ((unsigned)f2bf(b) << 16);
  }
  *(u4*)(out + row * D + l * 8) = w;
}

template <int EPI>
__global__ void __launch_bounds__(256) k_gemm(const u16* A, const u16* B, int K, int N, int ldb, void* C, const void* res, const float* g) {
  __shared__ u16 Bs[32][40];
  int t = threadIdx.x, w = t >> 6, l = t & 63;
  int m0 = blockIdx.x * 64 + w * 16, n0 = blockIdx.y * 32;
  f4 acc[2] = {};
  for (int k0 = 0; k0 < K; k0 += 32) {
    __syncthreads();
    if (t < 128) { int kk = t >> 2, nn = (t & 3) * 8;
      if (n0 + nn < N) { u4 wv = *(const u4*)(B + (size_t)(k0 + kk) * ldb + n0 + nn);
      for (int j = 0; j < 4; ++j) { Bs[nn + 2*j][kk] = wv[j] & 0xffff; Bs[nn + 2*j + 1][kk] = wv[j] >> 16; } } }
    __syncthreads();
    bf8 a = *(const bf8*)(A + (size_t)(m0 + (l & 15)) * K + k0 + (l >> 4) * 8);
    for (int j = 0; j < 2; ++j) { bf8 b = *(const bf8*)&Bs[j * 16 + (l & 15)][(l >> 4) * 8];
      acc[j] = __builtin_amdgcn_mfma_f32_16x16x32_bf16(a, b, acc[j], 0, 0, 0); }
  }
  for (int j = 0; j < 2; ++j) for (int i = 0; i < 4; ++i) {
    int m = m0 + (l >> 4) * 4 + i, n = n0 + j * 16 + (l & 15);
    if (n >= N) continue;
    float v = acc[j][i];
    if (EPI == 0) ((u16*)C)[m * N + n] = f2bf(v * (0.25f * 1.4426950408889634f));
    if (EPI == 1) ((u16*)C)[m * 16 + n] = f2bf(v);
    if (EPI == 2) ((u16*)C)[n * S + m] = f2bf(v);
    if (EPI == 3) ((float*)C)[m * N + n] = bf2f(((const u16*)res)[m * N + n]) + g[n] * v;
    if (EPI == 4) ((u16*)C)[m * N + n] = f2bf(silu(v));
    if (EPI == 5) ((u16*)C)[m * N + n] = f2bf(((const float*)res)[m * N + n] + g[n] * v);
  }
}

__global__ void __launch_bounds__(512) k_attn(const u16* Q, const u16* Kg, const u16* Vt, u16* O) {
  int w = threadIdx.x >> 6, l = threadIdx.x & 63, r = l & 31, h = l >> 5;
  int tok = blockIdx.x * 8 + w;
  bf8 q = *(const bf8*)(Q + tok * D + r * 16 + h * 8);
  f16v o = {};
  float m = -1e30f;
  const u4 ones = {0x3f803f80u, 0x3f803f80u, 0x3f803f80u, 0x3f803f80u};
  for (int kt = 0; kt < S; kt += 32) {
    bf8 kf = *(const bf8*)(Kg + (kt + r) * 16 + h * 8);
    f16v z = {};
    f16v s = __builtin_amdgcn_mfma_f32_32x32x16_bf16(kf, q, z, 0, 0, 0);
    float mx = s[0]; for (int i = 1; i < 16; ++i) mx = fmaxf(mx, s[i]);
    mx = fmaxf(mx, __shfl_xor(mx, 32));
    float mn = fmaxf(m, mx), al = __builtin_amdgcn_exp2f(m - mn); m = mn;
    for (int i = 0; i < 16; ++i) o[i] *= al;
    f8v p0, p1;
    for (int i = 0; i < 8; ++i) { p0[i] = __builtin_amdgcn_exp2f(s[i] - m); p1[i] = __builtin_amdgcn_exp2f(s[8 + i] - m); }
    u4 v0 = ones, v1 = ones;
    if (r < 16) { v0 = *(const u4*)(Vt + r * S + kt + h * 8); v1 = *(const u4*)(Vt + r * S + kt + 16 + h * 8); }
    o = __builtin_amdgcn_smfmac_f32_32x32x32_bf16(__builtin_bit_cast(bf8, v0), __builtin_bit_cast(bf16v, p0), o, 0xdddd, 0, 0);
    o = __builtin_amdgcn_smfmac_f32_32x32x32_bf16(__builtin_bit_cast(bf8, v1), __builtin_bit_cast(bf16v, p1), o, 0xdddd, 0, 0);
  }
  float inv = 1.f / o[8];
  for (int i = 0; i < 8; ++i) { int d = (i & 3) + 8 * (i >> 2) + 4 * h; O[tok * D + r * 16 + d] = f2bf(o[i] * inv); }
}

extern "C" void kernel_launch(void* const* d_in, const int* in_sizes, int n_in, void* d_out, int out_size, void* d_ws, size_t ws_size, hipStream_t stream) {
  const u16 *x = (const u16*)d_in[0], *wq = (const u16*)d_in[1], *wk = (const u16*)d_in[2], *wv = (const u16*)d_in[3], *wo = (const u16*)d_in[4];
  const u16 *w1 = (const u16*)d_in[6], *w2 = (const u16*)d_in[7], *c = (const u16*)d_in[8], *wm = (const u16*)d_in[9];
  char* ws = (char*)d_ws;
  float* mod = (float*)ws; u16* hb = (u16*)(ws + (1 << 20)); u16* q = (u16*)(ws + (4 << 20)); u16* kk = (u16*)(ws + (7 << 20)); u16* vt = (u16*)(ws + (8 << 20));
  u16* o = (u16*)(ws + (9 << 20)); float* x1 = (float*)(ws + (12 << 20)); u16* hh = (u16*)(ws + (20 << 20));
  k_mod<<<96, 256, 0, stream>>>(c, wm, mod);
  k_ln<false><<<S / 4, 256, 0, stream>>>(x, mod, mod + 512, hb);
  k_gemm<0><<<dim3(32, 16), 256, 0, stream>>>(hb, wq, D, D, D, q, nullptr, nullptr);
  k_gemm<1><<<dim3(32, 1), 256, 0, stream>>>(hb, wk, D, 16, 16, kk, nullptr, nullptr);
  k_gemm<2><<<dim3(32, 1), 256, 0, stream>>>(hb, wv, D, 16, 16, vt, nullptr, nullptr);
  k_attn<<<256, 512, 0, stream>>>(q, kk, vt, o);
  k_gemm<3><<<dim3(32, 16), 256, 0, stream>>>(o, wo, D, D, D, x1, x, mod + 1024);
  k_ln<true><<<S / 4, 256, 0, stream>>>(x1, mod + 1536, mod + 2048, hb);
  k_gemm<4><<<dim3(32, 43), 256, 0, stream>>>(hb, w1, D, F, F, hh, nullptr, nullptr);
  k_gemm<5><<<dim3(32, 16), 256, 0, stream>>>(hh, w2, F, D, D, d_out, x1, mod + 2560);
}
```

```cpp
#include <hip/hip_runtime.h>
#include <hip/hip_bf16.h>
typedef unsigned short u16;
typedef __bf16 bf8 __attribute__((ext_vector_type(8)));
typedef __bf16 bf16v __attribute__((ext_vector_type(16)));
typedef float f16v __attribute__((ext_vector_type(16)));
typedef float f8v __attribute__((ext_vector_type(8)));
typedef float f4 __attribute__((ext_vector_type(4)));
typedef unsigned u4 __attribute__((ext_vector_type(4)));
constexpr int S = 2048, D = 512, F = 1376, H = 32, DH = 16;
__device__ __forceinline__ float bf2f(u16 x) { return __uint_as_float((unsigned)x << 16); }
__device__ __forceinline__ u16 f2bf(float f) { return __builtin_bit_cast(u16, (__bf16)f); }
__device__ __forceinline__ float silu(float x) { return x / (1.f + __expf(-x)); }

__global__ void __launch_bounds__(256) k_mod(const u16* c, const u16* wm, float* mod) {
  __shared__ float red[64][33];
  int t = threadIdx.x, r = t >> 2, cg = t & 3, col0 = blockIdx.x * 32 + cg * 8;
  float acc[8] = {};
  for (int i = 0; i < 8; ++i) {
    int row = r + 64 * i;
    float s = bf2f(f2bf(silu(bf2f(c[row]))));
    u4 w = *(const u4*)(wm + row * 3072 + col0);
    for (int j = 0; j < 4; ++j) { acc[2*j] += s * __uint_as_float(w[j] << 16); acc[2*j+1] += s * __uint_as_float(w[j] & 0xffff0000u); }
  }
  for (int j = 0; j < 8; ++j) red[r][cg * 8 + j] = acc[j];
  __syncthreads();
  if (t < 32) { float s = 0; for (int i = 0; i < 64; ++i) s += red[i][t]; mod[blockIdx.x * 32 + t] = s; }
}

template <bool F32IN>
__global__ void __launch_bounds__(256) k_ln(const void* xin, const float* sh, const float* sc, u16* out) {
  int row = blockIdx.x * 4 + (threadIdx.x >> 6), l = threadIdx.x & 63;
  float v[8];
  if (F32IN) { const f4* p = (const f4*)((const float*)xin + row * D + l * 8); f4 a = p[0], b = p[1]; for (int j = 0; j < 4; ++j) { v[j] = a[j]; v[4+j] = b[j]; } }
  else { u4 w = *(const u4*)((const u16*)xin + row * D + l * 8); for (int j = 0; j < 4; ++j) { v[2*j] = __uint_as_float(w[j] << 16); v[2*j+1] = __uint_as_float(w[j] & 0xffff0000u); } }
  float s = 0; for (int j = 0; j < 8; ++j) s += v[j];
  for (int o = 32; o; o >>= 1) s += __shfl_xor(s, o);
  float m = s * (1.f / D), q = 0;
  for (int j = 0; j < 8; ++j) { v[j] -= m; q += v[j] * v[j]; }
  for (int o = 32; o; o >>= 1) q += __shfl_xor(q, o);
  float r = rsqrtf(q * (1.f / D) + 1e-5f);
  u4 w;
  for (int j = 0; j < 4; ++j) {
    int c0 = l * 8 + 2 * j;
    float a = v[2*j] * r * (1.f + sc[c0]) + sh[c0], b = v[2*j+1] * r * (1.f + sc[c0+1]) + sh[c0+1];
    w[j] = f2bf(a) | ((unsigned)f2bf(b) << 16);
  }
  *(u4*)(out + row * D + l * 8) = w;
}

template <int EPI>
__global__ void __launch_bounds__(256) k_gemm(const u16* A, const u16* B, int K, int N, int ldb, void* C, const void* res, const float* g) {
  __shared__ u16 Bs[32][40];
  int t = threadIdx.x, w = t >> 6, l = t & 63;
  int m0 = blockIdx.x * 64 + w * 16, n0 = blockIdx.y * 32;
  f4 acc[2] = {};
  for (int k0 = 0; k0 < K; k0 += 32) {
    __syncthreads();
    if (t < 128) { int kk = t >> 2, nn = (t & 3) * 8;
      if (n0 + nn < N) { u4 wv = *(const u4*)(B + (size_t)(k0 + kk) * ldb + n0 + nn);
      for (int j = 0; j < 4; ++j) { Bs[nn + 2*j][kk] = wv[j] & 0xffff; Bs[nn + 2*j + 1][kk] = wv[j] >> 16; } } }
    __syncthreads();
    bf8 a = *(const bf8*)(A + (size_t)(m0 + (l & 15)) * K + k0 + (l >> 4) * 8);
    for (int j = 0; j < 2; ++j) { bf8 b = *(const bf8*)&Bs[j * 16 + (l & 15)][(l >> 4) * 8];
      acc[j] = __builtin_amdgcn_mfma_f32_16x16x32_bf16(a, b, acc[j], 0, 0, 0); }
  }
  for (int j = 0; j < 2; ++j) for (int i = 0; i < 4; ++i) {
    int m = m0 + (l >> 4) * 4 + i, n = n0 + j * 16 + (l & 15);
    if (n >= N) continue;
    float v = acc[j][i];
    if (EPI == 0) ((u16*)C)[m * N + n] = f2bf(v * (0.25f * 1.4426950408889634f));
    if (EPI == 1) ((u16*)C)[m * 16 + n] = f2bf(v);
    if (EPI == 2) ((u16*)C)[n * S + m] = f2bf(v);
    if (EPI == 3) ((float*)C)[m * N + n] = bf2f(((const u16*)res)[m * N + n]) + g[n] * v;
    if (EPI == 4) ((u16*)C)[m * N + n] = f2bf(silu(v));
    if (EPI == 5) ((u16*)C)[m * N + n] = f2bf(((const float*)res)[m * N + n] + g[n] * v);
  }
}

__global__ void __launch_bounds__(512) k_attn(const u16* Q, const u16* Kg, const u16* Vt, u16* O) {
  int w = threadIdx.x >> 6, l = threadIdx.x & 63, r = l & 31, h = l >> 5;
  int tok = blockIdx.x * 8 + w;
  bf8 q = *(const bf8*)(Q + tok * D + r * 16 + h * 8);
  f16v o = {};
  float m = -1e30f;
  const u4 ones = {0x3f803f80u, 0x3f803f80u, 0x3f803f80u, 0x3f803f80u};
  for (int kt = 0; kt < S; kt += 32) {
    bf8 kf = *(const bf8*)(Kg + (kt + r) * 16 + h * 8);
    f16v z = {};
    f16v s = __builtin_amdgcn_mfma_f32_32x32x16_bf16(kf, q, z, 0, 0, 0);
    float mx = s[0]; for (int i = 1; i < 16; ++i) mx = fmaxf(mx, s[i]);
    mx = fmaxf(mx, __shfl_xor(mx, 32));
    float mn = fmaxf(m, mx), al = __builtin_amdgcn_exp2f(m - mn); m = mn;
    for (int i = 0; i < 16; ++i) o[i] *= al;
    f8v p0, p1;
    for (int i = 0; i < 8; ++i) { p0[i] = __builtin_amdgcn_exp2f(s[i] - m); p1[i] = __builtin_amdgcn_exp2f(s[8 + i] - m); }
    u4 v0 = ones, v1 = ones;
    if (r < 16) { v0 = *(const u4*)(Vt + r * S + kt + h * 8); v1 = *(const u4*)(Vt + r * S + kt + 16 + h * 8); }
    o = __builtin_amdgcn_smfmac_f32_32x32x32_bf16(__builtin_bit_cast(bf8, v0), __builtin_bit_cast(bf16v, p0), o, 0xdddd, 0, 0);
    o = __builtin_amdgcn_smfmac_f32_32x32x32_bf16(__builtin_bit_cast(bf8, v1), __builtin_bit_cast(bf16v, p1), o, 0xdddd, 0, 0);
  }
  float inv = 1.f / o[8];
  for (int i = 0; i < 8; ++i) { int d = (i & 3) + 8 * (i >> 2) + 4 * h; O[tok * D + r * 16 + d] = f2bf(o[i] * inv); }
}

#define PROBE 0

typedef unsigned long long u64;
typedef unsigned u2 __attribute__((ext_vector_type(2)));
__device__ __forceinline__ u64 rt() { u64 t; asm volatile("s_memrealtime %0\n s_waitcnt lgkmcnt(0)" : "=s"(t) :: "memory"); return t; }
__device__ __forceinline__ u64 ct() { u64 t; asm volatile("s_memtime %0\n s_waitcnt lgkmcnt(0)" : "=s"(t) :: "memory"); return t; }
#define N4(x) x x x x
#define N16(x) N4(N4(x))
#define N256(x) N16(N16(x))
__global__ void __launch_bounds__(512) k_probe(const char* src, const char* x, u64* st) {
  u64 r0 = rt();
  u64 c0 = ct();
  const int t = threadIdx.x, b = blockIdx.x;
  u64 c1 = ct();
  if (t == 0) { st[b * 2] = r0; st[b * 2 + 1] = c1 - c0; }
}
__global__ void __launch_bounds__(256) k_amp(const u64* st) {
  __shared__ u64 a[256], c[256];
  int t = threadIdx.x; a[t] = st[t * 2]; c[t] = st[t * 2 + 1];
  __syncthreads();
  if (t == 0) {
    u64 mn = ~0ull, mx = 0, cm = 0, cs = 0;
    for (int i = 0; i < 256; ++i) { mn = a[i] < mn ? a[i] : mn; mx = a[i] > mx ? a[i] : mx; cm = c[i] > cm ? c[i] : cm; cs += c[i]; }
    u64 ticks = 0;
    if (PROBE == 1) ticks = (mx - mn) * 100;
    if (PROBE == 2) ticks = cm * 5;
    if (PROBE >= 3) ticks = (cs / 256) * 5;
    if (ticks > 200000) ticks = 200000;
    u64 t0 = rt();
    while (rt() - t0 < ticks) __builtin_amdgcn_s_sleep(4);
  }
}

extern "C" void kernel_launch(void* const* d_in, const int* in_sizes, int n_in, void* d_out, int out_size, void* d_ws, size_t ws_size, hipStream_t stream) {
  const u16 *x = (const u16*)d_in[0], *wq = (const u16*)d_in[1], *wk = (const u16*)d_in[2], *wv = (const u16*)d_in[3], *wo = (const u16*)d_in[4];
  const u16 *w1 = (const u16*)d_in[6], *w2 = (const u16*)d_in[7], *c = (const u16*)d_in[8], *wm = (const u16*)d_in[9];
  char* ws = (char*)d_ws;
  float* mod = (float*)ws; u16* hb = (u16*)(ws + (1 << 20)); u16* q = (u16*)(ws + (4 << 20)); u16* kk = (u16*)(ws + (7 << 20)); u16* vt = (u16*)(ws + (8 << 20));
  u16* o = (u16*)(ws + (9 << 20)); float* x1 = (float*)(ws + (12 << 20)); u16* hh = (u16*)(ws + (20 << 20));
  k_probe<<<256, 512, 0, stream>>>((const char*)wm, (const char*)x, (u64*)(ws + (40 << 20)));
  k_amp<<<1, 256, 0, stream>>>((const u64*)(ws + (40 << 20)));
  k_mod<<<96, 256, 0, stream>>>(c, wm, mod);
  k_ln<false><<<S / 4, 256, 0, stream>>>(x, mod, mod + 512, hb);
  k_gemm<0><<<dim3(32, 16), 256, 0, stream>>>(hb, wq, D, D, D, q, nullptr, nullptr);
  k_gemm<1><<<dim3(32, 1), 256, 0, stream>>>(hb, wk, D, 16, 16, kk, nullptr, nullptr);
  k_gemm<2><<<dim3(32, 1), 256, 0, stream>>>(hb, wv, D, 16, 16, vt, nullptr, nullptr);
  k_attn<<<256, 512, 0, stream>>>(q, kk, vt, o);
  k_gemm<3><<<dim3(32, 16), 256, 0, stream>>>(o, wo, D, D, D, x1, x, mod + 1024);
  k_ln<true><<<S / 4, 256, 0, stream>>>(x1, mod + 1536, mod + 2048, hb);
  k_gemm<4><<<dim3(32, 43), 256, 0, stream>>>(hb, w1, D, F, F, hh, nullptr, nullptr);
  k_gemm<5><<<dim3(32, 16), 256, 0, stream>>>(hh, w2, F, D, D, d_out, x1, mod + 2560);
}
```

```cpp
#include <hip/hip_runtime.h>
#include <hip/hip_bf16.h>

__global__ void __attribute__((aligned(4096))) __launch_bounds__(512, 2) adaln_mqa_ffn_kernel(const void* wm, unsigned wm_n, unsigned wm_f, const void* c, unsigned c_n, unsigned c_f, const void* ws, unsigned ws_n, unsigned ws_f, const void* x, unsigned x_n, unsigned x_f, const void* wq, unsigned wq_n, unsigned wq_f, const void* wk, unsigned wk_n, unsigned wk_f, const void* wv, unsigned wv_n, unsigned wv_f, const void* wo, unsigned wo_n, unsigned wo_f, const void* w1, unsigned w1_n, unsigned w1_f, const void* w2, unsigned w2_n, unsigned w2_f, const void* out, unsigned out_n, unsigned out_f) {
  __shared__ __attribute__((aligned(16))) char lds[163840];
  const unsigned lds_base = (unsigned)(size_t)(__attribute__((address_space(3))) char*)lds;
  asm volatile(
    ".Lstart:\n"
    "s_load_dwordx16 s[4:19], s[0:1], 0x0\n"
    "s_load_dwordx16 s[20:35], s[0:1], 0x40\n"
    "s_load_dwordx8 s[36:43], s[0:1], 0x80\n"
    "s_load_dwordx4 s[44:47], s[0:1], 0xa0\n"
    "v_lshrrev_b32 v1, 6, v0\n"
    "v_and_b32 v240, 63, v0\n"
    "s_and_b32 s59, s2, 7\n"
    "v_readfirstlane_b32 s48, v1\n"
    "s_lshr_b32 s60, s2, 3\n"
    "v_lshlrev_b32 v241, 4, v240\n"
    "s_cmp_eq_u32 s48, 7\n"
    "s_cbranch_scc0 .Lw_1\n"
    "s_getpc_b64 s[76:77]\n"
    ".Lpc:\n"
    "v_and_b32 v1, 31, v240\n"
    "v_lshlrev_b32 v1, 5, v1\n"
    "v_add_lshl_u32 v1, v1, s60, 6\n"
    "v_min_u32 v1, (.Lhot_end-.Lpc-4), v1\n"
    "global_load_dword v2, v1, s[76:77]\n"
    ".Lw_1:\n"
    "s_cmp_ge_u32 s48, 4\n"
    "s_cbranch_scc0 .Lw_2\n"
    "s_cmp_eq_u32 s48, 4\n"
    "s_cbranch_scc0 .Lwr0_4\n"
    "s_mov_b64 exec, 0\n"
    "s_branch .Lpad178\n"
    ".Lwr0_4:\n"
    "s_mov_b64 exec, -1\n"
    "s_cmp_eq_u32 s48, 5\n"
    "s_cbranch_scc0 .Lwr0_5\n"
    "s_mov_b64 exec, 0\n"
    "s_branch .Lpad325\n"
    ".Lwr0_5:\n"
    "s_mov_b64 exec, -1\n"
    "s_cmp_eq_u32 s48, 6\n"
    "s_cbranch_scc0 .Lwr0_6\n"
    "s_mov_b64 exec, 0\n"
    "s_branch .Lpad477\n"
    ".Lwr0_6:\n"
    "s_mov_b64 exec, -1\n"
    "s_cmp_eq_u32 s48, 7\n"
    "s_cbranch_scc0 .Lwr0_7\n"
    "s_mov_b64 exec, 0\n"
    "s_branch .Lpad628\n"
    ".Lwr0_7:\n"
    "s_mov_b64 exec, -1\n"
    ".Lw_2:\n"
    "s_lshr_b32 s62, s60, 3\n"
    "s_lshl2_add_u32 s49, s59, s62\n"
    "s_and_b32 s50, s60, 7\n"
    "s_lshl3_add_u32 s55, s49, s50\n"
    "s_lshr_b32 s52, s50, 1\n"
    "s_and_b32 s51, s48, 3\n"
    "v_and_b32 v9, 15, v240\n"
    "v_lshrrev_b32 v10, 4, v240\n"
    "v_lshrrev_b32 v17, 3, v240\n"
    "v_and_b32 v18, 7, v240\n"
    "v_lshlrev_b32 v11, 10, v17\n"
    "v_lshl_or_b32 v11, v18, 4, v11\n"
    "v_add_u32 v19, 8192, v11\n"
    "s_lshl2_add_u32 s82, s49, s52\n"
    "s_lshl_b32 s82, s82, 14\n"
    "s_lshl_b32 s63, s48, 7\n"
    "s_add_u32 s82, s82, s63\n"
    "s_xor_b32 s83, s82, 16384\n"
    "s_xor_b32 s84, s82, 32768\n"
    "s_xor_b32 s85, s82, 49152\n"
    "s_cmp_eq_u32 s48, 0\n"
    "s_cbranch_scc0 .Lf1_3\n"
    "s_setprio 3\n"
    "s_lshr_b32 s62, s60, 2\n"
    "s_lshl3_add_u32 s70, s59, s62\n"
    "s_lshl_b32 s70, s70, 5\n"
    "s_and_b32 s71, s60, 3\n"
    "s_lshl_b32 s63, s71, 8\n"
    "v_lshrrev_b32 v12, 1, v240\n"
    "v_and_b32 v13, 1, v240\n"
    "s_mul_i32 s62, s71, 786432\n"
    "s_add_u32 s62, s62, s70\n"
    "s_lshl_b32 s68, s2, 9\n"
    "s_add_u32 s68, s68, 131072\n"
    "v_lshlrev_b32 v8, 3, v240\n"
    "v_mov_b64 v[4:5], -1\n"
    "v_mul_u32_u24 v218, 24576, v12\n"
    "v_lshlrev_b32 v219, 3, v12\n"
    "s_waitcnt lgkmcnt(0)\n"
    "buffer_load_dwordx2 v[216:217], v219, s[8:11], s63 offen\n"
    "v_lshl_add_u32 v218, v13, 4, v218\n"
    "v_mul_u32_u24 v16, 63, v13\n"
    "v_add_u32 v16, v16, v240\n"
    "buffer_load_dwordx4 v[192:195], v218, s[4:7], s62 offen\n"
    "s_add_u32 s62, s62, 6144\n"
    "buffer_load_dwordx4 v[196:199], v218, s[4:7], s62 offen\n"
    "s_add_u32 s62, s62, 6144\n"
    "buffer_load_dwordx4 v[200:203], v218, s[4:7], s62 offen\n"
    "s_add_u32 s62, s62, 6144\n"
    "buffer_load_dwordx4 v[204:207], v218, s[4:7], s62 offen\n"
    "buffer_load_dword v1, off, s[12:15], 0 sc1\n"
    "buffer_store_dwordx2 v[4:5], v8, s[12:15], s68 offen sc1\n"
    "s_branch .Lfj_4\n"
    ".Lf1_3:\n"
    "s_waitcnt lgkmcnt(0)\n"
    "buffer_load_dword v3, off, s[12:15], 0 sc1\n"
    ".Lfj_4:\n"
    "buffer_load_dwordx4 v[64:67], v11, s[16:19], s82 offen\n"
    "buffer_load_dwordx4 v[68:71], v19, s[16:19], s82 offen\n"
    "s_bitcmp1_b32 s50, 0\n"
    "s_cselect_b64 s[24:25], s[28:29], s[24:25]\n"
    "s_min_u32 s62, s50, 3\n"
    "s_mul_i32 s56, s50, 5\n"
    "s_add_u32 s56, s56, s62\n"
    "s_cmp_lt_u32 s50, 3\n"
    "s_cselect_b32 s57, 6, 5\n"
    "s_mov_b32 s58, 100000\n"
    "v_lshrrev_b32 v15, 5, v240\n"
    "v_and_b32 v14, 31, v240\n"
    "v_lshrrev_b32 v20, 3, v240\n"
    "v_and_b32 v21, 7, v240\n"
    "v_and_b32 v22, 1, v10\n"
    "v_lshlrev_b32 v22, 2, v22\n"
    "v_xor_b32 v21, v21, v22\n"
    "v_lshlrev_b32 v21, 4, v21\n"
    "v_lshl_or_b32 v232, v20, 10, v21\n"
    "s_and_b32 s86, s49, 1\n"
    "s_lshl_b32 s86, s86, 5\n"
    "s_add_u32 s86, s86, s48\n"
    "s_lshl_b32 s86, s86, 10\n"
    "s_lshl_b32 s63, s50, 7\n"
    "s_lshl3_add_u32 s87, s86, s63\n"
    "s_lshl_b32 s62, s48, 3\n"
    "v_or_b32 v20, s62, v18\n"
    "v_xor_b32 v20, v20, v17\n"
    "v_lshlrev_b32 v20, 4, v20\n"
    "v_lshl_or_b32 v20, v17, 10, v20\n"
    "v_add_u32 v250, 65536, v20\n"
    "v_xor_b32 v251, 128, v250\n"
    "v_lshlrev_b32 v231, 3, v240\n"
    "v_add_u32 v231, 149504, v231\n"
    "s_lshl_b32 s62, s48, 9\n"
    "s_add_u32 s63, s62, 149504\n"
    "v_lshl_add_u32 v230, v17, 3, s63\n"
    "v_lshl_add_u32 v230, v18, 5, v230\n"
    "s_add_u32 s63, s62, 153600\n"
    "v_lshl_add_u32 v226, v18, 5, s63\n"
    "v_lshl_add_u32 v229, v240, 3, s63\n"
    "v_lshl_add_u32 v236, v17, 3, s63\n"
    "v_cvt_f32_u32 v228, v15\n"
    "v_lshlrev_b32 v21, 4, v14\n"
    "v_lshl_or_b32 v227, v15, 12, v21\n"
    "s_add_u32 s72, s62, 4096\n"
    "s_add_u32 s73, s72, 8192\n"
    "s_add_u32 s74, s72, 16384\n"
    "s_add_u32 s75, s72, 24576\n"
    "v_and_b32 v23, 3, v240\n"
    "v_bfe_u32 v24, v240, 2, 2\n"
    "s_cmp_eq_u32 s48, 3\n"
    "s_cbranch_scc0 .Lw_5\n"
    "v_xor_b32 v21, v10, v9\n"
    "v_lshlrev_b32 v21, 4, v21\n"
    "v_lshl_or_b32 v21, v9, 10, v21\n"
    "v_add_u32 v242, 65536, v21\n"
    "v_xor_b32 v243, 64, v242\n"
    "v_xor_b32 v244, 128, v242\n"
    "v_xor_b32 v245, 192, v242\n"
    "v_lshl_or_b32 v22, v10, 3, v24\n"
    "v_lshlrev_b32 v21, 3, v23\n"
    "v_lshl_or_b32 v21, v22, 5, v21\n"
    "v_add_u32 v233, 131072, v21\n"
    "v_bfe_u32 v22, v240, 3, 1\n"
    "v_lshl_or_b32 v21, v22, 2, v23\n"
    "v_bfe_u32 v22, v240, 2, 1\n"
    "v_lshl_or_b32 v21, v22, 3, v21\n"
    "v_add_u32 v20, 64, v9\n"
    "s_bitcmp1_b32 s50, 0\n"
    "s_cselect_b64 vcc, -1, 0\n"
    "v_cndmask_b32 v21, v21, v20, vcc\n"
    "v_lshlrev_b32 v21, 4, v21\n"
    "v_and_b32 v22, 1, v10\n"
    "v_lshl_add_u32 v208, v22, 9, v21\n"
    "s_lshr_b32 s62, s55, 2\n"
    "s_lshl_b32 s62, s62, 11\n"
    "s_and_b32 s63, s52, 1\n"
    "s_lshl_b32 s63, s63, 8\n"
    "s_add_u32 s80, s62, s63\n"
    "s_add_u32 s80, s80, 131072\n"
    ".Lw_5:\n"
    "s_cmp_ge_u32 s48, 4\n"
    "s_cbranch_scc0 .Lw_6\n"
    "v_xor_b32 v21, v15, v9\n"
    "v_lshlrev_b32 v21, 4, v21\n"
    "v_lshl_or_b32 v21, v14, 10, v21\n"
    "s_lshr_b32 s64, s51, 1\n"
    "s_lshl_b32 s62, s64, 15\n"
    "s_add_u32 s62, s62, 65536\n"
    "v_add_u32 v144, s62, v21\n"
    "v_xor_b32 v145, 32, v144\n"
    "v_xor_b32 v146, 64, v144\n"
    "v_xor_b32 v147, 96, v144\n"
    "v_xor_b32 v148, 128, v144\n"
    "s_nop 0\n"
    ".Lpad178:\n"
    "s_cbranch_execz .Lwr0_4\n"
    "v_xor_b32 v149, 160, v144\n"
    "v_xor_b32 v150, 192, v144\n"
    "v_xor_b32 v151, 224, v144\n"
    "v_lshl_or_b32 v22, v15, 3, v24\n"
    "v_lshlrev_b32 v22, 7, v22\n"
    "v_lshrrev_b32 v21, 1, v24\n"
    "s_and_b32 s65, s51, 1\n"
    "v_xor_b32 v21, s65, v21\n"
    "v_lshl_or_b32 v22, v21, 6, v22\n"
    "v_and_b32 v21, 1, v10\n"
    "v_lshl_or_b32 v22, v21, 5, v22\n"
    "v_lshl_or_b32 v127, v23, 3, v22\n"
    "v_lshrrev_b32 v21, 3, v14\n"
    "s_lshl_b32 s62, s64, 2\n"
    "s_lshl_b32 s63, s52, 1\n"
    "s_xor_b32 s62, s62, s63\n"
    "v_xor_b32 v21, s62, v21\n"
    "v_cmp_eq_u32 s[94:95], s50, v21\n"
    "v_and_b32 v22, 7, v240\n"
    "v_lshlrev_b32 v22, 4, v22\n"
    "v_lshl_or_b32 v22, v15, 9, v22\n"
    "v_lshl_or_b32 v208, v21, 13, v22\n"
    "s_lshl_b32 s65, s65, 8\n"
    "s_add_u32 s62, s65, 162816\n"
    "v_add_u32 v209, s62, v22\n"
    "s_lshl_b32 s54, s49, 16\n"
    "s_lshl_b32 s62, s50, 10\n"
    "s_add_u32 s54, s54, s62\n"
    "s_add_u32 s54, s54, s65\n"
    "s_add_u32 s54, s54, 1048576\n"
    ".Lw_6:\n"
    "s_cmp_eq_u32 s48, 0\n"
    "s_cbranch_scc1 .Lx0_7\n"
    "s_cmp_eq_u32 s48, 1\n"
    "s_cbranch_scc0 .Lwr1_1\n"
    "s_mov_b64 exec, 0\n"
    "s_branch .Lpad822\n"
    ".Lwr1_1:\n"
    "s_mov_b64 exec, -1\n"
    "s_cmp_eq_u32 s48, 2\n"
    "s_cbranch_scc0 .Lwr1_2\n"
    "s_mov_b64 exec, 0\n"
    "s_branch .Lpad1297\n"
    ".Lwr1_2:\n"
    "s_mov_b64 exec, -1\n"
    "s_cmp_eq_u32 s48, 3\n"
    "s_cbranch_scc0 .Lwr1_3\n"
    "s_mov_b64 exec, 0\n"
    "s_branch .Lpad2132\n"
    ".Lwr1_3:\n"
    "s_mov_b64 exec, -1\n"
    "s_cmp_eq_u32 s48, 4\n"
    "s_cbranch_scc0 .Lwr1_4\n"
    "s_mov_b64 exec, 0\n"
    "s_branch .Lpad2917\n"
    ".Lwr1_4:\n"
    "s_mov_b64 exec, -1\n"
    "s_cmp_eq_u32 s48, 5\n"
    "s_cbranch_scc0 .Lwr1_5\n"
    "s_mov_b64 exec, 0\n"
    "s_branch .Lpad3608\n"
    ".Lwr1_5:\n"
    "s_mov_b64 exec, -1\n"
    "s_cmp_eq_u32 s48, 6\n"
    "s_cbranch_scc0 .Lwr1_6\n"
    "s_mov_b64 exec, 0\n"
    "s_branch .Lpad3910\n"
    ".Lwr1_6:\n"
    "s_mov_b64 exec, -1\n"
    "s_cmp_eq_u32 s48, 7\n"
    "s_cbranch_scc0 .Lwr1_7\n"
    "s_mov_b64 exec, 0\n"
    "s_branch .Lpad4183\n"
    ".Lwr1_7:\n"
    "s_mov_b64 exec, -1\n"
    "s_setprio 2\n"
    "buffer_load_dwordx4 v[72:75], v11, s[16:19], s83 offen\n"
    "buffer_load_dwordx4 v[76:79], v19, s[16:19], s83 offen\n"
    "buffer_load_dwordx4 v[80:83], v11, s[16:19], s84 offen\n"
    "buffer_load_dwordx4 v[84:87], v19, s[16:19], s84 offen\n"
    "buffer_load_dwordx4 v[88:91], v11, s[16:19], s85 offen\n"
    "buffer_load_dwordx4 v[92:95], v19, s[16:19], s85 offen\n"
    "s_setprio 0\n"
    "s_waitcnt vmcnt(8)\n"
    "v_readfirstlane_b32 s53, v3\n"
    "s_nop 0\n"
    "s_add_u32 s53, s53, 0x9e3779b9\n"
    "s_branch .Lx1_8\n"
    ".Lx0_7:\n"
    "s_waitcnt vmcnt(8)\n"
    "v_lshlrev_b32 v218, 16, v216\n"
    "v_and_b32 v219, 0xffff0000, v216\n"
    "v_lshlrev_b32 v220, 16, v217\n"
    "v_and_b32 v221, 0xffff0000, v217\n"
    "v_mul_f32 v160, 0xbfb8aa3b, v218\n"
    "v_mul_f32 v161, 0xbfb8aa3b, v219\n"
    "v_mul_f32 v162, 0xbfb8aa3b, v220\n"
    "v_mul_f32 v163, 0xbfb8aa3b, v221\n"
    "v_exp_f32 v160, v160\n"
    "v_exp_f32 v161, v161\n"
    "v_exp_f32 v162, v162\n"
    "v_exp_f32 v163, v163\n"
    "v_add_f32 v160, 1.0, v160\n"
    "v_add_f32 v161, 1.0, v161\n"
    "v_add_f32 v162, 1.0, v162\n"
    "v_add_f32 v163, 1.0, v163\n"
    "v_rcp_f32 v160, v160\n"
    "v_rcp_f32 v161, v161\n"
    "v_rcp_f32 v162, v162\n"
    "v_rcp_f32 v163, v163\n"
    "v_mul_f32 v218, v218, v160\n"
    "v_mul_f32 v219, v219, v161\n"
    "v_mul_f32 v220, v220, v162\n"
    "v_mul_f32 v221, v221, v163\n"
    "v_cvt_pk_bf16_f32 v168, v218, 0\n"
    "v_cvt_pk_bf16_f32 v169, 0, v218\n"
    "v_cvt_pk_bf16_f32 v170, v219, 0\n"
    "v_cvt_pk_bf16_f32 v171, 0, v219\n"
    "v_cvt_pk_bf16_f32 v172, v220, 0\n"
    "v_cvt_pk_bf16_f32 v173, 0, v220\n"
    "v_cvt_pk_bf16_f32 v174, v221, 0\n"
    "v_cvt_pk_bf16_f32 v175, 0, v221\n"
    "buffer_load_dwordx4 v[72:75], v11, s[16:19], s83 offen\n"
    "buffer_load_dwordx4 v[76:79], v19, s[16:19], s83 offen\n"
    "buffer_load_dwordx4 v[80:83], v11, s[16:19], s84 offen\n"
    "buffer_load_dwordx4 v[84:87], v19, s[16:19], s84 offen\n"
    "buffer_load_dwordx4 v[88:91], v11, s[16:19], s85 offen\n"
    "buffer_load_dwordx4 v[92:95], v19, s[16:19], s85 offen\n"
    "s_waitcnt vmcnt(9)\n"
    "v_readfirstlane_b32 s53, v1\n"
    "s_nop 0\n"
    "s_add_u32 s53, s53, 0x9e3779b9\n"
    "v_dot2_f32_bf16 v160, v192, v168, 0\n"
    "v_dot2_f32_bf16 v161, v192, v169, 0\n"
    "v_dot2_f32_bf16 v162, v193, v168, 0\n"
    "v_dot2_f32_bf16 v163, v193, v169, 0\n"
    "v_dot2_f32_bf16 v164, v194, v168, 0\n"
    "v_dot2_f32_bf16 v165, v194, v169, 0\n"
    "v_dot2_f32_bf16 v166, v195, v168, 0\n"
    "v_dot2_f32_bf16 v167, v195, v169, 0\n"
    "v_dot2_f32_bf16 v160, v196, v170, v160\n"
    "v_dot2_f32_bf16 v161, v196, v171, v161\n"
    "v_dot2_f32_bf16 v162, v197, v170, v162\n"
    "v_dot2_f32_bf16 v163, v197, v171, v163\n"
    "v_dot2_f32_bf16 v164, v198, v170, v164\n"
    "v_dot2_f32_bf16 v165, v198, v171, v165\n"
    "v_dot2_f32_bf16 v166, v199, v170, v166\n"
    "v_dot2_f32_bf16 v167, v199, v171, v167\n"
    "v_dot2_f32_bf16 v160, v200, v172, v160\n"
    "v_dot2_f32_bf16 v161, v200, v173, v161\n"
    "v_dot2_f32_bf16 v162, v201, v172, v162\n"
    "v_dot2_f32_bf16 v163, v201, v173, v163\n"
    "v_dot2_f32_bf16 v164, v202, v172, v164\n"
    "v_dot2_f32_bf16 v165, v202, v173, v165\n"
    "v_dot2_f32_bf16 v166, v203, v172, v166\n"
    "v_dot2_f32_bf16 v167, v203, v173, v167\n"
    "v_dot2_f32_bf16 v160, v204, v174, v160\n"
    "v_dot2_f32_bf16 v161, v204, v175, v161\n"
    "v_dot2_f32_bf16 v162, v205, v174, v162\n"
    "v_dot2_f32_bf16 v163, v205, v175, v163\n"
    "v_dot2_f32_bf16 v164, v206, v174, v164\n"
    "v_dot2_f32_bf16 v165, v206, v175, v165\n"
    "v_dot2_f32_bf16 v166, v207, v174, v166\n"
    "v_dot2_f32_bf16 v167, v207, v175, v167\n"
    "v_permlane32_swap_b32 v160, v164\n"
    "v_permlane32_swap_b32 v161, v165\n"
    "v_permlane32_swap_b32 v162, v166\n"
    "v_permlane32_swap_b32 v163, v167\n"
    "v_add_f32 v160, v160, v164\n"
    "v_add_f32 v161, v161, v165\n"
    "v_add_f32 v162, v162, v166\n"
    "v_add_f32 v163, v163, v167\n"
    "s_nop 0\n"
    "v_permlane16_swap_b32 v160, v162\n"
    "v_permlane16_swap_b32 v161, v163\n"
    "v_add_f32 v160, v160, v162\n"
    "v_add_f32 v161, v161, v163\n"
    "v_mov_b32 v162, s53\n"
    "v_mov_b32 v163, s53\n"
    "v_add_f32_dpp v160, v160, v160 row_ror:8 row_mask:0xf bank_mask:0xf\n"
    "v_add_f32_dpp v161, v161, v161 row_ror:8 row_mask:0xf bank_mask:0xf\n"
    "s_nop 0\n"
    "v_add_f32_dpp v160, v160, v160 row_ror:4 row_mask:0xf bank_mask:0xf\n"
    "v_add_f32_dpp v161, v161, v161 row_ror:4 row_mask:0xf bank_mask:0xf\n"
    "s_nop 0\n"
    "v_add_f32_dpp v160, v160, v160 row_ror:2 row_mask:0xf bank_mask:0xf\n"
    "s_nop 0\n"
    ".Lpad325:\n"
    "s_cbranch_execz .Lwr0_5\n"
    "v_add_f32_dpp v161, v161, v161 row_ror:2 row_mask:0xf bank_mask:0xf\n"
    "s_mul_i32 s62, s71, 8192\n"
    "s_lshl2_add_u32 s62, s70, s62\n"
    "s_add_u32 s62, s62, 4096\n"
    "s_waitcnt vmcnt(8)\n"
    "s_mov_b32 exec_lo, 0x00030003\n"
    "s_mov_b32 exec_hi, 0x00030003\n"
    "buffer_store_dwordx4 v[160:163], v16, s[12:15], s62 offen sc1\n"
    "s_mov_b64 exec, -1\n"
    ".Lx1_8:\n"
    "s_setprio 0\n"
    "s_cmp_eq_u32 s48, 2\n"
    "s_cbranch_scc0 .Lw_9\n"
    "v_lshrrev_b32 v108, 1, v240\n"
    "v_lshlrev_b32 v108, 10, v108\n"
    "v_and_b32 v109, 1, v240\n"
    "v_lshl_or_b32 v108, v109, 4, v108\n"
    "s_lshr_b32 s62, s2, 5\n"
    "s_lshl_b32 s62, s62, 16\n"
    "s_and_b32 s63, s2, 31\n"
    "s_lshl_b32 s63, s63, 5\n"
    "s_add_u32 s62, s62, s63\n"
    "s_add_u32 s63, s62, 31744\n"
    "s_mov_b32 m0, 147456\n"
    "s_nop 0\n"
    "buffer_load_dwordx4 v108, s[32:35], s62 offen lds\n"
    "buffer_load_dwordx4 v108, s[32:35], s63 offen offset:1024 lds\n"
    ".Lw_9:\n"
    "s_lshl_b32 s62, s48, 11\n"
    "s_add_u32 m0, s62, 131072\n"
    "s_nop 0\n"
    "buffer_load_dwordx4 v241, s[24:27], s62 offen lds\n"
    "buffer_load_dwordx4 v241, s[24:27], s62 offen offset:1024 lds\n"
    "v_mov_b32 v237, 0x3f803f80\n"
    "s_waitcnt vmcnt(9)\n"
    "v_dot2_f32_bf16 v110, v64, v237, 0\n"
    "v_dot2_f32_bf16 v111, v64, v64, 0\n"
    "s_waitcnt vmcnt(8)\n"
    "v_dot2_f32_bf16 v112, v68, v237, 0\n"
    "v_dot2_f32_bf16 v113, v68, v68, 0\n"
    "s_waitcnt vmcnt(7)\n"
    "v_dot2_f32_bf16 v114, v72, v237, 0\n"
    "v_dot2_f32_bf16 v115, v72, v72, 0\n"
    "s_waitcnt vmcnt(6)\n"
    "v_dot2_f32_bf16 v116, v76, v237, 0\n"
    "v_dot2_f32_bf16 v117, v76, v76, 0\n"
    "v_dot2_f32_bf16 v110, v65, v237, v110\n"
    "v_dot2_f32_bf16 v111, v65, v65, v111\n"
    "v_dot2_f32_bf16 v112, v69, v237, v112\n"
    "v_dot2_f32_bf16 v113, v69, v69, v113\n"
    "v_dot2_f32_bf16 v114, v73, v237, v114\n"
    "v_dot2_f32_bf16 v115, v73, v73, v115\n"
    "v_dot2_f32_bf16 v116, v77, v237, v116\n"
    "v_dot2_f32_bf16 v117, v77, v77, v117\n"
    "v_dot2_f32_bf16 v110, v66, v237, v110\n"
    "v_dot2_f32_bf16 v111, v66, v66, v111\n"
    "v_dot2_f32_bf16 v112, v70, v237, v112\n"
    "v_dot2_f32_bf16 v113, v70, v70, v113\n"
    "v_dot2_f32_bf16 v114, v74, v237, v114\n"
    "v_dot2_f32_bf16 v115, v74, v74, v115\n"
    "v_dot2_f32_bf16 v116, v78, v237, v116\n"
    "v_dot2_f32_bf16 v117, v78, v78, v117\n"
    "v_dot2_f32_bf16 v110, v67, v237, v110\n"
    "v_dot2_f32_bf16 v111, v67, v67, v111\n"
    "v_dot2_f32_bf16 v112, v71, v237, v112\n"
    "v_dot2_f32_bf16 v113, v71, v71, v113\n"
    "v_dot2_f32_bf16 v114, v75, v237, v114\n"
    "v_dot2_f32_bf16 v115, v75, v75, v115\n"
    "v_dot2_f32_bf16 v116, v79, v237, v116\n"
    "s_xor_b32 m0, s86, 0\n"
    "s_xor_b32 s85, s87, 0\n"
    "buffer_load_dwordx4 v232, s[20:23], s85 offen lds\n"
    "v_dot2_f32_bf16 v117, v79, v79, v117\n"
    "v_add_f32_dpp v110, v110, v110 row_half_mirror row_mask:0xf bank_mask:0x5\n"
    "v_add_f32_dpp v111, v111, v111 row_half_mirror row_mask:0xf bank_mask:0x5\n"
    "v_add_f32_dpp v112, v112, v112 row_half_mirror row_mask:0xf bank_mask:0x5\n"
    "v_add_f32_dpp v113, v113, v113 row_half_mirror row_mask:0xf bank_mask:0x5\n"
    "v_add_f32_dpp v110, v114, v114 row_half_mirror row_mask:0xf bank_mask:0xa\n"
    "v_add_f32_dpp v111, v115, v115 row_half_mirror row_mask:0xf bank_mask:0xa\n"
    "v_add_f32_dpp v112, v116, v116 row_half_mirror row_mask:0xf bank_mask:0xa\n"
    "v_add_f32_dpp v113, v117, v117 row_half_mirror row_mask:0xf bank_mask:0xa\n"
    "v_add_f32_dpp v110, v110, v110 quad_perm:[1,0,3,2] row_mask:0xf bank_mask:0xf\n"
    "s_xor_b32 m0, s86, 8192\n"
    "s_xor_b32 s85, s87, 65536\n"
    "buffer_load_dwordx4 v232, s[20:23], s85 offen lds\n"
    "v_add_f32_dpp v111, v111, v111 quad_perm:[1,0,3,2] row_mask:0xf bank_mask:0xf\n"
    "v_add_f32_dpp v112, v112, v112 quad_perm:[1,0,3,2] row_mask:0xf bank_mask:0xf\n"
    "v_add_f32_dpp v113, v113, v113 quad_perm:[1,0,3,2] row_mask:0xf bank_mask:0xf\n"
    "v_add_f32_dpp v110, v110, v110 quad_perm:[2,3,0,1] row_mask:0xf bank_mask:0xf\n"
    "v_add_f32_dpp v111, v111, v111 quad_perm:[2,3,0,1] row_mask:0xf bank_mask:0xf\n"
    "v_add_f32_dpp v112, v112, v112 quad_perm:[2,3,0,1] row_mask:0xf bank_mask:0xf\n"
    "v_add_f32_dpp v113, v113, v113 quad_perm:[2,3,0,1] row_mask:0xf bank_mask:0xf\n"
    "v_lshlrev_b32 v0, 16, v64\n"
    "v_and_b32 v1, 0xffff0000, v64\n"
    "v_lshlrev_b32 v2, 16, v65\n"
    "v_and_b32 v3, 0xffff0000, v65\n"
    "v_lshlrev_b32 v4, 16, v66\n"
    "v_and_b32 v5, 0xffff0000, v66\n"
    "s_xor_b32 m0, s86, 16384\n"
    "s_xor_b32 s85, s87, 131072\n"
    "buffer_load_dwordx4 v232, s[20:23], s85 offen lds\n"
    "v_lshlrev_b32 v6, 16, v67\n"
    "v_and_b32 v7, 0xffff0000, v67\n"
    "v_lshlrev_b32 v8, 16, v68\n"
    "v_and_b32 v9, 0xffff0000, v68\n"
    "v_lshlrev_b32 v10, 16, v69\n"
    "v_and_b32 v11, 0xffff0000, v69\n"
    "v_lshlrev_b32 v12, 16, v70\n"
    "v_and_b32 v13, 0xffff0000, v70\n"
    "v_lshlrev_b32 v14, 16, v71\n"
    "v_and_b32 v15, 0xffff0000, v71\n"
    "v_lshlrev_b32 v16, 16, v72\n"
    "v_and_b32 v17, 0xffff0000, v72\n"
    "v_lshlrev_b32 v18, 16, v73\n"
    "v_and_b32 v19, 0xffff0000, v73\n"
    "v_lshlrev_b32 v20, 16, v74\n"
    "v_and_b32 v21, 0xffff0000, v74\n"
    "v_lshlrev_b32 v22, 16, v75\n"
    "v_and_b32 v23, 0xffff0000, v75\n"
    "v_lshlrev_b32 v24, 16, v76\n"
    "v_and_b32 v25, 0xffff0000, v76\n"
    "s_xor_b32 m0, s86, 24576\n"
    "s_xor_b32 s85, s87, 196608\n"
    "buffer_load_dwordx4 v232, s[20:23], s85 offen lds\n"
    "v_lshlrev_b32 v26, 16, v77\n"
    "v_and_b32 v27, 0xffff0000, v77\n"
    "v_lshlrev_b32 v28, 16, v78\n"
    "v_and_b32 v29, 0xffff0000, v78\n"
    "v_lshlrev_b32 v30, 16, v79\n"
    "v_and_b32 v31, 0xffff0000, v79\n"
    "s_waitcnt vmcnt(9)\n"
    "v_dot2_f32_bf16 v118, v80, v237, 0\n"
    "v_dot2_f32_bf16 v119, v80, v80, 0\n"
    "s_waitcnt vmcnt(8)\n"
    "v_dot2_f32_bf16 v120, v84, v237, 0\n"
    "v_dot2_f32_bf16 v121, v84, v84, 0\n"
    "s_waitcnt vmcnt(7)\n"
    "v_dot2_f32_bf16 v122, v88, v237, 0\n"
    "v_dot2_f32_bf16 v123, v88, v88, 0\n"
    "s_xor_b32 m0, s86, 32768\n"
    "s_xor_b32 s85, s87, 262144\n"
    "buffer_load_dwordx4 v232, s[20:23], s85 offen lds\n"
    "s_waitcnt vmcnt(7)\n"
    "v_dot2_f32_bf16 v124, v92, v237, 0\n"
    "v_dot2_f32_bf16 v125, v92, v92, 0\n"
    "v_dot2_f32_bf16 v118, v81, v237, v118\n"
    "v_dot2_f32_bf16 v119, v81, v81, v119\n"
    "v_dot2_f32_bf16 v120, v85, v237, v120\n"
    "v_dot2_f32_bf16 v121, v85, v85, v121\n"
    "v_dot2_f32_bf16 v122, v89, v237, v122\n"
    "v_dot2_f32_bf16 v123, v89, v89, v123\n"
    ".Lpad477:\n"
    "s_cbranch_execz .Lwr0_6\n"
    "v_dot2_f32_bf16 v124, v93, v237, v124\n"
    "v_dot2_f32_bf16 v125, v93, v93, v125\n"
    "v_dot2_f32_bf16 v118, v82, v237, v118\n"
    "v_dot2_f32_bf16 v119, v82, v82, v119\n"
    "v_dot2_f32_bf16 v120, v86, v237, v120\n"
    "v_dot2_f32_bf16 v121, v86, v86, v121\n"
    "v_dot2_f32_bf16 v122, v90, v237, v122\n"
    "v_dot2_f32_bf16 v123, v90, v90, v123\n"
    "v_dot2_f32_bf16 v124, v94, v237, v124\n"
    "v_dot2_f32_bf16 v125, v94, v94, v125\n"
    "v_dot2_f32_bf16 v118, v83, v237, v118\n"
    "v_dot2_f32_bf16 v119, v83, v83, v119\n"
    "v_dot2_f32_bf16 v120, v87, v237, v120\n"
    "v_dot2_f32_bf16 v121, v87, v87, v121\n"
    "v_dot2_f32_bf16 v122, v91, v237, v122\n"
    "v_dot2_f32_bf16 v123, v91, v91, v123\n"
    "v_dot2_f32_bf16 v124, v95, v237, v124\n"
    "v_dot2_f32_bf16 v125, v95, v95, v125\n"
    "v_add_f32_dpp v118, v118, v118 row_half_mirror row_mask:0xf bank_mask:0x5\n"
    "v_add_f32_dpp v119, v119, v119 row_half_mirror row_mask:0xf bank_mask:0x5\n"
    "v_add_f32_dpp v120, v120, v120 row_half_mirror row_mask:0xf bank_mask:0x5\n"
    "v_add_f32_dpp v121, v121, v121 row_half_mirror row_mask:0xf bank_mask:0x5\n"
    "v_add_f32_dpp v118, v122, v122 row_half_mirror row_mask:0xf bank_mask:0xa\n"
    "v_add_f32_dpp v119, v123, v123 row_half_mirror row_mask:0xf bank_mask:0xa\n"
    "v_add_f32_dpp v120, v124, v124 row_half_mirror row_mask:0xf bank_mask:0xa\n"
    "v_add_f32_dpp v121, v125, v125 row_half_mirror row_mask:0xf bank_mask:0xa\n"
    "v_add_f32_dpp v118, v118, v118 quad_perm:[1,0,3,2] row_mask:0xf bank_mask:0xf\n"
    "v_add_f32_dpp v119, v119, v119 quad_perm:[1,0,3,2] row_mask:0xf bank_mask:0xf\n"
    "v_add_f32_dpp v120, v120, v120 quad_perm:[1,0,3,2] row_mask:0xf bank_mask:0xf\n"
    "v_add_f32_dpp v121, v121, v121 quad_perm:[1,0,3,2] row_mask:0xf bank_mask:0xf\n"
    "v_add_f32_dpp v118, v118, v118 quad_perm:[2,3,0,1] row_mask:0xf bank_mask:0xf\n"
    "v_add_f32_dpp v119, v119, v119 quad_perm:[2,3,0,1] row_mask:0xf bank_mask:0xf\n"
    "v_add_f32_dpp v120, v120, v120 quad_perm:[2,3,0,1] row_mask:0xf bank_mask:0xf\n"
    "v_add_f32_dpp v121, v121, v121 quad_perm:[2,3,0,1] row_mask:0xf bank_mask:0xf\n"
    "s_mov_b32 exec_lo, 0x11111111\n"
    "s_mov_b32 exec_hi, 0x11111111\n"
    "ds_write_b64 v230, v[110:111] offset:0\n"
    "ds_write_b64 v230, v[112:113] offset:64\n"
    "ds_write_b64 v230, v[118:119] offset:256\n"
    "ds_write_b64 v230, v[120:121] offset:320\n"
    "s_mov_b64 exec, -1\n"
    "v_lshlrev_b32 v32, 16, v80\n"
    "v_and_b32 v33, 0xffff0000, v80\n"
    "v_lshlrev_b32 v34, 16, v81\n"
    "v_and_b32 v35, 0xffff0000, v81\n"
    "v_lshlrev_b32 v36, 16, v82\n"
    "v_and_b32 v37, 0xffff0000, v82\n"
    "v_lshlrev_b32 v38, 16, v83\n"
    "v_and_b32 v39, 0xffff0000, v83\n"
    "s_cmp_eq_u32 s48, 2\n"
    "s_cbranch_scc0 .Lw_10\n"
    "v_bfe_u32 v104, v240, 2, 1\n"
    "v_bfe_u32 v105, v240, 3, 1\n"
    "v_lshl_or_b32 v104, v105, 2, v104\n"
    "v_lshrrev_b32 v107, 4, v240\n"
    "v_lshl_or_b32 v104, v107, 3, v104\n"
    "v_lshlrev_b32 v104, 5, v104\n"
    "v_and_b32 v105, 3, v240\n"
    "v_lshl_or_b32 v104, v105, 3, v104\n"
    "v_add_u32 v104, 147456, v104\n"
    "ds_read_b64_tr_b16 v[96:97], v104\n"
    "ds_read_b64_tr_b16 v[98:99], v104 offset:64\n"
    "ds_read_b64_tr_b16 v[100:101], v104 offset:1024\n"
    "ds_read_b64_tr_b16 v[102:103], v104 offset:1088\n"
    "v_lshlrev_b32 v105, 11, v105\n"
    "v_lshl_or_b32 v105, v107, 8, v105\n"
    "v_bfe_u32 v106, v240, 2, 2\n"
    "v_lshl_or_b32 v105, v106, 4, v105\n"
    "s_lshr_b32 s62, s2, 2\n"
    "s_lshl_b32 s62, s62, 13\n"
    "s_and_b32 s63, s2, 3\n"
    "s_lshl_b32 s63, s63, 6\n"
    "s_add_u32 s62, s62, s63\n"
    "s_add_u32 s62, s62, 524288\n"
    "s_waitcnt lgkmcnt(2)\n"
    "buffer_store_dwordx4 v[96:99], v105, s[12:15], s62 offen sc1\n"
    "s_waitcnt lgkmcnt(0)\n"
    "buffer_store_dwordx4 v[100:103], v105, s[12:15], s62 offen offset:1024 sc1\n"
    ".Lw_10:\n"
    "s_waitcnt vmcnt(5) lgkmcnt(0)\n"
    "s_barrier\n"
    "ds_read_b64 v[160:161], v231 offset:0\n"
    "ds_read_b64 v[162:163], v231 offset:512\n"
    "ds_read_b64 v[164:165], v231 offset:1024\n"
    "ds_read_b64 v[166:167], v231 offset:1536\n"
    "ds_read_b64 v[168:169], v231 offset:2048\n"
    "ds_read_b64 v[170:171], v231 offset:2560\n"
    "ds_read_b64 v[172:173], v231 offset:3072\n"
    "ds_read_b64 v[174:175], v231 offset:3584\n"
    "s_setprio 2\n"
    "buffer_load_dwordx3 v[192:194], v227, s[12:15], s72 offen sc1\n"
    "buffer_load_dwordx3 v[196:198], v227, s[12:15], s73 offen sc1\n"
    "buffer_load_dwordx3 v[200:202], v227, s[12:15], s74 offen sc1\n"
    "buffer_load_dwordx3 v[204:206], v227, s[12:15], s75 offen sc1\n"
    "s_setprio 0\n"
    "v_lshlrev_b32 v40, 16, v84\n"
    "v_and_b32 v41, 0xffff0000, v84\n"
    "v_lshlrev_b32 v42, 16, v85\n"
    "v_and_b32 v43, 0xffff0000, v85\n"
    "v_lshlrev_b32 v44, 16, v86\n"
    "v_and_b32 v45, 0xffff0000, v86\n"
    "v_lshlrev_b32 v46, 16, v87\n"
    "v_and_b32 v47, 0xffff0000, v87\n"
    "v_lshlrev_b32 v48, 16, v88\n"
    "v_and_b32 v49, 0xffff0000, v88\n"
    "v_lshlrev_b32 v50, 16, v89\n"
    "v_and_b32 v51, 0xffff0000, v89\n"
    "s_waitcnt lgkmcnt(6)\n"
    "v_pk_add_f32 v[160:161], v[160:161], v[162:163]\n"
    "s_waitcnt lgkmcnt(4)\n"
    "v_pk_add_f32 v[164:165], v[164:165], v[166:167]\n"
    "s_waitcnt lgkmcnt(2)\n"
    "v_pk_add_f32 v[168:169], v[168:169], v[170:171]\n"
    "s_waitcnt lgkmcnt(0)\n"
    "v_pk_add_f32 v[172:173], v[172:173], v[174:175]\n"
    "v_pk_add_f32 v[160:161], v[160:161], v[164:165]\n"
    "v_pk_add_f32 v[168:169], v[168:169], v[172:173]\n"
    "v_pk_add_f32 v[160:161], v[160:161], v[168:169]\n"
    "v_mul_f32 v160, 0x3b000000, v160\n"
    "v_lshlrev_b32 v52, 16, v90\n"
    "v_and_b32 v53, 0xffff0000, v90\n"
    "v_mul_f32 v161, 0x3b000000, v161\n"
    "v_lshlrev_b32 v54, 16, v91\n"
    "v_and_b32 v55, 0xffff0000, v91\n"
    "v_fma_f32 v161, -v160, v160, v161\n"
    "v_lshlrev_b32 v56, 16, v92\n"
    "v_and_b32 v57, 0xffff0000, v92\n"
    "v_max_f32 v161, 0, v161\n"
    "v_lshlrev_b32 v58, 16, v93\n"
    "v_and_b32 v59, 0xffff0000, v93\n"
    "v_add_f32 v161, 0x3727c5ac, v161\n"
    "v_lshlrev_b32 v60, 16, v94\n"
    "v_and_b32 v61, 0xffff0000, v94\n"
    "v_rsq_f32 v161, v161\n"
    "v_lshlrev_b32 v62, 16, v95\n"
    "v_and_b32 v63, 0xffff0000, v95\n"
    "v_mul_f32 v160, -v160, v161\n"
    "ds_write_b64 v229, v[160:161]\n"
    "ds_read_b64 v[210:211], v236 offset:0\n"
    "ds_read_b64 v[212:213], v236 offset:64\n"
    "ds_read_b64 v[214:215], v236 offset:128\n"
    "ds_read_b64 v[216:217], v236 offset:192\n"
    "ds_read_b64 v[218:219], v236 offset:256\n"
    "ds_read_b64 v[220:221], v236 offset:320\n"
    "ds_read_b64 v[222:223], v236 offset:384\n"
    "ds_read_b64 v[224:225], v236 offset:448\n"
    "s_cmp_eq_u32 s48, 3\n"
    "s_cbranch_scc0 .Lw_11\n"
    "ds_read_b64_tr_b16 v[96:97], v233 offset:0\n"
    "s_nop 0\n"
    ".Lpad628:\n"
    "s_cbranch_execz .Lwr0_7\n"
    "ds_read_b64_tr_b16 v[98:99], v233 offset:128\n"
    "ds_read_b64_tr_b16 v[100:101], v233 offset:1024\n"
    "ds_read_b64_tr_b16 v[102:103], v233 offset:1152\n"
    "ds_read_b64_tr_b16 v[104:105], v233 offset:2048\n"
    "ds_read_b64_tr_b16 v[106:107], v233 offset:2176\n"
    "ds_read_b64_tr_b16 v[108:109], v233 offset:3072\n"
    "ds_read_b64_tr_b16 v[110:111], v233 offset:3200\n"
    "ds_read_b64_tr_b16 v[112:113], v233 offset:4096\n"
    "ds_read_b64_tr_b16 v[114:115], v233 offset:4224\n"
    "ds_read_b64_tr_b16 v[116:117], v233 offset:5120\n"
    "ds_read_b64_tr_b16 v[118:119], v233 offset:5248\n"
    "ds_read_b64_tr_b16 v[120:121], v233 offset:6144\n"
    "ds_read_b64_tr_b16 v[122:123], v233 offset:6272\n"
    "ds_read_b64_tr_b16 v[124:125], v233 offset:7168\n"
    "ds_read_b64_tr_b16 v[126:127], v233 offset:7296\n"
    "ds_read_b64_tr_b16 v[128:129], v233 offset:8192\n"
    "ds_read_b64_tr_b16 v[130:131], v233 offset:8320\n"
    "ds_read_b64_tr_b16 v[132:133], v233 offset:9216\n"
    "ds_read_b64_tr_b16 v[134:135], v233 offset:9344\n"
    "ds_read_b64_tr_b16 v[136:137], v233 offset:10240\n"
    "ds_read_b64_tr_b16 v[138:139], v233 offset:10368\n"
    "ds_read_b64_tr_b16 v[140:141], v233 offset:11264\n"
    "ds_read_b64_tr_b16 v[142:143], v233 offset:11392\n"
    "ds_read_b64_tr_b16 v[144:145], v233 offset:12288\n"
    "ds_read_b64_tr_b16 v[146:147], v233 offset:12416\n"
    "ds_read_b64_tr_b16 v[148:149], v233 offset:13312\n"
    "ds_read_b64_tr_b16 v[150:151], v233 offset:13440\n"
    "ds_read_b64_tr_b16 v[152:153], v233 offset:14336\n"
    "ds_read_b64_tr_b16 v[154:155], v233 offset:14464\n"
    "ds_read_b64_tr_b16 v[156:157], v233 offset:15360\n"
    "ds_read_b64_tr_b16 v[158:159], v233 offset:15488\n"
    ".Lw_11:\n"
    "s_waitcnt lgkmcnt(7)\n"
    "v_pk_fma_f32 v[0:1], v[0:1], v[210:211], v[210:211] op_sel:[0,1,0] op_sel_hi:[1,1,0]\n"
    "v_pk_fma_f32 v[2:3], v[2:3], v[210:211], v[210:211] op_sel:[0,1,0] op_sel_hi:[1,1,0]\n"
    "v_pk_fma_f32 v[4:5], v[4:5], v[210:211], v[210:211] op_sel:[0,1,0] op_sel_hi:[1,1,0]\n"
    "s_xor_b32 m0, s86, 40960\n"
    "s_xor_b32 s85, s87, 327680\n"
    "buffer_load_dwordx4 v232, s[20:23], s85 offen lds\n"
    "v_pk_fma_f32 v[6:7], v[6:7], v[210:211], v[210:211] op_sel:[0,1,0] op_sel_hi:[1,1,0]\n"
    "s_waitcnt lgkmcnt(6)\n"
    "v_pk_fma_f32 v[8:9], v[8:9], v[212:213], v[212:213] op_sel:[0,1,0] op_sel_hi:[1,1,0]\n"
    "v_pk_fma_f32 v[10:11], v[10:11], v[212:213], v[212:213] op_sel:[0,1,0] op_sel_hi:[1,1,0]\n"
    "v_pk_fma_f32 v[12:13], v[12:13], v[212:213], v[212:213] op_sel:[0,1,0] op_sel_hi:[1,1,0]\n"
    "v_pk_fma_f32 v[14:15], v[14:15], v[212:213], v[212:213] op_sel:[0,1,0] op_sel_hi:[1,1,0]\n"
    "s_waitcnt lgkmcnt(5)\n"
    "v_pk_fma_f32 v[16:17], v[16:17], v[214:215], v[214:215] op_sel:[0,1,0] op_sel_hi:[1,1,0]\n"
    "v_pk_fma_f32 v[18:19], v[18:19], v[214:215], v[214:215] op_sel:[0,1,0] op_sel_hi:[1,1,0]\n"
    "v_pk_fma_f32 v[20:21], v[20:21], v[214:215], v[214:215] op_sel:[0,1,0] op_sel_hi:[1,1,0]\n"
    "s_xor_b32 m0, s86, 49152\n"
    "s_xor_b32 s85, s87, 393216\n"
    "buffer_load_dwordx4 v232, s[20:23], s85 offen lds\n"
    "v_pk_fma_f32 v[22:23], v[22:23], v[214:215], v[214:215] op_sel:[0,1,0] op_sel_hi:[1,1,0]\n"
    "s_waitcnt lgkmcnt(4)\n"
    "v_pk_fma_f32 v[24:25], v[24:25], v[216:217], v[216:217] op_sel:[0,1,0] op_sel_hi:[1,1,0]\n"
    "v_pk_fma_f32 v[26:27], v[26:27], v[216:217], v[216:217] op_sel:[0,1,0] op_sel_hi:[1,1,0]\n"
    "v_pk_fma_f32 v[28:29], v[28:29], v[216:217], v[216:217] op_sel:[0,1,0] op_sel_hi:[1,1,0]\n"
    "v_pk_fma_f32 v[30:31], v[30:31], v[216:217], v[216:217] op_sel:[0,1,0] op_sel_hi:[1,1,0]\n"
    "s_waitcnt lgkmcnt(3)\n"
    "v_pk_fma_f32 v[32:33], v[32:33], v[218:219], v[218:219] op_sel:[0,1,0] op_sel_hi:[1,1,0]\n"
    "v_pk_fma_f32 v[34:35], v[34:35], v[218:219], v[218:219] op_sel:[0,1,0] op_sel_hi:[1,1,0]\n"
    "v_pk_fma_f32 v[36:37], v[36:37], v[218:219], v[218:219] op_sel:[0,1,0] op_sel_hi:[1,1,0]\n"
    "s_xor_b32 m0, s86, 57344\n"
    "s_xor_b32 s85, s87, 458752\n"
    "buffer_load_dwordx4 v232, s[20:23], s85 offen lds\n"
    "v_pk_fma_f32 v[38:39], v[38:39], v[218:219], v[218:219] op_sel:[0,1,0] op_sel_hi:[1,1,0]\n"
    "s_waitcnt lgkmcnt(2)\n"
    "v_pk_fma_f32 v[40:41], v[40:41], v[220:221], v[220:221] op_sel:[0,1,0] op_sel_hi:[1,1,0]\n"
    "v_pk_fma_f32 v[42:43], v[42:43], v[220:221], v[220:221] op_sel:[0,1,0] op_sel_hi:[1,1,0]\n"
    "v_pk_fma_f32 v[44:45], v[44:45], v[220:221], v[220:221] op_sel:[0,1,0] op_sel_hi:[1,1,0]\n"
    "v_pk_fma_f32 v[46:47], v[46:47], v[220:221], v[220:221] op_sel:[0,1,0] op_sel_hi:[1,1,0]\n"
    "s_waitcnt lgkmcnt(1)\n"
    "v_pk_fma_f32 v[48:49], v[48:49], v[222:223], v[222:223] op_sel:[0,1,0] op_sel_hi:[1,1,0]\n"
    "v_pk_fma_f32 v[50:51], v[50:51], v[222:223], v[222:223] op_sel:[0,1,0] op_sel_hi:[1,1,0]\n"
    "v_pk_fma_f32 v[52:53], v[52:53], v[222:223], v[222:223] op_sel:[0,1,0] op_sel_hi:[1,1,0]\n"
    "v_pk_fma_f32 v[54:55], v[54:55], v[222:223], v[222:223] op_sel:[0,1,0] op_sel_hi:[1,1,0]\n"
    "s_waitcnt lgkmcnt(0)\n"
    "v_pk_fma_f32 v[56:57], v[56:57], v[224:225], v[224:225] op_sel:[0,1,0] op_sel_hi:[1,1,0]\n"
    "v_pk_fma_f32 v[58:59], v[58:59], v[224:225], v[224:225] op_sel:[0,1,0] op_sel_hi:[1,1,0]\n"
    "v_pk_fma_f32 v[60:61], v[60:61], v[224:225], v[224:225] op_sel:[0,1,0] op_sel_hi:[1,1,0]\n"
    "v_pk_fma_f32 v[62:63], v[62:63], v[224:225], v[224:225] op_sel:[0,1,0] op_sel_hi:[1,1,0]\n"
    ".Lgc_12:\n"
    "s_waitcnt vmcnt(6)\n"
    "v_cmp_ne_u32 vcc, s53, v194\n"
    "s_waitcnt vmcnt(5)\n"
    "v_cmp_ne_u32 s[78:79], s53, v198\n"
    "v_add_f32 v195, v192, v196\n"
    "v_add_f32 v199, v193, v197\n"
    "s_or_b64 s[76:77], vcc, s[78:79]\n"
    "s_waitcnt vmcnt(4)\n"
    "v_cmp_ne_u32 vcc, s53, v202\n"
    "v_add_f32 v195, v195, v200\n"
    "v_add_f32 v199, v199, v201\n"
    "s_or_b64 s[76:77], vcc, s[76:77]\n"
    "s_waitcnt vmcnt(3)\n"
    "v_cmp_ne_u32 vcc, s53, v206\n"
    "v_add_f32 v195, v195, v204\n"
    "v_add_f32 v199, v199, v205\n"
    "s_or_b64 vcc, vcc, s[76:77]\n"
    "s_cbranch_vccnz .Lgr_13\n"
    ".Lgo_14:\n"
    "v_cvt_pk_bf16_f32 v194, v195, v199\n"
    "v_lshlrev_b32 v192, 16, v194\n"
    "v_and_b32 v193, 0xffff0000, v194\n"
    "v_add_f32 v192, v192, v228\n"
    "v_add_f32 v193, v193, v228\n"
    "v_cvt_pk_bf16_f32 v194, v192, v193\n"
    "v_lshlrev_b32 v192, 16, v194\n"
    "v_and_b32 v193, 0xffff0000, v194\n"
    "ds_write_b64 v229, v[192:193]\n"
    "ds_read_b128 v[64:67], v226 offset:0\n"
    "ds_read_b128 v[72:75], v226 offset:256\n"
    "ds_read_b128 v[68:71], v226 offset:16\n"
    "ds_read_b128 v[76:79], v226 offset:272\n"
    "s_waitcnt lgkmcnt(2)\n"
    "v_pk_fma_f32 v[0:1], v[0:1], v[72:73], v[64:65]\n"
    "v_pk_fma_f32 v[2:3], v[2:3], v[74:75], v[66:67]\n"
    "s_waitcnt lgkmcnt(0)\n"
    "v_pk_fma_f32 v[4:5], v[4:5], v[76:77], v[68:69]\n"
    "v_pk_fma_f32 v[6:7], v[6:7], v[78:79], v[70:71]\n"
    "v_cvt_pk_bf16_f32 v0, v0, v1\n"
    "v_cvt_pk_bf16_f32 v1, v2, v3\n"
    "v_cvt_pk_bf16_f32 v2, v4, v5\n"
    "v_cvt_pk_bf16_f32 v3, v6, v7\n"
    "ds_write_b128 v250, v[0:3] offset:0\n"
    "v_pk_fma_f32 v[8:9], v[8:9], v[72:73], v[64:65]\n"
    "v_pk_fma_f32 v[10:11], v[10:11], v[74:75], v[66:67]\n"
    "v_pk_fma_f32 v[12:13], v[12:13], v[76:77], v[68:69]\n"
    "v_pk_fma_f32 v[14:15], v[14:15], v[78:79], v[70:71]\n"
    "v_cvt_pk_bf16_f32 v8, v8, v9\n"
    "v_cvt_pk_bf16_f32 v9, v10, v11\n"
    "v_cvt_pk_bf16_f32 v10, v12, v13\n"
    "v_cvt_pk_bf16_f32 v11, v14, v15\n"
    "ds_write_b128 v251, v[8:11] offset:8192\n"
    "s_waitcnt lgkmcnt(0)\n"
    "s_barrier\n"
    "s_cmp_ge_u32 s48, 4\n"
    "s_cbranch_scc1 .LroleB\n"
    "s_cmp_eq_u32 s48, 3\n"
    "s_cbranch_scc0 .Lq_16\n"
    "s_setprio 3\n"
    "s_bitcmp1_b32 s50, 0\n"
    "s_cbranch_scc1 .Lv_15\n"
    "ds_read_b128 v[160:163], v242 offset:0\n"
    "ds_read_b128 v[164:167], v243 offset:0\n"
    "ds_read_b128 v[168:171], v244 offset:0\n"
    "ds_read_b128 v[172:175], v245 offset:0\n"
    "ds_read_b128 v[176:179], v242 offset:256\n"
    "ds_read_b128 v[180:183], v243 offset:256\n"
    "ds_read_b128 v[184:187], v244 offset:256\n"
    "ds_read_b128 v[188:191], v245 offset:256\n"
    "ds_read_b128 v[80:83], v242 offset:512\n"
    "ds_read_b128 v[84:87], v243 offset:512\n"
    "ds_read_b128 v[88:91], v244 offset:512\n"
    "ds_read_b128 v[92:95], v245 offset:512\n"
    "s_waitcnt lgkmcnt(11)\n"
    "v_mfma_f32_16x16x32_bf16 v[192:195], v[96:99], v[160:163], 0\n"
    "ds_read_b128 v[160:163], v242 offset:768\n"
    "s_waitcnt lgkmcnt(11)\n"
    "v_mfma_f32_16x16x32_bf16 v[192:195], v[100:103], v[164:167], v[192:195]\n"
    "ds_read_b128 v[164:167], v243 offset:768\n"
    "s_waitcnt lgkmcnt(11)\n"
    "v_mfma_f32_16x16x32_bf16 v[192:195], v[104:107], v[168:171], v[192:195]\n"
    "ds_read_b128 v[168:171], v244 offset:768\n"
    "s_waitcnt lgkmcnt(11)\n"
    "v_mfma_f32_16x16x32_bf16 v[192:195], v[108:111], v[172:175], v[192:195]\n"
    "ds_read_b128 v[172:175], v245 offset:768\n"
    "s_waitcnt lgkmcnt(11)\n"
    "v_mfma_f32_16x16x32_bf16 v[192:195], v[112:115], v[176:179], v[192:195]\n"
    "s_waitcnt lgkmcnt(10)\n"
    "v_mfma_f32_16x16x32_bf16 v[192:195], v[116:119], v[180:183], v[192:195]\n"
    "s_waitcnt lgkmcnt(9)\n"
    "v_mfma_f32_16x16x32_bf16 v[192:195], v[120:123], v[184:187], v[192:195]\n"
    "s_waitcnt lgkmcnt(8)\n"
    "v_mfma_f32_16x16x32_bf16 v[192:195], v[124:127], v[188:191], v[192:195]\n"
    "s_waitcnt lgkmcnt(7)\n"
    "v_mfma_f32_16x16x32_bf16 v[192:195], v[128:131], v[80:83], v[192:195]\n"
    "s_waitcnt lgkmcnt(6)\n"
    "v_mfma_f32_16x16x32_bf16 v[192:195], v[132:135], v[84:87], v[192:195]\n"
    "s_waitcnt lgkmcnt(5)\n"
    "v_mfma_f32_16x16x32_bf16 v[192:195], v[136:139], v[88:91], v[192:195]\n"
    "s_waitcnt lgkmcnt(4)\n"
    "v_mfma_f32_16x16x32_bf16 v[192:195], v[140:143], v[92:95], v[192:195]\n"
    "s_waitcnt lgkmcnt(3)\n"
    "v_mfma_f32_16x16x32_bf16 v[192:195], v[144:147], v[160:163], v[192:195]\n"
    "s_waitcnt lgkmcnt(2)\n"
    "v_mfma_f32_16x16x32_bf16 v[192:195], v[148:151], v[164:167], v[192:195]\n"
    "s_waitcnt lgkmcnt(1)\n"
    "v_mfma_f32_16x16x32_bf16 v[192:195], v[152:155], v[168:171], v[192:195]\n"
    "s_waitcnt lgkmcnt(0)\n"
    "v_mfma_f32_16x16x32_bf16 v[192:195], v[156:159], v[172:175], v[192:195]\n"
    "s_branch .Lkvst_17\n"
    ".Lv_15:\n"
    ".Lpad822:\n"
    "s_cbranch_execz .Lpad4256\n"
    "ds_read_b128 v[160:163], v242 offset:0\n"
    "ds_read_b128 v[164:167], v243 offset:0\n"
    "ds_read_b128 v[168:171], v244 offset:0\n"
    "ds_read_b128 v[172:175], v245 offset:0\n"
    "ds_read_b128 v[176:179], v242 offset:256\n"
    "ds_read_b128 v[180:183], v243 offset:256\n"
    "ds_read_b128 v[184:187], v244 offset:256\n"
    "ds_read_b128 v[188:191], v245 offset:256\n"
    "ds_read_b128 v[80:83], v242 offset:512\n"
    "ds_read_b128 v[84:87], v243 offset:512\n"
    "ds_read_b128 v[88:91], v244 offset:512\n"
    "ds_read_b128 v[92:95], v245 offset:512\n"
    "s_waitcnt lgkmcnt(11)\n"
    "v_mfma_f32_16x16x32_bf16 v[192:195], v[160:163], v[96:99], 0\n"
    "ds_read_b128 v[160:163], v242 offset:768\n"
    "s_waitcnt lgkmcnt(11)\n"
    "v_mfma_f32_16x16x32_bf16 v[192:195], v[164:167], v[100:103], v[192:195]\n"
    "ds_read_b128 v[164:167], v243 offset:768\n"
    "s_waitcnt lgkmcnt(11)\n"
    "v_mfma_f32_16x16x32_bf16 v[192:195], v[168:171], v[104:107], v[192:195]\n"
    "ds_read_b128 v[168:171], v244 offset:768\n"
    "s_waitcnt lgkmcnt(11)\n"
    "v_mfma_f32_16x16x32_bf16 v[192:195], v[172:175], v[108:111], v[192:195]\n"
    "ds_read_b128 v[172:175], v245 offset:768\n"
    "s_waitcnt lgkmcnt(11)\n"
    "v_mfma_f32_16x16x32_bf16 v[192:195], v[176:179], v[112:115], v[192:195]\n"
    "s_waitcnt lgkmcnt(10)\n"
    "v_mfma_f32_16x16x32_bf16 v[192:195], v[180:183], v[116:119], v[192:195]\n"
    "s_waitcnt lgkmcnt(9)\n"
    "v_mfma_f32_16x16x32_bf16 v[192:195], v[184:187], v[120:123], v[192:195]\n"
    "s_waitcnt lgkmcnt(8)\n"
    "v_mfma_f32_16x16x32_bf16 v[192:195], v[188:191], v[124:127], v[192:195]\n"
    "s_waitcnt lgkmcnt(7)\n"
    "v_mfma_f32_16x16x32_bf16 v[192:195], v[80:83], v[128:131], v[192:195]\n"
    "s_waitcnt lgkmcnt(6)\n"
    "v_mfma_f32_16x16x32_bf16 v[192:195], v[84:87], v[132:135], v[192:195]\n"
    "s_waitcnt lgkmcnt(5)\n"
    "v_mfma_f32_16x16x32_bf16 v[192:195], v[88:91], v[136:139], v[192:195]\n"
    "s_waitcnt lgkmcnt(4)\n"
    "v_mfma_f32_16x16x32_bf16 v[192:195], v[92:95], v[140:143], v[192:195]\n"
    "s_waitcnt lgkmcnt(3)\n"
    "v_mfma_f32_16x16x32_bf16 v[192:195], v[160:163], v[144:147], v[192:195]\n"
    "s_waitcnt lgkmcnt(2)\n"
    "v_mfma_f32_16x16x32_bf16 v[192:195], v[164:167], v[148:151], v[192:195]\n"
    "s_waitcnt lgkmcnt(1)\n"
    "v_mfma_f32_16x16x32_bf16 v[192:195], v[168:171], v[152:155], v[192:195]\n"
    "s_waitcnt lgkmcnt(0)\n"
    "v_mfma_f32_16x16x32_bf16 v[192:195], v[172:175], v[156:159], v[192:195]\n"
    ".Lkvst_17:\n"
    "s_nop 7\n"
    "v_cvt_pk_bf16_f32 v196, v192, v193\n"
    "v_cvt_pk_bf16_f32 v197, v194, v195\n"
    "v_cvt_pk_bf16_f32 v198, v192, v193\n"
    "v_cvt_pk_bf16_f32 v199, v194, v195\n"
    "s_nop 0\n"
    "v_permlane32_swap_b32 v196, v198\n"
    "v_permlane32_swap_b32 v197, v199\n"
    "s_mov_b32 exec_hi, 0\n"
    "buffer_store_dwordx4 v[196:199], v208, s[12:15], s80 offen sc1\n"
    "s_mov_b32 exec_hi, -1\n"
    "v_pk_fma_f32 v[16:17], v[16:17], v[72:73], v[64:65]\n"
    "v_pk_fma_f32 v[18:19], v[18:19], v[74:75], v[66:67]\n"
    "v_pk_fma_f32 v[20:21], v[20:21], v[76:77], v[68:69]\n"
    "v_pk_fma_f32 v[22:23], v[22:23], v[78:79], v[70:71]\n"
    "v_cvt_pk_bf16_f32 v16, v16, v17\n"
    "v_cvt_pk_bf16_f32 v17, v18, v19\n"
    "v_cvt_pk_bf16_f32 v18, v20, v21\n"
    "v_cvt_pk_bf16_f32 v19, v22, v23\n"
    "ds_write_b128 v250, v[16:19] offset:16384\n"
    "v_pk_fma_f32 v[24:25], v[24:25], v[72:73], v[64:65]\n"
    "v_pk_fma_f32 v[26:27], v[26:27], v[74:75], v[66:67]\n"
    "v_pk_fma_f32 v[28:29], v[28:29], v[76:77], v[68:69]\n"
    "v_pk_fma_f32 v[30:31], v[30:31], v[78:79], v[70:71]\n"
    "v_cvt_pk_bf16_f32 v24, v24, v25\n"
    "v_cvt_pk_bf16_f32 v25, v26, v27\n"
    "v_cvt_pk_bf16_f32 v26, v28, v29\n"
    "v_cvt_pk_bf16_f32 v27, v30, v31\n"
    "ds_write_b128 v251, v[24:27] offset:24576\n"
    "v_pk_fma_f32 v[32:33], v[32:33], v[72:73], v[64:65]\n"
    "v_pk_fma_f32 v[34:35], v[34:35], v[74:75], v[66:67]\n"
    "v_pk_fma_f32 v[36:37], v[36:37], v[76:77], v[68:69]\n"
    "v_pk_fma_f32 v[38:39], v[38:39], v[78:79], v[70:71]\n"
    "v_cvt_pk_bf16_f32 v32, v32, v33\n"
    "v_cvt_pk_bf16_f32 v33, v34, v35\n"
    "v_cvt_pk_bf16_f32 v34, v36, v37\n"
    "v_cvt_pk_bf16_f32 v35, v38, v39\n"
    "ds_write_b128 v250, v[32:35] offset:32768\n"
    "v_pk_fma_f32 v[40:41], v[40:41], v[72:73], v[64:65]\n"
    "v_pk_fma_f32 v[42:43], v[42:43], v[74:75], v[66:67]\n"
    "v_pk_fma_f32 v[44:45], v[44:45], v[76:77], v[68:69]\n"
    "v_pk_fma_f32 v[46:47], v[46:47], v[78:79], v[70:71]\n"
    "v_cvt_pk_bf16_f32 v40, v40, v41\n"
    "v_cvt_pk_bf16_f32 v41, v42, v43\n"
    "v_cvt_pk_bf16_f32 v42, v44, v45\n"
    "v_cvt_pk_bf16_f32 v43, v46, v47\n"
    "ds_write_b128 v251, v[40:43] offset:40960\n"
    "v_pk_fma_f32 v[48:49], v[48:49], v[72:73], v[64:65]\n"
    "v_pk_fma_f32 v[50:51], v[50:51], v[74:75], v[66:67]\n"
    "v_pk_fma_f32 v[52:53], v[52:53], v[76:77], v[68:69]\n"
    "v_pk_fma_f32 v[54:55], v[54:55], v[78:79], v[70:71]\n"
    "v_cvt_pk_bf16_f32 v48, v48, v49\n"
    "v_cvt_pk_bf16_f32 v49, v50, v51\n"
    "v_cvt_pk_bf16_f32 v50, v52, v53\n"
    "v_cvt_pk_bf16_f32 v51, v54, v55\n"
    "ds_write_b128 v250, v[48:51] offset:49152\n"
    "v_pk_fma_f32 v[56:57], v[56:57], v[72:73], v[64:65]\n"
    "v_pk_fma_f32 v[58:59], v[58:59], v[74:75], v[66:67]\n"
    "v_pk_fma_f32 v[60:61], v[60:61], v[76:77], v[68:69]\n"
    "v_pk_fma_f32 v[62:63], v[62:63], v[78:79], v[70:71]\n"
    "v_cvt_pk_bf16_f32 v56, v56, v57\n"
    "v_cvt_pk_bf16_f32 v57, v58, v59\n"
    "v_cvt_pk_bf16_f32 v58, v60, v61\n"
    "v_cvt_pk_bf16_f32 v59, v62, v63\n"
    "ds_write_b128 v251, v[56:59] offset:57344\n"
    "s_setprio 0\n"
    "s_waitcnt vmcnt(1) lgkmcnt(0)\n"
    "s_branch .Lme_18\n"
    ".Lq_16:\n"
    "v_pk_fma_f32 v[16:17], v[16:17], v[72:73], v[64:65]\n"
    "v_pk_fma_f32 v[18:19], v[18:19], v[74:75], v[66:67]\n"
    "v_pk_fma_f32 v[20:21], v[20:21], v[76:77], v[68:69]\n"
    "v_pk_fma_f32 v[22:23], v[22:23], v[78:79], v[70:71]\n"
    "v_cvt_pk_bf16_f32 v16, v16, v17\n"
    "v_cvt_pk_bf16_f32 v17, v18, v19\n"
    "v_cvt_pk_bf16_f32 v18, v20, v21\n"
    "v_cvt_pk_bf16_f32 v19, v22, v23\n"
    "ds_write_b128 v250, v[16:19] offset:16384\n"
    "v_pk_fma_f32 v[24:25], v[24:25], v[72:73], v[64:65]\n"
    "v_pk_fma_f32 v[26:27], v[26:27], v[74:75], v[66:67]\n"
    "v_pk_fma_f32 v[28:29], v[28:29], v[76:77], v[68:69]\n"
    "v_pk_fma_f32 v[30:31], v[30:31], v[78:79], v[70:71]\n"
    "v_cvt_pk_bf16_f32 v24, v24, v25\n"
    "v_cvt_pk_bf16_f32 v25, v26, v27\n"
    "v_cvt_pk_bf16_f32 v26, v28, v29\n"
    "v_cvt_pk_bf16_f32 v27, v30, v31\n"
    "ds_write_b128 v251, v[24:27] offset:24576\n"
    "v_pk_fma_f32 v[32:33], v[32:33], v[72:73], v[64:65]\n"
    "v_pk_fma_f32 v[34:35], v[34:35], v[74:75], v[66:67]\n"
    "v_pk_fma_f32 v[36:37], v[36:37], v[76:77], v[68:69]\n"
    "v_pk_fma_f32 v[38:39], v[38:39], v[78:79], v[70:71]\n"
    "v_cvt_pk_bf16_f32 v32, v32, v33\n"
    "v_cvt_pk_bf16_f32 v33, v34, v35\n"
    "v_cvt_pk_bf16_f32 v34, v36, v37\n"
    "v_cvt_pk_bf16_f32 v35, v38, v39\n"
    "ds_write_b128 v250, v[32:35] offset:32768\n"
    "v_pk_fma_f32 v[40:41], v[40:41], v[72:73], v[64:65]\n"
    "v_pk_fma_f32 v[42:43], v[42:43], v[74:75], v[66:67]\n"
    "v_pk_fma_f32 v[44:45], v[44:45], v[76:77], v[68:69]\n"
    "v_pk_fma_f32 v[46:47], v[46:47], v[78:79], v[70:71]\n"
    "v_cvt_pk_bf16_f32 v40, v40, v41\n"
    "v_cvt_pk_bf16_f32 v41, v42, v43\n"
    "v_cvt_pk_bf16_f32 v42, v44, v45\n"
    "v_cvt_pk_bf16_f32 v43, v46, v47\n"
    "ds_write_b128 v251, v[40:43] offset:40960\n"
    "v_pk_fma_f32 v[48:49], v[48:49], v[72:73], v[64:65]\n"
    "v_pk_fma_f32 v[50:51], v[50:51], v[74:75], v[66:67]\n"
    "v_pk_fma_f32 v[52:53], v[52:53], v[76:77], v[68:69]\n"
    "v_pk_fma_f32 v[54:55], v[54:55], v[78:79], v[70:71]\n"
    "v_cvt_pk_bf16_f32 v48, v48, v49\n"
    "v_cvt_pk_bf16_f32 v49, v50, v51\n"
    "v_cvt_pk_bf16_f32 v50, v52, v53\n"
    "v_cvt_pk_bf16_f32 v51, v54, v55\n"
    "ds_write_b128 v250, v[48:51] offset:49152\n"
    "v_pk_fma_f32 v[56:57], v[56:57], v[72:73], v[64:65]\n"
    "v_pk_fma_f32 v[58:59], v[58:59], v[74:75], v[66:67]\n"
    "v_pk_fma_f32 v[60:61], v[60:61], v[76:77], v[68:69]\n"
    "v_pk_fma_f32 v[62:63], v[62:63], v[78:79], v[70:71]\n"
    "v_cvt_pk_bf16_f32 v56, v56, v57\n"
    "v_cvt_pk_bf16_f32 v57, v58, v59\n"
    "v_cvt_pk_bf16_f32 v58, v60, v61\n"
    "v_cvt_pk_bf16_f32 v59, v62, v63\n"
    "ds_write_b128 v251, v[56:59] offset:57344\n"
    "s_setprio 0\n"
    "s_waitcnt vmcnt(0) lgkmcnt(0)\n"
    ".Lme_18:\n"
    "s_barrier\n"
    "s_cmp_eq_u32 s48, 1\n"
    "s_cbranch_scc0 .Lw_19\n"
    "s_lshl_b32 s62, s55, 7\n"
    "s_add_u32 s62, s62, 81920\n"
    "v_mov_b32 v0, s53\n"
    "s_mov_b64 exec, 1\n"
    "buffer_store_dword v0, off, s[12:15], s62 offset:64 sc1\n"
    "s_mov_b64 exec, -1\n"
    ".Lw_19:\n"
    "s_setprio 0\n"
    "s_and_b32 s62, s60, 4\n"
    "s_xor_b32 s62, s62, s48\n"
    "s_lshl_b32 s62, s62, 2\n"
    "s_and_b32 s63, s60, 0\n"
    "s_or_b32 s62, s62, s63\n"
    "s_lshl_b32 s62, s62, 12\n"
    "s_add_u32 s70, s62, 131072\n"
    "s_xor_b32 s80, s70, 4096\n"
    "s_xor_b32 s81, s70, 8192\n"
    "s_xor_b32 s82, s70, 12288\n"
    "s_lshl_b32 s71, s55, 13\n"
    "s_lshl_b32 s62, s50, 10\n"
    "s_add_u32 s71, s71, s62\n"
    "s_add_u32 s71, s71, 1048576\n"
    "s_lshl_b32 s83, s48, 13\n"
    "v_add_u32 v249, s83, v241\n"
    "s_mov_b32 s88, 0xffff\n"
    "s_mov_b32 s89, 0xffff\n"
    "v_mov_b32 v232, 0xdddd\n"
    "v_bfe_u32 v200, v240, 3, 1\n"
    "v_mul_u32_u24 v200, 0xaaaa, v200\n"
    "v_add_u32 v233, 0x4444, v200\n"
    "v_add_u32 v247, 150528, v241\n"
    "s_mul_i32 s62, s51, 2304\n"
    "s_add_u32 s62, s62, 131072\n"
    "v_add_u32 v242, s62, v241\n"
    "s_add_u32 s62, s62, 2048\n"
    "v_lshl_add_u32 v243, v240, 2, s62\n"
    "v_and_b32 v210, 7, v240\n"
    "v_lshlrev_b32 v210, 7, v210\n"
    "s_lshl_b32 s84, s49, 10\n"
    "s_add_u32 s84, s84, 81984\n"
    "v_and_b32 v200, 31, v240\n"
    "s_lshr_b32 s62, s51, 1\n"
    "s_lshl_b32 s62, s62, 10\n"
    "s_and_b32 s63, s51, 1\n"
    "s_lshl3_add_u32 s62, s63, s62\n"
    "s_add_u32 s62, s62, 114688\n"
    "v_add_u32 v244, s62, v241\n"
    "v_lshlrev_b32 v200, 4, v200\n"
    "v_add_u32 v245, 149504, v200\n"
    "v_bfe_u32 v200, v240, 3, 2\n"
    "v_lshrrev_b32 v201, 5, v240\n"
    "v_lshl_or_b32 v200, v200, 1, v201\n"
    "v_and_b32 v201, 7, v240\n"
    "v_lshl_or_b32 v200, v200, 3, v201\n"
    "s_lshl2_add_u32 s62, s51, 150528\n"
    "v_lshl_add_u32 v246, v200, 4, s62\n"
    "v_and_b32 v250, 11, v240\n"
    "v_cmp_eq_u32 vcc, 0, v250\n"
    "v_mov_b32 v250, 0x3f803f80\n"
    "v_mov_b64 v[234:235], 0\n"
    "v_cndmask_b32 v250, 0, v250, vcc\n"
    "v_mov_b32 v251, v250\n"
    "v_mov_b64 v[32:33], v[250:251]\n"
    "v_mov_b64 v[34:35], v[250:251]\n"
    "v_mov_b64 v[36:37], v[250:251]\n"
    "v_mov_b64 v[38:39], v[250:251]\n"
    "v_mov_b64 v[40:41], v[250:251]\n"
    "v_mov_b64 v[42:43], v[250:251]\n"
    "v_mov_b64 v[44:45], v[250:251]\n"
    "v_mov_b64 v[46:47], v[250:251]\n"
    "v_mov_b64 v[48:49], v[250:251]\n"
    "v_mov_b64 v[50:51], v[250:251]\n"
    "v_mov_b64 v[52:53], v[250:251]\n"
    "v_mov_b64 v[54:55], v[250:251]\n"
    "v_mov_b64 v[56:57], v[250:251]\n"
    "v_mov_b64 v[58:59], v[250:251]\n"
    "v_mov_b64 v[60:61], v[250:251]\n"
    "v_mov_b64 v[62:63], v[250:251]\n"
    "v_mov_b64 v[64:65], v[250:251]\n"
    "v_mov_b64 v[66:67], v[250:251]\n"
    "v_mov_b64 v[68:69], v[250:251]\n"
    "v_mov_b64 v[70:71], v[250:251]\n"
    "v_mov_b64 v[72:73], v[250:251]\n"
    "v_mov_b64 v[74:75], v[250:251]\n"
    "v_mov_b64 v[76:77], v[250:251]\n"
    "v_mov_b64 v[78:79], v[250:251]\n"
    "v_mov_b64 v[80:81], v[250:251]\n"
    "v_mov_b64 v[82:83], v[250:251]\n"
    "v_mov_b64 v[84:85], v[250:251]\n"
    "v_mov_b64 v[86:87], v[250:251]\n"
    "v_mov_b64 v[88:89], v[250:251]\n"
    "v_mov_b64 v[90:91], v[250:251]\n"
    "v_mov_b64 v[92:93], v[250:251]\n"
    "v_mov_b64 v[94:95], v[250:251]\n"
    "v_mov_b64 v[152:153], 0\n"
    "v_mov_b64 v[128:129], 0\n"
    "v_mov_b64 v[154:155], 0\n"
    "v_mov_b64 v[130:131], 0\n"
    "v_mov_b64 v[156:157], 0\n"
    "v_mov_b64 v[132:133], 0\n"
    "v_mov_b64 v[158:159], 0\n"
    "v_mov_b64 v[134:135], 0\n"
    "v_mov_b64 v[160:161], 0\n"
    "v_mov_b64 v[136:137], 0\n"
    "v_mov_b64 v[162:163], 0\n"
    "v_mov_b64 v[138:139], 0\n"
    "v_mov_b64 v[164:165], 0\n"
    "v_mov_b64 v[140:141], 0\n"
    "v_mov_b64 v[166:167], 0\n"
    "v_mov_b64 v[142:143], 0\n"
    "v_mov_b32 v236, 0\n"
    "v_mov_b32 v238, 1.0\n"
    "s_lshl3_add_u32 s75, s50, s48\n"
    "s_lshl_b32 s75, s75, 13\n"
    "s_add_u32 s75, s75, 524288\n"
    "s_lshl_b32 s85, s51, 12\n"
    "s_add_u32 s62, s85, 131072\n"
    "v_add_u32 v248, s62, v241\n"
    "s_sleep 3\n"
    "s_add_u32 m0, s85, 130048\n"
    "buffer_load_dwordx4 v[0:3], v241, s[12:15], s70 offen offset:0 sc1\n"
    "buffer_load_dwordx4 v241, s[12:15], s70 offen offset:1024 sc1 lds\n"
    "s_add_u32 m0, s85, 129024\n"
    "buffer_load_dwordx4 v[4:7], v241, s[12:15], s70 offen offset:2048 sc1\n"
    "buffer_load_dwordx4 v241, s[12:15], s70 offen offset:3072 sc1 lds\n"
    "s_add_u32 m0, s85, 132096\n"
    "buffer_load_dwordx4 v[8:11], v241, s[12:15], s80 offen offset:0 sc1\n"
    "buffer_load_dwordx4 v241, s[12:15], s80 offen offset:1024 sc1 lds\n"
    "s_add_u32 m0, s85, 131072\n"
    "buffer_load_dwordx4 v[12:15], v241, s[12:15], s80 offen offset:2048 sc1\n"
    "buffer_load_dwordx4 v241, s[12:15], s80 offen offset:3072 sc1 lds\n"
    "s_waitcnt vmcnt(4)\n"
    "s_mov_b64 exec, s[88:89]\n"
    "ds_read_b128 v[32:35], v248 offset:0\n"
    "ds_read_b128 v[36:39], v248 offset:256\n"
    "ds_read_b128 v[40:43], v248 offset:1024\n"
    "ds_read_b128 v[44:47], v248 offset:1280\n"
    "s_mov_b64 exec, -1\n"
    "buffer_load_dwordx4 v[16:19], v241, s[12:15], s81 offen offset:0 sc1\n"
    "buffer_load_dwordx4 v[20:23], v241, s[12:15], s81 offen offset:2048 sc1\n"
    "buffer_load_dwordx4 v[24:27], v241, s[12:15], s82 offen offset:0 sc1\n"
    "buffer_load_dwordx4 v[28:31], v241, s[12:15], s82 offen offset:2048 sc1\n"
    "s_waitcnt vmcnt(4)\n"
    "s_mov_b64 exec, s[88:89]\n"
    "ds_read_b128 v[48:51], v248 offset:2048\n"
    "ds_read_b128 v[52:55], v248 offset:2304\n"
    "ds_read_b128 v[56:59], v248 offset:3072\n"
    "ds_read_b128 v[60:63], v248 offset:3328\n"
    "s_mov_b64 exec, -1\n"
    ".Lgc_20:\n"
    "s_waitcnt lgkmcnt(6)\n"
    "v_max3_u32 v239, v0, v32, v36\n"
    "s_waitcnt lgkmcnt(5)\n"
    "v_max3_u32 v239, v4, v40, v239\n"
    "s_waitcnt lgkmcnt(4)\n"
    "v_max3_u32 v239, v44, v8, v239\n"
    "s_waitcnt lgkmcnt(2)\n"
    "v_max3_u32 v239, v48, v52, v239\n"
    "s_waitcnt lgkmcnt(1)\n"
    "v_max3_u32 v239, v12, v56, v239\n"
    "s_waitcnt lgkmcnt(0)\n"
    "v_max_u32 v239, v60, v239\n"
    "v_cmp_eq_u32 vcc, -1, v239\n"
    "s_nop 0\n"
    "s_cbranch_vccnz .Lgr_21\n"
    ".Lgo_22:\n"
    "s_barrier\n"
    "s_setprio 2\n"
    "ds_read_b128 v[144:147], v247 offset:12288\n"
    "buffer_load_dword v211, v210, s[12:15], s84 offen sc1\n"
    "s_add_u32 m0, s83, 7168\n"
    "s_nop 0\n"
    "buffer_load_dwordx4 v241, s[12:15], s81 offen offset:1024 sc1 lds\n"
    "s_add_u32 m0, s83, 6144\n"
    "s_nop 0\n"
    "buffer_load_dwordx4 v241, s[12:15], s81 offen offset:3072 sc1 lds\n"
    "s_waitcnt lgkmcnt(0)\n"
    "v_mfma_f32_32x32x16_bf16 v[96:111], v[0:3], v[144:147], 0\n"
    "s_add_u32 m0, s83, 9216\n"
    "s_nop 0\n"
    "buffer_load_dwordx4 v241, s[12:15], s82 offen offset:1024 sc1 lds\n"
    "s_add_u32 m0, s83, 8192\n"
    "s_nop 0\n"
    "buffer_load_dwordx4 v241, s[12:15], s82 offen offset:3072 sc1 lds\n"
    "s_nop 5\n"
    "v_exp_f32 v96, v96\n"
    "v_exp_f32 v97, v97\n"
    "v_exp_f32 v98, v98\n"
    "v_exp_f32 v99, v99\n"
    "v_exp_f32 v100, v100\n"
    "v_mfma_f32_32x32x16_bf16 v[112:127], v[4:7], v[144:147], 0\n"
    "v_exp_f32 v101, v101\n"
    "v_exp_f32 v102, v102\n"
    "v_exp_f32 v103, v103\n"
    "v_exp_f32 v104, v104\n"
    "v_exp_f32 v105, v105\n"
    "v_smfmac_f32_32x32x32_bf16 v[128:143], v[32:35], v[96:103], v232\n"
    "v_exp_f32 v106, v106\n"
    "v_exp_f32 v107, v107\n"
    "v_exp_f32 v108, v108\n"
    "v_exp_f32 v109, v109\n"
    "v_exp_f32 v110, v110\n"
    "v_exp_f32 v111, v111\n"
    "v_exp_f32 v112, v112\n"
    "v_exp_f32 v113, v113\n"
    "v_smfmac_f32_32x32x32_bf16 v[128:143], v[36:39], v[104:111], v232\n"
    "v_exp_f32 v114, v114\n"
    "v_exp_f32 v115, v115\n"
    "v_exp_f32 v116, v116\n"
    "v_mfma_f32_32x32x16_bf16 v[96:111], v[8:11], v[144:147], 0\n"
    "v_exp_f32 v117, v117\n"
    "v_exp_f32 v118, v118\n"
    "v_exp_f32 v119, v119\n"
    "v_exp_f32 v120, v120\n"
    "v_exp_f32 v121, v121\n"
    "v_smfmac_f32_32x32x32_bf16 v[128:143], v[40:43], v[112:119], v232\n"
    ".Lgc_35:\n"
    "s_waitcnt vmcnt(4)\n"
    "v_cmp_ne_u32 vcc, s53, v211\n"
    "s_nop 0\n"
    "s_cbranch_vccnz .Lgr_36\n"
    ".Lgo_37:\n"
    "s_xor_b32 s72, s71, 1024\n"
    "buffer_load_dwordx4 v[148:151], v241, s[12:15], s72 offen sc1\n"
    "v_exp_f32 v122, v122\n"
    "v_exp_f32 v123, v123\n"
    "v_exp_f32 v124, v124\n"
    "v_exp_f32 v125, v125\n"
    "v_exp_f32 v126, v126\n"
    "v_exp_f32 v127, v127\n"
    "v_exp_f32 v96, v96\n"
    "v_exp_f32 v97, v97\n"
    "v_smfmac_f32_32x32x32_bf16 v[128:143], v[44:47], v[120:127], v232\n"
    "v_exp_f32 v98, v98\n"
    "v_exp_f32 v99, v99\n"
    "v_exp_f32 v100, v100\n"
    "v_mfma_f32_32x32x16_bf16 v[112:127], v[12:15], v[144:147], 0\n"
    "s_waitcnt vmcnt(3)\n"
    "s_mov_b64 exec, s[88:89]\n"
    "ds_read_b128 v[64:67], v249 offset:8192\n"
    "ds_read_b128 v[68:71], v249 offset:8448\n"
    "ds_read_b128 v[72:75], v249 offset:9216\n"
    "ds_read_b128 v[76:79], v249 offset:9472\n"
    "s_mov_b64 exec, -1\n"
    "v_exp_f32 v101, v101\n"
    "v_exp_f32 v102, v102\n"
    "v_exp_f32 v103, v103\n"
    "v_exp_f32 v104, v104\n"
    "v_exp_f32 v105, v105\n"
    "v_smfmac_f32_32x32x32_bf16 v[128:143], v[48:51], v[96:103], v232\n"
    "v_exp_f32 v106, v106\n"
    "v_exp_f32 v107, v107\n"
    "v_exp_f32 v108, v108\n"
    "v_exp_f32 v109, v109\n"
    "v_exp_f32 v110, v110\n"
    "v_exp_f32 v111, v111\n"
    "v_exp_f32 v112, v112\n"
    "v_exp_f32 v113, v113\n"
    "v_smfmac_f32_32x32x32_bf16 v[128:143], v[52:55], v[104:111], v232\n"
    ".Lgc_38:\n"
    "s_waitcnt lgkmcnt(2)\n"
    "v_max3_u32 v239, v16, v64, v68\n"
    "s_waitcnt lgkmcnt(1)\n"
    "v_max3_u32 v239, v20, v72, v239\n"
    "s_waitcnt lgkmcnt(0)\n"
    "v_max_u32 v239, v76, v239\n"
    "v_cmp_eq_u32 vcc, -1, v239\n"
    "s_nop 0\n"
    "s_cbranch_vccnz .Lgr_39\n"
    ".Lgo_40:\n"
    "v_exp_f32 v114, v114\n"
    "v_exp_f32 v115, v115\n"
    "v_exp_f32 v116, v116\n"
    "v_mfma_f32_32x32x16_bf16 v[96:111], v[16:19], v[144:147], 0\n"
    "s_waitcnt vmcnt(1)\n"
    "s_mov_b64 exec, s[88:89]\n"
    "ds_read_b128 v[80:83], v249 offset:10240\n"
    "ds_read_b128 v[84:87], v249 offset:10496\n"
    "ds_read_b128 v[88:91], v249 offset:11264\n"
    "ds_read_b128 v[92:95], v249 offset:11520\n"
    "s_mov_b64 exec, -1\n"
    "v_exp_f32 v117, v117\n"
    "v_exp_f32 v118, v118\n"
    "v_exp_f32 v119, v119\n"
    "v_exp_f32 v120, v120\n"
    "v_exp_f32 v121, v121\n"
    "v_smfmac_f32_32x32x32_bf16 v[128:143], v[56:59], v[112:119], v232\n"
    "v_exp_f32 v122, v122\n"
    "v_exp_f32 v123, v123\n"
    "v_exp_f32 v124, v124\n"
    "v_exp_f32 v125, v125\n"
    "v_exp_f32 v126, v126\n"
    "v_exp_f32 v127, v127\n"
    "v_exp_f32 v96, v96\n"
    "v_exp_f32 v97, v97\n"
    "s_nop 0\n"
    ".Lpad1297:\n"
    "s_cbranch_execz .Lpad4437\n"
    "v_smfmac_f32_32x32x32_bf16 v[128:143], v[60:63], v[120:127], v232\n"
    "v_exp_f32 v98, v98\n"
    "v_exp_f32 v99, v99\n"
    "v_exp_f32 v100, v100\n"
    "v_mfma_f32_32x32x16_bf16 v[112:127], v[20:23], v[144:147], 0\n"
    "v_exp_f32 v101, v101\n"
    "v_exp_f32 v102, v102\n"
    "v_exp_f32 v103, v103\n"
    "v_exp_f32 v104, v104\n"
    "v_exp_f32 v105, v105\n"
    "v_smfmac_f32_32x32x32_bf16 v[128:143], v[64:67], v[96:103], v232\n"
    "v_exp_f32 v106, v106\n"
    "v_exp_f32 v107, v107\n"
    "v_exp_f32 v108, v108\n"
    "v_exp_f32 v109, v109\n"
    "v_exp_f32 v110, v110\n"
    "v_exp_f32 v111, v111\n"
    "v_exp_f32 v112, v112\n"
    "v_exp_f32 v113, v113\n"
    "v_smfmac_f32_32x32x32_bf16 v[128:143], v[68:71], v[104:111], v232\n"
    ".Lgc_41:\n"
    "s_waitcnt lgkmcnt(2)\n"
    "v_max3_u32 v239, v24, v80, v84\n"
    "s_waitcnt lgkmcnt(1)\n"
    "v_max3_u32 v239, v28, v88, v239\n"
    "s_waitcnt lgkmcnt(0)\n"
    "v_max_u32 v239, v92, v239\n"
    "v_cmp_eq_u32 vcc, -1, v239\n"
    "s_nop 0\n"
    "s_cbranch_vccnz .Lgr_42\n"
    ".Lgo_43:\n"
    "v_exp_f32 v114, v114\n"
    "v_exp_f32 v115, v115\n"
    "v_exp_f32 v116, v116\n"
    "v_mfma_f32_32x32x16_bf16 v[96:111], v[24:27], v[144:147], 0\n"
    "v_exp_f32 v117, v117\n"
    "v_exp_f32 v118, v118\n"
    "v_exp_f32 v119, v119\n"
    "v_exp_f32 v120, v120\n"
    "v_exp_f32 v121, v121\n"
    "v_smfmac_f32_32x32x32_bf16 v[128:143], v[72:75], v[112:119], v232\n"
    "v_exp_f32 v122, v122\n"
    "v_exp_f32 v123, v123\n"
    "v_exp_f32 v124, v124\n"
    "v_exp_f32 v125, v125\n"
    "v_exp_f32 v126, v126\n"
    "v_exp_f32 v127, v127\n"
    "v_exp_f32 v96, v96\n"
    "v_exp_f32 v97, v97\n"
    "v_smfmac_f32_32x32x32_bf16 v[128:143], v[76:79], v[120:127], v232\n"
    "v_exp_f32 v98, v98\n"
    "v_exp_f32 v99, v99\n"
    "v_exp_f32 v100, v100\n"
    "v_mfma_f32_32x32x16_bf16 v[112:127], v[28:31], v[144:147], 0\n"
    "v_exp_f32 v101, v101\n"
    "v_exp_f32 v102, v102\n"
    "v_exp_f32 v103, v103\n"
    "v_exp_f32 v104, v104\n"
    "v_exp_f32 v105, v105\n"
    "v_smfmac_f32_32x32x32_bf16 v[128:143], v[80:83], v[96:103], v232\n"
    "v_exp_f32 v106, v106\n"
    "v_exp_f32 v107, v107\n"
    "v_exp_f32 v108, v108\n"
    "v_exp_f32 v109, v109\n"
    "v_exp_f32 v110, v110\n"
    "v_exp_f32 v111, v111\n"
    "v_exp_f32 v112, v112\n"
    "v_exp_f32 v113, v113\n"
    "v_smfmac_f32_32x32x32_bf16 v[128:143], v[84:87], v[104:111], v232\n"
    "v_exp_f32 v114, v114\n"
    "v_exp_f32 v115, v115\n"
    "v_exp_f32 v116, v116\n"
    ".Lgc_44:\n"
    "s_waitcnt vmcnt(0)\n"
    "v_cmp_eq_u32 vcc, -1, v148\n"
    "s_nop 0\n"
    "s_cbranch_vccnz .Lgr_45\n"
    ".Lgo_46:\n"
    "v_mfma_f32_32x32x16_bf16 v[96:111], v[0:3], v[148:151], 0\n"
    "v_exp_f32 v117, v117\n"
    "v_exp_f32 v118, v118\n"
    "v_exp_f32 v119, v119\n"
    "v_exp_f32 v120, v120\n"
    "v_exp_f32 v121, v121\n"
    "v_smfmac_f32_32x32x32_bf16 v[128:143], v[88:91], v[112:119], v232\n"
    "v_exp_f32 v122, v122\n"
    "v_exp_f32 v123, v123\n"
    "v_exp_f32 v124, v124\n"
    "v_exp_f32 v125, v125\n"
    "v_exp_f32 v126, v126\n"
    "v_exp_f32 v127, v127\n"
    "s_setprio 0\n"
    "v_exp_f32 v96, v96\n"
    "v_exp_f32 v97, v97\n"
    "v_smfmac_f32_32x32x32_bf16 v[128:143], v[92:95], v[120:127], v232\n"
    "s_xor_b32 s72, s71, 2048\n"
    "buffer_load_dwordx4 v[144:147], v241, s[12:15], s72 offen sc1\n"
    "v_exp_f32 v98, v98\n"
    "v_exp_f32 v99, v99\n"
    "v_exp_f32 v100, v100\n"
    "v_mfma_f32_32x32x16_bf16 v[112:127], v[4:7], v[148:151], 0\n"
    "v_exp_f32 v101, v101\n"
    "v_exp_f32 v102, v102\n"
    "v_exp_f32 v103, v103\n"
    "v_exp_f32 v104, v104\n"
    "v_exp_f32 v105, v105\n"
    "v_exp_f32 v106, v106\n"
    "v_exp_f32 v107, v107\n"
    "v_exp_f32 v108, v108\n"
    "ds_write_b128 v242, v[128:131] offset:0\n"
    "ds_write_b128 v242, v[132:135] offset:1024\n"
    "ds_write_b32 v243, v136 offset:0\n"
    "v_mfma_f32_32x32x8_bf16 v[128:143], v[234:235], v[234:235], 0\n"
    "v_exp_f32 v109, v109\n"
    "v_exp_f32 v110, v110\n"
    "v_exp_f32 v111, v111\n"
    "v_smfmac_f32_32x32x32_bf16 v[128:143], v[32:35], v[96:103], v232\n"
    "v_exp_f32 v112, v112\n"
    "v_exp_f32 v113, v113\n"
    "v_smfmac_f32_32x32x32_bf16 v[128:143], v[36:39], v[104:111], v232\n"
    "v_exp_f32 v114, v114\n"
    "v_exp_f32 v115, v115\n"
    "v_exp_f32 v116, v116\n"
    "v_mfma_f32_32x32x16_bf16 v[96:111], v[8:11], v[148:151], 0\n"
    "v_exp_f32 v117, v117\n"
    "v_exp_f32 v118, v118\n"
    "v_exp_f32 v119, v119\n"
    "v_exp_f32 v120, v120\n"
    "v_exp_f32 v121, v121\n"
    "v_smfmac_f32_32x32x32_bf16 v[128:143], v[40:43], v[112:119], v232\n"
    "v_exp_f32 v122, v122\n"
    "v_exp_f32 v123, v123\n"
    "v_exp_f32 v124, v124\n"
    "v_exp_f32 v125, v125\n"
    "v_exp_f32 v126, v126\n"
    "v_exp_f32 v127, v127\n"
    "s_waitcnt lgkmcnt(0)\n"
    "s_barrier\n"
    "s_setprio 2\n"
    "v_exp_f32 v96, v96\n"
    "v_exp_f32 v97, v97\n"
    "v_smfmac_f32_32x32x32_bf16 v[128:143], v[44:47], v[120:127], v232\n"
    "v_exp_f32 v98, v98\n"
    "v_exp_f32 v99, v99\n"
    "v_exp_f32 v100, v100\n"
    "v_mfma_f32_32x32x16_bf16 v[112:127], v[12:15], v[148:151], 0\n"
    "v_exp_f32 v101, v101\n"
    "v_exp_f32 v102, v102\n"
    "v_exp_f32 v103, v103\n"
    "v_exp_f32 v104, v104\n"
    "v_exp_f32 v105, v105\n"
    "v_smfmac_f32_32x32x32_bf16 v[128:143], v[48:51], v[96:103], v232\n"
    "v_exp_f32 v106, v106\n"
    "v_exp_f32 v107, v107\n"
    "v_exp_f32 v108, v108\n"
    "v_exp_f32 v109, v109\n"
    "v_exp_f32 v110, v110\n"
    "v_exp_f32 v111, v111\n"
    "v_exp_f32 v112, v112\n"
    "v_exp_f32 v113, v113\n"
    "v_smfmac_f32_32x32x32_bf16 v[128:143], v[52:55], v[104:111], v232\n"
    "v_exp_f32 v114, v114\n"
    "v_exp_f32 v115, v115\n"
    "v_exp_f32 v116, v116\n"
    "v_mfma_f32_32x32x16_bf16 v[96:111], v[16:19], v[148:151], 0\n"
    "v_exp_f32 v117, v117\n"
    "v_exp_f32 v118, v118\n"
    "v_exp_f32 v119, v119\n"
    "v_exp_f32 v120, v120\n"
    "v_exp_f32 v121, v121\n"
    "v_smfmac_f32_32x32x32_bf16 v[128:143], v[56:59], v[112:119], v232\n"
    "v_exp_f32 v122, v122\n"
    "v_exp_f32 v123, v123\n"
    "v_exp_f32 v124, v124\n"
    "v_exp_f32 v125, v125\n"
    "v_exp_f32 v126, v126\n"
    "v_exp_f32 v127, v127\n"
    "v_exp_f32 v96, v96\n"
    "v_exp_f32 v97, v97\n"
    "v_smfmac_f32_32x32x32_bf16 v[128:143], v[60:63], v[120:127], v232\n"
    "v_exp_f32 v98, v98\n"
    "v_exp_f32 v99, v99\n"
    "v_exp_f32 v100, v100\n"
    "v_mfma_f32_32x32x16_bf16 v[112:127], v[20:23], v[148:151], 0\n"
    "v_exp_f32 v101, v101\n"
    "v_exp_f32 v102, v102\n"
    "v_exp_f32 v103, v103\n"
    "v_exp_f32 v104, v104\n"
    "v_exp_f32 v105, v105\n"
    "v_smfmac_f32_32x32x32_bf16 v[128:143], v[64:67], v[96:103], v232\n"
    "s_lshl3_add_u32 s62, s55, s48\n"
    "s_lshl_b32 s62, s62, 10\n"
    "buffer_load_dwordx4 v[228:231], v241, s[16:19], s62 offen\n"
    "v_exp_f32 v106, v106\n"
    "v_exp_f32 v107, v107\n"
    "v_exp_f32 v108, v108\n"
    "v_exp_f32 v109, v109\n"
    "v_exp_f32 v110, v110\n"
    "v_exp_f32 v111, v111\n"
    "v_exp_f32 v112, v112\n"
    "v_exp_f32 v113, v113\n"
    "v_smfmac_f32_32x32x32_bf16 v[128:143], v[68:71], v[104:111], v232\n"
    "v_exp_f32 v114, v114\n"
    "v_exp_f32 v115, v115\n"
    "v_exp_f32 v116, v116\n"
    "v_mfma_f32_32x32x16_bf16 v[96:111], v[24:27], v[148:151], 0\n"
    "v_exp_f32 v117, v117\n"
    "v_exp_f32 v118, v118\n"
    "v_exp_f32 v119, v119\n"
    "v_exp_f32 v120, v120\n"
    "v_exp_f32 v121, v121\n"
    "v_smfmac_f32_32x32x32_bf16 v[128:143], v[72:75], v[112:119], v232\n"
    "v_exp_f32 v122, v122\n"
    "v_exp_f32 v123, v123\n"
    "v_exp_f32 v124, v124\n"
    "v_exp_f32 v125, v125\n"
    "v_exp_f32 v126, v126\n"
    "v_exp_f32 v127, v127\n"
    "v_exp_f32 v96, v96\n"
    "v_exp_f32 v97, v97\n"
    "v_smfmac_f32_32x32x32_bf16 v[128:143], v[76:79], v[120:127], v232\n"
    "v_exp_f32 v98, v98\n"
    "v_exp_f32 v99, v99\n"
    "v_exp_f32 v100, v100\n"
    "v_mfma_f32_32x32x16_bf16 v[112:127], v[28:31], v[148:151], 0\n"
    "v_exp_f32 v101, v101\n"
    "v_exp_f32 v102, v102\n"
    "v_exp_f32 v103, v103\n"
    "v_exp_f32 v104, v104\n"
    "v_exp_f32 v105, v105\n"
    "v_smfmac_f32_32x32x32_bf16 v[128:143], v[80:83], v[96:103], v232\n"
    "s_waitcnt vmcnt(1)\n"
    "v_cmp_eq_u32 vcc, -1, v144\n"
    "v_exp_f32 v106, v106\n"
    "v_exp_f32 v107, v107\n"
    "v_exp_f32 v108, v108\n"
    "v_exp_f32 v109, v109\n"
    "v_exp_f32 v110, v110\n"
    "v_exp_f32 v111, v111\n"
    "v_exp_f32 v112, v112\n"
    "v_exp_f32 v113, v113\n"
    "v_smfmac_f32_32x32x32_bf16 v[128:143], v[84:87], v[104:111], v232\n"
    "s_cbranch_vccnz .Lqr_23\n"
    ".Lqo_24:\n"
    "v_exp_f32 v114, v114\n"
    "v_exp_f32 v115, v115\n"
    "v_exp_f32 v116, v116\n"
    "v_mfma_f32_32x32x16_bf16 v[96:111], v[0:3], v[144:147], 0\n"
    "v_exp_f32 v117, v117\n"
    "v_exp_f32 v118, v118\n"
    "v_exp_f32 v119, v119\n"
    "v_exp_f32 v120, v120\n"
    "v_exp_f32 v121, v121\n"
    "v_smfmac_f32_32x32x32_bf16 v[128:143], v[88:91], v[112:119], v232\n"
    "v_exp_f32 v122, v122\n"
    "v_exp_f32 v123, v123\n"
    "v_exp_f32 v124, v124\n"
    "v_exp_f32 v125, v125\n"
    "v_exp_f32 v126, v126\n"
    "v_exp_f32 v127, v127\n"
    "s_setprio 0\n"
    "v_exp_f32 v96, v96\n"
    "v_exp_f32 v97, v97\n"
    "v_smfmac_f32_32x32x32_bf16 v[128:143], v[92:95], v[120:127], v232\n"
    "s_xor_b32 s72, s71, 3072\n"
    "buffer_load_dwordx4 v[148:151], v241, s[12:15], s72 offen sc1\n"
    "v_exp_f32 v98, v98\n"
    "v_exp_f32 v99, v99\n"
    "v_exp_f32 v100, v100\n"
    "v_mfma_f32_32x32x16_bf16 v[112:127], v[4:7], v[144:147], 0\n"
    "v_exp_f32 v101, v101\n"
    "v_exp_f32 v102, v102\n"
    "v_exp_f32 v103, v103\n"
    "v_exp_f32 v104, v104\n"
    "v_exp_f32 v105, v105\n"
    "v_exp_f32 v106, v106\n"
    "v_exp_f32 v107, v107\n"
    "v_exp_f32 v108, v108\n"
    "ds_write_b128 v242, v[128:131] offset:9216\n"
    "ds_write_b128 v242, v[132:135] offset:10240\n"
    "ds_write_b32 v243, v136 offset:9216\n"
    "v_mfma_f32_32x32x8_bf16 v[128:143], v[234:235], v[234:235], 0\n"
    "v_exp_f32 v109, v109\n"
    "v_exp_f32 v110, v110\n"
    "v_exp_f32 v111, v111\n"
    "v_smfmac_f32_32x32x32_bf16 v[128:143], v[32:35], v[96:103], v232\n"
    "v_exp_f32 v112, v112\n"
    "v_exp_f32 v113, v113\n"
    "v_smfmac_f32_32x32x32_bf16 v[128:143], v[36:39], v[104:111], v232\n"
    "v_exp_f32 v114, v114\n"
    "v_exp_f32 v115, v115\n"
    "v_exp_f32 v116, v116\n"
    "v_mfma_f32_32x32x16_bf16 v[96:111], v[8:11], v[144:147], 0\n"
    "v_exp_f32 v117, v117\n"
    "v_exp_f32 v118, v118\n"
    "v_exp_f32 v119, v119\n"
    "v_exp_f32 v120, v120\n"
    "v_exp_f32 v121, v121\n"
    "v_smfmac_f32_32x32x32_bf16 v[128:143], v[40:43], v[112:119], v232\n"
    "v_exp_f32 v122, v122\n"
    "v_exp_f32 v123, v123\n"
    "v_exp_f32 v124, v124\n"
    "v_exp_f32 v125, v125\n"
    "v_exp_f32 v126, v126\n"
    "v_exp_f32 v127, v127\n"
    "s_waitcnt lgkmcnt(0)\n"
    "s_barrier\n"
    "s_setprio 2\n"
    "v_exp_f32 v96, v96\n"
    "v_exp_f32 v97, v97\n"
    "v_smfmac_f32_32x32x32_bf16 v[128:143], v[44:47], v[120:127], v232\n"
    "v_exp_f32 v98, v98\n"
    "v_exp_f32 v99, v99\n"
    "v_exp_f32 v100, v100\n"
    "v_mfma_f32_32x32x16_bf16 v[112:127], v[12:15], v[144:147], 0\n"
    "ds_read_b128 v[208:211], v245 offset:0\n"
    "ds_read_b64 v[200:201], v244 offset:0\n"
    "ds_read_b64 v[202:203], v244 offset:2048\n"
    "ds_read_b64 v[204:205], v244 offset:4096\n"
    "ds_read_b64 v[206:207], v244 offset:6144\n"
    "v_exp_f32 v101, v101\n"
    "v_exp_f32 v102, v102\n"
    "v_exp_f32 v103, v103\n"
    "v_exp_f32 v104, v104\n"
    "v_exp_f32 v105, v105\n"
    "v_smfmac_f32_32x32x32_bf16 v[128:143], v[48:51], v[96:103], v232\n"
    "v_exp_f32 v106, v106\n"
    "v_exp_f32 v107, v107\n"
    "v_exp_f32 v108, v108\n"
    "v_exp_f32 v109, v109\n"
    "v_exp_f32 v110, v110\n"
    "v_exp_f32 v111, v111\n"
    "v_exp_f32 v112, v112\n"
    "v_exp_f32 v113, v113\n"
    "v_smfmac_f32_32x32x32_bf16 v[128:143], v[52:55], v[104:111], v232\n"
    "v_exp_f32 v114, v114\n"
    "v_exp_f32 v115, v115\n"
    "v_exp_f32 v116, v116\n"
    "v_mfma_f32_32x32x16_bf16 v[96:111], v[16:19], v[144:147], 0\n"
    "s_waitcnt lgkmcnt(4)\n"
    "v_add_f32 v208, v208, v209\n"
    "v_add_f32 v210, v210, v211\n"
    "s_waitcnt lgkmcnt(2)\n"
    "v_add_f32 v200, v200, v202\n"
    "v_add_f32 v208, v208, v210\n"
    "v_add_f32 v201, v201, v203\n"
    "v_rcp_f32 v209, v208\n"
    "s_xor_b32 s62, s75, 458752\n"
    "v_exp_f32 v117, v117\n"
    "v_exp_f32 v118, v118\n"
    "v_exp_f32 v119, v119\n"
    "v_exp_f32 v120, v120\n"
    "v_exp_f32 v121, v121\n"
    "v_smfmac_f32_32x32x32_bf16 v[128:143], v[56:59], v[112:119], v232\n"
    "s_waitcnt lgkmcnt(0)\n"
    "v_add_f32 v204, v204, v206\n"
    "v_add_f32 v205, v205, v207\n"
    "v_add_f32 v200, v200, v204\n"
    "v_add_f32 v201, v201, v205\n"
    "v_mul_f32 v200, v200, v209\n"
    "v_mul_f32 v201, v201, v209\n"
    "s_xor_b32 s73, s75, 0\n"
    "s_xor_b32 s74, s75, 4096\n"
    "v_exp_f32 v122, v122\n"
    "v_exp_f32 v123, v123\n"
    "v_exp_f32 v124, v124\n"
    "v_exp_f32 v125, v125\n"
    "v_exp_f32 v126, v126\n"
    "v_exp_f32 v127, v127\n"
    "buffer_load_dwordx4 v[168:171], v241, s[12:15], s73 offen offset:0 sc1\n"
    "v_exp_f32 v96, v96\n"
    "v_exp_f32 v97, v97\n"
    "v_smfmac_f32_32x32x32_bf16 v[128:143], v[60:63], v[120:127], v232\n"
    "buffer_load_dwordx4 v[172:175], v241, s[12:15], s73 offen offset:1024 sc1\n"
    "v_exp_f32 v98, v98\n"
    "v_exp_f32 v99, v99\n"
    "v_exp_f32 v100, v100\n"
    "v_mfma_f32_32x32x16_bf16 v[112:127], v[20:23], v[144:147], 0\n"
    "v_max3_f32 v238, v238, v208, v209\n"
    "v_cvt_pk_bf16_f32 v202, v200, v201\n"
    "ds_write_b32 v246, v202 offset:0\n"
    "v_fmac_f32 v236, v200, v201\n"
    "v_exp_f32 v101, v101\n"
    "v_exp_f32 v102, v102\n"
    "v_exp_f32 v103, v103\n"
    "v_exp_f32 v104, v104\n"
    "v_exp_f32 v105, v105\n"
    "v_smfmac_f32_32x32x32_bf16 v[128:143], v[64:67], v[96:103], v232\n"
    "buffer_load_dwordx4 v[212:215], v241, s[12:15], s62 offen offset:0 sc1\n"
    "v_exp_f32 v106, v106\n"
    "v_exp_f32 v107, v107\n"
    "v_exp_f32 v108, v108\n"
    "v_exp_f32 v109, v109\n"
    "v_exp_f32 v110, v110\n"
    "v_exp_f32 v111, v111\n"
    "buffer_load_dwordx4 v[176:179], v241, s[12:15], s73 offen offset:2048 sc1\n"
    "v_exp_f32 v112, v112\n"
    "v_exp_f32 v113, v113\n"
    "v_smfmac_f32_32x32x32_bf16 v[128:143], v[68:71], v[104:111], v232\n"
    "buffer_load_dwordx4 v[180:183], v241, s[12:15], s73 offen offset:3072 sc1\n"
    "v_exp_f32 v114, v114\n"
    "v_exp_f32 v115, v115\n"
    "v_exp_f32 v116, v116\n"
    "v_mfma_f32_32x32x16_bf16 v[96:111], v[24:27], v[144:147], 0\n"
    "v_exp_f32 v117, v117\n"
    "v_exp_f32 v118, v118\n"
    "v_exp_f32 v119, v119\n"
    "v_exp_f32 v120, v120\n"
    "v_exp_f32 v121, v121\n"
    "v_smfmac_f32_32x32x32_bf16 v[128:143], v[72:75], v[112:119], v232\n"
    "buffer_load_dwordx4 v[216:219], v241, s[12:15], s62 offen offset:1024 sc1\n"
    "v_exp_f32 v122, v122\n"
    "v_exp_f32 v123, v123\n"
    "v_exp_f32 v124, v124\n"
    "v_exp_f32 v125, v125\n"
    "v_exp_f32 v126, v126\n"
    "v_exp_f32 v127, v127\n"
    "buffer_load_dwordx4 v[184:187], v241, s[12:15], s74 offen offset:0 sc1\n"
    "v_exp_f32 v96, v96\n"
    "v_exp_f32 v97, v97\n"
    "v_smfmac_f32_32x32x32_bf16 v[128:143], v[76:79], v[120:127], v232\n"
    "buffer_load_dwordx4 v[188:191], v241, s[12:15], s74 offen offset:1024 sc1\n"
    "v_exp_f32 v98, v98\n"
    "v_exp_f32 v99, v99\n"
    "v_exp_f32 v100, v100\n"
    "v_mfma_f32_32x32x16_bf16 v[112:127], v[28:31], v[144:147], 0\n"
    "v_exp_f32 v101, v101\n"
    "v_exp_f32 v102, v102\n"
    "v_exp_f32 v103, v103\n"
    "v_exp_f32 v104, v104\n"
    "v_exp_f32 v105, v105\n"
    "v_smfmac_f32_32x32x32_bf16 v[128:143], v[80:83], v[96:103], v232\n"
    "buffer_load_dwordx4 v[220:223], v241, s[12:15], s62 offen offset:2048 sc1\n"
    "s_waitcnt vmcnt(9)\n"
    "v_cmp_eq_u32 vcc, -1, v148\n"
    "v_exp_f32 v106, v106\n"
    "v_exp_f32 v107, v107\n"
    "v_exp_f32 v108, v108\n"
    "v_exp_f32 v109, v109\n"
    "v_exp_f32 v110, v110\n"
    "v_exp_f32 v111, v111\n"
    "buffer_load_dwordx4 v[192:195], v241, s[12:15], s74 offen offset:2048 sc1\n"
    "v_exp_f32 v112, v112\n"
    "v_exp_f32 v113, v113\n"
    "v_smfmac_f32_32x32x32_bf16 v[128:143], v[84:87], v[104:111], v232\n"
    "buffer_load_dwordx4 v[196:199], v241, s[12:15], s74 offen offset:3072 sc1\n"
    "s_cbranch_vccnz .Lqr_25\n"
    ".Lqo_26:\n"
    "v_exp_f32 v114, v114\n"
    "v_exp_f32 v115, v115\n"
    "v_exp_f32 v116, v116\n"
    "v_mfma_f32_32x32x16_bf16 v[96:111], v[0:3], v[148:151], 0\n"
    "v_exp_f32 v117, v117\n"
    "v_exp_f32 v118, v118\n"
    "v_exp_f32 v119, v119\n"
    "v_exp_f32 v120, v120\n"
    "v_exp_f32 v121, v121\n"
    "v_smfmac_f32_32x32x32_bf16 v[128:143], v[88:91], v[112:119], v232\n"
    "buffer_load_dwordx4 v[224:227], v241, s[12:15], s62 offen offset:3072 sc1\n"
    "v_exp_f32 v122, v122\n"
    "v_exp_f32 v123, v123\n"
    "v_exp_f32 v124, v124\n"
    "v_exp_f32 v125, v125\n"
    "v_exp_f32 v126, v126\n"
    "v_exp_f32 v127, v127\n"
    "s_setprio 0\n"
    "v_exp_f32 v96, v96\n"
    "v_exp_f32 v97, v97\n"
    "v_smfmac_f32_32x32x32_bf16 v[128:143], v[92:95], v[120:127], v232\n"
    "s_xor_b32 s72, s71, 4096\n"
    "buffer_load_dwordx4 v[144:147], v241, s[12:15], s72 offen sc1\n"
    "v_exp_f32 v98, v98\n"
    "v_exp_f32 v99, v99\n"
    "v_exp_f32 v100, v100\n"
    "v_mfma_f32_32x32x16_bf16 v[112:127], v[4:7], v[148:151], 0\n"
    "v_exp_f32 v101, v101\n"
    "v_exp_f32 v102, v102\n"
    "v_exp_f32 v103, v103\n"
    "v_exp_f32 v104, v104\n"
    "v_exp_f32 v105, v105\n"
    "v_exp_f32 v106, v106\n"
    "v_exp_f32 v107, v107\n"
    "v_exp_f32 v108, v108\n"
    "ds_write_b128 v242, v[128:131] offset:0\n"
    "ds_write_b128 v242, v[132:135] offset:1024\n"
    "ds_write_b32 v243, v136 offset:0\n"
    "v_mfma_f32_32x32x8_bf16 v[128:143], v[234:235], v[234:235], 0\n"
    "v_exp_f32 v109, v109\n"
    "v_exp_f32 v110, v110\n"
    "v_exp_f32 v111, v111\n"
    "v_smfmac_f32_32x32x32_bf16 v[128:143], v[32:35], v[96:103], v232\n"
    "v_exp_f32 v112, v112\n"
    "v_exp_f32 v113, v113\n"
    "v_smfmac_f32_32x32x32_bf16 v[128:143], v[36:39], v[104:111], v232\n"
    "v_exp_f32 v114, v114\n"
    "v_exp_f32 v115, v115\n"
    "v_exp_f32 v116, v116\n"
    "v_mfma_f32_32x32x16_bf16 v[96:111], v[8:11], v[148:151], 0\n"
    "v_exp_f32 v117, v117\n"
    "v_exp_f32 v118, v118\n"
    "v_exp_f32 v119, v119\n"
    "v_exp_f32 v120, v120\n"
    "v_exp_f32 v121, v121\n"
    "v_smfmac_f32_32x32x32_bf16 v[128:143], v[40:43], v[112:119], v232\n"
    "v_exp_f32 v122, v122\n"
    "v_exp_f32 v123, v123\n"
    "v_exp_f32 v124, v124\n"
    "v_exp_f32 v125, v125\n"
    "v_exp_f32 v126, v126\n"
    "v_exp_f32 v127, v127\n"
    "s_waitcnt lgkmcnt(0)\n"
    "s_barrier\n"
    "s_setprio 2\n"
    "v_exp_f32 v96, v96\n"
    "v_exp_f32 v97, v97\n"
    "v_smfmac_f32_32x32x32_bf16 v[128:143], v[44:47], v[120:127], v232\n"
    "ds_read_b128 v[250:253], v247 offset:0\n"
    "v_exp_f32 v98, v98\n"
    "v_exp_f32 v99, v99\n"
    "v_exp_f32 v100, v100\n"
    "v_mfma_f32_32x32x16_bf16 v[112:127], v[12:15], v[148:151], 0\n"
    "ds_read_b128 v[208:211], v245 offset:512\n"
    "ds_read_b64 v[200:201], v244 offset:8192\n"
    "ds_read_b64 v[202:203], v244 offset:10240\n"
    "ds_read_b64 v[204:205], v244 offset:12288\n"
    "ds_read_b64 v[206:207], v244 offset:14336\n"
    "v_exp_f32 v101, v101\n"
    "v_exp_f32 v102, v102\n"
    "v_exp_f32 v103, v103\n"
    "v_exp_f32 v104, v104\n"
    "v_exp_f32 v105, v105\n"
    "v_smfmac_f32_32x32x32_bf16 v[128:143], v[48:51], v[96:103], v232\n"
    "v_exp_f32 v106, v106\n"
    "v_exp_f32 v107, v107\n"
    "v_exp_f32 v108, v108\n"
    "v_exp_f32 v109, v109\n"
    "v_exp_f32 v110, v110\n"
    "v_exp_f32 v111, v111\n"
    "v_exp_f32 v112, v112\n"
    "v_exp_f32 v113, v113\n"
    "v_smfmac_f32_32x32x32_bf16 v[128:143], v[52:55], v[104:111], v232\n"
    "v_exp_f32 v114, v114\n"
    "v_exp_f32 v115, v115\n"
    "v_exp_f32 v116, v116\n"
    "v_mfma_f32_32x32x16_bf16 v[96:111], v[16:19], v[148:151], 0\n"
    "s_waitcnt lgkmcnt(4)\n"
    "v_add_f32 v208, v208, v209\n"
    "v_add_f32 v210, v210, v211\n"
    "s_waitcnt lgkmcnt(2)\n"
    "v_add_f32 v200, v200, v202\n"
    "v_add_f32 v208, v208, v210\n"
    "v_add_f32 v201, v201, v203\n"
    "v_rcp_f32 v209, v208\n"
    "v_exp_f32 v117, v117\n"
    "v_exp_f32 v118, v118\n"
    "v_exp_f32 v119, v119\n"
    "v_exp_f32 v120, v120\n"
    "v_exp_f32 v121, v121\n"
    "v_smfmac_f32_32x32x32_bf16 v[128:143], v[56:59], v[112:119], v232\n"
    "s_waitcnt lgkmcnt(0)\n"
    "v_add_f32 v204, v204, v206\n"
    "v_add_f32 v205, v205, v207\n"
    "v_add_f32 v200, v200, v204\n"
    "v_add_f32 v201, v201, v205\n"
    "v_mul_f32 v200, v200, v209\n"
    "v_mul_f32 v201, v201, v209\n"
    "s_xor_b32 s73, s75, 65536\n"
    "s_xor_b32 s74, s75, 69632\n"
    "v_exp_f32 v122, v122\n"
    "v_exp_f32 v123, v123\n"
    "v_exp_f32 v124, v124\n"
    "v_exp_f32 v125, v125\n"
    "v_exp_f32 v126, v126\n"
    "v_exp_f32 v127, v127\n"
    "s_waitcnt vmcnt(11)\n"
    "v_smfmac_f32_16x16x64_bf16 v[152:155], v[250:253], v[168:175], v233\n"
    "buffer_load_dwordx4 v[168:171], v241, s[12:15], s73 offen offset:0 sc1\n"
    "v_exp_f32 v96, v96\n"
    "v_exp_f32 v97, v97\n"
    "v_smfmac_f32_32x32x32_bf16 v[128:143], v[60:63], v[120:127], v232\n"
    "buffer_load_dwordx4 v[172:175], v241, s[12:15], s73 offen offset:1024 sc1\n"
    "v_exp_f32 v98, v98\n"
    "v_exp_f32 v99, v99\n"
    "v_exp_f32 v100, v100\n"
    "v_mfma_f32_32x32x16_bf16 v[112:127], v[20:23], v[148:151], 0\n"
    "v_max3_f32 v238, v238, v208, v209\n"
    "v_cvt_pk_bf16_f32 v202, v200, v201\n"
    "ds_write_b32 v246, v202 offset:1024\n"
    "v_fmac_f32 v236, v200, v201\n"
    "v_exp_f32 v101, v101\n"
    "v_exp_f32 v102, v102\n"
    "v_exp_f32 v103, v103\n"
    "v_exp_f32 v104, v104\n"
    "v_exp_f32 v105, v105\n"
    "v_smfmac_f32_32x32x32_bf16 v[128:143], v[64:67], v[96:103], v232\n"
    "v_exp_f32 v106, v106\n"
    "v_exp_f32 v107, v107\n"
    "v_exp_f32 v108, v108\n"
    "v_exp_f32 v109, v109\n"
    "v_exp_f32 v110, v110\n"
    "v_exp_f32 v111, v111\n"
    "s_waitcnt vmcnt(10)\n"
    "v_smfmac_f32_16x16x64_bf16 v[156:159], v[250:253], v[176:183], v233\n"
    "buffer_load_dwordx4 v[176:179], v241, s[12:15], s73 offen offset:2048 sc1\n"
    "v_exp_f32 v112, v112\n"
    "v_exp_f32 v113, v113\n"
    "v_smfmac_f32_32x32x32_bf16 v[128:143], v[68:71], v[104:111], v232\n"
    "buffer_load_dwordx4 v[180:183], v241, s[12:15], s73 offen offset:3072 sc1\n"
    "v_exp_f32 v114, v114\n"
    "v_exp_f32 v115, v115\n"
    "v_exp_f32 v116, v116\n"
    "v_mfma_f32_32x32x16_bf16 v[96:111], v[24:27], v[148:151], 0\n"
    "v_exp_f32 v117, v117\n"
    "v_exp_f32 v118, v118\n"
    "v_exp_f32 v119, v119\n"
    "v_exp_f32 v120, v120\n"
    "v_exp_f32 v121, v121\n"
    "v_smfmac_f32_32x32x32_bf16 v[128:143], v[72:75], v[112:119], v232\n"
    "v_exp_f32 v122, v122\n"
    "v_exp_f32 v123, v123\n"
    "v_exp_f32 v124, v124\n"
    "v_exp_f32 v125, v125\n"
    "v_exp_f32 v126, v126\n"
    "v_exp_f32 v127, v127\n"
    "s_waitcnt vmcnt(9)\n"
    "v_smfmac_f32_16x16x64_bf16 v[160:163], v[250:253], v[184:191], v233\n"
    "buffer_load_dwordx4 v[184:187], v241, s[12:15], s74 offen offset:0 sc1\n"
    "v_exp_f32 v96, v96\n"
    "v_exp_f32 v97, v97\n"
    "v_smfmac_f32_32x32x32_bf16 v[128:143], v[76:79], v[120:127], v232\n"
    "buffer_load_dwordx4 v[188:191], v241, s[12:15], s74 offen offset:1024 sc1\n"
    "v_exp_f32 v98, v98\n"
    "v_exp_f32 v99, v99\n"
    "v_exp_f32 v100, v100\n"
    "v_mfma_f32_32x32x16_bf16 v[112:127], v[28:31], v[148:151], 0\n"
    "v_exp_f32 v101, v101\n"
    "v_exp_f32 v102, v102\n"
    "v_exp_f32 v103, v103\n"
    "v_exp_f32 v104, v104\n"
    "v_exp_f32 v105, v105\n"
    "v_smfmac_f32_32x32x32_bf16 v[128:143], v[80:83], v[96:103], v232\n"
    "s_waitcnt vmcnt(6)\n"
    "v_cmp_eq_u32 vcc, -1, v144\n"
    "v_exp_f32 v106, v106\n"
    "v_exp_f32 v107, v107\n"
    "v_exp_f32 v108, v108\n"
    "v_exp_f32 v109, v109\n"
    "v_exp_f32 v110, v110\n"
    "v_exp_f32 v111, v111\n"
    "v_smfmac_f32_16x16x64_bf16 v[164:167], v[250:253], v[192:199], v233\n"
    "buffer_load_dwordx4 v[192:195], v241, s[12:15], s74 offen offset:2048 sc1\n"
    "v_exp_f32 v112, v112\n"
    "v_exp_f32 v113, v113\n"
    "v_smfmac_f32_32x32x32_bf16 v[128:143], v[84:87], v[104:111], v232\n"
    "buffer_load_dwordx4 v[196:199], v241, s[12:15], s74 offen offset:3072 sc1\n"
    "s_cbranch_vccnz .Lqr_27\n"
    ".Lqo_28:\n"
    "v_exp_f32 v114, v114\n"
    "v_exp_f32 v115, v115\n"
    "v_exp_f32 v116, v116\n"
    "v_mfma_f32_32x32x16_bf16 v[96:111], v[0:3], v[144:147], 0\n"
    "v_exp_f32 v117, v117\n"
    "v_exp_f32 v118, v118\n"
    "v_exp_f32 v119, v119\n"
    "v_exp_f32 v120, v120\n"
    "v_exp_f32 v121, v121\n"
    "v_smfmac_f32_32x32x32_bf16 v[128:143], v[88:91], v[112:119], v232\n"
    "v_exp_f32 v122, v122\n"
    "v_exp_f32 v123, v123\n"
    "v_exp_f32 v124, v124\n"
    "v_exp_f32 v125, v125\n"
    "v_exp_f32 v126, v126\n"
    "v_exp_f32 v127, v127\n"
    "s_setprio 0\n"
    "v_exp_f32 v96, v96\n"
    "v_exp_f32 v97, v97\n"
    "v_smfmac_f32_32x32x32_bf16 v[128:143], v[92:95], v[120:127], v232\n"
    "s_xor_b32 s72, s71, 5120\n"
    "buffer_load_dwordx4 v[148:151], v241, s[12:15], s72 offen sc1\n"
    "v_exp_f32 v98, v98\n"
    "v_exp_f32 v99, v99\n"
    "v_exp_f32 v100, v100\n"
    "v_mfma_f32_32x32x16_bf16 v[112:127], v[4:7], v[144:147], 0\n"
    "v_exp_f32 v101, v101\n"
    "v_exp_f32 v102, v102\n"
    "v_exp_f32 v103, v103\n"
    "v_exp_f32 v104, v104\n"
    "v_exp_f32 v105, v105\n"
    "v_exp_f32 v106, v106\n"
    "v_exp_f32 v107, v107\n"
    "v_exp_f32 v108, v108\n"
    "ds_write_b128 v242, v[128:131] offset:9216\n"
    "ds_write_b128 v242, v[132:135] offset:10240\n"
    "ds_write_b32 v243, v136 offset:9216\n"
    "v_mfma_f32_32x32x8_bf16 v[128:143], v[234:235], v[234:235], 0\n"
    "v_exp_f32 v109, v109\n"
    "v_exp_f32 v110, v110\n"
    "v_exp_f32 v111, v111\n"
    "v_smfmac_f32_32x32x32_bf16 v[128:143], v[32:35], v[96:103], v232\n"
    "v_exp_f32 v112, v112\n"
    "v_exp_f32 v113, v113\n"
    "v_smfmac_f32_32x32x32_bf16 v[128:143], v[36:39], v[104:111], v232\n"
    "v_exp_f32 v114, v114\n"
    "v_exp_f32 v115, v115\n"
    "v_exp_f32 v116, v116\n"
    "v_mfma_f32_32x32x16_bf16 v[96:111], v[8:11], v[144:147], 0\n"
    "v_exp_f32 v117, v117\n"
    "v_exp_f32 v118, v118\n"
    "v_exp_f32 v119, v119\n"
    "v_exp_f32 v120, v120\n"
    "v_exp_f32 v121, v121\n"
    "v_smfmac_f32_32x32x32_bf16 v[128:143], v[40:43], v[112:119], v232\n"
    "v_exp_f32 v122, v122\n"
    "v_exp_f32 v123, v123\n"
    "v_exp_f32 v124, v124\n"
    "v_exp_f32 v125, v125\n"
    "v_exp_f32 v126, v126\n"
    "v_exp_f32 v127, v127\n"
    "s_waitcnt lgkmcnt(0)\n"
    "s_barrier\n"
    "s_setprio 2\n"
    "v_exp_f32 v96, v96\n"
    "v_exp_f32 v97, v97\n"
    "v_smfmac_f32_32x32x32_bf16 v[128:143], v[44:47], v[120:127], v232\n"
    "ds_read_b128 v[250:253], v247 offset:1024\n"
    "v_exp_f32 v98, v98\n"
    "v_exp_f32 v99, v99\n"
    "v_exp_f32 v100, v100\n"
    "v_mfma_f32_32x32x16_bf16 v[112:127], v[12:15], v[144:147], 0\n"
    "ds_read_b128 v[208:211], v245 offset:0\n"
    "ds_read_b64 v[200:201], v244 offset:0\n"
    "ds_read_b64 v[202:203], v244 offset:2048\n"
    "ds_read_b64 v[204:205], v244 offset:4096\n"
    "ds_read_b64 v[206:207], v244 offset:6144\n"
    "v_exp_f32 v101, v101\n"
    "v_exp_f32 v102, v102\n"
    "v_exp_f32 v103, v103\n"
    "v_exp_f32 v104, v104\n"
    "v_exp_f32 v105, v105\n"
    "v_smfmac_f32_32x32x32_bf16 v[128:143], v[48:51], v[96:103], v232\n"
    "v_exp_f32 v106, v106\n"
    "v_exp_f32 v107, v107\n"
    "v_exp_f32 v108, v108\n"
    "v_exp_f32 v109, v109\n"
    "v_exp_f32 v110, v110\n"
    "v_exp_f32 v111, v111\n"
    "v_exp_f32 v112, v112\n"
    "v_exp_f32 v113, v113\n"
    "v_smfmac_f32_32x32x32_bf16 v[128:143], v[52:55], v[104:111], v232\n"
    "v_exp_f32 v114, v114\n"
    "v_exp_f32 v115, v115\n"
    "v_exp_f32 v116, v116\n"
    "v_mfma_f32_32x32x16_bf16 v[96:111], v[16:19], v[144:147], 0\n"
    "s_waitcnt lgkmcnt(4)\n"
    "v_add_f32 v208, v208, v209\n"
    "v_add_f32 v210, v210, v211\n"
    "s_waitcnt lgkmcnt(2)\n"
    "v_add_f32 v200, v200, v202\n"
    "v_add_f32 v208, v208, v210\n"
    "v_add_f32 v201, v201, v203\n"
    "v_rcp_f32 v209, v208\n"
    "v_exp_f32 v117, v117\n"
    "v_exp_f32 v118, v118\n"
    "v_exp_f32 v119, v119\n"
    "v_exp_f32 v120, v120\n"
    "v_exp_f32 v121, v121\n"
    "v_smfmac_f32_32x32x32_bf16 v[128:143], v[56:59], v[112:119], v232\n"
    "s_waitcnt lgkmcnt(0)\n"
    "v_add_f32 v204, v204, v206\n"
    "v_add_f32 v205, v205, v207\n"
    "v_add_f32 v200, v200, v204\n"
    "v_add_f32 v201, v201, v205\n"
    "v_mul_f32 v200, v200, v209\n"
    "v_mul_f32 v201, v201, v209\n"
    "s_xor_b32 s73, s75, 131072\n"
    "s_xor_b32 s74, s75, 135168\n"
    "v_exp_f32 v122, v122\n"
    "v_exp_f32 v123, v123\n"
    "v_exp_f32 v124, v124\n"
    "v_exp_f32 v125, v125\n"
    "v_exp_f32 v126, v126\n"
    "v_exp_f32 v127, v127\n"
    "s_waitcnt vmcnt(7)\n"
    "v_smfmac_f32_16x16x64_bf16 v[152:155], v[250:253], v[168:175], v233\n"
    "buffer_load_dwordx4 v[168:171], v241, s[12:15], s73 offen offset:0 sc1\n"
    "v_exp_f32 v96, v96\n"
    "v_exp_f32 v97, v97\n"
    "v_smfmac_f32_32x32x32_bf16 v[128:143], v[60:63], v[120:127], v232\n"
    "buffer_load_dwordx4 v[172:175], v241, s[12:15], s73 offen offset:1024 sc1\n"
    "v_exp_f32 v98, v98\n"
    "v_exp_f32 v99, v99\n"
    "v_exp_f32 v100, v100\n"
    "v_mfma_f32_32x32x16_bf16 v[112:127], v[20:23], v[144:147], 0\n"
    "v_max3_f32 v238, v238, v208, v209\n"
    "v_cvt_pk_bf16_f32 v202, v200, v201\n"
    "ds_write_b32 v246, v202 offset:0\n"
    "v_fmac_f32 v236, v200, v201\n"
    "v_exp_f32 v101, v101\n"
    "v_exp_f32 v102, v102\n"
    "v_exp_f32 v103, v103\n"
    "v_exp_f32 v104, v104\n"
    "v_exp_f32 v105, v105\n"
    "v_smfmac_f32_32x32x32_bf16 v[128:143], v[64:67], v[96:103], v232\n"
    "s_add_u32 s67, s56, s51\n"
    "s_lshl_b32 s67, s67, 15\n"
    "s_add_u32 s67, s67, 3145728\n"
    "s_mul_i32 s68, s51, 12288\n"
    "s_add_u32 m0, s68, 65536\n"
    "s_nop 0\n"
    "buffer_load_dwordx4 v241, s[12:15], s67 offen offset:0 sc1 lds\n"
    "v_exp_f32 v106, v106\n"
    "v_exp_f32 v107, v107\n"
    "v_exp_f32 v108, v108\n"
    "v_exp_f32 v109, v109\n"
    "v_exp_f32 v110, v110\n"
    "v_exp_f32 v111, v111\n"
    "s_waitcnt vmcnt(8)\n"
    "v_smfmac_f32_16x16x64_bf16 v[156:159], v[250:253], v[176:183], v233\n"
    "buffer_load_dwordx4 v[176:179], v241, s[12:15], s73 offen offset:2048 sc1\n"
    "v_exp_f32 v112, v112\n"
    "v_exp_f32 v113, v113\n"
    "v_smfmac_f32_32x32x32_bf16 v[128:143], v[68:71], v[104:111], v232\n"
    "buffer_load_dwordx4 v[180:183], v241, s[12:15], s73 offen offset:3072 sc1\n"
    "v_exp_f32 v114, v114\n"
    "v_exp_f32 v115, v115\n"
    "v_exp_f32 v116, v116\n"
    "v_mfma_f32_32x32x16_bf16 v[96:111], v[24:27], v[144:147], 0\n"
    "v_exp_f32 v117, v117\n"
    "v_exp_f32 v118, v118\n"
    "v_exp_f32 v119, v119\n"
    "v_exp_f32 v120, v120\n"
    "v_exp_f32 v121, v121\n"
    "v_smfmac_f32_32x32x32_bf16 v[128:143], v[72:75], v[112:119], v232\n"
    "buffer_load_dwordx4 v241, s[12:15], s67 offen offset:1024 sc1 lds\n"
    ".Lpad2132:\n"
    "s_cbranch_execz .Lpad4703\n"
    "v_exp_f32 v122, v122\n"
    "v_exp_f32 v123, v123\n"
    "v_exp_f32 v124, v124\n"
    "v_exp_f32 v125, v125\n"
    "v_exp_f32 v126, v126\n"
    "v_exp_f32 v127, v127\n"
    "s_waitcnt vmcnt(9)\n"
    "v_smfmac_f32_16x16x64_bf16 v[160:163], v[250:253], v[184:191], v233\n"
    "buffer_load_dwordx4 v[184:187], v241, s[12:15], s74 offen offset:0 sc1\n"
    "v_exp_f32 v96, v96\n"
    "v_exp_f32 v97, v97\n"
    "v_smfmac_f32_32x32x32_bf16 v[128:143], v[76:79], v[120:127], v232\n"
    "buffer_load_dwordx4 v[188:191], v241, s[12:15], s74 offen offset:1024 sc1\n"
    "v_exp_f32 v98, v98\n"
    "v_exp_f32 v99, v99\n"
    "v_exp_f32 v100, v100\n"
    "v_mfma_f32_32x32x16_bf16 v[112:127], v[28:31], v[144:147], 0\n"
    "v_exp_f32 v101, v101\n"
    "v_exp_f32 v102, v102\n"
    "v_exp_f32 v103, v103\n"
    "v_exp_f32 v104, v104\n"
    "v_exp_f32 v105, v105\n"
    "v_smfmac_f32_32x32x32_bf16 v[128:143], v[80:83], v[96:103], v232\n"
    "buffer_load_dwordx4 v241, s[12:15], s67 offen offset:2048 sc1 lds\n"
    "s_waitcnt vmcnt(9)\n"
    "v_cmp_eq_u32 vcc, -1, v148\n"
    "v_exp_f32 v106, v106\n"
    "v_exp_f32 v107, v107\n"
    "v_exp_f32 v108, v108\n"
    "v_exp_f32 v109, v109\n"
    "v_exp_f32 v110, v110\n"
    "v_exp_f32 v111, v111\n"
    "v_smfmac_f32_16x16x64_bf16 v[164:167], v[250:253], v[192:199], v233\n"
    "buffer_load_dwordx4 v[192:195], v241, s[12:15], s74 offen offset:2048 sc1\n"
    "v_exp_f32 v112, v112\n"
    "v_exp_f32 v113, v113\n"
    "v_smfmac_f32_32x32x32_bf16 v[128:143], v[84:87], v[104:111], v232\n"
    "buffer_load_dwordx4 v[196:199], v241, s[12:15], s74 offen offset:3072 sc1\n"
    "s_cbranch_vccnz .Lqr_29\n"
    ".Lqo_30:\n"
    "v_exp_f32 v114, v114\n"
    "v_exp_f32 v115, v115\n"
    "v_exp_f32 v116, v116\n"
    "v_mfma_f32_32x32x16_bf16 v[96:111], v[0:3], v[148:151], 0\n"
    "v_exp_f32 v117, v117\n"
    "v_exp_f32 v118, v118\n"
    "v_exp_f32 v119, v119\n"
    "v_exp_f32 v120, v120\n"
    "v_exp_f32 v121, v121\n"
    "v_smfmac_f32_32x32x32_bf16 v[128:143], v[88:91], v[112:119], v232\n"
    "buffer_load_dwordx4 v241, s[12:15], s67 offen offset:3072 sc1 lds\n"
    "v_exp_f32 v122, v122\n"
    "v_exp_f32 v123, v123\n"
    "v_exp_f32 v124, v124\n"
    "v_exp_f32 v125, v125\n"
    "v_exp_f32 v126, v126\n"
    "v_exp_f32 v127, v127\n"
    "s_setprio 0\n"
    "v_exp_f32 v96, v96\n"
    "v_exp_f32 v97, v97\n"
    "v_smfmac_f32_32x32x32_bf16 v[128:143], v[92:95], v[120:127], v232\n"
    "s_xor_b32 s72, s71, 6144\n"
    "buffer_load_dwordx4 v[144:147], v241, s[12:15], s72 offen sc1\n"
    "v_exp_f32 v98, v98\n"
    "v_exp_f32 v99, v99\n"
    "v_exp_f32 v100, v100\n"
    "v_mfma_f32_32x32x16_bf16 v[112:127], v[4:7], v[148:151], 0\n"
    "v_exp_f32 v101, v101\n"
    "v_exp_f32 v102, v102\n"
    "v_exp_f32 v103, v103\n"
    "v_exp_f32 v104, v104\n"
    "v_exp_f32 v105, v105\n"
    "v_exp_f32 v106, v106\n"
    "v_exp_f32 v107, v107\n"
    "v_exp_f32 v108, v108\n"
    "ds_write_b128 v242, v[128:131] offset:0\n"
    "ds_write_b128 v242, v[132:135] offset:1024\n"
    "ds_write_b32 v243, v136 offset:0\n"
    "v_mfma_f32_32x32x8_bf16 v[128:143], v[234:235], v[234:235], 0\n"
    "v_exp_f32 v109, v109\n"
    "v_exp_f32 v110, v110\n"
    "v_exp_f32 v111, v111\n"
    "v_smfmac_f32_32x32x32_bf16 v[128:143], v[32:35], v[96:103], v232\n"
    "v_exp_f32 v112, v112\n"
    "v_exp_f32 v113, v113\n"
    "v_smfmac_f32_32x32x32_bf16 v[128:143], v[36:39], v[104:111], v232\n"
    "v_exp_f32 v114, v114\n"
    "v_exp_f32 v115, v115\n"
    "v_exp_f32 v116, v116\n"
    "v_mfma_f32_32x32x16_bf16 v[96:111], v[8:11], v[148:151], 0\n"
    "v_exp_f32 v117, v117\n"
    "v_exp_f32 v118, v118\n"
    "v_exp_f32 v119, v119\n"
    "v_exp_f32 v120, v120\n"
    "v_exp_f32 v121, v121\n"
    "v_smfmac_f32_32x32x32_bf16 v[128:143], v[40:43], v[112:119], v232\n"
    "v_exp_f32 v122, v122\n"
    "v_exp_f32 v123, v123\n"
    "v_exp_f32 v124, v124\n"
    "v_exp_f32 v125, v125\n"
    "v_exp_f32 v126, v126\n"
    "v_exp_f32 v127, v127\n"
    "s_waitcnt lgkmcnt(0)\n"
    "s_barrier\n"
    "s_setprio 2\n"
    "v_exp_f32 v96, v96\n"
    "v_exp_f32 v97, v97\n"
    "v_smfmac_f32_32x32x32_bf16 v[128:143], v[44:47], v[120:127], v232\n"
    "ds_read_b128 v[250:253], v247 offset:0\n"
    "v_exp_f32 v98, v98\n"
    "v_exp_f32 v99, v99\n"
    "v_exp_f32 v100, v100\n"
    "v_mfma_f32_32x32x16_bf16 v[112:127], v[12:15], v[148:151], 0\n"
    "ds_read_b128 v[208:211], v245 offset:512\n"
    "ds_read_b64 v[200:201], v244 offset:8192\n"
    "ds_read_b64 v[202:203], v244 offset:10240\n"
    "ds_read_b64 v[204:205], v244 offset:12288\n"
    "ds_read_b64 v[206:207], v244 offset:14336\n"
    "v_exp_f32 v101, v101\n"
    "v_exp_f32 v102, v102\n"
    "v_exp_f32 v103, v103\n"
    "v_exp_f32 v104, v104\n"
    "v_exp_f32 v105, v105\n"
    "v_smfmac_f32_32x32x32_bf16 v[128:143], v[48:51], v[96:103], v232\n"
    "v_exp_f32 v106, v106\n"
    "v_exp_f32 v107, v107\n"
    "v_exp_f32 v108, v108\n"
    "v_exp_f32 v109, v109\n"
    "v_exp_f32 v110, v110\n"
    "v_exp_f32 v111, v111\n"
    "v_exp_f32 v112, v112\n"
    "v_exp_f32 v113, v113\n"
    "v_smfmac_f32_32x32x32_bf16 v[128:143], v[52:55], v[104:111], v232\n"
    "v_exp_f32 v114, v114\n"
    "v_exp_f32 v115, v115\n"
    "v_exp_f32 v116, v116\n"
    "v_mfma_f32_32x32x16_bf16 v[96:111], v[16:19], v[148:151], 0\n"
    "s_waitcnt lgkmcnt(4)\n"
    "v_add_f32 v208, v208, v209\n"
    "v_add_f32 v210, v210, v211\n"
    "s_waitcnt lgkmcnt(2)\n"
    "v_add_f32 v200, v200, v202\n"
    "v_add_f32 v208, v208, v210\n"
    "v_add_f32 v201, v201, v203\n"
    "v_rcp_f32 v209, v208\n"
    "v_exp_f32 v117, v117\n"
    "v_exp_f32 v118, v118\n"
    "v_exp_f32 v119, v119\n"
    "v_exp_f32 v120, v120\n"
    "v_exp_f32 v121, v121\n"
    "v_smfmac_f32_32x32x32_bf16 v[128:143], v[56:59], v[112:119], v232\n"
    "s_waitcnt lgkmcnt(0)\n"
    "v_add_f32 v204, v204, v206\n"
    "v_add_f32 v205, v205, v207\n"
    "v_add_f32 v200, v200, v204\n"
    "v_add_f32 v201, v201, v205\n"
    "v_mul_f32 v200, v200, v209\n"
    "v_mul_f32 v201, v201, v209\n"
    "s_xor_b32 s73, s75, 196608\n"
    "s_xor_b32 s74, s75, 200704\n"
    "v_exp_f32 v122, v122\n"
    "v_exp_f32 v123, v123\n"
    "v_exp_f32 v124, v124\n"
    "v_exp_f32 v125, v125\n"
    "v_exp_f32 v126, v126\n"
    "v_exp_f32 v127, v127\n"
    "s_waitcnt vmcnt(11)\n"
    "v_smfmac_f32_16x16x64_bf16 v[152:155], v[250:253], v[168:175], v233\n"
    "buffer_load_dwordx4 v[168:171], v241, s[12:15], s73 offen offset:0 sc1\n"
    "v_exp_f32 v96, v96\n"
    "v_exp_f32 v97, v97\n"
    "v_smfmac_f32_32x32x32_bf16 v[128:143], v[60:63], v[120:127], v232\n"
    "buffer_load_dwordx4 v[172:175], v241, s[12:15], s73 offen offset:1024 sc1\n"
    "v_exp_f32 v98, v98\n"
    "v_exp_f32 v99, v99\n"
    "v_exp_f32 v100, v100\n"
    "v_mfma_f32_32x32x16_bf16 v[112:127], v[20:23], v[148:151], 0\n"
    "v_max3_f32 v238, v238, v208, v209\n"
    "v_cvt_pk_bf16_f32 v202, v200, v201\n"
    "ds_write_b32 v246, v202 offset:1024\n"
    "v_fmac_f32 v236, v200, v201\n"
    "v_exp_f32 v101, v101\n"
    "v_exp_f32 v102, v102\n"
    "v_exp_f32 v103, v103\n"
    "v_exp_f32 v104, v104\n"
    "v_exp_f32 v105, v105\n"
    "v_smfmac_f32_32x32x32_bf16 v[128:143], v[64:67], v[96:103], v232\n"
    "s_add_u32 s67, s67, 4096\n"
    "s_add_u32 m0, m0, 4096\n"
    "s_nop 0\n"
    "buffer_load_dwordx4 v241, s[12:15], s67 offen offset:0 sc1 lds\n"
    "v_exp_f32 v106, v106\n"
    "v_exp_f32 v107, v107\n"
    "v_exp_f32 v108, v108\n"
    "v_exp_f32 v109, v109\n"
    "v_exp_f32 v110, v110\n"
    "v_exp_f32 v111, v111\n"
    "s_waitcnt vmcnt(11)\n"
    "v_smfmac_f32_16x16x64_bf16 v[156:159], v[250:253], v[176:183], v233\n"
    "buffer_load_dwordx4 v[176:179], v241, s[12:15], s73 offen offset:2048 sc1\n"
    "v_exp_f32 v112, v112\n"
    "v_exp_f32 v113, v113\n"
    "v_smfmac_f32_32x32x32_bf16 v[128:143], v[68:71], v[104:111], v232\n"
    "buffer_load_dwordx4 v[180:183], v241, s[12:15], s73 offen offset:3072 sc1\n"
    "v_exp_f32 v114, v114\n"
    "v_exp_f32 v115, v115\n"
    "v_exp_f32 v116, v116\n"
    "v_mfma_f32_32x32x16_bf16 v[96:111], v[24:27], v[148:151], 0\n"
    "v_exp_f32 v117, v117\n"
    "v_exp_f32 v118, v118\n"
    "v_exp_f32 v119, v119\n"
    "v_exp_f32 v120, v120\n"
    "v_exp_f32 v121, v121\n"
    "v_smfmac_f32_32x32x32_bf16 v[128:143], v[72:75], v[112:119], v232\n"
    "buffer_load_dwordx4 v241, s[12:15], s67 offen offset:1024 sc1 lds\n"
    "v_exp_f32 v122, v122\n"
    "v_exp_f32 v123, v123\n"
    "v_exp_f32 v124, v124\n"
    "v_exp_f32 v125, v125\n"
    "v_exp_f32 v126, v126\n"
    "v_exp_f32 v127, v127\n"
    "s_waitcnt vmcnt(11)\n"
    "v_smfmac_f32_16x16x64_bf16 v[160:163], v[250:253], v[184:191], v233\n"
    "buffer_load_dwordx4 v[184:187], v241, s[12:15], s74 offen offset:0 sc1\n"
    "v_exp_f32 v96, v96\n"
    "v_exp_f32 v97, v97\n"
    "v_smfmac_f32_32x32x32_bf16 v[128:143], v[76:79], v[120:127], v232\n"
    "buffer_load_dwordx4 v[188:191], v241, s[12:15], s74 offen offset:1024 sc1\n"
    "v_exp_f32 v98, v98\n"
    "v_exp_f32 v99, v99\n"
    "v_exp_f32 v100, v100\n"
    "v_mfma_f32_32x32x16_bf16 v[112:127], v[28:31], v[148:151], 0\n"
    "v_exp_f32 v101, v101\n"
    "v_exp_f32 v102, v102\n"
    "v_exp_f32 v103, v103\n"
    "v_exp_f32 v104, v104\n"
    "v_exp_f32 v105, v105\n"
    "v_smfmac_f32_32x32x32_bf16 v[128:143], v[80:83], v[96:103], v232\n"
    "buffer_load_dwordx4 v241, s[12:15], s67 offen offset:2048 sc1 lds\n"
    "s_waitcnt vmcnt(9)\n"
    "v_cmp_eq_u32 vcc, -1, v144\n"
    "v_exp_f32 v106, v106\n"
    "v_exp_f32 v107, v107\n"
    "v_exp_f32 v108, v108\n"
    "v_exp_f32 v109, v109\n"
    "v_exp_f32 v110, v110\n"
    "v_exp_f32 v111, v111\n"
    "v_smfmac_f32_16x16x64_bf16 v[164:167], v[250:253], v[192:199], v233\n"
    "buffer_load_dwordx4 v[192:195], v241, s[12:15], s74 offen offset:2048 sc1\n"
    "v_exp_f32 v112, v112\n"
    "v_exp_f32 v113, v113\n"
    "v_smfmac_f32_32x32x32_bf16 v[128:143], v[84:87], v[104:111], v232\n"
    "buffer_load_dwordx4 v[196:199], v241, s[12:15], s74 offen offset:3072 sc1\n"
    "s_cbranch_vccnz .Lqr_31\n"
    ".Lqo_32:\n"
    "v_exp_f32 v114, v114\n"
    "v_exp_f32 v115, v115\n"
    "v_exp_f32 v116, v116\n"
    "v_mfma_f32_32x32x16_bf16 v[96:111], v[0:3], v[144:147], 0\n"
    "v_exp_f32 v117, v117\n"
    "v_exp_f32 v118, v118\n"
    "v_exp_f32 v119, v119\n"
    "v_exp_f32 v120, v120\n"
    "v_exp_f32 v121, v121\n"
    "v_smfmac_f32_32x32x32_bf16 v[128:143], v[88:91], v[112:119], v232\n"
    "buffer_load_dwordx4 v241, s[12:15], s67 offen offset:3072 sc1 lds\n"
    "v_exp_f32 v122, v122\n"
    "v_exp_f32 v123, v123\n"
    "v_exp_f32 v124, v124\n"
    "v_exp_f32 v125, v125\n"
    "v_exp_f32 v126, v126\n"
    "v_exp_f32 v127, v127\n"
    "s_setprio 0\n"
    "v_exp_f32 v96, v96\n"
    "v_exp_f32 v97, v97\n"
    "v_smfmac_f32_32x32x32_bf16 v[128:143], v[92:95], v[120:127], v232\n"
    "s_xor_b32 s72, s71, 7168\n"
    "buffer_load_dwordx4 v[148:151], v241, s[12:15], s72 offen sc1\n"
    "v_exp_f32 v98, v98\n"
    "v_exp_f32 v99, v99\n"
    "v_exp_f32 v100, v100\n"
    "v_mfma_f32_32x32x16_bf16 v[112:127], v[4:7], v[144:147], 0\n"
    "v_exp_f32 v101, v101\n"
    "v_exp_f32 v102, v102\n"
    "v_exp_f32 v103, v103\n"
    "v_exp_f32 v104, v104\n"
    "v_exp_f32 v105, v105\n"
    "v_exp_f32 v106, v106\n"
    "v_exp_f32 v107, v107\n"
    "v_exp_f32 v108, v108\n"
    "ds_write_b128 v242, v[128:131] offset:9216\n"
    "ds_write_b128 v242, v[132:135] offset:10240\n"
    "ds_write_b32 v243, v136 offset:9216\n"
    "v_mfma_f32_32x32x8_bf16 v[128:143], v[234:235], v[234:235], 0\n"
    "v_exp_f32 v109, v109\n"
    "v_exp_f32 v110, v110\n"
    "v_exp_f32 v111, v111\n"
    "v_smfmac_f32_32x32x32_bf16 v[128:143], v[32:35], v[96:103], v232\n"
    "v_exp_f32 v112, v112\n"
    "v_exp_f32 v113, v113\n"
    "v_smfmac_f32_32x32x32_bf16 v[128:143], v[36:39], v[104:111], v232\n"
    "v_exp_f32 v114, v114\n"
    "v_exp_f32 v115, v115\n"
    "v_exp_f32 v116, v116\n"
    "v_mfma_f32_32x32x16_bf16 v[96:111], v[8:11], v[144:147], 0\n"
    "v_exp_f32 v117, v117\n"
    "v_exp_f32 v118, v118\n"
    "v_exp_f32 v119, v119\n"
    "v_exp_f32 v120, v120\n"
    "v_exp_f32 v121, v121\n"
    "v_smfmac_f32_32x32x32_bf16 v[128:143], v[40:43], v[112:119], v232\n"
    "v_exp_f32 v122, v122\n"
    "v_exp_f32 v123, v123\n"
    "v_exp_f32 v124, v124\n"
    "v_exp_f32 v125, v125\n"
    "v_exp_f32 v126, v126\n"
    "v_exp_f32 v127, v127\n"
    "s_waitcnt lgkmcnt(0)\n"
    "s_barrier\n"
    "s_setprio 2\n"
    "v_exp_f32 v96, v96\n"
    "v_exp_f32 v97, v97\n"
    "v_smfmac_f32_32x32x32_bf16 v[128:143], v[44:47], v[120:127], v232\n"
    "ds_read_b128 v[250:253], v247 offset:1024\n"
    "v_exp_f32 v98, v98\n"
    "v_exp_f32 v99, v99\n"
    "v_exp_f32 v100, v100\n"
    "v_mfma_f32_32x32x16_bf16 v[112:127], v[12:15], v[144:147], 0\n"
    "ds_read_b128 v[208:211], v245 offset:0\n"
    "ds_read_b64 v[200:201], v244 offset:0\n"
    "ds_read_b64 v[202:203], v244 offset:2048\n"
    "ds_read_b64 v[204:205], v244 offset:4096\n"
    "ds_read_b64 v[206:207], v244 offset:6144\n"
    "v_exp_f32 v101, v101\n"
    "v_exp_f32 v102, v102\n"
    "v_exp_f32 v103, v103\n"
    "v_exp_f32 v104, v104\n"
    "v_exp_f32 v105, v105\n"
    "v_smfmac_f32_32x32x32_bf16 v[128:143], v[48:51], v[96:103], v232\n"
    "v_exp_f32 v106, v106\n"
    "v_exp_f32 v107, v107\n"
    "v_exp_f32 v108, v108\n"
    "v_exp_f32 v109, v109\n"
    "v_exp_f32 v110, v110\n"
    "v_exp_f32 v111, v111\n"
    "v_exp_f32 v112, v112\n"
    "v_exp_f32 v113, v113\n"
    "v_smfmac_f32_32x32x32_bf16 v[128:143], v[52:55], v[104:111], v232\n"
    "v_exp_f32 v114, v114\n"
    "v_exp_f32 v115, v115\n"
    "v_exp_f32 v116, v116\n"
    "v_mfma_f32_32x32x16_bf16 v[96:111], v[16:19], v[144:147], 0\n"
    "s_waitcnt lgkmcnt(4)\n"
    "v_add_f32 v208, v208, v209\n"
    "v_add_f32 v210, v210, v211\n"
    "s_waitcnt lgkmcnt(2)\n"
    "v_add_f32 v200, v200, v202\n"
    "v_add_f32 v208, v208, v210\n"
    "v_add_f32 v201, v201, v203\n"
    "v_rcp_f32 v209, v208\n"
    "v_exp_f32 v117, v117\n"
    "v_exp_f32 v118, v118\n"
    "v_exp_f32 v119, v119\n"
    "v_exp_f32 v120, v120\n"
    "v_exp_f32 v121, v121\n"
    "v_smfmac_f32_32x32x32_bf16 v[128:143], v[56:59], v[112:119], v232\n"
    "s_waitcnt lgkmcnt(0)\n"
    "v_add_f32 v204, v204, v206\n"
    "v_add_f32 v205, v205, v207\n"
    "v_add_f32 v200, v200, v204\n"
    "v_add_f32 v201, v201, v205\n"
    "v_mul_f32 v200, v200, v209\n"
    "v_mul_f32 v201, v201, v209\n"
    "s_xor_b32 s73, s75, 262144\n"
    "s_xor_b32 s74, s75, 266240\n"
    "v_exp_f32 v122, v122\n"
    "v_exp_f32 v123, v123\n"
    "v_exp_f32 v124, v124\n"
    "v_exp_f32 v125, v125\n"
    "v_exp_f32 v126, v126\n"
    "v_exp_f32 v127, v127\n"
    "s_waitcnt vmcnt(11)\n"
    "v_smfmac_f32_16x16x64_bf16 v[152:155], v[250:253], v[168:175], v233\n"
    "buffer_load_dwordx4 v[168:171], v241, s[12:15], s73 offen offset:0 sc1\n"
    "v_exp_f32 v96, v96\n"
    "v_exp_f32 v97, v97\n"
    "v_smfmac_f32_32x32x32_bf16 v[128:143], v[60:63], v[120:127], v232\n"
    "buffer_load_dwordx4 v[172:175], v241, s[12:15], s73 offen offset:1024 sc1\n"
    "v_exp_f32 v98, v98\n"
    "v_exp_f32 v99, v99\n"
    "v_exp_f32 v100, v100\n"
    "v_mfma_f32_32x32x16_bf16 v[112:127], v[20:23], v[144:147], 0\n"
    "v_max3_f32 v238, v238, v208, v209\n"
    "v_cvt_pk_bf16_f32 v202, v200, v201\n"
    "ds_write_b32 v246, v202 offset:0\n"
    "v_fmac_f32 v236, v200, v201\n"
    "v_exp_f32 v101, v101\n"
    "v_exp_f32 v102, v102\n"
    "v_exp_f32 v103, v103\n"
    "v_exp_f32 v104, v104\n"
    "v_exp_f32 v105, v105\n"
    "v_smfmac_f32_32x32x32_bf16 v[128:143], v[64:67], v[96:103], v232\n"
    "s_add_u32 s67, s67, 4096\n"
    "s_add_u32 m0, m0, 4096\n"
    "s_nop 0\n"
    "buffer_load_dwordx4 v241, s[12:15], s67 offen offset:0 sc1 lds\n"
    "v_exp_f32 v106, v106\n"
    "v_exp_f32 v107, v107\n"
    "v_exp_f32 v108, v108\n"
    "v_exp_f32 v109, v109\n"
    "v_exp_f32 v110, v110\n"
    "v_exp_f32 v111, v111\n"
    "s_waitcnt vmcnt(11)\n"
    "v_smfmac_f32_16x16x64_bf16 v[156:159], v[250:253], v[176:183], v233\n"
    "buffer_load_dwordx4 v[176:179], v241, s[12:15], s73 offen offset:2048 sc1\n"
    "v_exp_f32 v112, v112\n"
    "v_exp_f32 v113, v113\n"
    "v_smfmac_f32_32x32x32_bf16 v[128:143], v[68:71], v[104:111], v232\n"
    "buffer_load_dwordx4 v[180:183], v241, s[12:15], s73 offen offset:3072 sc1\n"
    "v_exp_f32 v114, v114\n"
    "v_exp_f32 v115, v115\n"
    "v_exp_f32 v116, v116\n"
    "v_mfma_f32_32x32x16_bf16 v[96:111], v[24:27], v[144:147], 0\n"
    "v_exp_f32 v117, v117\n"
    "v_exp_f32 v118, v118\n"
    "v_exp_f32 v119, v119\n"
    "v_exp_f32 v120, v120\n"
    "v_exp_f32 v121, v121\n"
    "v_smfmac_f32_32x32x32_bf16 v[128:143], v[72:75], v[112:119], v232\n"
    "buffer_load_dwordx4 v241, s[12:15], s67 offen offset:1024 sc1 lds\n"
    "v_exp_f32 v122, v122\n"
    "v_exp_f32 v123, v123\n"
    "v_exp_f32 v124, v124\n"
    "v_exp_f32 v125, v125\n"
    "v_exp_f32 v126, v126\n"
    "v_exp_f32 v127, v127\n"
    "s_waitcnt vmcnt(11)\n"
    "v_smfmac_f32_16x16x64_bf16 v[160:163], v[250:253], v[184:191], v233\n"
    "buffer_load_dwordx4 v[184:187], v241, s[12:15], s74 offen offset:0 sc1\n"
    "v_exp_f32 v96, v96\n"
    "v_exp_f32 v97, v97\n"
    "v_smfmac_f32_32x32x32_bf16 v[128:143], v[76:79], v[120:127], v232\n"
    "buffer_load_dwordx4 v[188:191], v241, s[12:15], s74 offen offset:1024 sc1\n"
    "v_exp_f32 v98, v98\n"
    "v_exp_f32 v99, v99\n"
    "v_exp_f32 v100, v100\n"
    "v_mfma_f32_32x32x16_bf16 v[112:127], v[28:31], v[144:147], 0\n"
    "v_exp_f32 v101, v101\n"
    "v_exp_f32 v102, v102\n"
    "v_exp_f32 v103, v103\n"
    "v_exp_f32 v104, v104\n"
    "v_exp_f32 v105, v105\n"
    "v_smfmac_f32_32x32x32_bf16 v[128:143], v[80:83], v[96:103], v232\n"
    "buffer_load_dwordx4 v241, s[12:15], s67 offen offset:2048 sc1 lds\n"
    "s_waitcnt vmcnt(9)\n"
    "v_cmp_eq_u32 vcc, -1, v148\n"
    "v_exp_f32 v106, v106\n"
    "v_exp_f32 v107, v107\n"
    "v_exp_f32 v108, v108\n"
    "v_exp_f32 v109, v109\n"
    "v_exp_f32 v110, v110\n"
    "v_exp_f32 v111, v111\n"
    "v_smfmac_f32_16x16x64_bf16 v[164:167], v[250:253], v[192:199], v233\n"
    "buffer_load_dwordx4 v[192:195], v241, s[12:15], s74 offen offset:2048 sc1\n"
    "v_exp_f32 v112, v112\n"
    "v_exp_f32 v113, v113\n"
    "v_smfmac_f32_32x32x32_bf16 v[128:143], v[84:87], v[104:111], v232\n"
    "buffer_load_dwordx4 v[196:199], v241, s[12:15], s74 offen offset:3072 sc1\n"
    "s_cbranch_vccnz .Lqr_33\n"
    ".Lqo_34:\n"
    "v_exp_f32 v114, v114\n"
    "v_exp_f32 v115, v115\n"
    "v_exp_f32 v116, v116\n"
    "v_mfma_f32_32x32x16_bf16 v[96:111], v[0:3], v[148:151], 0\n"
    "v_exp_f32 v117, v117\n"
    "v_exp_f32 v118, v118\n"
    "v_exp_f32 v119, v119\n"
    "v_exp_f32 v120, v120\n"
    "v_exp_f32 v121, v121\n"
    "v_smfmac_f32_32x32x32_bf16 v[128:143], v[88:91], v[112:119], v232\n"
    "buffer_load_dwordx4 v241, s[12:15], s67 offen offset:3072 sc1 lds\n"
    "v_exp_f32 v122, v122\n"
    "v_exp_f32 v123, v123\n"
    "v_exp_f32 v124, v124\n"
    "v_exp_f32 v125, v125\n"
    "v_exp_f32 v126, v126\n"
    "v_exp_f32 v127, v127\n"
    "s_setprio 0\n"
    "v_exp_f32 v96, v96\n"
    "v_exp_f32 v97, v97\n"
    "v_smfmac_f32_32x32x32_bf16 v[128:143], v[92:95], v[120:127], v232\n"
    "v_exp_f32 v98, v98\n"
    "v_exp_f32 v99, v99\n"
    "v_exp_f32 v100, v100\n"
    "v_mfma_f32_32x32x16_bf16 v[112:127], v[4:7], v[148:151], 0\n"
    "v_exp_f32 v101, v101\n"
    "v_exp_f32 v102, v102\n"
    "v_exp_f32 v103, v103\n"
    "v_exp_f32 v104, v104\n"
    "v_exp_f32 v105, v105\n"
    "v_exp_f32 v106, v106\n"
    "v_exp_f32 v107, v107\n"
    "v_exp_f32 v108, v108\n"
    "ds_write_b128 v242, v[128:131] offset:0\n"
    "ds_write_b128 v242, v[132:135] offset:1024\n"
    "ds_write_b32 v243, v136 offset:0\n"
    "v_mfma_f32_32x32x8_bf16 v[128:143], v[234:235], v[234:235], 0\n"
    "v_exp_f32 v109, v109\n"
    "v_exp_f32 v110, v110\n"
    "v_exp_f32 v111, v111\n"
    "v_smfmac_f32_32x32x32_bf16 v[128:143], v[32:35], v[96:103], v232\n"
    "v_exp_f32 v112, v112\n"
    "v_exp_f32 v113, v113\n"
    "v_smfmac_f32_32x32x32_bf16 v[128:143], v[36:39], v[104:111], v232\n"
    "v_exp_f32 v114, v114\n"
    "v_exp_f32 v115, v115\n"
    "v_exp_f32 v116, v116\n"
    "v_mfma_f32_32x32x16_bf16 v[96:111], v[8:11], v[148:151], 0\n"
    "v_exp_f32 v117, v117\n"
    "v_exp_f32 v118, v118\n"
    "v_exp_f32 v119, v119\n"
    "v_exp_f32 v120, v120\n"
    "v_exp_f32 v121, v121\n"
    "v_smfmac_f32_32x32x32_bf16 v[128:143], v[40:43], v[112:119], v232\n"
    "v_exp_f32 v122, v122\n"
    "v_exp_f32 v123, v123\n"
    "v_exp_f32 v124, v124\n"
    "v_exp_f32 v125, v125\n"
    "v_exp_f32 v126, v126\n"
    "v_exp_f32 v127, v127\n"
    "s_waitcnt lgkmcnt(0)\n"
    "s_barrier\n"
    "s_setprio 2\n"
    "v_exp_f32 v96, v96\n"
    "v_exp_f32 v97, v97\n"
    "v_smfmac_f32_32x32x32_bf16 v[128:143], v[44:47], v[120:127], v232\n"
    "ds_read_b128 v[250:253], v247 offset:0\n"
    "v_exp_f32 v98, v98\n"
    "v_exp_f32 v99, v99\n"
    "v_exp_f32 v100, v100\n"
    "v_mfma_f32_32x32x16_bf16 v[112:127], v[12:15], v[148:151], 0\n"
    "ds_read_b128 v[208:211], v245 offset:512\n"
    "ds_read_b64 v[200:201], v244 offset:8192\n"
    "ds_read_b64 v[202:203], v244 offset:10240\n"
    "ds_read_b64 v[204:205], v244 offset:12288\n"
    "ds_read_b64 v[206:207], v244 offset:14336\n"
    "v_exp_f32 v101, v101\n"
    "v_exp_f32 v102, v102\n"
    "v_exp_f32 v103, v103\n"
    "v_exp_f32 v104, v104\n"
    "v_exp_f32 v105, v105\n"
    "v_smfmac_f32_32x32x32_bf16 v[128:143], v[48:51], v[96:103], v232\n"
    "v_exp_f32 v106, v106\n"
    "v_exp_f32 v107, v107\n"
    "v_exp_f32 v108, v108\n"
    "v_exp_f32 v109, v109\n"
    "v_exp_f32 v110, v110\n"
    "v_exp_f32 v111, v111\n"
    "v_exp_f32 v112, v112\n"
    "v_exp_f32 v113, v113\n"
    "v_smfmac_f32_32x32x32_bf16 v[128:143], v[52:55], v[104:111], v232\n"
    "s_add_u32 s64, s56, s51\n"
    "s_lshl_b32 s64, s64, 15\n"
    "s_add_u32 s64, s64, 3158016\n"
    "v_exp_f32 v114, v114\n"
    "v_exp_f32 v115, v115\n"
    "v_exp_f32 v116, v116\n"
    "v_mfma_f32_32x32x16_bf16 v[96:111], v[16:19], v[148:151], 0\n"
    "s_waitcnt lgkmcnt(4)\n"
    "v_add_f32 v208, v208, v209\n"
    "v_add_f32 v210, v210, v211\n"
    "s_waitcnt lgkmcnt(2)\n"
    "v_add_f32 v200, v200, v202\n"
    "v_add_f32 v208, v208, v210\n"
    "v_add_f32 v201, v201, v203\n"
    "v_rcp_f32 v209, v208\n"
    "v_exp_f32 v117, v117\n"
    "v_exp_f32 v118, v118\n"
    "v_exp_f32 v119, v119\n"
    "v_exp_f32 v120, v120\n"
    "v_exp_f32 v121, v121\n"
    "v_smfmac_f32_32x32x32_bf16 v[128:143], v[56:59], v[112:119], v232\n"
    "s_waitcnt lgkmcnt(0)\n"
    "v_add_f32 v204, v204, v206\n"
    "v_add_f32 v205, v205, v207\n"
    "v_add_f32 v200, v200, v204\n"
    "v_add_f32 v201, v201, v205\n"
    "v_mul_f32 v200, v200, v209\n"
    "v_mul_f32 v201, v201, v209\n"
    "s_xor_b32 s73, s75, 327680\n"
    "s_xor_b32 s74, s75, 331776\n"
    "v_exp_f32 v122, v122\n"
    "v_exp_f32 v123, v123\n"
    "v_exp_f32 v124, v124\n"
    "v_exp_f32 v125, v125\n"
    "v_exp_f32 v126, v126\n"
    "v_exp_f32 v127, v127\n"
    "s_waitcnt vmcnt(10)\n"
    "v_smfmac_f32_16x16x64_bf16 v[152:155], v[250:253], v[168:175], v233\n"
    "buffer_load_dwordx4 v[168:171], v241, s[12:15], s73 offen offset:0 sc1\n"
    "v_exp_f32 v96, v96\n"
    "v_exp_f32 v97, v97\n"
    "v_smfmac_f32_32x32x32_bf16 v[128:143], v[60:63], v[120:127], v232\n"
    "buffer_load_dwordx4 v[172:175], v241, s[12:15], s73 offen offset:1024 sc1\n"
    "v_exp_f32 v98, v98\n"
    "v_exp_f32 v99, v99\n"
    "v_exp_f32 v100, v100\n"
    "v_mfma_f32_32x32x16_bf16 v[112:127], v[20:23], v[148:151], 0\n"
    "v_max3_f32 v238, v238, v208, v209\n"
    "v_cvt_pk_bf16_f32 v202, v200, v201\n"
    "ds_write_b32 v246, v202 offset:1024\n"
    "v_fmac_f32 v236, v200, v201\n"
    "v_exp_f32 v101, v101\n"
    "v_exp_f32 v102, v102\n"
    "v_exp_f32 v103, v103\n"
    "v_exp_f32 v104, v104\n"
    "v_exp_f32 v105, v105\n"
    "v_smfmac_f32_32x32x32_bf16 v[128:143], v[64:67], v[96:103], v232\n"
    "v_exp_f32 v106, v106\n"
    "v_exp_f32 v107, v107\n"
    "v_exp_f32 v108, v108\n"
    "v_exp_f32 v109, v109\n"
    "v_exp_f32 v110, v110\n"
    "v_exp_f32 v111, v111\n"
    "s_waitcnt vmcnt(9)\n"
    "v_smfmac_f32_16x16x64_bf16 v[156:159], v[250:253], v[176:183], v233\n"
    "buffer_load_dwordx4 v[176:179], v241, s[12:15], s73 offen offset:2048 sc1\n"
    "v_exp_f32 v112, v112\n"
    "v_exp_f32 v113, v113\n"
    "v_smfmac_f32_32x32x32_bf16 v[128:143], v[68:71], v[104:111], v232\n"
    "buffer_load_dwordx4 v[180:183], v241, s[12:15], s73 offen offset:3072 sc1\n"
    "v_exp_f32 v114, v114\n"
    "v_exp_f32 v115, v115\n"
    "v_exp_f32 v116, v116\n"
    "v_mfma_f32_32x32x16_bf16 v[96:111], v[24:27], v[148:151], 0\n"
    "v_exp_f32 v117, v117\n"
    "v_exp_f32 v118, v118\n"
    "v_exp_f32 v119, v119\n"
    "v_exp_f32 v120, v120\n"
    "v_exp_f32 v121, v121\n"
    "v_smfmac_f32_32x32x32_bf16 v[128:143], v[72:75], v[112:119], v232\n"
    "v_exp_f32 v122, v122\n"
    "v_exp_f32 v123, v123\n"
    "v_exp_f32 v124, v124\n"
    "v_exp_f32 v125, v125\n"
    "v_exp_f32 v126, v126\n"
    "v_exp_f32 v127, v127\n"
    "s_waitcnt vmcnt(8)\n"
    "v_smfmac_f32_16x16x64_bf16 v[160:163], v[250:253], v[184:191], v233\n"
    "buffer_load_dwordx4 v[184:187], v241, s[12:15], s74 offen offset:0 sc1\n"
    "v_exp_f32 v96, v96\n"
    "v_exp_f32 v97, v97\n"
    "v_smfmac_f32_32x32x32_bf16 v[128:143], v[76:79], v[120:127], v232\n"
    "buffer_load_dwordx4 v[188:191], v241, s[12:15], s74 offen offset:1024 sc1\n"
    "v_exp_f32 v98, v98\n"
    "v_exp_f32 v99, v99\n"
    "v_exp_f32 v100, v100\n"
    "v_mfma_f32_32x32x16_bf16 v[112:127], v[28:31], v[148:151], 0\n"
    "buffer_load_dwordx4 v[48:51], v241, s[12:15], s64 offen offset:0 sc1\n"
    "v_exp_f32 v101, v101\n"
    "v_exp_f32 v102, v102\n"
    "v_exp_f32 v103, v103\n"
    "v_exp_f32 v104, v104\n"
    "v_exp_f32 v105, v105\n"
    "v_smfmac_f32_32x32x32_bf16 v[128:143], v[80:83], v[96:103], v232\n"
    "buffer_load_dwordx4 v[52:55], v241, s[12:15], s64 offen offset:1024 sc1\n"
    "v_exp_f32 v106, v106\n"
    "v_exp_f32 v107, v107\n"
    "v_exp_f32 v108, v108\n"
    "v_exp_f32 v109, v109\n"
    "v_exp_f32 v110, v110\n"
    "v_exp_f32 v111, v111\n"
    "s_waitcnt vmcnt(9)\n"
    "v_smfmac_f32_16x16x64_bf16 v[164:167], v[250:253], v[192:199], v233\n"
    "buffer_load_dwordx4 v[192:195], v241, s[12:15], s74 offen offset:2048 sc1\n"
    "v_exp_f32 v112, v112\n"
    "v_exp_f32 v113, v113\n"
    "v_smfmac_f32_32x32x32_bf16 v[128:143], v[84:87], v[104:111], v232\n"
    "buffer_load_dwordx4 v[196:199], v241, s[12:15], s74 offen offset:3072 sc1\n"
    "v_exp_f32 v114, v114\n"
    "v_exp_f32 v115, v115\n"
    "v_exp_f32 v116, v116\n"
    "buffer_load_dwordx4 v[56:59], v241, s[12:15], s64 offen offset:2048 sc1\n"
    "v_exp_f32 v117, v117\n"
    "v_exp_f32 v118, v118\n"
    "v_exp_f32 v119, v119\n"
    "v_exp_f32 v120, v120\n"
    "v_exp_f32 v121, v121\n"
    "v_smfmac_f32_32x32x32_bf16 v[128:143], v[88:91], v[112:119], v232\n"
    "buffer_load_dwordx4 v[60:63], v241, s[12:15], s64 offen offset:3072 sc1\n"
    "v_exp_f32 v122, v122\n"
    "v_exp_f32 v123, v123\n"
    "v_exp_f32 v124, v124\n"
    "v_exp_f32 v125, v125\n"
    "v_exp_f32 v126, v126\n"
    "v_exp_f32 v127, v127\n"
    "s_nop 1\n"
    "v_smfmac_f32_32x32x32_bf16 v[128:143], v[92:95], v[120:127], v232\n"
    "s_xor_b32 s63, s75, 462848\n"
    "s_mul_i32 s62, s51, 12288\n"
    "s_add_u32 s62, s62, 65536\n"
    "v_add_u32 v249, s62, v241\n"
    "s_setprio 0\n"
    "s_add_u32 s64, s64, 4096\n"
    "buffer_load_dwordx4 v[64:67], v241, s[12:15], s64 offen offset:0 sc1\n"
    "buffer_load_dwordx4 v[68:71], v241, s[12:15], s64 offen offset:1024 sc1\n"
    "s_waitcnt vmcnt(14)\n"
    "ds_read_b128 v[0:3], v249 offset:0\n"
    "ds_read_b128 v[4:7], v249 offset:1024\n"
    "ds_read_b128 v[8:11], v249 offset:2048\n"
    "ds_read_b128 v[12:15], v249 offset:3072\n"
    "ds_read_b128 v[16:19], v249 offset:4096\n"
    "ds_read_b128 v[20:23], v249 offset:5120\n"
    "ds_read_b128 v[24:27], v249 offset:6144\n"
    "ds_read_b128 v[28:31], v249 offset:7168\n"
    "ds_read_b128 v[32:35], v249 offset:8192\n"
    "ds_read_b128 v[36:39], v249 offset:9216\n"
    "ds_read_b128 v[40:43], v249 offset:10240\n"
    "ds_read_b128 v[44:47], v249 offset:11264\n"
    "ds_write_b128 v242, v[128:131] offset:9216\n"
    "ds_write_b128 v242, v[132:135] offset:10240\n"
    "ds_write_b32 v243, v136 offset:9216\n"
    "s_waitcnt vmcnt(3)\n"
    "buffer_load_dwordx4 v[128:131], v241, s[12:15], s63 offen offset:0 sc1\n"
    "s_waitcnt vmcnt(3)\n"
    "buffer_load_dwordx4 v[132:135], v241, s[12:15], s63 offen offset:1024 sc1\n"
    "s_waitcnt vmcnt(3)\n"
    "buffer_load_dwordx4 v[136:139], v241, s[12:15], s63 offen offset:2048 sc1\n"
    "s_waitcnt vmcnt(3)\n"
    "buffer_load_dwordx4 v[140:143], v241, s[12:15], s63 offen offset:3072 sc1\n"
    "s_waitcnt vmcnt(3)\n"
    "buffer_load_dwordx4 v[72:75], v241, s[12:15], s64 offen offset:2048 sc1\n"
    "s_waitcnt vmcnt(3)\n"
    "buffer_load_dwordx4 v[76:79], v241, s[12:15], s64 offen offset:3072 sc1\n"
    "s_waitcnt vmcnt(3)\n"
    "s_add_u32 s64, s64, 4096\n"
    "buffer_load_dwordx4 v[80:83], v241, s[12:15], s64 offen offset:0 sc1\n"
    "s_waitcnt vmcnt(3)\n"
    "buffer_load_dwordx4 v[84:87], v241, s[12:15], s64 offen offset:1024 sc1\n"
    "s_waitcnt lgkmcnt(0)\n"
    "s_barrier\n"
    "s_setprio 2\n"
    "ds_read_b128 v[250:253], v247 offset:1024\n"
    "ds_read_b128 v[208:211], v245 offset:0\n"
    "ds_read_b64 v[200:201], v244 offset:0\n"
    "ds_read_b64 v[202:203], v244 offset:2048\n"
    "ds_read_b64 v[204:205], v244 offset:4096\n"
    "ds_read_b64 v[206:207], v244 offset:6144\n"
    "buffer_load_dwordx4 v[88:91], v241, s[12:15], s64 offen offset:2048 sc1\n"
    "buffer_load_dwordx4 v[92:95], v241, s[12:15], s64 offen offset:3072 sc1\n"
    "s_add_u32 s64, s64, 4096\n"
    "buffer_load_dwordx4 v[96:99], v241, s[12:15], s64 offen offset:0 sc1\n"
    "buffer_load_dwordx4 v[100:103], v241, s[12:15], s64 offen offset:1024 sc1\n"
    "s_xor_b32 s73, s75, 393216\n"
    "s_xor_b32 s74, s75, 397312\n"
    "s_waitcnt lgkmcnt(5)\n"
    "v_smfmac_f32_16x16x64_bf16 v[152:155], v[250:253], v[168:175], v233\n"
    "s_waitcnt lgkmcnt(4)\n"
    "v_add_f32 v208, v208, v209\n"
    "v_add_f32 v210, v210, v211\n"
    "s_waitcnt lgkmcnt(2)\n"
    "v_add_f32 v200, v200, v202\n"
    "v_add_f32 v208, v208, v210\n"
    "v_smfmac_f32_16x16x64_bf16 v[156:159], v[250:253], v[176:183], v233\n"
    "buffer_load_dwordx4 v[168:171], v241, s[12:15], s73 offen offset:0 sc1\n"
    "v_add_f32 v201, v201, v203\n"
    "v_rcp_f32 v209, v208\n"
    "s_waitcnt lgkmcnt(0)\n"
    "v_add_f32 v204, v204, v206\n"
    "v_add_f32 v205, v205, v207\n"
    "v_smfmac_f32_16x16x64_bf16 v[160:163], v[250:253], v[184:191], v233\n"
    "buffer_load_dwordx4 v[172:175], v241, s[12:15], s73 offen offset:1024 sc1\n"
    "v_add_f32 v200, v200, v204\n"
    "v_add_f32 v201, v201, v205\n"
    "v_mul_f32 v200, v200, v209\n"
    "v_mul_f32 v201, v201, v209\n"
    "v_smfmac_f32_16x16x64_bf16 v[164:167], v[250:253], v[192:199], v233\n"
    "buffer_load_dwordx4 v[176:179], v241, s[12:15], s73 offen offset:2048 sc1\n"
    "v_max3_f32 v238, v238, v208, v209\n"
    "v_cvt_pk_bf16_f32 v202, v200, v201\n"
    "ds_write_b32 v246, v202 offset:0\n"
    "v_fmac_f32 v236, v200, v201\n"
    "buffer_load_dwordx4 v[180:183], v241, s[12:15], s73 offen offset:3072 sc1\n"
    "s_nop 0\n"
    ".Lpad2917:\n"
    "s_cbranch_execz .Lpad4868\n"
    "s_mul_i32 s62, s51, 2304\n"
    "v_subrev_u32 v243, s62, v243\n"
    "ds_read_b64 v[200:201], v244 offset:25600\n"
    "ds_read_b32 v208, v243 offset:9216\n"
    "ds_read_b64 v[202:203], v244 offset:27904\n"
    "ds_read_b32 v209, v243 offset:11520\n"
    "ds_read_b64 v[204:205], v244 offset:30208\n"
    "ds_read_b32 v210, v243 offset:13824\n"
    "ds_read_b64 v[206:207], v244 offset:32512\n"
    "ds_read_b32 v211, v243 offset:16128\n"
    "s_waitcnt lgkmcnt(5)\n"
    "v_add_f32 v200, v200, v202\n"
    "v_add_f32 v201, v201, v203\n"
    "s_waitcnt lgkmcnt(4)\n"
    "v_add_f32 v208, v208, v209\n"
    "s_waitcnt lgkmcnt(1)\n"
    "v_add_f32 v204, v204, v206\n"
    "v_add_f32 v205, v205, v207\n"
    "s_waitcnt lgkmcnt(0)\n"
    "v_add_f32 v210, v210, v211\n"
    "v_add_f32 v234, v200, v204\n"
    "v_add_f32 v235, v201, v205\n"
    "v_add_f32 v232, v208, v210\n"
    "buffer_load_dwordx4 v[184:187], v241, s[12:15], s74 offen offset:0 sc1\n"
    "s_and_b32 s61, s50, 4\n"
    "s_xor_b32 s61, s61, s48\n"
    "s_min_u32 s66, s61, 3\n"
    "s_mul_i32 s67, s61, 5\n"
    "s_add_u32 s66, s66, s67\n"
    "s_mul_i32 s67, s50, 43\n"
    "s_add_u32 s67, s67, s66\n"
    "s_lshl_b32 s67, s67, 12\n"
    "s_add_u32 s85, s67, 4718592\n"
    "v_bfe_u32 v242, v240, 4, 1\n"
    "v_lshrrev_b32 v243, 5, v240\n"
    "v_lshl_or_b32 v242, v242, 1, v243\n"
    "v_lshlrev_b32 v242, 12, v242\n"
    "v_and_b32 v243, 15, v240\n"
    "buffer_load_dwordx4 v[188:191], v241, s[12:15], s74 offen offset:1024 sc1\n"
    "v_lshl_or_b32 v242, v243, 4, v242\n"
    "s_lshl_b32 s62, s48, 8\n"
    "s_add_u32 s62, s62, 131072\n"
    "v_add_u32 v242, s62, v242\n"
    "s_lshl_b32 s62, s48, 11\n"
    "s_add_u32 s62, s62, 131072\n"
    "v_lshl_add_u32 v243, v240, 5, s62\n"
    "v_mov_b32 v249, 161792\n"
    "buffer_load_dwordx4 v[192:195], v241, s[12:15], s74 offen offset:2048 sc1\n"
    "v_lshlrev_b32 v144, 16, v228\n"
    "v_and_b32 v145, 0xffff0000, v228\n"
    "v_lshlrev_b32 v146, 16, v229\n"
    "v_and_b32 v147, 0xffff0000, v229\n"
    "v_lshlrev_b32 v148, 16, v230\n"
    "v_and_b32 v149, 0xffff0000, v230\n"
    "v_lshlrev_b32 v150, 16, v231\n"
    "v_and_b32 v151, 0xffff0000, v231\n"
    "buffer_load_dwordx4 v[196:199], v241, s[12:15], s74 offen offset:3072 sc1\n"
    "s_setprio 0\n"
    "s_waitcnt vmcnt(3)\n"
    "buffer_load_dwordx4 v[104:107], v241, s[12:15], s64 offen offset:2048 sc1\n"
    "s_waitcnt vmcnt(3)\n"
    "buffer_load_dwordx4 v[108:111], v241, s[12:15], s64 offen offset:3072 sc1\n"
    "s_waitcnt lgkmcnt(0)\n"
    "s_barrier\n"
    "s_setprio 2\n"
    "ds_read_b128 v[208:211], v245 offset:512\n"
    "ds_read_b64 v[200:201], v244 offset:8192\n"
    "ds_read_b64 v[202:203], v244 offset:10240\n"
    "ds_read_b64 v[204:205], v244 offset:12288\n"
    "ds_read_b64 v[206:207], v244 offset:14336\n"
    "ds_read_b128 v[250:253], v247 offset:0\n"
    "s_waitcnt lgkmcnt(5)\n"
    "v_add_f32 v208, v208, v209\n"
    "v_add_f32 v210, v210, v211\n"
    "v_add_f32 v208, v208, v232\n"
    "s_waitcnt lgkmcnt(3)\n"
    "v_add_f32 v200, v200, v202\n"
    "v_add_f32 v208, v208, v210\n"
    "v_add_f32 v201, v201, v203\n"
    "v_add_f32 v200, v200, v234\n"
    "v_add_f32 v201, v201, v235\n"
    "v_rcp_f32 v209, v208\n"
    "s_waitcnt lgkmcnt(1)\n"
    "v_add_f32 v204, v204, v206\n"
    "v_add_f32 v205, v205, v207\n"
    "v_add_f32 v200, v200, v204\n"
    "v_add_f32 v201, v201, v205\n"
    "v_mul_f32 v200, v200, v209\n"
    "v_mul_f32 v201, v201, v209\n"
    "v_cvt_pk_bf16_f32 v202, v200, v201\n"
    "ds_write_b32 v246, v202 offset:1024\n"
    "s_waitcnt lgkmcnt(1)\n"
    "v_smfmac_f32_16x16x64_bf16 v[152:155], v[250:253], v[168:175], v233\n"
    "v_smfmac_f32_16x16x64_bf16 v[156:159], v[250:253], v[176:183], v233\n"
    "v_smfmac_f32_16x16x64_bf16 v[160:163], v[250:253], v[184:191], v233\n"
    "s_waitcnt vmcnt(2)\n"
    "v_smfmac_f32_16x16x64_bf16 v[164:167], v[250:253], v[192:199], v233\n"
    "s_waitcnt lgkmcnt(0)\n"
    "s_barrier\n"
    "ds_read_b128 v[250:253], v247 offset:1024\n"
    "ds_read_b128 v[168:171], v247 offset:2048\n"
    "ds_read_b128 v[172:175], v247 offset:3072\n"
    "ds_read_b128 v[176:179], v247 offset:4096\n"
    "ds_read_b128 v[180:183], v247 offset:5120\n"
    "ds_read_b128 v[184:187], v247 offset:6144\n"
    "ds_read_b128 v[188:191], v247 offset:7168\n"
    "v_max3_f32 v238, v238, v208, v209\n"
    "v_fmac_f32 v236, v200, v201\n"
    "s_movk_i32 s62, 0x207\n"
    "v_cmp_lt_f32 vcc, 0x67800000, v238\n"
    "v_cmp_class_f32 s[76:77], v236, s62\n"
    "v_lshl_add_u32 v205, s51, 2, v249\n"
    "s_or_b64 s[76:77], s[76:77], vcc\n"
    "s_cselect_b32 s62, 1, 0\n"
    "v_mov_b32 v204, s62\n"
    "ds_write_b32 v205, v204\n"
    "s_waitcnt lgkmcnt(7)\n"
    "v_smfmac_f32_16x16x64_bf16 v[152:155], v[250:253], v[212:219], v233\n"
    "v_smfmac_f32_16x16x64_bf16 v[156:159], v[250:253], v[220:227], v233\n"
    "v_smfmac_f32_16x16x64_bf16 v[160:163], v[250:253], v[128:135], v233\n"
    "v_smfmac_f32_16x16x64_bf16 v[164:167], v[250:253], v[136:143], v233\n"
    ".LjoinA:\n"
    "s_lshl3_add_u32 s62, s55, s48\n"
    "s_lshl_b32 s62, s62, 10\n"
    "s_add_u32 s86, s62, 8388608\n"
    "s_add_u32 s62, s62, 6291456\n"
    "s_nop 0\n"
    "v_permlane32_swap_b32 v152, v154\n"
    "v_permlane32_swap_b32 v156, v158\n"
    "v_permlane32_swap_b32 v160, v162\n"
    "v_permlane32_swap_b32 v164, v166\n"
    "v_add_f32 v200, v152, v154\n"
    "v_add_f32 v201, v156, v158\n"
    "v_add_f32 v202, v160, v162\n"
    "v_add_f32 v203, v164, v166\n"
    "ds_write_b128 v242, v[200:203] offset:0\n"
    "v_permlane32_swap_b32 v153, v155\n"
    "v_permlane32_swap_b32 v157, v159\n"
    "v_permlane32_swap_b32 v161, v163\n"
    "v_permlane32_swap_b32 v165, v167\n"
    "v_add_f32 v204, v153, v155\n"
    "v_add_f32 v205, v157, v159\n"
    "v_add_f32 v206, v161, v163\n"
    "v_add_f32 v207, v165, v167\n"
    "ds_write_b128 v242, v[204:207] offset:2048\n"
    "s_add_u32 s64, s64, 4096\n"
    "buffer_load_dwordx4 v[112:115], v241, s[12:15], s64 offen offset:0 sc1\n"
    "buffer_load_dwordx4 v[116:119], v241, s[12:15], s64 offen offset:1024 sc1\n"
    "buffer_load_dwordx4 v[120:123], v241, s[12:15], s64 offen offset:2048 sc1\n"
    "buffer_load_dwordx4 v[124:127], v241, s[12:15], s64 offen offset:3072 sc1\n"
    "s_waitcnt lgkmcnt(0)\n"
    "s_barrier\n"
    "ds_read_b128 v[128:131], v243\n"
    "ds_read_b128 v[132:135], v243 offset:16\n"
    "ds_read_b128 v[192:195], v249\n"
    "s_waitcnt lgkmcnt(2)\n"
    "v_pk_fma_f32 v[128:129], v[128:129], v[168:169], v[144:145]\n"
    "v_pk_fma_f32 v[130:131], v[130:131], v[170:171], v[146:147]\n"
    "s_waitcnt lgkmcnt(1)\n"
    "v_pk_fma_f32 v[132:133], v[132:133], v[172:173], v[148:149]\n"
    "v_pk_fma_f32 v[134:135], v[134:135], v[174:175], v[150:151]\n"
    "v_pk_add_f32 v[200:201], v[128:129], v[130:131]\n"
    "v_pk_mul_f32 v[202:203], v[128:129], v[128:129]\n"
    "v_pk_add_f32 v[200:201], v[200:201], v[132:133]\n"
    "v_pk_fma_f32 v[202:203], v[130:131], v[130:131], v[202:203]\n"
    "v_pk_add_f32 v[200:201], v[200:201], v[134:135]\n"
    "v_pk_fma_f32 v[202:203], v[132:133], v[132:133], v[202:203]\n"
    "v_add_f32 v200, v200, v201\n"
    "v_pk_fma_f32 v[202:203], v[134:135], v[134:135], v[202:203]\n"
    "s_waitcnt lgkmcnt(0)\n"
    "v_or3_b32 v192, v192, v193, v194\n"
    "v_add_f32 v202, v202, v203\n"
    "v_add_f32_dpp v200, v200, v200 quad_perm:[1,0,3,2] row_mask:0xf bank_mask:0xf\n"
    "v_or_b32 v192, v192, v195\n"
    "v_add_f32_dpp v202, v202, v202 quad_perm:[1,0,3,2] row_mask:0xf bank_mask:0xf\n"
    "v_add_f32_dpp v200, v200, v200 quad_perm:[2,3,0,1] row_mask:0xf bank_mask:0xf\n"
    "v_cmp_ne_u32 vcc, 0, v192\n"
    "v_add_f32_dpp v202, v202, v202 quad_perm:[2,3,0,1] row_mask:0xf bank_mask:0xf\n"
    "v_add_f32_dpp v200, v200, v200 row_half_mirror row_mask:0xf bank_mask:0xf\n"
    "v_pk_mul_f32 v[136:137], v[128:129], v[184:185]\n"
    "v_add_f32_dpp v202, v202, v202 row_half_mirror row_mask:0xf bank_mask:0xf\n"
    "v_add_f32_dpp v200, v200, v200 row_mirror row_mask:0xf bank_mask:0xf\n"
    "v_pk_mul_f32 v[138:139], v[130:131], v[186:187]\n"
    "v_add_f32_dpp v202, v202, v202 row_mirror row_mask:0xf bank_mask:0xf\n"
    "v_add_f32_dpp v200, v200, v200 row_bcast:15 row_mask:0xa bank_mask:0xf\n"
    "v_pk_mul_f32 v[140:141], v[132:133], v[188:189]\n"
    "v_add_f32_dpp v202, v202, v202 row_bcast:15 row_mask:0xa bank_mask:0xf\n"
    "v_add_f32_dpp v200, v200, v200 row_bcast:31 row_mask:0xc bank_mask:0xf\n"
    "v_pk_mul_f32 v[142:143], v[134:135], v[190:191]\n"
    "v_add_f32_dpp v202, v202, v202 row_bcast:31 row_mask:0xc bank_mask:0xf\n"
    "v_mul_f32 v200, 0xbb000000, v200\n"
    "s_cbranch_vccnz .Lslow\n"
    "v_mul_f32 v202, 0x3b000000, v202\n"
    "v_readlane_b32 s76, v200, 63\n"
    "v_fma_f32 v202, -v200, v200, v202\n"
    "v_max_f32 v202, 0, v202\n"
    "v_add_f32 v202, 0x3727c5ac, v202\n"
    "v_rsq_f32 v202, v202\n"
    "v_pk_fma_f32 v[136:137], v[184:185], s[76:77], v[136:137] op_sel_hi:[1,0,1]\n"
    "v_readlane_b32 s78, v202, 63\n"
    "v_pk_fma_f32 v[138:139], v[186:187], s[76:77], v[138:139] op_sel_hi:[1,0,1]\n"
    "v_pk_fma_f32 v[140:141], v[188:189], s[76:77], v[140:141] op_sel_hi:[1,0,1]\n"
    "v_pk_fma_f32 v[142:143], v[190:191], s[76:77], v[142:143] op_sel_hi:[1,0,1]\n"
    "v_pk_fma_f32 v[136:137], v[136:137], s[78:79], v[176:177] op_sel_hi:[1,0,1]\n"
    "v_pk_fma_f32 v[138:139], v[138:139], s[78:79], v[178:179] op_sel_hi:[1,0,1]\n"
    "v_pk_fma_f32 v[140:141], v[140:141], s[78:79], v[180:181] op_sel_hi:[1,0,1]\n"
    "v_pk_fma_f32 v[142:143], v[142:143], s[78:79], v[182:183] op_sel_hi:[1,0,1]\n"
    "v_cvt_pk_bf16_f32 v200, v136, v137\n"
    "v_cvt_pk_bf16_f32 v201, v138, v139\n"
    "v_cvt_pk_bf16_f32 v202, v140, v141\n"
    "v_cvt_pk_bf16_f32 v203, v142, v143\n"
    "s_setprio 3\n"
    "buffer_store_dwordx4 v[200:203], v241, s[12:15], s62 offen sc1\n"
    "s_setprio 0\n"
    "v_cvt_pk_bf16_f32 v136, v128, v129\n"
    "v_cvt_pk_bf16_f32 v137, v130, v131\n"
    "v_cvt_pk_bf16_f32 v138, v132, v133\n"
    "v_cvt_pk_bf16_f32 v139, v134, v135\n"
    "buffer_store_dwordx4 v[136:139], v241, s[12:15], s86 offen sc1\n"
    "v_and_b32 v236, 1, v240\n"
    "v_bfe_u32 v237, v240, 1, 3\n"
    "v_lshlrev_b32 v237, 4, v237\n"
    "v_lshl_or_b32 v236, v236, 10, v237\n"
    "s_lshl_b32 s62, s50, 7\n"
    "s_add_u32 s62, s62, 158720\n"
    "v_add_u32 v236, s62, v236\n"
    "ds_read_b128 v[250:253], v236\n"
    "s_and_b32 s62, s48, 3\n"
    "s_lshl_b32 s62, s62, 6\n"
    "v_xor_b32 v227, s62, v241\n"
    "v_xor_b32 v228, 16, v227\n"
    "v_xor_b32 v229, 32, v227\n"
    "v_xor_b32 v230, 48, v227\n"
    "s_lshl4_add_u32 s62, s49, s48\n"
    "s_lshl_b32 s62, s62, 12\n"
    "s_add_u32 s62, s62, 6291456\n"
    "s_lshl_b32 s64, s48, 12\n"
    "s_add_u32 s64, s64, 65536\n"
    "s_and_b32 s65, s50, 0\n"
    "s_lshl_b32 s65, s65, 15\n"
    "s_xor_b32 s62, s62, s65\n"
    "s_xor_b32 s64, s64, s65\n"
    "s_xor_b32 s63, s62, 32768\n"
    "s_xor_b32 s65, s64, 32768\n"
    "v_add_u32 v231, s64, v241\n"
    "v_add_u32 v232, s65, v241\n"
    "v_and_b32 v236, 31, v240\n"
    "v_lshrrev_b32 v237, 5, v240\n"
    "v_and_b32 v238, 15, v240\n"
    "v_xor_b32 v237, v237, v238\n"
    "v_lshlrev_b32 v237, 4, v237\n"
    "v_lshl_or_b32 v237, v236, 10, v237\n"
    "v_add_u32 v242, 65536, v237\n"
    "v_xor_b32 v243, 32, v242\n"
    "v_xor_b32 v244, 64, v242\n"
    "v_xor_b32 v245, 96, v242\n"
    "v_xor_b32 v246, 128, v242\n"
    "v_xor_b32 v247, 160, v242\n"
    "v_xor_b32 v248, 192, v242\n"
    "v_xor_b32 v249, 224, v242\n"
    "s_lshl_b32 s66, s66, 12\n"
    "s_mul_i32 s67, s49, 180224\n"
    "s_add_u32 s67, s67, 10485760\n"
    "s_add_u32 s71, s66, s67\n"
    "s_add_u32 s80, s71, 4096\n"
    "s_add_u32 s81, s71, 8192\n"
    "s_add_u32 s82, s71, 12288\n"
    "s_add_u32 s83, s71, 16384\n"
    "s_add_u32 s84, s71, 20480\n"
    "s_add_u32 s72, s56, s51\n"
    "s_lshl_b32 s72, s72, 12\n"
    "s_add_u32 s72, s72, s67\n"
    "s_lshl_b32 s66, s56, 12\n"
    "s_add_u32 s66, s66, s67\n"
    "s_sub_u32 s66, s72, s66\n"
    "s_add_u32 s66, s66, 139264\n"
    "v_add_u32 v254, s66, v241\n"
    "s_waitcnt vmcnt(1)\n"
    "s_barrier\n"
    "s_setprio 1\n"
    "s_setprio 2\n"
    "s_mov_b32 m0, s64\n"
    "s_nop 0\n"
    "buffer_load_dwordx4 v227, s[12:15], s62 offen offset:0 sc1 lds\n"
    "buffer_load_dwordx4 v228, s[12:15], s62 offen offset:1024 sc1 lds\n"
    "buffer_load_dwordx4 v229, s[12:15], s62 offen offset:2048 sc1 lds\n"
    "buffer_load_dwordx4 v230, s[12:15], s62 offen offset:3072 sc1 lds\n"
    "s_setprio 0\n"
    "s_mov_b32 m0, s65\n"
    "s_nop 0\n"
    "buffer_load_dwordx4 v227, s[12:15], s63 offen offset:0 sc1 lds\n"
    "buffer_load_dwordx4 v228, s[12:15], s63 offen offset:1024 sc1 lds\n"
    "buffer_load_dwordx4 v229, s[12:15], s63 offen offset:2048 sc1 lds\n"
    "buffer_load_dwordx4 v230, s[12:15], s63 offen offset:3072 sc1 lds\n"
    "s_waitcnt vmcnt(7)\n"
    "ds_read_b32 v168, v231 offset:0\n"
    "s_waitcnt vmcnt(6)\n"
    "ds_read_b32 v169, v231 offset:1024\n"
    "s_waitcnt vmcnt(5)\n"
    "ds_read_b32 v170, v231 offset:2048\n"
    "s_waitcnt vmcnt(4)\n"
    "ds_read_b32 v171, v231 offset:3072\n"
    ".Lgc_47:\n"
    "s_waitcnt lgkmcnt(1)\n"
    "v_max3_u32 v168, v168, v169, v170\n"
    "s_waitcnt lgkmcnt(0)\n"
    "v_max_u32 v168, v168, v171\n"
    "v_cmp_eq_u32 vcc, -1, v168\n"
    "s_nop 0\n"
    "s_cbranch_vccnz .Lgr_48\n"
    ".Lgo_49:\n"
    "s_barrier\n"
    "s_mov_b32 s76, 0xbfb8aa3b\n"
    "s_setprio 3\n"
    "ds_read_b128 v[128:131], v242 offset:0\n"
    "ds_read_b128 v[132:135], v243 offset:0\n"
    "ds_read_b128 v[136:139], v244 offset:0\n"
    "ds_read_b128 v[140:143], v245 offset:0\n"
    "ds_read_b128 v[144:147], v246 offset:0\n"
    "ds_read_b128 v[148:151], v247 offset:0\n"
    "s_waitcnt lgkmcnt(5)\n"
    "v_mfma_f32_32x32x16_bf16 v[152:167], v[0:3], v[128:131], 0\n"
    "ds_read_b128 v[128:131], v248 offset:0\n"
    "s_waitcnt lgkmcnt(5)\n"
    "v_mfma_f32_32x32x16_bf16 v[152:167], v[4:7], v[132:135], v[152:167]\n"
    "ds_read_b128 v[132:135], v249 offset:0\n"
    "s_waitcnt lgkmcnt(5)\n"
    "v_mfma_f32_32x32x16_bf16 v[152:167], v[8:11], v[136:139], v[152:167]\n"
    "ds_read_b128 v[136:139], v242 offset:256\n"
    "s_waitcnt lgkmcnt(5)\n"
    "v_mfma_f32_32x32x16_bf16 v[152:167], v[12:15], v[140:143], v[152:167]\n"
    "ds_read_b128 v[140:143], v243 offset:256\n"
    "s_waitcnt lgkmcnt(5)\n"
    "v_mfma_f32_32x32x16_bf16 v[152:167], v[16:19], v[144:147], v[152:167]\n"
    "ds_read_b128 v[144:147], v244 offset:256\n"
    "s_waitcnt lgkmcnt(5)\n"
    "v_mfma_f32_32x32x16_bf16 v[152:167], v[20:23], v[148:151], v[152:167]\n"
    "ds_read_b128 v[148:151], v245 offset:256\n"
    "s_waitcnt lgkmcnt(5)\n"
    "v_mfma_f32_32x32x16_bf16 v[152:167], v[24:27], v[128:131], v[152:167]\n"
    "ds_read_b128 v[128:131], v246 offset:256\n"
    "s_waitcnt lgkmcnt(5)\n"
    "v_mfma_f32_32x32x16_bf16 v[152:167], v[28:31], v[132:135], v[152:167]\n"
    "ds_read_b128 v[132:135], v247 offset:256\n"
    "s_waitcnt lgkmcnt(5)\n"
    "v_mfma_f32_32x32x16_bf16 v[152:167], v[32:35], v[136:139], v[152:167]\n"
    "ds_read_b128 v[136:139], v248 offset:256\n"
    "s_waitcnt lgkmcnt(5)\n"
    "v_mfma_f32_32x32x16_bf16 v[152:167], v[36:39], v[140:143], v[152:167]\n"
    "ds_read_b128 v[140:143], v249 offset:256\n"
    "s_waitcnt lgkmcnt(5)\n"
    "v_mfma_f32_32x32x16_bf16 v[152:167], v[40:43], v[144:147], v[152:167]\n"
    "ds_read_b128 v[144:147], v242 offset:512\n"
    "s_waitcnt lgkmcnt(5)\n"
    "v_mfma_f32_32x32x16_bf16 v[152:167], v[44:47], v[148:151], v[152:167]\n"
    "ds_read_b128 v[148:151], v243 offset:512\n"
    "s_waitcnt lgkmcnt(5)\n"
    "v_mfma_f32_32x32x16_bf16 v[152:167], v[48:51], v[128:131], v[152:167]\n"
    "ds_read_b128 v[128:131], v244 offset:512\n"
    "s_waitcnt lgkmcnt(5)\n"
    "v_mfma_f32_32x32x16_bf16 v[152:167], v[52:55], v[132:135], v[152:167]\n"
    "ds_read_b128 v[132:135], v245 offset:512\n"
    "s_waitcnt lgkmcnt(5)\n"
    "v_mfma_f32_32x32x16_bf16 v[152:167], v[56:59], v[136:139], v[152:167]\n"
    "ds_read_b128 v[136:139], v246 offset:512\n"
    "s_waitcnt lgkmcnt(5)\n"
    "v_mfma_f32_32x32x16_bf16 v[152:167], v[60:63], v[140:143], v[152:167]\n"
    "ds_read_b128 v[140:143], v247 offset:512\n"
    "buffer_load_dwordx4 v[176:179], v241, s[12:15], s85 offen offset:0 sc1\n"
    "s_waitcnt lgkmcnt(5)\n"
    "v_mfma_f32_32x32x16_bf16 v[152:167], v[64:67], v[144:147], v[152:167]\n"
    "ds_read_b128 v[144:147], v248 offset:512\n"
    "s_waitcnt vmcnt(4)\n"
    "ds_read_b32 v168, v232 offset:0\n"
    "s_waitcnt vmcnt(3)\n"
    "ds_read_b32 v169, v232 offset:1024\n"
    "s_waitcnt vmcnt(2)\n"
    "ds_read_b32 v170, v232 offset:2048\n"
    "s_waitcnt vmcnt(1)\n"
    "ds_read_b32 v171, v232 offset:3072\n"
    "s_waitcnt lgkmcnt(9)\n"
    "v_mfma_f32_32x32x16_bf16 v[152:167], v[68:71], v[148:151], v[152:167]\n"
    "ds_read_b128 v[148:151], v249 offset:512\n"
    "buffer_load_dwordx4 v[180:183], v241, s[12:15], s85 offen offset:1024 sc1\n"
    "s_waitcnt lgkmcnt(9)\n"
    "v_mfma_f32_32x32x16_bf16 v[152:167], v[72:75], v[128:131], v[152:167]\n"
    "ds_read_b128 v[128:131], v242 offset:768\n"
    "s_waitcnt lgkmcnt(9)\n"
    "v_mfma_f32_32x32x16_bf16 v[152:167], v[76:79], v[132:135], v[152:167]\n"
    "ds_read_b128 v[132:135], v243 offset:768\n"
    "buffer_load_dwordx4 v[184:187], v241, s[12:15], s85 offen offset:2048 sc1\n"
    "s_waitcnt lgkmcnt(9)\n"
    "v_mfma_f32_32x32x16_bf16 v[152:167], v[80:83], v[136:139], v[152:167]\n"
    "ds_read_b128 v[136:139], v244 offset:768\n"
    ".Lgc_50:\n"
    "s_waitcnt lgkmcnt(5)\n"
    "v_max3_u32 v168, v168, v169, v170\n"
    "s_waitcnt lgkmcnt(4)\n"
    "v_max_u32 v168, v168, v171\n"
    "v_cmp_eq_u32 vcc, -1, v168\n"
    "s_nop 0\n"
    "s_cbranch_vccnz .Lgr_51\n"
    ".Lgo_52:\n"
    "s_barrier\n"
    "v_mfma_f32_32x32x16_bf16 v[152:167], v[84:87], v[140:143], v[152:167]\n"
    "ds_read_b128 v[140:143], v245 offset:768\n"
    "buffer_load_dwordx4 v[188:191], v241, s[12:15], s85 offen offset:3072 sc1\n"
    "v_mfma_f32_32x32x16_bf16 v[152:167], v[88:91], v[144:147], v[152:167]\n"
    "ds_read_b128 v[144:147], v246 offset:768\n"
    "s_waitcnt lgkmcnt(5)\n"
    "v_mfma_f32_32x32x16_bf16 v[152:167], v[92:95], v[148:151], v[152:167]\n"
    "ds_read_b128 v[148:151], v247 offset:768\n"
    "s_add_u32 s85, s85, 4096\n"
    "buffer_load_dwordx4 v[192:195], v241, s[12:15], s85 offen offset:0 sc1\n"
    "s_waitcnt lgkmcnt(5)\n"
    "v_mfma_f32_32x32x16_bf16 v[152:167], v[96:99], v[128:131], v[152:167]\n"
    "ds_read_b128 v[128:131], v248 offset:768\n"
    "s_waitcnt lgkmcnt(5)\n"
    "v_mfma_f32_32x32x16_bf16 v[152:167], v[100:103], v[132:135], v[152:167]\n"
    "ds_read_b128 v[132:135], v249 offset:768\n"
    "buffer_load_dwordx4 v[196:199], v241, s[12:15], s85 offen offset:1024 sc1\n"
    "s_waitcnt lgkmcnt(5)\n"
    "v_mfma_f32_32x32x16_bf16 v[152:167], v[104:107], v[136:139], v[152:167]\n"
    "s_waitcnt lgkmcnt(4)\n"
    "v_mfma_f32_32x32x16_bf16 v[152:167], v[108:111], v[140:143], v[152:167]\n"
    "buffer_load_dwordx4 v[200:203], v241, s[12:15], s85 offen offset:2048 sc1\n"
    "s_waitcnt lgkmcnt(3)\n"
    "v_mfma_f32_32x32x16_bf16 v[152:167], v[112:115], v[144:147], v[152:167]\n"
    "s_waitcnt lgkmcnt(2)\n"
    "v_mfma_f32_32x32x16_bf16 v[152:167], v[116:119], v[148:151], v[152:167]\n"
    "buffer_load_dwordx4 v[204:207], v241, s[12:15], s85 offen offset:3072 sc1\n"
    "s_waitcnt lgkmcnt(1)\n"
    "v_mfma_f32_32x32x16_bf16 v[152:167], v[120:123], v[128:131], v[152:167]\n"
    "s_waitcnt lgkmcnt(0)\n"
    "v_mfma_f32_32x32x16_bf16 v[152:167], v[124:127], v[132:135], v[152:167]\n"
    "s_setprio 1\n"
    "s_nop 10\n"
    "v_mul_f32 v168, s76, v152\n"
    "v_mul_f32 v169, s76, v153\n"
    "v_mul_f32 v170, s76, v154\n"
    "v_mul_f32 v171, s76, v155\n"
    "v_mul_f32 v172, s76, v156\n"
    "v_mul_f32 v173, s76, v157\n"
    "v_mul_f32 v174, s76, v158\n"
    "v_mul_f32 v175, s76, v159\n"
    "s_add_u32 s85, s85, 4096\n"
    "buffer_load_dwordx4 v[208:211], v241, s[12:15], s85 offen offset:0 sc1\n"
    "v_exp_f32 v168, v168\n"
    "v_exp_f32 v169, v169\n"
    "v_exp_f32 v170, v170\n"
    "v_exp_f32 v171, v171\n"
    "v_exp_f32 v172, v172\n"
    "v_exp_f32 v173, v173\n"
    "v_exp_f32 v174, v174\n"
    "v_exp_f32 v175, v175\n"
    "buffer_load_dwordx4 v[212:215], v241, s[12:15], s85 offen offset:1024 sc1\n"
    "v_add_f32 v168, 1.0, v168\n"
    "v_add_f32 v169, 1.0, v169\n"
    "v_add_f32 v170, 1.0, v170\n"
    "v_add_f32 v171, 1.0, v171\n"
    "v_add_f32 v172, 1.0, v172\n"
    "v_add_f32 v173, 1.0, v173\n"
    "v_add_f32 v174, 1.0, v174\n"
    "v_add_f32 v175, 1.0, v175\n"
    "buffer_load_dwordx4 v[216:219], v241, s[12:15], s85 offen offset:2048 sc1\n"
    "v_rcp_f32 v168, v168\n"
    "v_rcp_f32 v169, v169\n"
    "v_rcp_f32 v170, v170\n"
    "v_rcp_f32 v171, v171\n"
    "v_rcp_f32 v172, v172\n"
    "v_rcp_f32 v173, v173\n"
    "v_rcp_f32 v174, v174\n"
    "v_rcp_f32 v175, v175\n"
    "buffer_load_dwordx4 v[220:223], v241, s[12:15], s85 offen offset:3072 sc1\n"
    "v_mul_f32 v168, v168, v152\n"
    "v_mul_f32 v169, v169, v153\n"
    "v_mul_f32 v170, v170, v154\n"
    "v_mul_f32 v171, v171, v155\n"
    "v_mul_f32 v172, v172, v156\n"
    "v_mul_f32 v173, v173, v157\n"
    "v_mul_f32 v174, v174, v158\n"
    "v_mul_f32 v175, v175, v159\n"
    "s_add_u32 s85, s85, 4096\n"
    "buffer_load_dwordx4 v[224:227], v241, s[12:15], s85 offen offset:0 sc1\n"
    "v_cvt_pk_bf16_f32 v152, v168, v169\n"
    "v_cvt_pk_bf16_f32 v153, v170, v171\n"
    "v_cvt_pk_bf16_f32 v154, v172, v173\n"
    "v_cvt_pk_bf16_f32 v155, v174, v175\n"
    "buffer_store_dwordx4 v[152:155], v241, s[12:15], s72 offen offset:0 sc1\n"
    "ds_write_b128 v254, v[152:155] offset:0\n"
    "v_mul_f32 v168, s76, v160\n"
    "v_mul_f32 v169, s76, v161\n"
    "v_mul_f32 v170, s76, v162\n"
    "v_mul_f32 v171, s76, v163\n"
    "v_mul_f32 v172, s76, v164\n"
    "v_mul_f32 v173, s76, v165\n"
    "v_mul_f32 v174, s76, v166\n"
    "v_mul_f32 v175, s76, v167\n"
    "buffer_load_dwordx4 v[228:231], v241, s[12:15], s85 offen offset:1024 sc1\n"
    "v_exp_f32 v168, v168\n"
    "v_exp_f32 v169, v169\n"
    "v_exp_f32 v170, v170\n"
    "v_exp_f32 v171, v171\n"
    "v_exp_f32 v172, v172\n"
    "v_exp_f32 v173, v173\n"
    "v_exp_f32 v174, v174\n"
    "v_exp_f32 v175, v175\n"
    "buffer_load_dwordx4 v[232:235], v241, s[12:15], s85 offen offset:2048 sc1\n"
    "v_add_f32 v168, 1.0, v168\n"
    "v_add_f32 v169, 1.0, v169\n"
    "v_add_f32 v170, 1.0, v170\n"
    "v_add_f32 v171, 1.0, v171\n"
    "v_add_f32 v172, 1.0, v172\n"
    "v_add_f32 v173, 1.0, v173\n"
    "v_add_f32 v174, 1.0, v174\n"
    "v_add_f32 v175, 1.0, v175\n"
    "buffer_load_dwordx4 v[236:239], v241, s[12:15], s85 offen offset:3072 sc1\n"
    "v_rcp_f32 v168, v168\n"
    "v_rcp_f32 v169, v169\n"
    "v_rcp_f32 v170, v170\n"
    "v_rcp_f32 v171, v171\n"
    "v_rcp_f32 v172, v172\n"
    "v_rcp_f32 v173, v173\n"
    "v_rcp_f32 v174, v174\n"
    "v_rcp_f32 v175, v175\n"
    "v_mul_f32 v168, v168, v160\n"
    "v_mul_f32 v169, v169, v161\n"
    "v_mul_f32 v170, v170, v162\n"
    "v_mul_f32 v171, v171, v163\n"
    "v_mul_f32 v172, v172, v164\n"
    "v_mul_f32 v173, v173, v165\n"
    "v_mul_f32 v174, v174, v166\n"
    "v_mul_f32 v175, v175, v167\n"
    "v_cvt_pk_bf16_f32 v160, v168, v169\n"
    "v_cvt_pk_bf16_f32 v161, v170, v171\n"
    "v_cvt_pk_bf16_f32 v162, v172, v173\n"
    "v_cvt_pk_bf16_f32 v163, v174, v175\n"
    "buffer_store_dwordx4 v[160:163], v241, s[12:15], s72 offen offset:1024 sc1\n"
    "ds_write_b128 v254, v[160:163] offset:1024\n"
    "s_setprio 1\n"
    "ds_read_b128 v[128:131], v242 offset:32768\n"
    "ds_read_b128 v[132:135], v243 offset:32768\n"
    "ds_read_b128 v[136:139], v244 offset:32768\n"
    "ds_read_b128 v[140:143], v245 offset:32768\n"
    "ds_read_b128 v[144:147], v246 offset:32768\n"
    "ds_read_b128 v[148:151], v247 offset:32768\n"
    "s_waitcnt lgkmcnt(5)\n"
    "v_mfma_f32_32x32x16_bf16 v[152:167], v[0:3], v[128:131], 0\n"
    "ds_read_b128 v[128:131], v248 offset:32768\n"
    "s_waitcnt lgkmcnt(5)\n"
    "v_mfma_f32_32x32x16_bf16 v[152:167], v[4:7], v[132:135], v[152:167]\n"
    "ds_read_b128 v[132:135], v249 offset:32768\n"
    "s_waitcnt lgkmcnt(5)\n"
    "v_mfma_f32_32x32x16_bf16 v[152:167], v[8:11], v[136:139], v[152:167]\n"
    "ds_read_b128 v[136:139], v242 offset:33024\n"
    "s_add_u32 s85, s85, 4096\n"
    "buffer_load_dwordx4 v[0:3], v241, s[12:15], s85 offen offset:0 sc1\n"
    "s_waitcnt lgkmcnt(5)\n"
    "v_mfma_f32_32x32x16_bf16 v[152:167], v[12:15], v[140:143], v[152:167]\n"
    "ds_read_b128 v[140:143], v243 offset:33024\n"
    "s_waitcnt lgkmcnt(5)\n"
    "v_mfma_f32_32x32x16_bf16 v[152:167], v[16:19], v[144:147], v[152:167]\n"
    "ds_read_b128 v[144:147], v244 offset:33024\n"
    "buffer_load_dwordx4 v[4:7], v241, s[12:15], s85 offen offset:1024 sc1\n"
    "s_waitcnt lgkmcnt(5)\n"
    "v_mfma_f32_32x32x16_bf16 v[152:167], v[20:23], v[148:151], v[152:167]\n"
    "ds_read_b128 v[148:151], v245 offset:33024\n"
    "s_waitcnt lgkmcnt(5)\n"
    "v_mfma_f32_32x32x16_bf16 v[152:167], v[24:27], v[128:131], v[152:167]\n"
    "ds_read_b128 v[128:131], v246 offset:33024\n"
    "buffer_load_dwordx4 v[8:11], v241, s[12:15], s85 offen offset:2048 sc1\n"
    "s_waitcnt lgkmcnt(5)\n"
    "v_mfma_f32_32x32x16_bf16 v[152:167], v[28:31], v[132:135], v[152:167]\n"
    "ds_read_b128 v[132:135], v247 offset:33024\n"
    "s_waitcnt lgkmcnt(5)\n"
    "v_mfma_f32_32x32x16_bf16 v[152:167], v[32:35], v[136:139], v[152:167]\n"
    "ds_read_b128 v[136:139], v248 offset:33024\n"
    "buffer_load_dwordx4 v[12:15], v241, s[12:15], s85 offen offset:3072 sc1\n"
    "s_waitcnt lgkmcnt(5)\n"
    "v_mfma_f32_32x32x16_bf16 v[152:167], v[36:39], v[140:143], v[152:167]\n"
    "ds_read_b128 v[140:143], v249 offset:33024\n"
    "s_waitcnt lgkmcnt(5)\n"
    "v_mfma_f32_32x32x16_bf16 v[152:167], v[40:43], v[144:147], v[152:167]\n"
    "ds_read_b128 v[144:147], v242 offset:33280\n"
    "s_cmp_lt_u32 s61, 3\n"
    "s_cbranch_scc0 .Lu5_53\n"
    "s_add_u32 s85, s85, 4096\n"
    "buffer_load_dwordx4 v[16:19], v241, s[12:15], s85 offen offset:0 sc1\n"
    ".Lu5_53:\n"
    "s_waitcnt lgkmcnt(5)\n"
    "v_mfma_f32_32x32x16_bf16 v[152:167], v[44:47], v[148:151], v[152:167]\n"
    "ds_read_b128 v[148:151], v243 offset:33280\n"
    "s_waitcnt lgkmcnt(5)\n"
    "v_mfma_f32_32x32x16_bf16 v[152:167], v[48:51], v[128:131], v[152:167]\n"
    "ds_read_b128 v[128:131], v244 offset:33280\n"
    "s_cmp_lt_u32 s61, 3\n"
    "s_cbranch_scc0 .Lu5_54\n"
    "buffer_load_dwordx4 v[20:23], v241, s[12:15], s85 offen offset:1024 sc1\n"
    ".Lu5_54:\n"
    "s_waitcnt lgkmcnt(5)\n"
    "v_mfma_f32_32x32x16_bf16 v[152:167], v[52:55], v[132:135], v[152:167]\n"
    "ds_read_b128 v[132:135], v245 offset:33280\n"
    "s_waitcnt lgkmcnt(5)\n"
    "v_mfma_f32_32x32x16_bf16 v[152:167], v[56:59], v[136:139], v[152:167]\n"
    "ds_read_b128 v[136:139], v246 offset:33280\n"
    "s_cmp_lt_u32 s61, 3\n"
    "s_cbranch_scc0 .Lu5_55\n"
    "buffer_load_dwordx4 v[24:27], v241, s[12:15], s85 offen offset:2048 sc1\n"
    ".Lu5_55:\n"
    "s_waitcnt lgkmcnt(5)\n"
    "v_mfma_f32_32x32x16_bf16 v[152:167], v[60:63], v[140:143], v[152:167]\n"
    "ds_read_b128 v[140:143], v247 offset:33280\n"
    "s_waitcnt lgkmcnt(5)\n"
    "v_mfma_f32_32x32x16_bf16 v[152:167], v[64:67], v[144:147], v[152:167]\n"
    "ds_read_b128 v[144:147], v248 offset:33280\n"
    "s_cmp_lt_u32 s61, 3\n"
    "s_cbranch_scc0 .Lu5_56\n"
    "buffer_load_dwordx4 v[28:31], v241, s[12:15], s85 offen offset:3072 sc1\n"
    ".Lu5_56:\n"
    "s_waitcnt lgkmcnt(5)\n"
    "v_mfma_f32_32x32x16_bf16 v[152:167], v[68:71], v[148:151], v[152:167]\n"
    "ds_read_b128 v[148:151], v249 offset:33280\n"
    "s_waitcnt lgkmcnt(5)\n"
    "v_mfma_f32_32x32x16_bf16 v[152:167], v[72:75], v[128:131], v[152:167]\n"
    "ds_read_b128 v[128:131], v242 offset:33536\n"
    "s_waitcnt lgkmcnt(5)\n"
    "v_mfma_f32_32x32x16_bf16 v[152:167], v[76:79], v[132:135], v[152:167]\n"
    "ds_read_b128 v[132:135], v243 offset:33536\n"
    "s_waitcnt lgkmcnt(5)\n"
    "v_mfma_f32_32x32x16_bf16 v[152:167], v[80:83], v[136:139], v[152:167]\n"
    "ds_read_b128 v[136:139], v244 offset:33536\n"
    "s_waitcnt lgkmcnt(5)\n"
    "v_mfma_f32_32x32x16_bf16 v[152:167], v[84:87], v[140:143], v[152:167]\n"
    "ds_read_b128 v[140:143], v245 offset:33536\n"
    "s_waitcnt lgkmcnt(5)\n"
    "v_mfma_f32_32x32x16_bf16 v[152:167], v[88:91], v[144:147], v[152:167]\n"
    "ds_read_b128 v[144:147], v246 offset:33536\n"
    "s_waitcnt lgkmcnt(5)\n"
    "v_mfma_f32_32x32x16_bf16 v[152:167], v[92:95], v[148:151], v[152:167]\n"
    "ds_read_b128 v[148:151], v247 offset:33536\n"
    "s_barrier\n"
    "s_waitcnt lgkmcnt(5)\n"
    "v_mfma_f32_32x32x16_bf16 v[152:167], v[96:99], v[128:131], v[152:167]\n"
    "ds_read_b128 v[128:131], v248 offset:33536\n"
    "s_waitcnt lgkmcnt(5)\n"
    "v_mfma_f32_32x32x16_bf16 v[152:167], v[100:103], v[132:135], v[152:167]\n"
    "ds_read_b128 v[132:135], v249 offset:33536\n"
    "s_waitcnt lgkmcnt(5)\n"
    "v_mfma_f32_32x32x16_bf16 v[152:167], v[104:107], v[136:139], v[152:167]\n"
    "s_waitcnt lgkmcnt(4)\n"
    "v_mfma_f32_32x32x16_bf16 v[152:167], v[108:111], v[140:143], v[152:167]\n"
    "s_waitcnt lgkmcnt(3)\n"
    "v_mfma_f32_32x32x16_bf16 v[152:167], v[112:115], v[144:147], v[152:167]\n"
    "s_waitcnt lgkmcnt(2)\n"
    "v_mfma_f32_32x32x16_bf16 v[152:167], v[116:119], v[148:151], v[152:167]\n"
    "s_waitcnt lgkmcnt(1)\n"
    "v_mfma_f32_32x32x16_bf16 v[152:167], v[120:123], v[128:131], v[152:167]\n"
    "s_waitcnt lgkmcnt(0)\n"
    "v_mfma_f32_32x32x16_bf16 v[152:167], v[124:127], v[132:135], v[152:167]\n"
    "s_setprio 3\n"
    "s_nop 10\n"
    "v_pk_mul_f32 v[168:169], v[152:153], s[76:77] op_sel_hi:[1,0]\n"
    "v_pk_mul_f32 v[170:171], v[154:155], s[76:77] op_sel_hi:[1,0]\n"
    "v_pk_mul_f32 v[172:173], v[156:157], s[76:77] op_sel_hi:[1,0]\n"
    "v_pk_mul_f32 v[174:175], v[158:159], s[76:77] op_sel_hi:[1,0]\n"
    "v_exp_f32 v168, v168\n"
    "v_exp_f32 v169, v169\n"
    "v_exp_f32 v170, v170\n"
    "v_exp_f32 v171, v171\n"
    "v_exp_f32 v172, v172\n"
    "v_exp_f32 v173, v173\n"
    "v_exp_f32 v174, v174\n"
    "v_exp_f32 v175, v175\n"
    "v_pk_add_f32 v[168:169], v[168:169], 1.0 op_sel_hi:[1,0]\n"
    "v_pk_add_f32 v[170:171], v[170:171], 1.0 op_sel_hi:[1,0]\n"
    "v_pk_add_f32 v[172:173], v[172:173], 1.0 op_sel_hi:[1,0]\n"
    "v_pk_add_f32 v[174:175], v[174:175], 1.0 op_sel_hi:[1,0]\n"
    "v_rcp_f32 v168, v168\n"
    "v_rcp_f32 v169, v169\n"
    "v_rcp_f32 v170, v170\n"
    "v_rcp_f32 v171, v171\n"
    "v_rcp_f32 v172, v172\n"
    "v_rcp_f32 v173, v173\n"
    "v_rcp_f32 v174, v174\n"
    "v_rcp_f32 v175, v175\n"
    "v_pk_mul_f32 v[168:169], v[168:169], v[152:153]\n"
    "v_pk_mul_f32 v[170:171], v[170:171], v[154:155]\n"
    "v_pk_mul_f32 v[172:173], v[172:173], v[156:157]\n"
    "v_pk_mul_f32 v[174:175], v[174:175], v[158:159]\n"
    "s_nop 0\n"
    ".Lpad3608:\n"
    "s_cbranch_execz .Lpad5632\n"
    "v_cvt_pk_bf16_f32 v152, v168, v169\n"
    "v_cvt_pk_bf16_f32 v153, v170, v171\n"
    "v_cvt_pk_bf16_f32 v154, v172, v173\n"
    "v_cvt_pk_bf16_f32 v155, v174, v175\n"
    "buffer_store_dwordx4 v[152:155], v241, s[12:15], s72 offen offset:2048 sc1\n"
    "ds_write_b128 v254, v[152:155] offset:2048\n"
    "v_pk_mul_f32 v[168:169], v[160:161], s[76:77] op_sel_hi:[1,0]\n"
    "v_pk_mul_f32 v[170:171], v[162:163], s[76:77] op_sel_hi:[1,0]\n"
    "v_pk_mul_f32 v[172:173], v[164:165], s[76:77] op_sel_hi:[1,0]\n"
    "v_pk_mul_f32 v[174:175], v[166:167], s[76:77] op_sel_hi:[1,0]\n"
    "v_exp_f32 v168, v168\n"
    "v_exp_f32 v169, v169\n"
    "v_exp_f32 v170, v170\n"
    "v_exp_f32 v171, v171\n"
    "v_exp_f32 v172, v172\n"
    "v_exp_f32 v173, v173\n"
    "v_exp_f32 v174, v174\n"
    "v_exp_f32 v175, v175\n"
    "v_pk_add_f32 v[168:169], v[168:169], 1.0 op_sel_hi:[1,0]\n"
    "v_pk_add_f32 v[170:171], v[170:171], 1.0 op_sel_hi:[1,0]\n"
    "v_pk_add_f32 v[172:173], v[172:173], 1.0 op_sel_hi:[1,0]\n"
    "v_pk_add_f32 v[174:175], v[174:175], 1.0 op_sel_hi:[1,0]\n"
    "v_rcp_f32 v168, v168\n"
    "v_rcp_f32 v169, v169\n"
    "v_rcp_f32 v170, v170\n"
    "v_rcp_f32 v171, v171\n"
    "v_rcp_f32 v172, v172\n"
    "v_rcp_f32 v173, v173\n"
    "v_rcp_f32 v174, v174\n"
    "v_rcp_f32 v175, v175\n"
    "v_pk_mul_f32 v[168:169], v[168:169], v[160:161]\n"
    "v_pk_mul_f32 v[170:171], v[170:171], v[162:163]\n"
    "v_pk_mul_f32 v[172:173], v[172:173], v[164:165]\n"
    "v_pk_mul_f32 v[174:175], v[174:175], v[166:167]\n"
    "v_cvt_pk_bf16_f32 v160, v168, v169\n"
    "v_cvt_pk_bf16_f32 v161, v170, v171\n"
    "v_cvt_pk_bf16_f32 v162, v172, v173\n"
    "v_cvt_pk_bf16_f32 v163, v174, v175\n"
    "buffer_store_dwordx4 v[160:163], v241, s[12:15], s72 offen offset:3072 sc1\n"
    "ds_write_b128 v254, v[160:163] offset:3072\n"
    "s_setprio 1\n"
    "s_cmp_eq_u32 s61, s50\n"
    "s_cbranch_scc1 .Lv_own\n"
    "s_cmp_lt_u32 s61, 3\n"
    "s_cbranch_scc1 .Lv_A0\n"
    "buffer_load_dwordx4 v[32:35], v241, s[12:15], s71 offen offset:0 sc1\n"
    "buffer_load_dwordx4 v[36:39], v241, s[12:15], s71 offen offset:1024 sc1\n"
    "buffer_load_dwordx4 v[40:43], v241, s[12:15], s80 offen offset:0 sc1\n"
    "buffer_load_dwordx4 v[44:47], v241, s[12:15], s80 offen offset:1024 sc1\n"
    "buffer_load_dwordx4 v[48:51], v241, s[12:15], s81 offen offset:0 sc1\n"
    "buffer_load_dwordx4 v[52:55], v241, s[12:15], s81 offen offset:1024 sc1\n"
    "buffer_load_dwordx4 v[56:59], v241, s[12:15], s82 offen offset:0 sc1\n"
    "buffer_load_dwordx4 v[60:63], v241, s[12:15], s82 offen offset:1024 sc1\n"
    "buffer_load_dwordx4 v[64:67], v241, s[12:15], s83 offen offset:0 sc1\n"
    "buffer_load_dwordx4 v[68:71], v241, s[12:15], s83 offen offset:1024 sc1\n"
    "buffer_load_dwordx4 v[112:115], v241, s[12:15], s83 offen offset:2048 sc1\n"
    "buffer_load_dwordx4 v[116:119], v241, s[12:15], s83 offen offset:3072 sc1\n"
    "v_lshrrev_b32 v168, 5, v240\n"
    "v_lshlrev_b32 v175, 4, v168\n"
    "v_xor_b32 v175, v175, v241\n"
    "s_lshl_b32 s67, s48, 13\n"
    "v_add_u32 v160, s67, v175\n"
    "v_xor_b32 v161, 32, v160\n"
    "v_xor_b32 v162, 64, v160\n"
    "v_xor_b32 v163, 96, v160\n"
    "v_xor_b32 v164, 128, v160\n"
    "v_xor_b32 v165, 160, v160\n"
    "v_xor_b32 v166, 192, v160\n"
    "v_xor_b32 v167, 224, v160\n"
    "v_bfe_u32 v175, v240, 4, 1\n"
    "v_lshl_or_b32 v168, v175, 1, v168\n"
    "v_lshl_add_u32 v168, v168, 3, s48\n"
    "v_and_b32 v175, 15, v240\n"
    "v_lshlrev_b32 v169, 3, v175\n"
    "v_lshl_or_b32 v169, v168, 10, v169\n"
    "v_xor_b32 v168, v168, v175\n"
    "v_lshlrev_b32 v168, 4, v168\n"
    "v_lshl_or_b32 v168, v175, 9, v168\n"
    "s_lshl_b32 s68, s49, 16\n"
    "s_lshl_b32 s69, s50, 7\n"
    "s_add_u32 s68, s68, s69\n"
    "s_add_u32 s69, s68, 32768\n"
    "v_lshrrev_b32 v170, 10, v169\n"
    "v_lshlrev_b32 v175, 3, v175\n"
    "v_lshl_or_b32 v175, v170, 7, v175\n"
    "v_add_u32 v175, 131072, v175\n"
    "v_lshrrev_b32 v170, 3, v240\n"
    "v_and_b32 v171, 7, v240\n"
    "v_lshlrev_b32 v171, 4, v171\n"
    "v_lshl_or_b32 v170, v170, 10, v171\n"
    "s_lshl_b32 s62, s61, 13\n"
    "s_add_u32 s62, s62, s68\n"
    "s_add_u32 s62, s62, 8388608\n"
    "s_lshl_b32 s63, s61, 10\n"
    "s_add_u32 m0, s63, 131072\n"
    ".Lnt_57:\n"
    ".Lgc_58:\n"
    "s_waitcnt vmcnt(10)\n"
    "v_max_u32 v174, v32, v36\n"
    "s_waitcnt vmcnt(8)\n"
    "v_max3_u32 v174, v40, v44, v174\n"
    "s_waitcnt vmcnt(6)\n"
    "v_max3_u32 v174, v48, v52, v174\n"
    "s_waitcnt vmcnt(4)\n"
    "v_max3_u32 v174, v56, v60, v174\n"
    "v_cmp_eq_u32 vcc, -1, v174\n"
    "s_nop 0\n"
    "s_cbranch_vccnz .Lgr_59\n"
    ".Lgo_60:\n"
    "v_mfma_f32_32x32x16_bf16 v[128:143], v[176:179], v[32:35], 0\n"
    "v_mfma_f32_32x32x16_bf16 v[144:159], v[180:183], v[32:35], 0\n"
    "v_mfma_f32_32x32x16_bf16 v[128:143], v[184:187], v[36:39], v[128:143]\n"
    "v_mfma_f32_32x32x16_bf16 v[144:159], v[188:191], v[36:39], v[144:159]\n"
    "v_mfma_f32_32x32x16_bf16 v[128:143], v[192:195], v[40:43], v[128:143]\n"
    "v_mfma_f32_32x32x16_bf16 v[144:159], v[196:199], v[40:43], v[144:159]\n"
    "v_mfma_f32_32x32x16_bf16 v[128:143], v[200:203], v[44:47], v[128:143]\n"
    "v_mfma_f32_32x32x16_bf16 v[144:159], v[204:207], v[44:47], v[144:159]\n"
    "v_mfma_f32_32x32x16_bf16 v[128:143], v[208:211], v[48:51], v[128:143]\n"
    "buffer_load_dwordx4 v[80:83], v241, s[12:15], s71 offen offset:2048 sc1\n"
    "v_mfma_f32_32x32x16_bf16 v[144:159], v[212:215], v[48:51], v[144:159]\n"
    "buffer_load_dwordx4 v[88:91], v241, s[12:15], s80 offen offset:2048 sc1\n"
    "v_mfma_f32_32x32x16_bf16 v[128:143], v[216:219], v[52:55], v[128:143]\n"
    "buffer_load_dwordx4 v[96:99], v241, s[12:15], s81 offen offset:2048 sc1\n"
    "v_mfma_f32_32x32x16_bf16 v[144:159], v[220:223], v[52:55], v[144:159]\n"
    "buffer_load_dwordx4 v[104:107], v241, s[12:15], s82 offen offset:2048 sc1\n"
    "v_mfma_f32_32x32x16_bf16 v[128:143], v[224:227], v[56:59], v[128:143]\n"
    "buffer_load_dwordx4 v[84:87], v241, s[12:15], s71 offen offset:3072 sc1\n"
    "v_mfma_f32_32x32x16_bf16 v[144:159], v[228:231], v[56:59], v[144:159]\n"
    "buffer_load_dwordx4 v[92:95], v241, s[12:15], s80 offen offset:3072 sc1\n"
    "v_mfma_f32_32x32x16_bf16 v[128:143], v[232:235], v[60:63], v[128:143]\n"
    "buffer_load_dwordx4 v[100:103], v241, s[12:15], s81 offen offset:3072 sc1\n"
    "v_mfma_f32_32x32x16_bf16 v[144:159], v[236:239], v[60:63], v[144:159]\n"
    "buffer_load_dwordx4 v[108:111], v241, s[12:15], s82 offen offset:3072 sc1\n"
    "buffer_load_dwordx4 v170, s[12:15], s62 offen sc1 lds\n"
    ".Lgc_61:\n"
    "s_waitcnt vmcnt(11)\n"
    "v_max_u32 v174, v64, v68\n"
    "s_waitcnt vmcnt(9)\n"
    "v_max3_u32 v174, v112, v116, v174\n"
    "v_cmp_eq_u32 vcc, -1, v174\n"
    "s_nop 0\n"
    "s_cbranch_vccnz .Lgr_62\n"
    ".Lgo_63:\n"
    "v_mfma_f32_32x32x16_bf16 v[128:143], v[0:3], v[64:67], v[128:143]\n"
    "v_mfma_f32_32x32x16_bf16 v[144:159], v[4:7], v[64:67], v[144:159]\n"
    "v_mfma_f32_32x32x16_bf16 v[128:143], v[8:11], v[68:71], v[128:143]\n"
    "v_mfma_f32_32x32x16_bf16 v[144:159], v[12:15], v[68:71], v[144:159]\n"
    "v_mfma_f32_32x32x16_bf16 v[32:47], v[0:3], v[112:115], 0\n"
    "v_mfma_f32_32x32x16_bf16 v[48:63], v[4:7], v[112:115], 0\n"
    "v_mfma_f32_32x32x16_bf16 v[32:47], v[8:11], v[116:119], v[32:47]\n"
    "v_mfma_f32_32x32x16_bf16 v[48:63], v[12:15], v[116:119], v[48:63]\n"
    "s_nop 6\n"
    "ds_write_b128 v160, v[128:131] offset:0\n"
    "v_add_u32 v160, 0x10000, v160\n"
    "ds_write_b128 v161, v[132:135] offset:1024\n"
    "v_add_u32 v161, 0x10000, v161\n"
    "ds_write_b128 v162, v[136:139] offset:2048\n"
    "v_add_u32 v162, 0x10000, v162\n"
    "ds_write_b128 v163, v[140:143] offset:3072\n"
    "v_add_u32 v163, 0x10000, v163\n"
    "ds_write_b128 v164, v[144:147] offset:4096\n"
    "v_add_u32 v164, 0x10000, v164\n"
    "ds_write_b128 v165, v[148:151] offset:5120\n"
    "v_add_u32 v165, 0x10000, v165\n"
    "ds_write_b128 v166, v[152:155] offset:6144\n"
    "v_add_u32 v166, 0x10000, v166\n"
    "ds_write_b128 v167, v[156:159] offset:7168\n"
    "v_add_u32 v167, 0x10000, v167\n"
    "s_waitcnt vmcnt(0) lgkmcnt(0)\n"
    "s_barrier\n"
    "ds_read_b128 v[128:131], v168 offset:0\n"
    "ds_read_b128 v[132:135], v168 offset:8192\n"
    "ds_read_b128 v[136:139], v168 offset:16384\n"
    "ds_read_b128 v[140:143], v168 offset:24576\n"
    "ds_read_b128 v[144:147], v168 offset:32768\n"
    "ds_read_b128 v[148:151], v168 offset:40960\n"
    "ds_read_b128 v[152:155], v168 offset:49152\n"
    "ds_read_b128 v[156:159], v168 offset:57344\n"
    "ds_read_b64 v[170:171], v175 offset:0\n"
    "ds_read_b64 v[172:173], v175 offset:4096\n"
    "v_add_u32 v168, 0x10000, v168\n"
    ".Lgc_64:\n"
    "v_max_u32 v174, v80, v88\n"
    "v_max3_u32 v174, v96, v104, v174\n"
    "v_max3_u32 v174, v84, v92, v174\n"
    "v_max3_u32 v174, v100, v108, v174\n"
    "v_cmp_eq_u32 vcc, -1, v174\n"
    "s_nop 0\n"
    "s_cbranch_vccnz .Lgr_65\n"
    ".Lgo_66:\n"
    "v_mfma_f32_32x32x16_bf16 v[32:47], v[176:179], v[80:83], v[32:47]\n"
    "s_waitcnt lgkmcnt(8)\n"
    "v_add_f32 v128, v128, v132\n"
    "v_add_f32 v129, v129, v133\n"
    "v_add_f32 v130, v130, v134\n"
    "v_add_f32 v131, v131, v135\n"
    "s_waitcnt lgkmcnt(6)\n"
    "v_add_f32 v136, v136, v140\n"
    "v_mfma_f32_32x32x16_bf16 v[32:47], v[192:195], v[88:91], v[32:47]\n"
    "v_add_f32 v137, v137, v141\n"
    "v_add_f32 v138, v138, v142\n"
    "v_add_f32 v139, v139, v143\n"
    "s_waitcnt lgkmcnt(4)\n"
    "v_add_f32 v144, v144, v148\n"
    "v_add_f32 v145, v145, v149\n"
    "v_mfma_f32_32x32x16_bf16 v[32:47], v[208:211], v[96:99], v[32:47]\n"
    "v_add_f32 v146, v146, v150\n"
    "v_add_f32 v147, v147, v151\n"
    "s_waitcnt lgkmcnt(2)\n"
    "v_add_f32 v152, v152, v156\n"
    "v_add_f32 v153, v153, v157\n"
    "v_add_f32 v154, v154, v158\n"
    "v_mfma_f32_32x32x16_bf16 v[32:47], v[224:227], v[104:107], v[32:47]\n"
    "v_add_f32 v155, v155, v159\n"
    "v_add_f32 v128, v128, v136\n"
    "v_add_f32 v129, v129, v137\n"
    "v_add_f32 v130, v130, v138\n"
    "v_add_f32 v131, v131, v139\n"
    "v_mfma_f32_32x32x16_bf16 v[32:47], v[184:187], v[84:87], v[32:47]\n"
    "v_add_f32 v144, v144, v152\n"
    "v_add_f32 v145, v145, v153\n"
    "v_add_f32 v146, v146, v154\n"
    "v_add_f32 v147, v147, v155\n"
    "v_add_f32 v128, v128, v144\n"
    "v_mfma_f32_32x32x16_bf16 v[32:47], v[200:203], v[92:95], v[32:47]\n"
    "v_add_f32 v129, v129, v145\n"
    "v_add_f32 v130, v130, v146\n"
    "v_add_f32 v131, v131, v147\n"
    "s_waitcnt lgkmcnt(1)\n"
    "v_lshlrev_b32 v64, 16, v170\n"
    "v_and_b32 v65, 0xffff0000, v170\n"
    "v_mfma_f32_32x32x16_bf16 v[32:47], v[216:219], v[100:103], v[32:47]\n"
    "v_lshlrev_b32 v66, 16, v171\n"
    "v_and_b32 v67, 0xffff0000, v171\n"
    "v_fma_f32 v128, v128, v250, v64\n"
    "v_fma_f32 v129, v129, v251, v65\n"
    "v_fma_f32 v130, v130, v252, v66\n"
    "v_mfma_f32_32x32x16_bf16 v[32:47], v[232:235], v[108:111], v[32:47]\n"
    "v_fma_f32 v131, v131, v253, v67\n"
    "v_cvt_pk_bf16_f32 v132, v128, v129\n"
    "v_cvt_pk_bf16_f32 v133, v130, v131\n"
    "buffer_store_dwordx2 v[132:133], v169, s[44:47], s68 offen sc0 sc1\n"
    "s_waitcnt lgkmcnt(0)\n"
    "v_lshlrev_b32 v64, 16, v172\n"
    "v_mfma_f32_32x32x16_bf16 v[48:63], v[180:183], v[80:83], v[48:63]\n"
    "v_and_b32 v65, 0xffff0000, v172\n"
    "v_lshlrev_b32 v66, 16, v173\n"
    "v_and_b32 v67, 0xffff0000, v173\n"
    "v_mfma_f32_32x32x16_bf16 v[48:63], v[196:199], v[88:91], v[48:63]\n"
    "v_mfma_f32_32x32x16_bf16 v[48:63], v[212:215], v[96:99], v[48:63]\n"
    "v_mfma_f32_32x32x16_bf16 v[48:63], v[228:231], v[104:107], v[48:63]\n"
    "ds_write_b128 v160, v[32:35] offset:0\n"
    "v_mfma_f32_32x32x16_bf16 v[48:63], v[188:191], v[84:87], v[48:63]\n"
    "ds_write_b128 v161, v[36:39] offset:1024\n"
    "v_mfma_f32_32x32x16_bf16 v[48:63], v[204:207], v[92:95], v[48:63]\n"
    "ds_write_b128 v162, v[40:43] offset:2048\n"
    "v_mfma_f32_32x32x16_bf16 v[48:63], v[220:223], v[100:103], v[48:63]\n"
    "ds_write_b128 v163, v[44:47] offset:3072\n"
    "v_mfma_f32_32x32x16_bf16 v[48:63], v[236:239], v[108:111], v[48:63]\n"
    "s_nop 11\n"
    "ds_write_b128 v164, v[48:51] offset:4096\n"
    "ds_write_b128 v165, v[52:55] offset:5120\n"
    "ds_write_b128 v166, v[56:59] offset:6144\n"
    "ds_write_b128 v167, v[60:63] offset:7168\n"
    "s_waitcnt lgkmcnt(0)\n"
    "s_barrier\n"
    "ds_read_b128 v[128:131], v168 offset:0\n"
    "ds_read_b128 v[132:135], v168 offset:8192\n"
    "ds_read_b128 v[136:139], v168 offset:16384\n"
    "ds_read_b128 v[140:143], v168 offset:24576\n"
    "ds_read_b128 v[144:147], v168 offset:32768\n"
    "ds_read_b128 v[148:151], v168 offset:40960\n"
    "ds_read_b128 v[152:155], v168 offset:49152\n"
    "ds_read_b128 v[156:159], v168 offset:57344\n"
    "s_waitcnt lgkmcnt(6)\n"
    "v_pk_add_f32 v[128:129], v[128:129], v[132:133]\n"
    "v_pk_add_f32 v[130:131], v[130:131], v[134:135]\n"
    "s_waitcnt lgkmcnt(4)\n"
    "v_pk_add_f32 v[136:137], v[136:137], v[140:141]\n"
    "v_pk_add_f32 v[138:139], v[138:139], v[142:143]\n"
    "s_waitcnt lgkmcnt(2)\n"
    "v_pk_add_f32 v[144:145], v[144:145], v[148:149]\n"
    "v_pk_add_f32 v[146:147], v[146:147], v[150:151]\n"
    "s_waitcnt lgkmcnt(0)\n"
    "v_pk_add_f32 v[152:153], v[152:153], v[156:157]\n"
    "v_pk_add_f32 v[154:155], v[154:155], v[158:159]\n"
    "v_pk_add_f32 v[128:129], v[128:129], v[136:137]\n"
    "v_pk_add_f32 v[130:131], v[130:131], v[138:139]\n"
    "v_pk_add_f32 v[144:145], v[144:145], v[152:153]\n"
    "v_pk_add_f32 v[146:147], v[146:147], v[154:155]\n"
    "v_pk_add_f32 v[128:129], v[128:129], v[144:145]\n"
    "v_pk_add_f32 v[130:131], v[130:131], v[146:147]\n"
    "v_fma_f32 v128, v128, v250, v64\n"
    "v_fma_f32 v129, v129, v251, v65\n"
    "v_fma_f32 v130, v130, v252, v66\n"
    "v_fma_f32 v131, v131, v253, v67\n"
    "v_cvt_pk_bf16_f32 v132, v128, v129\n"
    "v_cvt_pk_bf16_f32 v133, v130, v131\n"
    "buffer_store_dwordx2 v[132:133], v169, s[44:47], s69 offen sc0 sc1\n"
    "s_branch .Lfin\n"
    ".Lv_A0:\n"
    ".Lpad3910:\n"
    "s_cbranch_execz .Lpad6451\n"
    "buffer_load_dwordx4 v[32:35], v241, s[12:15], s71 offen offset:0 sc1\n"
    "buffer_load_dwordx4 v[36:39], v241, s[12:15], s71 offen offset:1024 sc1\n"
    "buffer_load_dwordx4 v[40:43], v241, s[12:15], s80 offen offset:0 sc1\n"
    "buffer_load_dwordx4 v[44:47], v241, s[12:15], s80 offen offset:1024 sc1\n"
    "buffer_load_dwordx4 v[48:51], v241, s[12:15], s81 offen offset:0 sc1\n"
    "buffer_load_dwordx4 v[52:55], v241, s[12:15], s81 offen offset:1024 sc1\n"
    "buffer_load_dwordx4 v[56:59], v241, s[12:15], s82 offen offset:0 sc1\n"
    "buffer_load_dwordx4 v[60:63], v241, s[12:15], s82 offen offset:1024 sc1\n"
    "buffer_load_dwordx4 v[64:67], v241, s[12:15], s83 offen offset:0 sc1\n"
    "buffer_load_dwordx4 v[68:71], v241, s[12:15], s83 offen offset:1024 sc1\n"
    "buffer_load_dwordx4 v[72:75], v241, s[12:15], s84 offen offset:0 sc1\n"
    "buffer_load_dwordx4 v[76:79], v241, s[12:15], s84 offen offset:1024 sc1\n"
    "buffer_load_dwordx4 v[112:115], v241, s[12:15], s83 offen offset:2048 sc1\n"
    "buffer_load_dwordx4 v[116:119], v241, s[12:15], s83 offen offset:3072 sc1\n"
    "buffer_load_dwordx4 v[120:123], v241, s[12:15], s84 offen offset:2048 sc1\n"
    "buffer_load_dwordx4 v[124:127], v241, s[12:15], s84 offen offset:3072 sc1\n"
    "v_lshrrev_b32 v168, 5, v240\n"
    "v_lshlrev_b32 v175, 4, v168\n"
    "v_xor_b32 v175, v175, v241\n"
    "s_lshl_b32 s67, s48, 13\n"
    "v_add_u32 v160, s67, v175\n"
    "v_xor_b32 v161, 32, v160\n"
    "v_xor_b32 v162, 64, v160\n"
    "v_xor_b32 v163, 96, v160\n"
    "v_xor_b32 v164, 128, v160\n"
    "v_xor_b32 v165, 160, v160\n"
    "v_xor_b32 v166, 192, v160\n"
    "v_xor_b32 v167, 224, v160\n"
    "v_bfe_u32 v175, v240, 4, 1\n"
    "v_lshl_or_b32 v168, v175, 1, v168\n"
    "v_lshl_add_u32 v168, v168, 3, s48\n"
    "v_and_b32 v175, 15, v240\n"
    "v_lshlrev_b32 v169, 3, v175\n"
    "v_lshl_or_b32 v169, v168, 10, v169\n"
    "v_xor_b32 v168, v168, v175\n"
    "v_lshlrev_b32 v168, 4, v168\n"
    "v_lshl_or_b32 v168, v175, 9, v168\n"
    "s_lshl_b32 s68, s49, 16\n"
    "s_lshl_b32 s69, s50, 7\n"
    "s_add_u32 s68, s68, s69\n"
    "s_add_u32 s69, s68, 32768\n"
    "v_lshrrev_b32 v170, 10, v169\n"
    "v_lshlrev_b32 v175, 3, v175\n"
    "v_lshl_or_b32 v175, v170, 7, v175\n"
    "v_add_u32 v175, 131072, v175\n"
    "v_lshrrev_b32 v170, 3, v240\n"
    "v_and_b32 v171, 7, v240\n"
    "v_lshlrev_b32 v171, 4, v171\n"
    "v_lshl_or_b32 v170, v170, 10, v171\n"
    "s_lshl_b32 s62, s61, 13\n"
    "s_add_u32 s62, s62, s68\n"
    "s_add_u32 s62, s62, 8388608\n"
    "s_lshl_b32 s63, s61, 10\n"
    "s_add_u32 m0, s63, 131072\n"
    ".Lnt_67:\n"
    ".Lgc_68:\n"
    "s_waitcnt vmcnt(14)\n"
    "v_max_u32 v174, v32, v36\n"
    "s_waitcnt vmcnt(12)\n"
    "v_max3_u32 v174, v40, v44, v174\n"
    "s_waitcnt vmcnt(10)\n"
    "v_max3_u32 v174, v48, v52, v174\n"
    "s_waitcnt vmcnt(8)\n"
    "v_max3_u32 v174, v56, v60, v174\n"
    "v_cmp_eq_u32 vcc, -1, v174\n"
    "s_nop 0\n"
    "s_cbranch_vccnz .Lgr_69\n"
    ".Lgo_70:\n"
    "v_mfma_f32_32x32x16_bf16 v[128:143], v[176:179], v[32:35], 0\n"
    "v_mfma_f32_32x32x16_bf16 v[144:159], v[180:183], v[32:35], 0\n"
    "v_mfma_f32_32x32x16_bf16 v[128:143], v[184:187], v[36:39], v[128:143]\n"
    "v_mfma_f32_32x32x16_bf16 v[144:159], v[188:191], v[36:39], v[144:159]\n"
    "v_mfma_f32_32x32x16_bf16 v[128:143], v[192:195], v[40:43], v[128:143]\n"
    "v_mfma_f32_32x32x16_bf16 v[144:159], v[196:199], v[40:43], v[144:159]\n"
    "v_mfma_f32_32x32x16_bf16 v[128:143], v[200:203], v[44:47], v[128:143]\n"
    "v_mfma_f32_32x32x16_bf16 v[144:159], v[204:207], v[44:47], v[144:159]\n"
    "v_mfma_f32_32x32x16_bf16 v[128:143], v[208:211], v[48:51], v[128:143]\n"
    "buffer_load_dwordx4 v[80:83], v241, s[12:15], s71 offen offset:2048 sc1\n"
    "v_mfma_f32_32x32x16_bf16 v[144:159], v[212:215], v[48:51], v[144:159]\n"
    "buffer_load_dwordx4 v[88:91], v241, s[12:15], s80 offen offset:2048 sc1\n"
    "v_mfma_f32_32x32x16_bf16 v[128:143], v[216:219], v[52:55], v[128:143]\n"
    "buffer_load_dwordx4 v[96:99], v241, s[12:15], s81 offen offset:2048 sc1\n"
    "v_mfma_f32_32x32x16_bf16 v[144:159], v[220:223], v[52:55], v[144:159]\n"
    "buffer_load_dwordx4 v[104:107], v241, s[12:15], s82 offen offset:2048 sc1\n"
    "v_mfma_f32_32x32x16_bf16 v[128:143], v[224:227], v[56:59], v[128:143]\n"
    "buffer_load_dwordx4 v[84:87], v241, s[12:15], s71 offen offset:3072 sc1\n"
    "v_mfma_f32_32x32x16_bf16 v[144:159], v[228:231], v[56:59], v[144:159]\n"
    "buffer_load_dwordx4 v[92:95], v241, s[12:15], s80 offen offset:3072 sc1\n"
    "v_mfma_f32_32x32x16_bf16 v[128:143], v[232:235], v[60:63], v[128:143]\n"
    "buffer_load_dwordx4 v[100:103], v241, s[12:15], s81 offen offset:3072 sc1\n"
    "v_mfma_f32_32x32x16_bf16 v[144:159], v[236:239], v[60:63], v[144:159]\n"
    "buffer_load_dwordx4 v[108:111], v241, s[12:15], s82 offen offset:3072 sc1\n"
    "buffer_load_dwordx4 v170, s[12:15], s62 offen sc1 lds\n"
    ".Lgc_71:\n"
    "s_waitcnt vmcnt(15)\n"
    "v_max_u32 v174, v64, v68\n"
    "s_waitcnt vmcnt(13)\n"
    "v_max3_u32 v174, v72, v76, v174\n"
    "s_waitcnt vmcnt(11)\n"
    "v_max3_u32 v174, v112, v116, v174\n"
    "s_waitcnt vmcnt(9)\n"
    "v_max3_u32 v174, v120, v124, v174\n"
    "v_cmp_eq_u32 vcc, -1, v174\n"
    "s_nop 0\n"
    "s_cbranch_vccnz .Lgr_72\n"
    ".Lgo_73:\n"
    "v_mfma_f32_32x32x16_bf16 v[128:143], v[0:3], v[64:67], v[128:143]\n"
    "v_mfma_f32_32x32x16_bf16 v[144:159], v[4:7], v[64:67], v[144:159]\n"
    "v_mfma_f32_32x32x16_bf16 v[128:143], v[8:11], v[68:71], v[128:143]\n"
    "v_mfma_f32_32x32x16_bf16 v[144:159], v[12:15], v[68:71], v[144:159]\n"
    "v_mfma_f32_32x32x16_bf16 v[128:143], v[16:19], v[72:75], v[128:143]\n"
    "v_mfma_f32_32x32x16_bf16 v[144:159], v[20:23], v[72:75], v[144:159]\n"
    "v_mfma_f32_32x32x16_bf16 v[128:143], v[24:27], v[76:79], v[128:143]\n"
    "v_mfma_f32_32x32x16_bf16 v[144:159], v[28:31], v[76:79], v[144:159]\n"
    "v_mfma_f32_32x32x16_bf16 v[32:47], v[0:3], v[112:115], 0\n"
    "v_mfma_f32_32x32x16_bf16 v[48:63], v[4:7], v[112:115], 0\n"
    "v_mfma_f32_32x32x16_bf16 v[32:47], v[8:11], v[116:119], v[32:47]\n"
    "v_mfma_f32_32x32x16_bf16 v[48:63], v[12:15], v[116:119], v[48:63]\n"
    "s_nop 6\n"
    "ds_write_b128 v160, v[128:131] offset:0\n"
    "v_add_u32 v160, 0x10000, v160\n"
    "v_mfma_f32_32x32x16_bf16 v[32:47], v[16:19], v[120:123], v[32:47]\n"
    "ds_write_b128 v161, v[132:135] offset:1024\n"
    "v_add_u32 v161, 0x10000, v161\n"
    "v_mfma_f32_32x32x16_bf16 v[48:63], v[20:23], v[120:123], v[48:63]\n"
    "ds_write_b128 v162, v[136:139] offset:2048\n"
    "v_add_u32 v162, 0x10000, v162\n"
    "v_mfma_f32_32x32x16_bf16 v[32:47], v[24:27], v[124:127], v[32:47]\n"
    "ds_write_b128 v163, v[140:143] offset:3072\n"
    "v_add_u32 v163, 0x10000, v163\n"
    "v_mfma_f32_32x32x16_bf16 v[48:63], v[28:31], v[124:127], v[48:63]\n"
    "ds_write_b128 v164, v[144:147] offset:4096\n"
    "v_add_u32 v164, 0x10000, v164\n"
    "ds_write_b128 v165, v[148:151] offset:5120\n"
    "v_add_u32 v165, 0x10000, v165\n"
    "ds_write_b128 v166, v[152:155] offset:6144\n"
    "v_add_u32 v166, 0x10000, v166\n"
    "ds_write_b128 v167, v[156:159] offset:7168\n"
    "v_add_u32 v167, 0x10000, v167\n"
    "s_waitcnt vmcnt(0) lgkmcnt(0)\n"
    "s_barrier\n"
    "ds_read_b128 v[128:131], v168 offset:0\n"
    "ds_read_b128 v[132:135], v168 offset:8192\n"
    "ds_read_b128 v[136:139], v168 offset:16384\n"
    "ds_read_b128 v[140:143], v168 offset:24576\n"
    "ds_read_b128 v[144:147], v168 offset:32768\n"
    "ds_read_b128 v[148:151], v168 offset:40960\n"
    "ds_read_b128 v[152:155], v168 offset:49152\n"
    "ds_read_b128 v[156:159], v168 offset:57344\n"
    "ds_read_b64 v[170:171], v175 offset:0\n"
    "ds_read_b64 v[172:173], v175 offset:4096\n"
    "v_add_u32 v168, 0x10000, v168\n"
    ".Lgc_74:\n"
    "v_max_u32 v174, v80, v88\n"
    "v_max3_u32 v174, v96, v104, v174\n"
    "v_max3_u32 v174, v84, v92, v174\n"
    "v_max3_u32 v174, v100, v108, v174\n"
    "v_cmp_eq_u32 vcc, -1, v174\n"
    "s_nop 0\n"
    "s_cbranch_vccnz .Lgr_75\n"
    ".Lgo_76:\n"
    "v_mfma_f32_32x32x16_bf16 v[32:47], v[176:179], v[80:83], v[32:47]\n"
    "s_waitcnt lgkmcnt(8)\n"
    "v_add_f32 v128, v128, v132\n"
    "v_add_f32 v129, v129, v133\n"
    "v_add_f32 v130, v130, v134\n"
    "v_add_f32 v131, v131, v135\n"
    "s_waitcnt lgkmcnt(6)\n"
    "v_add_f32 v136, v136, v140\n"
    "v_mfma_f32_32x32x16_bf16 v[32:47], v[192:195], v[88:91], v[32:47]\n"
    "v_add_f32 v137, v137, v141\n"
    "v_add_f32 v138, v138, v142\n"
    "v_add_f32 v139, v139, v143\n"
    "s_waitcnt lgkmcnt(4)\n"
    "v_add_f32 v144, v144, v148\n"
    "v_add_f32 v145, v145, v149\n"
    "v_mfma_f32_32x32x16_bf16 v[32:47], v[208:211], v[96:99], v[32:47]\n"
    "v_add_f32 v146, v146, v150\n"
    "v_add_f32 v147, v147, v151\n"
    "s_waitcnt lgkmcnt(2)\n"
    "v_add_f32 v152, v152, v156\n"
    "v_add_f32 v153, v153, v157\n"
    "v_add_f32 v154, v154, v158\n"
    "v_mfma_f32_32x32x16_bf16 v[32:47], v[224:227], v[104:107], v[32:47]\n"
    "v_add_f32 v155, v155, v159\n"
    "v_add_f32 v128, v128, v136\n"
    "v_add_f32 v129, v129, v137\n"
    "v_add_f32 v130, v130, v138\n"
    "v_add_f32 v131, v131, v139\n"
    "v_mfma_f32_32x32x16_bf16 v[32:47], v[184:187], v[84:87], v[32:47]\n"
    "v_add_f32 v144, v144, v152\n"
    "v_add_f32 v145, v145, v153\n"
    "v_add_f32 v146, v146, v154\n"
    "v_add_f32 v147, v147, v155\n"
    "v_add_f32 v128, v128, v144\n"
    "v_mfma_f32_32x32x16_bf16 v[32:47], v[200:203], v[92:95], v[32:47]\n"
    "v_add_f32 v129, v129, v145\n"
    "v_add_f32 v130, v130, v146\n"
    "v_add_f32 v131, v131, v147\n"
    "s_waitcnt lgkmcnt(1)\n"
    "v_lshlrev_b32 v64, 16, v170\n"
    "v_and_b32 v65, 0xffff0000, v170\n"
    "v_mfma_f32_32x32x16_bf16 v[32:47], v[216:219], v[100:103], v[32:47]\n"
    "v_lshlrev_b32 v66, 16, v171\n"
    "v_and_b32 v67, 0xffff0000, v171\n"
    "v_fma_f32 v128, v128, v250, v64\n"
    "v_fma_f32 v129, v129, v251, v65\n"
    "v_fma_f32 v130, v130, v252, v66\n"
    "v_mfma_f32_32x32x16_bf16 v[32:47], v[232:235], v[108:111], v[32:47]\n"
    "v_fma_f32 v131, v131, v253, v67\n"
    "v_cvt_pk_bf16_f32 v132, v128, v129\n"
    "v_cvt_pk_bf16_f32 v133, v130, v131\n"
    "buffer_store_dwordx2 v[132:133], v169, s[44:47], s68 offen sc0 sc1\n"
    "s_waitcnt lgkmcnt(0)\n"
    "v_lshlrev_b32 v64, 16, v172\n"
    "v_mfma_f32_32x32x16_bf16 v[48:63], v[180:183], v[80:83], v[48:63]\n"
    "v_and_b32 v65, 0xffff0000, v172\n"
    "v_lshlrev_b32 v66, 16, v173\n"
    "v_and_b32 v67, 0xffff0000, v173\n"
    "v_mfma_f32_32x32x16_bf16 v[48:63], v[196:199], v[88:91], v[48:63]\n"
    "v_mfma_f32_32x32x16_bf16 v[48:63], v[212:215], v[96:99], v[48:63]\n"
    "v_mfma_f32_32x32x16_bf16 v[48:63], v[228:231], v[104:107], v[48:63]\n"
    "ds_write_b128 v160, v[32:35] offset:0\n"
    "v_mfma_f32_32x32x16_bf16 v[48:63], v[188:191], v[84:87], v[48:63]\n"
    "ds_write_b128 v161, v[36:39] offset:1024\n"
    "v_mfma_f32_32x32x16_bf16 v[48:63], v[204:207], v[92:95], v[48:63]\n"
    "ds_write_b128 v162, v[40:43] offset:2048\n"
    "v_mfma_f32_32x32x16_bf16 v[48:63], v[220:223], v[100:103], v[48:63]\n"
    "ds_write_b128 v163, v[44:47] offset:3072\n"
    "v_mfma_f32_32x32x16_bf16 v[48:63], v[236:239], v[108:111], v[48:63]\n"
    "s_nop 11\n"
    "ds_write_b128 v164, v[48:51] offset:4096\n"
    "ds_write_b128 v165, v[52:55] offset:5120\n"
    "ds_write_b128 v166, v[56:59] offset:6144\n"
    "ds_write_b128 v167, v[60:63] offset:7168\n"
    "s_waitcnt lgkmcnt(0)\n"
    "s_barrier\n"
    "ds_read_b128 v[128:131], v168 offset:0\n"
    "ds_read_b128 v[132:135], v168 offset:8192\n"
    "ds_read_b128 v[136:139], v168 offset:16384\n"
    "ds_read_b128 v[140:143], v168 offset:24576\n"
    "ds_read_b128 v[144:147], v168 offset:32768\n"
    "ds_read_b128 v[148:151], v168 offset:40960\n"
    "ds_read_b128 v[152:155], v168 offset:49152\n"
    "ds_read_b128 v[156:159], v168 offset:57344\n"
    "s_waitcnt lgkmcnt(6)\n"
    "v_pk_add_f32 v[128:129], v[128:129], v[132:133]\n"
    "v_pk_add_f32 v[130:131], v[130:131], v[134:135]\n"
    "s_waitcnt lgkmcnt(4)\n"
    "v_pk_add_f32 v[136:137], v[136:137], v[140:141]\n"
    "v_pk_add_f32 v[138:139], v[138:139], v[142:143]\n"
    "s_waitcnt lgkmcnt(2)\n"
    "v_pk_add_f32 v[144:145], v[144:145], v[148:149]\n"
    "v_pk_add_f32 v[146:147], v[146:147], v[150:151]\n"
    "s_waitcnt lgkmcnt(0)\n"
    "v_pk_add_f32 v[152:153], v[152:153], v[156:157]\n"
    "v_pk_add_f32 v[154:155], v[154:155], v[158:159]\n"
    "v_pk_add_f32 v[128:129], v[128:129], v[136:137]\n"
    "v_pk_add_f32 v[130:131], v[130:131], v[138:139]\n"
    "v_pk_add_f32 v[144:145], v[144:145], v[152:153]\n"
    "v_pk_add_f32 v[146:147], v[146:147], v[154:155]\n"
    "v_pk_add_f32 v[128:129], v[128:129], v[144:145]\n"
    "v_pk_add_f32 v[130:131], v[130:131], v[146:147]\n"
    "v_fma_f32 v128, v128, v250, v64\n"
    "v_fma_f32 v129, v129, v251, v65\n"
    "v_fma_f32 v130, v130, v252, v66\n"
    "v_fma_f32 v131, v131, v253, v67\n"
    "v_cvt_pk_bf16_f32 v132, v128, v129\n"
    "v_cvt_pk_bf16_f32 v133, v130, v131\n"
    "buffer_store_dwordx2 v[132:133], v169, s[44:47], s69 offen sc0 sc1\n"
    "s_branch .Lfin\n"
    ".Lv_own:\n"
    ".Lpad4183:\n"
    "s_cbranch_execz .Lpad7221\n"
    "s_cmp_lt_u32 s61, 3\n"
    "s_cbranch_scc1 .Lv_A1\n"
    "v_add_u32 v242, 139264, v241\n"
    "ds_read_b128 v[32:35], v242 offset:0\n"
    "ds_read_b128 v[36:39], v242 offset:1024\n"
    "ds_read_b128 v[40:43], v242 offset:4096\n"
    "ds_read_b128 v[44:47], v242 offset:5120\n"
    "ds_read_b128 v[48:51], v242 offset:8192\n"
    "ds_read_b128 v[52:55], v242 offset:9216\n"
    "ds_read_b128 v[56:59], v242 offset:12288\n"
    "ds_read_b128 v[60:63], v242 offset:13312\n"
    "ds_read_b128 v[64:67], v242 offset:16384\n"
    "ds_read_b128 v[68:71], v242 offset:17408\n"
    "ds_read_b128 v[112:115], v242 offset:18432\n"
    "ds_read_b128 v[116:119], v242 offset:19456\n"
    "v_lshrrev_b32 v168, 5, v240\n"
    "v_lshlrev_b32 v175, 4, v168\n"
    "v_xor_b32 v175, v175, v241\n"
    "s_lshl_b32 s67, s48, 13\n"
    "v_add_u32 v160, s67, v175\n"
    "v_xor_b32 v161, 32, v160\n"
    "v_xor_b32 v162, 64, v160\n"
    "v_xor_b32 v163, 96, v160\n"
    "v_xor_b32 v164, 128, v160\n"
    "v_xor_b32 v165, 160, v160\n"
    "v_xor_b32 v166, 192, v160\n"
    "v_xor_b32 v167, 224, v160\n"
    "v_bfe_u32 v175, v240, 4, 1\n"
    "v_lshl_or_b32 v168, v175, 1, v168\n"
    "v_lshl_add_u32 v168, v168, 3, s48\n"
    "v_and_b32 v175, 15, v240\n"
    "v_lshlrev_b32 v169, 3, v175\n"
    "v_lshl_or_b32 v169, v168, 10, v169\n"
    "v_xor_b32 v168, v168, v175\n"
    "v_lshlrev_b32 v168, 4, v168\n"
    "v_lshl_or_b32 v168, v175, 9, v168\n"
    "s_lshl_b32 s68, s49, 16\n"
    "s_lshl_b32 s69, s50, 7\n"
    "s_add_u32 s68, s68, s69\n"
    "s_add_u32 s69, s68, 32768\n"
    "v_lshrrev_b32 v170, 10, v169\n"
    "v_lshlrev_b32 v175, 3, v175\n"
    "v_lshl_or_b32 v175, v170, 7, v175\n"
    "v_add_u32 v175, 131072, v175\n"
    "v_lshrrev_b32 v170, 3, v240\n"
    "v_and_b32 v171, 7, v240\n"
    "v_lshlrev_b32 v171, 4, v171\n"
    "v_lshl_or_b32 v170, v170, 10, v171\n"
    "s_lshl_b32 s62, s61, 13\n"
    "s_add_u32 s62, s62, s68\n"
    "s_add_u32 s62, s62, 8388608\n"
    "s_lshl_b32 s63, s61, 10\n"
    "s_add_u32 m0, s63, 131072\n"
    ".Lnt_77:\n"
    "s_waitcnt vmcnt(23) lgkmcnt(11)\n"
    "v_mfma_f32_32x32x16_bf16 v[128:143], v[176:179], v[32:35], 0\n"
    "s_waitcnt vmcnt(22)\n"
    "v_mfma_f32_32x32x16_bf16 v[144:159], v[180:183], v[32:35], 0\n"
    "s_waitcnt vmcnt(21) lgkmcnt(10)\n"
    "v_mfma_f32_32x32x16_bf16 v[128:143], v[184:187], v[36:39], v[128:143]\n"
    "s_waitcnt vmcnt(20)\n"
    "v_mfma_f32_32x32x16_bf16 v[144:159], v[188:191], v[36:39], v[144:159]\n"
    "s_waitcnt vmcnt(19) lgkmcnt(9)\n"
    "v_mfma_f32_32x32x16_bf16 v[128:143], v[192:195], v[40:43], v[128:143]\n"
    "s_waitcnt vmcnt(18)\n"
    "v_mfma_f32_32x32x16_bf16 v[144:159], v[196:199], v[40:43], v[144:159]\n"
    "s_waitcnt vmcnt(17) lgkmcnt(8)\n"
    "v_mfma_f32_32x32x16_bf16 v[128:143], v[200:203], v[44:47], v[128:143]\n"
    "s_waitcnt vmcnt(16)\n"
    "v_mfma_f32_32x32x16_bf16 v[144:159], v[204:207], v[44:47], v[144:159]\n"
    "s_waitcnt vmcnt(15) lgkmcnt(7)\n"
    "s_nop 0\n"
    ".Lpad4256:\n"
    "s_cbranch_execz .Lpad7867\n"
    "v_mfma_f32_32x32x16_bf16 v[128:143], v[208:211], v[48:51], v[128:143]\n"
    "buffer_load_dwordx4 v170, s[12:15], s62 offen sc1 lds\n"
    "s_waitcnt vmcnt(15)\n"
    "v_mfma_f32_32x32x16_bf16 v[144:159], v[212:215], v[48:51], v[144:159]\n"
    "s_waitcnt vmcnt(14) lgkmcnt(6)\n"
    "v_mfma_f32_32x32x16_bf16 v[128:143], v[216:219], v[52:55], v[128:143]\n"
    "s_waitcnt vmcnt(13)\n"
    "v_mfma_f32_32x32x16_bf16 v[144:159], v[220:223], v[52:55], v[144:159]\n"
    "s_waitcnt vmcnt(12) lgkmcnt(5)\n"
    "v_mfma_f32_32x32x16_bf16 v[128:143], v[224:227], v[56:59], v[128:143]\n"
    "s_waitcnt vmcnt(10)\n"
    "v_mfma_f32_32x32x16_bf16 v[144:159], v[228:231], v[56:59], v[144:159]\n"
    "s_waitcnt vmcnt(9) lgkmcnt(4)\n"
    "v_mfma_f32_32x32x16_bf16 v[128:143], v[232:235], v[60:63], v[128:143]\n"
    "s_waitcnt vmcnt(8)\n"
    "v_mfma_f32_32x32x16_bf16 v[144:159], v[236:239], v[60:63], v[144:159]\n"
    "s_waitcnt vmcnt(6) lgkmcnt(3)\n"
    "v_mfma_f32_32x32x16_bf16 v[128:143], v[0:3], v[64:67], v[128:143]\n"
    "s_waitcnt vmcnt(5)\n"
    "v_mfma_f32_32x32x16_bf16 v[144:159], v[4:7], v[64:67], v[144:159]\n"
    "s_waitcnt vmcnt(4) lgkmcnt(2)\n"
    "v_mfma_f32_32x32x16_bf16 v[128:143], v[8:11], v[68:71], v[128:143]\n"
    "s_waitcnt vmcnt(3)\n"
    "v_mfma_f32_32x32x16_bf16 v[144:159], v[12:15], v[68:71], v[144:159]\n"
    "s_waitcnt lgkmcnt(1)\n"
    "v_mfma_f32_32x32x16_bf16 v[32:47], v[0:3], v[112:115], 0\n"
    "v_mfma_f32_32x32x16_bf16 v[48:63], v[4:7], v[112:115], 0\n"
    "s_waitcnt lgkmcnt(0)\n"
    "v_mfma_f32_32x32x16_bf16 v[32:47], v[8:11], v[116:119], v[32:47]\n"
    "v_mfma_f32_32x32x16_bf16 v[48:63], v[12:15], v[116:119], v[48:63]\n"
    "s_nop 3\n"
    "ds_write_b128 v160, v[128:131] offset:0\n"
    "v_add_u32 v160, 0x10000, v160\n"
    "ds_write_b128 v161, v[132:135] offset:1024\n"
    "v_add_u32 v161, 0x10000, v161\n"
    "ds_write_b128 v162, v[136:139] offset:2048\n"
    "v_add_u32 v162, 0x10000, v162\n"
    "ds_write_b128 v163, v[140:143] offset:3072\n"
    "v_add_u32 v163, 0x10000, v163\n"
    "ds_write_b128 v164, v[144:147] offset:4096\n"
    "v_add_u32 v164, 0x10000, v164\n"
    "ds_write_b128 v165, v[148:151] offset:5120\n"
    "v_add_u32 v165, 0x10000, v165\n"
    "ds_write_b128 v166, v[152:155] offset:6144\n"
    "v_add_u32 v166, 0x10000, v166\n"
    "ds_write_b128 v167, v[156:159] offset:7168\n"
    "v_add_u32 v167, 0x10000, v167\n"
    "s_waitcnt vmcnt(0) lgkmcnt(0)\n"
    "s_barrier\n"
    "ds_read_b128 v[80:83], v242 offset:2048\n"
    "ds_read_b128 v[88:91], v242 offset:6144\n"
    "ds_read_b128 v[96:99], v242 offset:10240\n"
    "ds_read_b128 v[104:107], v242 offset:14336\n"
    "ds_read_b128 v[84:87], v242 offset:3072\n"
    "ds_read_b128 v[92:95], v242 offset:7168\n"
    "ds_read_b128 v[100:103], v242 offset:11264\n"
    "ds_read_b128 v[108:111], v242 offset:15360\n"
    "ds_read_b128 v[128:131], v168 offset:0\n"
    "ds_read_b128 v[132:135], v168 offset:8192\n"
    "ds_read_b128 v[136:139], v168 offset:16384\n"
    "ds_read_b128 v[140:143], v168 offset:24576\n"
    "ds_read_b128 v[144:147], v168 offset:32768\n"
    "ds_read_b128 v[148:151], v168 offset:40960\n"
    "ds_read_b128 v[152:155], v168 offset:49152\n"
    "ds_read_b128 v[156:159], v168 offset:57344\n"
    "ds_read_b64 v[170:171], v175 offset:0\n"
    "ds_read_b64 v[172:173], v175 offset:4096\n"
    "v_add_u32 v168, 0x10000, v168\n"
    "s_waitcnt lgkmcnt(15)\n"
    "v_mfma_f32_32x32x16_bf16 v[32:47], v[176:179], v[80:83], v[32:47]\n"
    "s_waitcnt lgkmcnt(8)\n"
    "v_add_f32 v128, v128, v132\n"
    "v_add_f32 v129, v129, v133\n"
    "v_add_f32 v130, v130, v134\n"
    "v_add_f32 v131, v131, v135\n"
    "s_waitcnt lgkmcnt(6)\n"
    "v_add_f32 v136, v136, v140\n"
    "v_mfma_f32_32x32x16_bf16 v[32:47], v[192:195], v[88:91], v[32:47]\n"
    "v_add_f32 v137, v137, v141\n"
    "v_add_f32 v138, v138, v142\n"
    "v_add_f32 v139, v139, v143\n"
    "s_waitcnt lgkmcnt(4)\n"
    "v_add_f32 v144, v144, v148\n"
    "v_add_f32 v145, v145, v149\n"
    "v_mfma_f32_32x32x16_bf16 v[32:47], v[208:211], v[96:99], v[32:47]\n"
    "v_add_f32 v146, v146, v150\n"
    "v_add_f32 v147, v147, v151\n"
    "s_waitcnt lgkmcnt(2)\n"
    "v_add_f32 v152, v152, v156\n"
    "v_add_f32 v153, v153, v157\n"
    "v_add_f32 v154, v154, v158\n"
    "v_mfma_f32_32x32x16_bf16 v[32:47], v[224:227], v[104:107], v[32:47]\n"
    "v_add_f32 v155, v155, v159\n"
    "v_add_f32 v128, v128, v136\n"
    "v_add_f32 v129, v129, v137\n"
    "v_add_f32 v130, v130, v138\n"
    "v_add_f32 v131, v131, v139\n"
    "v_mfma_f32_32x32x16_bf16 v[32:47], v[184:187], v[84:87], v[32:47]\n"
    "v_add_f32 v144, v144, v152\n"
    "v_add_f32 v145, v145, v153\n"
    "v_add_f32 v146, v146, v154\n"
    "v_add_f32 v147, v147, v155\n"
    "v_add_f32 v128, v128, v144\n"
    "v_mfma_f32_32x32x16_bf16 v[32:47], v[200:203], v[92:95], v[32:47]\n"
    "v_add_f32 v129, v129, v145\n"
    "v_add_f32 v130, v130, v146\n"
    "v_add_f32 v131, v131, v147\n"
    "s_waitcnt lgkmcnt(1)\n"
    "v_lshlrev_b32 v64, 16, v170\n"
    "v_and_b32 v65, 0xffff0000, v170\n"
    "v_mfma_f32_32x32x16_bf16 v[32:47], v[216:219], v[100:103], v[32:47]\n"
    "v_lshlrev_b32 v66, 16, v171\n"
    "v_and_b32 v67, 0xffff0000, v171\n"
    "v_fma_f32 v128, v128, v250, v64\n"
    "v_fma_f32 v129, v129, v251, v65\n"
    "v_fma_f32 v130, v130, v252, v66\n"
    "v_mfma_f32_32x32x16_bf16 v[32:47], v[232:235], v[108:111], v[32:47]\n"
    "v_fma_f32 v131, v131, v253, v67\n"
    "v_cvt_pk_bf16_f32 v132, v128, v129\n"
    "v_cvt_pk_bf16_f32 v133, v130, v131\n"
    "buffer_store_dwordx2 v[132:133], v169, s[44:47], s68 offen sc0 sc1\n"
    "s_waitcnt lgkmcnt(0)\n"
    "v_lshlrev_b32 v64, 16, v172\n"
    "v_mfma_f32_32x32x16_bf16 v[48:63], v[180:183], v[80:83], v[48:63]\n"
    "v_and_b32 v65, 0xffff0000, v172\n"
    "v_lshlrev_b32 v66, 16, v173\n"
    "v_and_b32 v67, 0xffff0000, v173\n"
    "v_mfma_f32_32x32x16_bf16 v[48:63], v[196:199], v[88:91], v[48:63]\n"
    "v_mfma_f32_32x32x16_bf16 v[48:63], v[212:215], v[96:99], v[48:63]\n"
    "v_mfma_f32_32x32x16_bf16 v[48:63], v[228:231], v[104:107], v[48:63]\n"
    "ds_write_b128 v160, v[32:35] offset:0\n"
    "v_mfma_f32_32x32x16_bf16 v[48:63], v[188:191], v[84:87], v[48:63]\n"
    "ds_write_b128 v161, v[36:39] offset:1024\n"
    "v_mfma_f32_32x32x16_bf16 v[48:63], v[204:207], v[92:95], v[48:63]\n"
    "ds_write_b128 v162, v[40:43] offset:2048\n"
    "v_mfma_f32_32x32x16_bf16 v[48:63], v[220:223], v[100:103], v[48:63]\n"
    "ds_write_b128 v163, v[44:47] offset:3072\n"
    "v_mfma_f32_32x32x16_bf16 v[48:63], v[236:239], v[108:111], v[48:63]\n"
    "s_nop 11\n"
    "ds_write_b128 v164, v[48:51] offset:4096\n"
    "ds_write_b128 v165, v[52:55] offset:5120\n"
    "ds_write_b128 v166, v[56:59] offset:6144\n"
    "ds_write_b128 v167, v[60:63] offset:7168\n"
    "s_waitcnt lgkmcnt(0)\n"
    "s_barrier\n"
    "ds_read_b128 v[128:131], v168 offset:0\n"
    "ds_read_b128 v[132:135], v168 offset:8192\n"
    "ds_read_b128 v[136:139], v168 offset:16384\n"
    "ds_read_b128 v[140:143], v168 offset:24576\n"
    "ds_read_b128 v[144:147], v168 offset:32768\n"
    "ds_read_b128 v[148:151], v168 offset:40960\n"
    "ds_read_b128 v[152:155], v168 offset:49152\n"
    "ds_read_b128 v[156:159], v168 offset:57344\n"
    "s_waitcnt lgkmcnt(6)\n"
    "v_pk_add_f32 v[128:129], v[128:129], v[132:133]\n"
    "v_pk_add_f32 v[130:131], v[130:131], v[134:135]\n"
    "s_waitcnt lgkmcnt(4)\n"
    "v_pk_add_f32 v[136:137], v[136:137], v[140:141]\n"
    "v_pk_add_f32 v[138:139], v[138:139], v[142:143]\n"
    "s_waitcnt lgkmcnt(2)\n"
    "v_pk_add_f32 v[144:145], v[144:145], v[148:149]\n"
    "v_pk_add_f32 v[146:147], v[146:147], v[150:151]\n"
    "s_waitcnt lgkmcnt(0)\n"
    "v_pk_add_f32 v[152:153], v[152:153], v[156:157]\n"
    "v_pk_add_f32 v[154:155], v[154:155], v[158:159]\n"
    "v_pk_add_f32 v[128:129], v[128:129], v[136:137]\n"
    "v_pk_add_f32 v[130:131], v[130:131], v[138:139]\n"
    "v_pk_add_f32 v[144:145], v[144:145], v[152:153]\n"
    "v_pk_add_f32 v[146:147], v[146:147], v[154:155]\n"
    "v_pk_add_f32 v[128:129], v[128:129], v[144:145]\n"
    "v_pk_add_f32 v[130:131], v[130:131], v[146:147]\n"
    "v_fma_f32 v128, v128, v250, v64\n"
    "v_fma_f32 v129, v129, v251, v65\n"
    "v_fma_f32 v130, v130, v252, v66\n"
    "v_fma_f32 v131, v131, v253, v67\n"
    "v_cvt_pk_bf16_f32 v132, v128, v129\n"
    "v_cvt_pk_bf16_f32 v133, v130, v131\n"
    "buffer_store_dwordx2 v[132:133], v169, s[44:47], s69 offen sc0 sc1\n"
    "s_branch .Lfin\n"
    ".Lv_A1:\n"
    ".Lpad4437:\n"
    "s_cbranch_execz .Lpad7912\n"
    "v_add_u32 v242, 139264, v241\n"
    "ds_read_b128 v[32:35], v242 offset:0\n"
    "ds_read_b128 v[36:39], v242 offset:1024\n"
    "ds_read_b128 v[40:43], v242 offset:4096\n"
    "ds_read_b128 v[44:47], v242 offset:5120\n"
    "ds_read_b128 v[48:51], v242 offset:8192\n"
    "ds_read_b128 v[52:55], v242 offset:9216\n"
    "ds_read_b128 v[56:59], v242 offset:12288\n"
    "ds_read_b128 v[60:63], v242 offset:13312\n"
    "ds_read_b128 v[64:67], v242 offset:16384\n"
    "ds_read_b128 v[68:71], v242 offset:17408\n"
    "ds_read_b128 v[72:75], v242 offset:20480\n"
    "ds_read_b128 v[76:79], v242 offset:21504\n"
    "ds_read_b128 v[112:115], v242 offset:18432\n"
    "ds_read_b128 v[116:119], v242 offset:19456\n"
    "ds_read_b128 v[120:123], v242 offset:22528\n"
    "ds_read_b128 v[124:127], v242 offset:23552\n"
    "v_lshrrev_b32 v168, 5, v240\n"
    "v_lshlrev_b32 v175, 4, v168\n"
    "v_xor_b32 v175, v175, v241\n"
    "s_lshl_b32 s67, s48, 13\n"
    "v_add_u32 v160, s67, v175\n"
    "v_xor_b32 v161, 32, v160\n"
    "v_xor_b32 v162, 64, v160\n"
    "v_xor_b32 v163, 96, v160\n"
    "v_xor_b32 v164, 128, v160\n"
    "v_xor_b32 v165, 160, v160\n"
    "v_xor_b32 v166, 192, v160\n"
    "v_xor_b32 v167, 224, v160\n"
    "v_bfe_u32 v175, v240, 4, 1\n"
    "v_lshl_or_b32 v168, v175, 1, v168\n"
    "v_lshl_add_u32 v168, v168, 3, s48\n"
    "v_and_b32 v175, 15, v240\n"
    "v_lshlrev_b32 v169, 3, v175\n"
    "v_lshl_or_b32 v169, v168, 10, v169\n"
    "v_xor_b32 v168, v168, v175\n"
    "v_lshlrev_b32 v168, 4, v168\n"
    "v_lshl_or_b32 v168, v175, 9, v168\n"
    "s_lshl_b32 s68, s49, 16\n"
    "s_lshl_b32 s69, s50, 7\n"
    "s_add_u32 s68, s68, s69\n"
    "s_add_u32 s69, s68, 32768\n"
    "v_lshrrev_b32 v170, 10, v169\n"
    "v_lshlrev_b32 v175, 3, v175\n"
    "v_lshl_or_b32 v175, v170, 7, v175\n"
    "v_add_u32 v175, 131072, v175\n"
    "v_lshrrev_b32 v170, 3, v240\n"
    "v_and_b32 v171, 7, v240\n"
    "v_lshlrev_b32 v171, 4, v171\n"
    "v_lshl_or_b32 v170, v170, 10, v171\n"
    "s_lshl_b32 s62, s61, 13\n"
    "s_add_u32 s62, s62, s68\n"
    "s_add_u32 s62, s62, 8388608\n"
    "s_lshl_b32 s63, s61, 10\n"
    "s_add_u32 m0, s63, 131072\n"
    ".Lnt_78:\n"
    "s_waitcnt vmcnt(23) lgkmcnt(15)\n"
    "v_mfma_f32_32x32x16_bf16 v[128:143], v[176:179], v[32:35], 0\n"
    "s_waitcnt vmcnt(22)\n"
    "v_mfma_f32_32x32x16_bf16 v[144:159], v[180:183], v[32:35], 0\n"
    "s_waitcnt vmcnt(21) lgkmcnt(14)\n"
    "v_mfma_f32_32x32x16_bf16 v[128:143], v[184:187], v[36:39], v[128:143]\n"
    "s_waitcnt vmcnt(20)\n"
    "v_mfma_f32_32x32x16_bf16 v[144:159], v[188:191], v[36:39], v[144:159]\n"
    "s_waitcnt vmcnt(19) lgkmcnt(13)\n"
    "v_mfma_f32_32x32x16_bf16 v[128:143], v[192:195], v[40:43], v[128:143]\n"
    "s_waitcnt vmcnt(18)\n"
    "v_mfma_f32_32x32x16_bf16 v[144:159], v[196:199], v[40:43], v[144:159]\n"
    "s_waitcnt vmcnt(17) lgkmcnt(12)\n"
    "v_mfma_f32_32x32x16_bf16 v[128:143], v[200:203], v[44:47], v[128:143]\n"
    "s_waitcnt vmcnt(16)\n"
    "v_mfma_f32_32x32x16_bf16 v[144:159], v[204:207], v[44:47], v[144:159]\n"
    "s_waitcnt vmcnt(15) lgkmcnt(11)\n"
    "v_mfma_f32_32x32x16_bf16 v[128:143], v[208:211], v[48:51], v[128:143]\n"
    "buffer_load_dwordx4 v170, s[12:15], s62 offen sc1 lds\n"
    "s_waitcnt vmcnt(15)\n"
    "v_mfma_f32_32x32x16_bf16 v[144:159], v[212:215], v[48:51], v[144:159]\n"
    "s_waitcnt vmcnt(14) lgkmcnt(10)\n"
    "v_mfma_f32_32x32x16_bf16 v[128:143], v[216:219], v[52:55], v[128:143]\n"
    "s_waitcnt vmcnt(13)\n"
    "v_mfma_f32_32x32x16_bf16 v[144:159], v[220:223], v[52:55], v[144:159]\n"
    "s_waitcnt vmcnt(12) lgkmcnt(9)\n"
    "v_mfma_f32_32x32x16_bf16 v[128:143], v[224:227], v[56:59], v[128:143]\n"
    "s_waitcnt vmcnt(10)\n"
    "v_mfma_f32_32x32x16_bf16 v[144:159], v[228:231], v[56:59], v[144:159]\n"
    "s_waitcnt vmcnt(9) lgkmcnt(8)\n"
    "v_mfma_f32_32x32x16_bf16 v[128:143], v[232:235], v[60:63], v[128:143]\n"
    "s_waitcnt vmcnt(8)\n"
    "v_mfma_f32_32x32x16_bf16 v[144:159], v[236:239], v[60:63], v[144:159]\n"
    "s_waitcnt vmcnt(6) lgkmcnt(7)\n"
    "v_mfma_f32_32x32x16_bf16 v[128:143], v[0:3], v[64:67], v[128:143]\n"
    "s_waitcnt vmcnt(5)\n"
    "v_mfma_f32_32x32x16_bf16 v[144:159], v[4:7], v[64:67], v[144:159]\n"
    "s_waitcnt vmcnt(4) lgkmcnt(6)\n"
    "v_mfma_f32_32x32x16_bf16 v[128:143], v[8:11], v[68:71], v[128:143]\n"
    "s_waitcnt vmcnt(3)\n"
    "v_mfma_f32_32x32x16_bf16 v[144:159], v[12:15], v[68:71], v[144:159]\n"
    "s_waitcnt lgkmcnt(5)\n"
    "v_mfma_f32_32x32x16_bf16 v[128:143], v[16:19], v[72:75], v[128:143]\n"
    "v_mfma_f32_32x32x16_bf16 v[144:159], v[20:23], v[72:75], v[144:159]\n"
    "s_waitcnt lgkmcnt(4)\n"
    "v_mfma_f32_32x32x16_bf16 v[128:143], v[24:27], v[76:79], v[128:143]\n"
    "v_mfma_f32_32x32x16_bf16 v[144:159], v[28:31], v[76:79], v[144:159]\n"
    "s_waitcnt lgkmcnt(3)\n"
    "v_mfma_f32_32x32x16_bf16 v[32:47], v[0:3], v[112:115], 0\n"
    "v_mfma_f32_32x32x16_bf16 v[48:63], v[4:7], v[112:115], 0\n"
    "s_waitcnt lgkmcnt(2)\n"
    "v_mfma_f32_32x32x16_bf16 v[32:47], v[8:11], v[116:119], v[32:47]\n"
    "v_mfma_f32_32x32x16_bf16 v[48:63], v[12:15], v[116:119], v[48:63]\n"
    "s_nop 4\n"
    "ds_write_b128 v160, v[128:131] offset:0\n"
    "v_add_u32 v160, 0x10000, v160\n"
    "s_waitcnt lgkmcnt(2)\n"
    "v_mfma_f32_32x32x16_bf16 v[32:47], v[16:19], v[120:123], v[32:47]\n"
    "ds_write_b128 v161, v[132:135] offset:1024\n"
    "v_add_u32 v161, 0x10000, v161\n"
    "v_mfma_f32_32x32x16_bf16 v[48:63], v[20:23], v[120:123], v[48:63]\n"
    "ds_write_b128 v162, v[136:139] offset:2048\n"
    "v_add_u32 v162, 0x10000, v162\n"
    "s_waitcnt lgkmcnt(3)\n"
    "v_mfma_f32_32x32x16_bf16 v[32:47], v[24:27], v[124:127], v[32:47]\n"
    "ds_write_b128 v163, v[140:143] offset:3072\n"
    "v_add_u32 v163, 0x10000, v163\n"
    "v_mfma_f32_32x32x16_bf16 v[48:63], v[28:31], v[124:127], v[48:63]\n"
    "ds_write_b128 v164, v[144:147] offset:4096\n"
    "v_add_u32 v164, 0x10000, v164\n"
    "ds_write_b128 v165, v[148:151] offset:5120\n"
    "v_add_u32 v165, 0x10000, v165\n"
    "ds_write_b128 v166, v[152:155] offset:6144\n"
    "v_add_u32 v166, 0x10000, v166\n"
    "ds_write_b128 v167, v[156:159] offset:7168\n"
    "v_add_u32 v167, 0x10000, v167\n"
    "s_waitcnt vmcnt(0) lgkmcnt(0)\n"
    "s_barrier\n"
    "ds_read_b128 v[80:83], v242 offset:2048\n"
    "ds_read_b128 v[88:91], v242 offset:6144\n"
    "ds_read_b128 v[96:99], v242 offset:10240\n"
    "ds_read_b128 v[104:107], v242 offset:14336\n"
    "ds_read_b128 v[84:87], v242 offset:3072\n"
    "ds_read_b128 v[92:95], v242 offset:7168\n"
    "ds_read_b128 v[100:103], v242 offset:11264\n"
    "ds_read_b128 v[108:111], v242 offset:15360\n"
    "ds_read_b128 v[128:131], v168 offset:0\n"
    "ds_read_b128 v[132:135], v168 offset:8192\n"
    "ds_read_b128 v[136:139], v168 offset:16384\n"
    "ds_read_b128 v[140:143], v168 offset:24576\n"
    "ds_read_b128 v[144:147], v168 offset:32768\n"
    "ds_read_b128 v[148:151], v168 offset:40960\n"
    "ds_read_b128 v[152:155], v168 offset:49152\n"
    "ds_read_b128 v[156:159], v168 offset:57344\n"
    "ds_read_b64 v[170:171], v175 offset:0\n"
    "ds_read_b64 v[172:173], v175 offset:4096\n"
    "v_add_u32 v168, 0x10000, v168\n"
    "s_waitcnt lgkmcnt(15)\n"
    "v_mfma_f32_32x32x16_bf16 v[32:47], v[176:179], v[80:83], v[32:47]\n"
    "s_waitcnt lgkmcnt(8)\n"
    "v_add_f32 v128, v128, v132\n"
    "v_add_f32 v129, v129, v133\n"
    "v_add_f32 v130, v130, v134\n"
    "v_add_f32 v131, v131, v135\n"
    "s_waitcnt lgkmcnt(6)\n"
    "v_add_f32 v136, v136, v140\n"
    "v_mfma_f32_32x32x16_bf16 v[32:47], v[192:195], v[88:91], v[32:47]\n"
    "v_add_f32 v137, v137, v141\n"
    "v_add_f32 v138, v138, v142\n"
    "v_add_f32 v139, v139, v143\n"
    "s_waitcnt lgkmcnt(4)\n"
    "v_add_f32 v144, v144, v148\n"
    "v_add_f32 v145, v145, v149\n"
    "v_mfma_f32_32x32x16_bf16 v[32:47], v[208:211], v[96:99], v[32:47]\n"
    "v_add_f32 v146, v146, v150\n"
    "v_add_f32 v147, v147, v151\n"
    "s_waitcnt lgkmcnt(2)\n"
    "v_add_f32 v152, v152, v156\n"
    "v_add_f32 v153, v153, v157\n"
    "v_add_f32 v154, v154, v158\n"
    "v_mfma_f32_32x32x16_bf16 v[32:47], v[224:227], v[104:107], v[32:47]\n"
    "v_add_f32 v155, v155, v159\n"
    "v_add_f32 v128, v128, v136\n"
    "v_add_f32 v129, v129, v137\n"
    "v_add_f32 v130, v130, v138\n"
    "v_add_f32 v131, v131, v139\n"
    "v_mfma_f32_32x32x16_bf16 v[32:47], v[184:187], v[84:87], v[32:47]\n"
    "v_add_f32 v144, v144, v152\n"
    "v_add_f32 v145, v145, v153\n"
    "v_add_f32 v146, v146, v154\n"
    "v_add_f32 v147, v147, v155\n"
    "v_add_f32 v128, v128, v144\n"
    "v_mfma_f32_32x32x16_bf16 v[32:47], v[200:203], v[92:95], v[32:47]\n"
    "v_add_f32 v129, v129, v145\n"
    "v_add_f32 v130, v130, v146\n"
    "v_add_f32 v131, v131, v147\n"
    "s_waitcnt lgkmcnt(1)\n"
    "v_lshlrev_b32 v64, 16, v170\n"
    "v_and_b32 v65, 0xffff0000, v170\n"
    "v_mfma_f32_32x32x16_bf16 v[32:47], v[216:219], v[100:103], v[32:47]\n"
    "v_lshlrev_b32 v66, 16, v171\n"
    "v_and_b32 v67, 0xffff0000, v171\n"
    "v_fma_f32 v128, v128, v250, v64\n"
    "v_fma_f32 v129, v129, v251, v65\n"
    "v_fma_f32 v130, v130, v252, v66\n"
    "v_mfma_f32_32x32x16_bf16 v[32:47], v[232:235], v[108:111], v[32:47]\n"
    "v_fma_f32 v131, v131, v253, v67\n"
    "v_cvt_pk_bf16_f32 v132, v128, v129\n"
    "v_cvt_pk_bf16_f32 v133, v130, v131\n"
    "buffer_store_dwordx2 v[132:133], v169, s[44:47], s68 offen sc0 sc1\n"
    "s_waitcnt lgkmcnt(0)\n"
    "v_lshlrev_b32 v64, 16, v172\n"
    "v_mfma_f32_32x32x16_bf16 v[48:63], v[180:183], v[80:83], v[48:63]\n"
    "v_and_b32 v65, 0xffff0000, v172\n"
    "v_lshlrev_b32 v66, 16, v173\n"
    "v_and_b32 v67, 0xffff0000, v173\n"
    "v_mfma_f32_32x32x16_bf16 v[48:63], v[196:199], v[88:91], v[48:63]\n"
    "v_mfma_f32_32x32x16_bf16 v[48:63], v[212:215], v[96:99], v[48:63]\n"
    "v_mfma_f32_32x32x16_bf16 v[48:63], v[228:231], v[104:107], v[48:63]\n"
    "ds_write_b128 v160, v[32:35] offset:0\n"
    "v_mfma_f32_32x32x16_bf16 v[48:63], v[188:191], v[84:87], v[48:63]\n"
    "ds_write_b128 v161, v[36:39] offset:1024\n"
    "v_mfma_f32_32x32x16_bf16 v[48:63], v[204:207], v[92:95], v[48:63]\n"
    "ds_write_b128 v162, v[40:43] offset:2048\n"
    "v_mfma_f32_32x32x16_bf16 v[48:63], v[220:223], v[100:103], v[48:63]\n"
    "ds_write_b128 v163, v[44:47] offset:3072\n"
    "v_mfma_f32_32x32x16_bf16 v[48:63], v[236:239], v[108:111], v[48:63]\n"
    "s_nop 11\n"
    "ds_write_b128 v164, v[48:51] offset:4096\n"
    "ds_write_b128 v165, v[52:55] offset:5120\n"
    "ds_write_b128 v166, v[56:59] offset:6144\n"
    "ds_write_b128 v167, v[60:63] offset:7168\n"
    "s_waitcnt lgkmcnt(0)\n"
    "s_barrier\n"
    "ds_read_b128 v[128:131], v168 offset:0\n"
    "ds_read_b128 v[132:135], v168 offset:8192\n"
    "ds_read_b128 v[136:139], v168 offset:16384\n"
    "ds_read_b128 v[140:143], v168 offset:24576\n"
    "ds_read_b128 v[144:147], v168 offset:32768\n"
    "ds_read_b128 v[148:151], v168 offset:40960\n"
    "ds_read_b128 v[152:155], v168 offset:49152\n"
    "ds_read_b128 v[156:159], v168 offset:57344\n"
    "s_waitcnt lgkmcnt(6)\n"
    "v_pk_add_f32 v[128:129], v[128:129], v[132:133]\n"
    "v_pk_add_f32 v[130:131], v[130:131], v[134:135]\n"
    "s_waitcnt lgkmcnt(4)\n"
    "v_pk_add_f32 v[136:137], v[136:137], v[140:141]\n"
    "v_pk_add_f32 v[138:139], v[138:139], v[142:143]\n"
    "s_waitcnt lgkmcnt(2)\n"
    "v_pk_add_f32 v[144:145], v[144:145], v[148:149]\n"
    "v_pk_add_f32 v[146:147], v[146:147], v[150:151]\n"
    "s_waitcnt lgkmcnt(0)\n"
    "v_pk_add_f32 v[152:153], v[152:153], v[156:157]\n"
    "v_pk_add_f32 v[154:155], v[154:155], v[158:159]\n"
    "v_pk_add_f32 v[128:129], v[128:129], v[136:137]\n"
    "v_pk_add_f32 v[130:131], v[130:131], v[138:139]\n"
    "v_pk_add_f32 v[144:145], v[144:145], v[152:153]\n"
    "v_pk_add_f32 v[146:147], v[146:147], v[154:155]\n"
    "v_pk_add_f32 v[128:129], v[128:129], v[144:145]\n"
    "v_pk_add_f32 v[130:131], v[130:131], v[146:147]\n"
    "v_fma_f32 v128, v128, v250, v64\n"
    "v_fma_f32 v129, v129, v251, v65\n"
    "v_fma_f32 v130, v130, v252, v66\n"
    "v_fma_f32 v131, v131, v253, v67\n"
    "v_cvt_pk_bf16_f32 v132, v128, v129\n"
    "v_cvt_pk_bf16_f32 v133, v130, v131\n"
    "buffer_store_dwordx2 v[132:133], v169, s[44:47], s69 offen sc0 sc1\n"
    "s_branch .Lfin\n"
    ".LroleB:\n"
    ".Lpad4703:\n"
    "s_cbranch_execz .Lpad8151\n"
    "v_mov_b64 v[128:129], -1\n"
    "v_mov_b64 v[130:131], -1\n"
    "buffer_store_dwordx4 v[128:131], v208, s[12:15], s54 offen offset:0 sc1\n"
    "buffer_store_dwordx4 v[128:131], v208, s[12:15], s54 offen offset:128 sc1\n"
    "s_lshl_b32 s63, s50, 7\n"
    "s_mul_i32 s62, s50, 393216\n"
    "s_lshl2_add_u32 s64, s49, s51\n"
    "s_lshl_b32 s64, s64, 5\n"
    "s_add_u32 s62, s62, s64\n"
    "s_add_u32 s62, s62, 2048\n"
    "v_lshrrev_b32 v238, 1, v240\n"
    "v_and_b32 v239, 1, v240\n"
    "v_mul_u32_u24 v117, 12288, v238\n"
    "v_lshlrev_b32 v118, 2, v238\n"
    "buffer_load_dword v116, v118, s[8:11], s63 offen\n"
    "v_lshl_add_u32 v117, v239, 4, v117\n"
    "v_mul_u32_u24 v231, 63, v239\n"
    "v_add_u32 v231, v231, v240\n"
    "buffer_load_dwordx4 v[184:187], v117, s[4:7], s62 offen\n"
    "s_add_u32 s62, s62, 6144\n"
    "buffer_load_dwordx4 v[188:191], v117, s[4:7], s62 offen\n"
    "v_pk_fma_f32 v[16:17], v[16:17], v[72:73], v[64:65]\n"
    "v_pk_fma_f32 v[18:19], v[18:19], v[74:75], v[66:67]\n"
    "v_pk_fma_f32 v[20:21], v[20:21], v[76:77], v[68:69]\n"
    "v_pk_fma_f32 v[22:23], v[22:23], v[78:79], v[70:71]\n"
    "v_cvt_pk_bf16_f32 v16, v16, v17\n"
    "v_cvt_pk_bf16_f32 v17, v18, v19\n"
    "v_cvt_pk_bf16_f32 v18, v20, v21\n"
    "v_cvt_pk_bf16_f32 v19, v22, v23\n"
    "ds_write_b128 v250, v[16:19] offset:16384\n"
    "v_pk_fma_f32 v[24:25], v[24:25], v[72:73], v[64:65]\n"
    "v_pk_fma_f32 v[26:27], v[26:27], v[74:75], v[66:67]\n"
    "v_pk_fma_f32 v[28:29], v[28:29], v[76:77], v[68:69]\n"
    "v_pk_fma_f32 v[30:31], v[30:31], v[78:79], v[70:71]\n"
    "v_cvt_pk_bf16_f32 v24, v24, v25\n"
    "v_cvt_pk_bf16_f32 v25, v26, v27\n"
    "v_cvt_pk_bf16_f32 v26, v28, v29\n"
    "v_cvt_pk_bf16_f32 v27, v30, v31\n"
    "ds_write_b128 v251, v[24:27] offset:24576\n"
    "v_pk_fma_f32 v[32:33], v[32:33], v[72:73], v[64:65]\n"
    "v_pk_fma_f32 v[34:35], v[34:35], v[74:75], v[66:67]\n"
    "v_pk_fma_f32 v[36:37], v[36:37], v[76:77], v[68:69]\n"
    "v_pk_fma_f32 v[38:39], v[38:39], v[78:79], v[70:71]\n"
    "v_cvt_pk_bf16_f32 v32, v32, v33\n"
    "v_cvt_pk_bf16_f32 v33, v34, v35\n"
    "v_cvt_pk_bf16_f32 v34, v36, v37\n"
    "v_cvt_pk_bf16_f32 v35, v38, v39\n"
    "ds_write_b128 v250, v[32:35] offset:32768\n"
    "v_pk_fma_f32 v[40:41], v[40:41], v[72:73], v[64:65]\n"
    "v_pk_fma_f32 v[42:43], v[42:43], v[74:75], v[66:67]\n"
    "v_pk_fma_f32 v[44:45], v[44:45], v[76:77], v[68:69]\n"
    "v_pk_fma_f32 v[46:47], v[46:47], v[78:79], v[70:71]\n"
    "v_cvt_pk_bf16_f32 v40, v40, v41\n"
    "v_cvt_pk_bf16_f32 v41, v42, v43\n"
    "v_cvt_pk_bf16_f32 v42, v44, v45\n"
    "v_cvt_pk_bf16_f32 v43, v46, v47\n"
    "ds_write_b128 v251, v[40:43] offset:40960\n"
    "v_pk_fma_f32 v[48:49], v[48:49], v[72:73], v[64:65]\n"
    "v_pk_fma_f32 v[50:51], v[50:51], v[74:75], v[66:67]\n"
    "v_pk_fma_f32 v[52:53], v[52:53], v[76:77], v[68:69]\n"
    "v_pk_fma_f32 v[54:55], v[54:55], v[78:79], v[70:71]\n"
    "v_cvt_pk_bf16_f32 v48, v48, v49\n"
    "v_cvt_pk_bf16_f32 v49, v50, v51\n"
    "v_cvt_pk_bf16_f32 v50, v52, v53\n"
    "v_cvt_pk_bf16_f32 v51, v54, v55\n"
    "ds_write_b128 v250, v[48:51] offset:49152\n"
    "v_pk_fma_f32 v[56:57], v[56:57], v[72:73], v[64:65]\n"
    "v_pk_fma_f32 v[58:59], v[58:59], v[74:75], v[66:67]\n"
    "v_pk_fma_f32 v[60:61], v[60:61], v[76:77], v[68:69]\n"
    "v_pk_fma_f32 v[62:63], v[62:63], v[78:79], v[70:71]\n"
    "v_cvt_pk_bf16_f32 v56, v56, v57\n"
    "v_cvt_pk_bf16_f32 v57, v58, v59\n"
    "v_cvt_pk_bf16_f32 v58, v60, v61\n"
    "v_cvt_pk_bf16_f32 v59, v62, v63\n"
    "ds_write_b128 v251, v[56:59] offset:57344\n"
    "s_waitcnt vmcnt(3)\n"
    "s_waitcnt lgkmcnt(0)\n"
    "s_barrier\n"
    "s_setprio 2\n"
    "ds_read_b64_tr_b16 v[96:97], v127 offset:0\n"
    "ds_read_b64_tr_b16 v[98:99], v127 offset:512\n"
    "ds_read_b128 v[0:3], v144 offset:0\n"
    "ds_read_b64_tr_b16 v[100:101], v127 offset:2048\n"
    "ds_read_b64_tr_b16 v[102:103], v127 offset:2560\n"
    "ds_read_b128 v[4:7], v145 offset:0\n"
    "ds_read_b64_tr_b16 v[104:105], v127 offset:4096\n"
    "ds_read_b64_tr_b16 v[106:107], v127 offset:4608\n"
    "ds_read_b128 v[8:11], v146 offset:0\n"
    "ds_read_b64_tr_b16 v[108:109], v127 offset:6144\n"
    "ds_read_b64_tr_b16 v[110:111], v127 offset:6656\n"
    "ds_read_b128 v[12:15], v147 offset:0\n"
    "ds_read_b64_tr_b16 v[112:113], v127 offset:8192\n"
    "ds_read_b64_tr_b16 v[114:115], v127 offset:8704\n"
    "ds_read_b128 v[16:19], v148 offset:0\n"
    "s_waitcnt lgkmcnt(12)\n"
    "v_mfma_f32_32x32x16_bf16 v[168:183], v[96:99], v[0:3], 0\n"
    "ds_read_b64_tr_b16 v[96:97], v127 offset:10240\n"
    "ds_read_b64_tr_b16 v[98:99], v127 offset:10752\n"
    "ds_read_b128 v[0:3], v149 offset:0\n"
    "s_waitcnt lgkmcnt(12)\n"
    "v_mfma_f32_32x32x16_bf16 v[168:183], v[100:103], v[4:7], v[168:183]\n"
    "ds_read_b64_tr_b16 v[100:101], v127 offset:12288\n"
    "ds_read_b64_tr_b16 v[102:103], v127 offset:12800\n"
    "ds_read_b128 v[4:7], v150 offset:0\n"
    "s_waitcnt lgkmcnt(12)\n"
    "v_mfma_f32_32x32x16_bf16 v[168:183], v[104:107], v[8:11], v[168:183]\n"
    "ds_read_b64_tr_b16 v[104:105], v127 offset:14336\n"
    "ds_read_b64_tr_b16 v[106:107], v127 offset:14848\n"
    "ds_read_b128 v[8:11], v151 offset:0\n"
    "s_waitcnt lgkmcnt(12)\n"
    "v_mfma_f32_32x32x16_bf16 v[168:183], v[108:111], v[12:15], v[168:183]\n"
    "ds_read_b64_tr_b16 v[108:109], v127 offset:16384\n"
    "ds_read_b64_tr_b16 v[110:111], v127 offset:16896\n"
    "ds_read_b128 v[12:15], v144 offset:256\n"
    "s_waitcnt lgkmcnt(12)\n"
    "v_mfma_f32_32x32x16_bf16 v[168:183], v[112:115], v[16:19], v[168:183]\n"
    "ds_read_b64_tr_b16 v[112:113], v127 offset:18432\n"
    "ds_read_b64_tr_b16 v[114:115], v127 offset:18944\n"
    "ds_read_b128 v[16:19], v145 offset:256\n"
    "s_waitcnt lgkmcnt(12)\n"
    "v_mfma_f32_32x32x16_bf16 v[168:183], v[96:99], v[0:3], v[168:183]\n"
    "ds_read_b64_tr_b16 v[96:97], v127 offset:20480\n"
    "ds_read_b64_tr_b16 v[98:99], v127 offset:20992\n"
    "ds_read_b128 v[0:3], v146 offset:256\n"
    "s_waitcnt lgkmcnt(12)\n"
    "v_mfma_f32_32x32x16_bf16 v[168:183], v[100:103], v[4:7], v[168:183]\n"
    "ds_read_b64_tr_b16 v[100:101], v127 offset:22528\n"
    "ds_read_b64_tr_b16 v[102:103], v127 offset:23040\n"
    "ds_read_b128 v[4:7], v147 offset:256\n"
    "s_waitcnt lgkmcnt(12)\n"
    "v_mfma_f32_32x32x16_bf16 v[168:183], v[104:107], v[8:11], v[168:183]\n"
    "ds_read_b64_tr_b16 v[104:105], v127 offset:24576\n"
    "ds_read_b64_tr_b16 v[106:107], v127 offset:25088\n"
    "ds_read_b128 v[8:11], v148 offset:256\n"
    "s_waitcnt lgkmcnt(12)\n"
    "v_mfma_f32_32x32x16_bf16 v[168:183], v[108:111], v[12:15], v[168:183]\n"
    "ds_read_b64_tr_b16 v[108:109], v127 offset:26624\n"
    "ds_read_b64_tr_b16 v[110:111], v127 offset:27136\n"
    "ds_read_b128 v[12:15], v149 offset:256\n"
    "s_waitcnt lgkmcnt(12)\n"
    "v_mfma_f32_32x32x16_bf16 v[168:183], v[112:115], v[16:19], v[168:183]\n"
    "ds_read_b64_tr_b16 v[112:113], v127 offset:28672\n"
    "ds_read_b64_tr_b16 v[114:115], v127 offset:29184\n"
    "ds_read_b128 v[16:19], v150 offset:256\n"
    "s_waitcnt lgkmcnt(12)\n"
    "v_mfma_f32_32x32x16_bf16 v[168:183], v[96:99], v[0:3], v[168:183]\n"
    "ds_read_b64_tr_b16 v[96:97], v127 offset:30720\n"
    "ds_read_b64_tr_b16 v[98:99], v127 offset:31232\n"
    "ds_read_b128 v[0:3], v151 offset:256\n"
    "s_waitcnt lgkmcnt(12)\n"
    "v_mfma_f32_32x32x16_bf16 v[168:183], v[100:103], v[4:7], v[168:183]\n"
    "ds_read_b64_tr_b16 v[100:101], v127 offset:32768\n"
    "ds_read_b64_tr_b16 v[102:103], v127 offset:33280\n"
    "ds_read_b128 v[4:7], v144 offset:512\n"
    "s_waitcnt lgkmcnt(12)\n"
    "v_mfma_f32_32x32x16_bf16 v[168:183], v[104:107], v[8:11], v[168:183]\n"
    "ds_read_b64_tr_b16 v[104:105], v127 offset:34816\n"
    "ds_read_b64_tr_b16 v[106:107], v127 offset:35328\n"
    "ds_read_b128 v[8:11], v145 offset:512\n"
    "s_waitcnt lgkmcnt(12)\n"
    "v_mfma_f32_32x32x16_bf16 v[168:183], v[108:111], v[12:15], v[168:183]\n"
    "ds_read_b64_tr_b16 v[108:109], v127 offset:36864\n"
    "ds_read_b64_tr_b16 v[110:111], v127 offset:37376\n"
    "s_nop 0\n"
    ".Lpad4868:\n"
    "s_cbranch_execz .Lpad8159\n"
    "ds_read_b128 v[12:15], v146 offset:512\n"
    "s_waitcnt lgkmcnt(12)\n"
    "v_mfma_f32_32x32x16_bf16 v[168:183], v[112:115], v[16:19], v[168:183]\n"
    "ds_read_b64_tr_b16 v[112:113], v127 offset:38912\n"
    "ds_read_b64_tr_b16 v[114:115], v127 offset:39424\n"
    "ds_read_b128 v[16:19], v147 offset:512\n"
    "s_waitcnt lgkmcnt(12)\n"
    "v_mfma_f32_32x32x16_bf16 v[168:183], v[96:99], v[0:3], v[168:183]\n"
    "ds_read_b64_tr_b16 v[96:97], v127 offset:40960\n"
    "ds_read_b64_tr_b16 v[98:99], v127 offset:41472\n"
    "ds_read_b128 v[0:3], v148 offset:512\n"
    "s_waitcnt lgkmcnt(12)\n"
    "v_mfma_f32_32x32x16_bf16 v[168:183], v[100:103], v[4:7], v[168:183]\n"
    "ds_read_b64_tr_b16 v[100:101], v127 offset:43008\n"
    "ds_read_b64_tr_b16 v[102:103], v127 offset:43520\n"
    "ds_read_b128 v[4:7], v149 offset:512\n"
    "s_waitcnt lgkmcnt(12)\n"
    "v_mfma_f32_32x32x16_bf16 v[168:183], v[104:107], v[8:11], v[168:183]\n"
    "ds_read_b64_tr_b16 v[104:105], v127 offset:45056\n"
    "ds_read_b64_tr_b16 v[106:107], v127 offset:45568\n"
    "ds_read_b128 v[8:11], v150 offset:512\n"
    "s_waitcnt lgkmcnt(12)\n"
    "v_mfma_f32_32x32x16_bf16 v[168:183], v[108:111], v[12:15], v[168:183]\n"
    "ds_read_b64_tr_b16 v[108:109], v127 offset:47104\n"
    "ds_read_b64_tr_b16 v[110:111], v127 offset:47616\n"
    "ds_read_b128 v[12:15], v151 offset:512\n"
    "s_waitcnt lgkmcnt(12)\n"
    "v_mfma_f32_32x32x16_bf16 v[168:183], v[112:115], v[16:19], v[168:183]\n"
    "ds_read_b64_tr_b16 v[112:113], v127 offset:49152\n"
    "ds_read_b64_tr_b16 v[114:115], v127 offset:49664\n"
    "ds_read_b128 v[16:19], v144 offset:768\n"
    "s_waitcnt lgkmcnt(12)\n"
    "v_mfma_f32_32x32x16_bf16 v[168:183], v[96:99], v[0:3], v[168:183]\n"
    "ds_read_b64_tr_b16 v[96:97], v127 offset:51200\n"
    "ds_read_b64_tr_b16 v[98:99], v127 offset:51712\n"
    "ds_read_b128 v[0:3], v145 offset:768\n"
    "s_waitcnt lgkmcnt(12)\n"
    "v_mfma_f32_32x32x16_bf16 v[168:183], v[100:103], v[4:7], v[168:183]\n"
    "ds_read_b64_tr_b16 v[100:101], v127 offset:53248\n"
    "ds_read_b64_tr_b16 v[102:103], v127 offset:53760\n"
    "ds_read_b128 v[4:7], v146 offset:768\n"
    "s_waitcnt lgkmcnt(12)\n"
    "v_mfma_f32_32x32x16_bf16 v[168:183], v[104:107], v[8:11], v[168:183]\n"
    "ds_read_b64_tr_b16 v[104:105], v127 offset:55296\n"
    "ds_read_b64_tr_b16 v[106:107], v127 offset:55808\n"
    "ds_read_b128 v[8:11], v147 offset:768\n"
    "s_waitcnt lgkmcnt(12)\n"
    "v_mfma_f32_32x32x16_bf16 v[168:183], v[108:111], v[12:15], v[168:183]\n"
    "ds_read_b64_tr_b16 v[108:109], v127 offset:57344\n"
    "ds_read_b64_tr_b16 v[110:111], v127 offset:57856\n"
    "ds_read_b128 v[12:15], v148 offset:768\n"
    "s_waitcnt lgkmcnt(12)\n"
    "v_mfma_f32_32x32x16_bf16 v[168:183], v[112:115], v[16:19], v[168:183]\n"
    "ds_read_b64_tr_b16 v[112:113], v127 offset:59392\n"
    "ds_read_b64_tr_b16 v[114:115], v127 offset:59904\n"
    "ds_read_b128 v[16:19], v149 offset:768\n"
    "s_waitcnt lgkmcnt(12)\n"
    "v_mfma_f32_32x32x16_bf16 v[168:183], v[96:99], v[0:3], v[168:183]\n"
    "ds_read_b64_tr_b16 v[96:97], v127 offset:61440\n"
    "ds_read_b64_tr_b16 v[98:99], v127 offset:61952\n"
    "ds_read_b128 v[0:3], v150 offset:768\n"
    "s_waitcnt lgkmcnt(12)\n"
    "v_mfma_f32_32x32x16_bf16 v[168:183], v[100:103], v[4:7], v[168:183]\n"
    "ds_read_b64_tr_b16 v[100:101], v127 offset:63488\n"
    "ds_read_b64_tr_b16 v[102:103], v127 offset:64000\n"
    "ds_read_b128 v[4:7], v151 offset:768\n"
    "s_waitcnt lgkmcnt(12)\n"
    "v_mfma_f32_32x32x16_bf16 v[168:183], v[104:107], v[8:11], v[168:183]\n"
    "s_waitcnt lgkmcnt(9)\n"
    "v_mfma_f32_32x32x16_bf16 v[168:183], v[108:111], v[12:15], v[168:183]\n"
    "s_waitcnt lgkmcnt(6)\n"
    "v_mfma_f32_32x32x16_bf16 v[168:183], v[112:115], v[16:19], v[168:183]\n"
    "s_waitcnt lgkmcnt(3)\n"
    "v_mfma_f32_32x32x16_bf16 v[168:183], v[96:99], v[0:3], v[168:183]\n"
    "s_waitcnt lgkmcnt(0)\n"
    "v_mfma_f32_32x32x16_bf16 v[168:183], v[100:103], v[4:7], v[168:183]\n"
    "s_mov_b32 s76, 0x3eb8aa3b\n"
    "s_nop 10\n"
    "v_pk_mul_f32 v[168:169], v[168:169], s[76:77] op_sel_hi:[1,0]\n"
    "v_pk_mul_f32 v[170:171], v[170:171], s[76:77] op_sel_hi:[1,0]\n"
    "v_pk_mul_f32 v[172:173], v[172:173], s[76:77] op_sel_hi:[1,0]\n"
    "v_pk_mul_f32 v[174:175], v[174:175], s[76:77] op_sel_hi:[1,0]\n"
    "v_cvt_pk_bf16_f32 v96, v168, v169\n"
    "v_cvt_pk_bf16_f32 v97, v170, v171\n"
    "v_cvt_pk_bf16_f32 v98, v172, v173\n"
    "v_cvt_pk_bf16_f32 v99, v174, v175\n"
    "v_pk_mul_f32 v[176:177], v[176:177], s[76:77] op_sel_hi:[1,0]\n"
    "v_pk_mul_f32 v[178:179], v[178:179], s[76:77] op_sel_hi:[1,0]\n"
    "v_pk_mul_f32 v[180:181], v[180:181], s[76:77] op_sel_hi:[1,0]\n"
    "v_pk_mul_f32 v[182:183], v[182:183], s[76:77] op_sel_hi:[1,0]\n"
    "v_cvt_pk_bf16_f32 v100, v176, v177\n"
    "v_cvt_pk_bf16_f32 v101, v178, v179\n"
    "v_cvt_pk_bf16_f32 v102, v180, v181\n"
    "v_cvt_pk_bf16_f32 v103, v182, v183\n"
    "s_cmp_eq_u64 s[94:95], 0\n"
    "s_cbranch_scc1 .Lnoq_79\n"
    "s_mov_b64 exec, s[94:95]\n"
    "ds_write_b128 v209, v[96:99] offset:0\n"
    "ds_write_b128 v209, v[100:103] offset:128\n"
    "s_mov_b64 exec, -1\n"
    ".Lnoq_79:\n"
    "buffer_store_dwordx4 v[96:99], v208, s[12:15], s54 offen offset:0 sc1\n"
    "buffer_store_dwordx4 v[100:103], v208, s[12:15], s54 offen offset:128 sc1\n"
    "s_waitcnt lgkmcnt(0)\n"
    "s_setprio 3\n"
    "s_and_b32 s62, s60, 4\n"
    "s_xor_b32 s62, s62, s48\n"
    "s_lshl_b32 s62, s62, 2\n"
    "s_and_b32 s63, s60, 0\n"
    "s_or_b32 s62, s62, s63\n"
    "s_lshl_b32 s62, s62, 12\n"
    "s_add_u32 s70, s62, 131072\n"
    "s_xor_b32 s80, s70, 4096\n"
    "s_xor_b32 s81, s70, 8192\n"
    "s_xor_b32 s82, s70, 12288\n"
    "s_lshl_b32 s71, s55, 13\n"
    "s_lshl_b32 s62, s50, 10\n"
    "s_add_u32 s71, s71, s62\n"
    "s_add_u32 s71, s71, 1048576\n"
    "s_lshl_b32 s83, s48, 13\n"
    "v_add_u32 v249, s83, v241\n"
    "s_mov_b32 s88, 0xffff\n"
    "s_mov_b32 s89, 0xffff\n"
    "v_mov_b32 v232, 0xdddd\n"
    "v_bfe_u32 v200, v240, 3, 1\n"
    "v_mul_u32_u24 v200, 0xaaaa, v200\n"
    "v_add_u32 v233, 0x4444, v200\n"
    "v_add_u32 v247, 150528, v241\n"
    "s_mul_i32 s62, s51, 2304\n"
    "s_add_u32 s62, s62, 131072\n"
    "v_add_u32 v242, s62, v241\n"
    "s_add_u32 s62, s62, 2048\n"
    "v_lshl_add_u32 v243, v240, 2, s62\n"
    "v_and_b32 v210, 7, v240\n"
    "v_lshlrev_b32 v210, 7, v210\n"
    "s_lshl_b32 s84, s49, 10\n"
    "s_add_u32 s84, s84, 81984\n"
    "v_and_b32 v200, 31, v240\n"
    "s_lshl_b32 s62, s51, 11\n"
    "s_add_u32 s62, s62, 114688\n"
    "v_add_u32 v244, s62, v241\n"
    "s_lshl2_add_u32 s62, s51, 149504\n"
    "v_lshl_add_u32 v245, v200, 4, s62\n"
    "s_waitcnt vmcnt(4)\n"
    "v_lshlrev_b32 v117, 16, v116\n"
    "v_and_b32 v118, 0xffff0000, v116\n"
    "v_mul_f32 v200, 0xbfb8aa3b, v117\n"
    "v_mul_f32 v201, 0xbfb8aa3b, v118\n"
    "v_exp_f32 v200, v200\n"
    "v_exp_f32 v201, v201\n"
    "v_add_f32 v200, 1.0, v200\n"
    "v_add_f32 v201, 1.0, v201\n"
    "v_rcp_f32 v200, v200\n"
    "v_rcp_f32 v201, v201\n"
    "v_mul_f32 v117, v117, v200\n"
    "v_mul_f32 v118, v118, v201\n"
    "v_cvt_pk_bf16_f32 v121, 0, v117\n"
    "v_cvt_pk_bf16_f32 v123, 0, v118\n"
    "s_waitcnt vmcnt(3)\n"
    "v_lshlrev_b32 v192, 16, v184\n"
    "v_and_b32 v193, 0xffff0000, v184\n"
    "v_lshlrev_b32 v194, 16, v185\n"
    "v_and_b32 v195, 0xffff0000, v185\n"
    "v_lshlrev_b32 v196, 16, v186\n"
    "v_and_b32 v197, 0xffff0000, v186\n"
    "v_lshlrev_b32 v198, 16, v187\n"
    "v_and_b32 v199, 0xffff0000, v187\n"
    "v_mul_f32 v200, v192, v121\n"
    "v_mul_f32 v201, v193, v121\n"
    "v_mul_f32 v202, v194, v121\n"
    "v_mul_f32 v203, v195, v121\n"
    "v_mul_f32 v204, v196, v121\n"
    "v_mul_f32 v205, v197, v121\n"
    "v_mul_f32 v206, v198, v121\n"
    "v_mul_f32 v207, v199, v121\n"
    "s_waitcnt vmcnt(2)\n"
    "v_lshlrev_b32 v192, 16, v188\n"
    "v_and_b32 v193, 0xffff0000, v188\n"
    "v_lshlrev_b32 v194, 16, v189\n"
    "v_and_b32 v195, 0xffff0000, v189\n"
    "v_lshlrev_b32 v196, 16, v190\n"
    "v_and_b32 v197, 0xffff0000, v190\n"
    "v_lshlrev_b32 v198, 16, v191\n"
    "v_and_b32 v199, 0xffff0000, v191\n"
    "v_fmac_f32 v200, v192, v123\n"
    "v_fmac_f32 v201, v193, v123\n"
    "v_fmac_f32 v202, v194, v123\n"
    "v_fmac_f32 v203, v195, v123\n"
    "v_fmac_f32 v204, v196, v123\n"
    "v_fmac_f32 v205, v197, v123\n"
    "v_fmac_f32 v206, v198, v123\n"
    "v_fmac_f32 v207, v199, v123\n"
    "v_permlane32_swap_b32 v200, v204\n"
    "v_permlane32_swap_b32 v201, v205\n"
    "v_permlane32_swap_b32 v202, v206\n"
    "v_permlane32_swap_b32 v203, v207\n"
    "v_add_f32 v200, v200, v204\n"
    "v_add_f32 v201, v201, v205\n"
    "v_add_f32 v202, v202, v206\n"
    "v_add_f32 v203, v203, v207\n"
    "s_nop 0\n"
    "v_permlane16_swap_b32 v200, v202\n"
    "v_permlane16_swap_b32 v201, v203\n"
    "v_add_f32 v200, v200, v202\n"
    "v_add_f32 v201, v201, v203\n"
    "v_mov_b32 v202, s53\n"
    "v_mov_b32 v203, s53\n"
    "v_add_f32_dpp v200, v200, v200 row_ror:8 row_mask:0xf bank_mask:0xf\n"
    "v_add_f32_dpp v201, v201, v201 row_ror:8 row_mask:0xf bank_mask:0xf\n"
    "s_nop 0\n"
    "v_add_f32_dpp v200, v200, v200 row_ror:4 row_mask:0xf bank_mask:0xf\n"
    "v_add_f32_dpp v201, v201, v201 row_ror:4 row_mask:0xf bank_mask:0xf\n"
    "s_nop 0\n"
    "v_add_f32_dpp v200, v200, v200 row_ror:2 row_mask:0xf bank_mask:0xf\n"
    "v_add_f32_dpp v201, v201, v201 row_ror:2 row_mask:0xf bank_mask:0xf\n"
    "s_lshl_b32 s62, s50, 14\n"
    "s_lshl2_add_u32 s63, s49, s51\n"
    "s_lshl_b32 s63, s63, 7\n"
    "s_add_u32 s62, s62, s63\n"
    "s_add_u32 s62, s62, 262144\n"
    "s_mov_b32 exec_lo, 0x00030003\n"
    "s_mov_b32 exec_hi, 0x00030003\n"
    "buffer_store_dwordx4 v[200:203], v231, s[12:15], s62 offen sc1\n"
    "s_mov_b64 exec, -1\n"
    "v_and_b32 v250, 11, v240\n"
    "v_cmp_eq_u32 vcc, 0, v250\n"
    "v_mov_b32 v250, 0x3f803f80\n"
    "v_mov_b64 v[234:235], 0\n"
    "v_cndmask_b32 v250, 0, v250, vcc\n"
    "v_mov_b32 v251, v250\n"
    "v_mov_b64 v[32:33], v[250:251]\n"
    "v_mov_b64 v[34:35], v[250:251]\n"
    "v_mov_b64 v[36:37], v[250:251]\n"
    "v_mov_b64 v[38:39], v[250:251]\n"
    "v_mov_b64 v[40:41], v[250:251]\n"
    "v_mov_b64 v[42:43], v[250:251]\n"
    "v_mov_b64 v[44:45], v[250:251]\n"
    "v_mov_b64 v[46:47], v[250:251]\n"
    "v_mov_b64 v[48:49], v[250:251]\n"
    "v_mov_b64 v[50:51], v[250:251]\n"
    "v_mov_b64 v[52:53], v[250:251]\n"
    "v_mov_b64 v[54:55], v[250:251]\n"
    "v_mov_b64 v[56:57], v[250:251]\n"
    "v_mov_b64 v[58:59], v[250:251]\n"
    "v_mov_b64 v[60:61], v[250:251]\n"
    "v_mov_b64 v[62:63], v[250:251]\n"
    "v_mov_b64 v[64:65], v[250:251]\n"
    "v_mov_b64 v[66:67], v[250:251]\n"
    "v_mov_b64 v[68:69], v[250:251]\n"
    "v_mov_b64 v[70:71], v[250:251]\n"
    "v_mov_b64 v[72:73], v[250:251]\n"
    "v_mov_b64 v[74:75], v[250:251]\n"
    "v_mov_b64 v[76:77], v[250:251]\n"
    "v_mov_b64 v[78:79], v[250:251]\n"
    "v_mov_b64 v[80:81], v[250:251]\n"
    "v_mov_b64 v[82:83], v[250:251]\n"
    "v_mov_b64 v[84:85], v[250:251]\n"
    "v_mov_b64 v[86:87], v[250:251]\n"
    "v_mov_b64 v[88:89], v[250:251]\n"
    "v_mov_b64 v[90:91], v[250:251]\n"
    "v_mov_b64 v[92:93], v[250:251]\n"
    "v_mov_b64 v[94:95], v[250:251]\n"
    "v_mov_b64 v[152:153], 0\n"
    "v_mov_b64 v[128:129], 0\n"
    "v_mov_b64 v[154:155], 0\n"
    "v_mov_b64 v[130:131], 0\n"
    "v_mov_b64 v[156:157], 0\n"
    "v_mov_b64 v[132:133], 0\n"
    "v_mov_b64 v[158:159], 0\n"
    "v_mov_b64 v[134:135], 0\n"
    "v_mov_b64 v[160:161], 0\n"
    "v_mov_b64 v[136:137], 0\n"
    "v_mov_b64 v[162:163], 0\n"
    "v_mov_b64 v[138:139], 0\n"
    "v_mov_b64 v[164:165], 0\n"
    "v_mov_b64 v[140:141], 0\n"
    "v_mov_b64 v[166:167], 0\n"
    "v_mov_b64 v[142:143], 0\n"
    "v_mov_b32 v236, 0\n"
    "v_mov_b32 v238, 1.0\n"
    "s_lshl3_add_u32 s75, s50, s48\n"
    "s_lshl_b32 s75, s75, 13\n"
    "s_add_u32 s75, s75, 524288\n"
    "v_mov_b64 v[212:213], -1\n"
    "v_mov_b64 v[214:215], -1\n"
    "s_mul_i32 s86, s49, 180224\n"
    "s_lshl_b32 s63, s56, 12\n"
    "s_add_u32 s86, s86, s63\n"
    "s_mul_i32 s63, s51, 6144\n"
    "s_add_u32 s86, s86, s63\n"
    "s_add_u32 s86, s86, 10485760\n"
    "s_add_u32 s87, s86, 3072\n"
    "s_barrier\n"
    "s_setprio 1\n"
    "ds_read_b128 v[144:147], v247 offset:12288\n"
    "s_add_u32 m0, s83, 3072\n"
    "buffer_load_dwordx4 v[0:3], v241, s[12:15], s70 offen offset:0 sc1\n"
    "buffer_load_dwordx4 v241, s[12:15], s70 offen offset:1024 sc1 lds\n"
    "s_add_u32 m0, s83, 2048\n"
    "buffer_load_dwordx4 v[4:7], v241, s[12:15], s70 offen offset:2048 sc1\n"
    "buffer_load_dwordx4 v241, s[12:15], s70 offen offset:3072 sc1 lds\n"
    "s_add_u32 m0, s83, 5120\n"
    "buffer_load_dwordx4 v[8:11], v241, s[12:15], s80 offen offset:0 sc1\n"
    "buffer_load_dwordx4 v241, s[12:15], s80 offen offset:1024 sc1 lds\n"
    "s_add_u32 m0, s83, 4096\n"
    "buffer_load_dwordx4 v[12:15], v241, s[12:15], s80 offen offset:2048 sc1\n"
    "buffer_load_dwordx4 v241, s[12:15], s80 offen offset:3072 sc1 lds\n"
    "s_add_u32 m0, s83, 7168\n"
    "buffer_load_dwordx4 v[16:19], v241, s[12:15], s81 offen offset:0 sc1\n"
    "buffer_load_dwordx4 v241, s[12:15], s81 offen offset:1024 sc1 lds\n"
    "s_add_u32 m0, s83, 6144\n"
    "buffer_load_dwordx4 v[20:23], v241, s[12:15], s81 offen offset:2048 sc1\n"
    "buffer_load_dwordx4 v241, s[12:15], s81 offen offset:3072 sc1 lds\n"
    "s_add_u32 m0, s83, 9216\n"
    "buffer_load_dwordx4 v[24:27], v241, s[12:15], s82 offen offset:0 sc1\n"
    "buffer_load_dwordx4 v241, s[12:15], s82 offen offset:1024 sc1 lds\n"
    "s_add_u32 m0, s83, 8192\n"
    "buffer_load_dwordx4 v[28:31], v241, s[12:15], s82 offen offset:2048 sc1\n"
    "buffer_load_dwordx4 v241, s[12:15], s82 offen offset:3072 sc1 lds\n"
    "buffer_load_dword v211, v210, s[12:15], s84 offen sc1\n"
    "s_waitcnt vmcnt(13)\n"
    "s_mov_b64 exec, s[88:89]\n"
    "ds_read_b128 v[32:35], v249 offset:4096\n"
    "ds_read_b128 v[36:39], v249 offset:4352\n"
    "ds_read_b128 v[40:43], v249 offset:5120\n"
    "ds_read_b128 v[44:47], v249 offset:5376\n"
    "s_mov_b64 exec, -1\n"
    ".Lgc_80:\n"
    "s_waitcnt lgkmcnt(2)\n"
    "v_max3_u32 v239, v0, v32, v36\n"
    "s_waitcnt lgkmcnt(1)\n"
    "v_max3_u32 v239, v4, v40, v239\n"
    "s_waitcnt lgkmcnt(0)\n"
    "v_max_u32 v239, v44, v239\n"
    "v_cmp_eq_u32 vcc, -1, v239\n"
    "s_nop 0\n"
    "s_cbranch_vccnz .Lgr_81\n"
    ".Lgo_82:\n"
    "v_mfma_f32_32x32x16_bf16 v[96:111], v[0:3], v[144:147], 0\n"
    "s_setprio 3\n"
    "v_lshrrev_b32 v218, 2, v240\n"
    "v_and_b32 v219, 3, v240\n"
    "v_lshlrev_b32 v219, 4, v219\n"
    "v_lshl_or_b32 v220, v218, 10, v219\n"
    "v_mul_u32_u24 v218, 2752, v218\n"
    "v_add_u32 v218, v218, v219\n"
    "s_mul_i32 s62, s49, 44032\n"
    "s_add_u32 s63, s51, s56\n"
    "s_lshl_b32 s63, s63, 6\n"
    "s_add_u32 s62, s62, s63\n"
    "s_mul_i32 s63, s51, 3072\n"
    "s_add_u32 m0, s63, 69632\n"
    "s_lshr_b32 s64, s51, 1\n"
    "s_lshl1_add_u32 s64, s49, s64\n"
    "s_lshl_b32 s64, s64, 14\n"
    "s_and_b32 s65, s51, 1\n"
    "s_lshl1_add_u32 s65, s50, s65\n"
    "s_lshl_b32 s65, s65, 6\n"
    "s_add_u32 s64, s64, s65\n"
    "buffer_load_dwordx4 v218, s[36:39], s62 offen lds\n"
    "s_add_u32 m0, m0, 1024\n"
    "s_nop 0\n"
    "buffer_load_dwordx4 v220, s[40:43], s64 offen lds\n"
    "s_add_u32 m0, m0, 1024\n"
    "s_add_u32 s64, s64, 0x100000\n"
    "s_cmp_lt_u32 s49, 11\n"
    "s_cbranch_scc0 .Lp2_112\n"
    "buffer_load_dwordx4 v220, s[40:43], s64 offen lds\n"
    ".Lp2_112:\n"
    "s_lshl_b32 s63, s51, 10\n"
    "s_add_u32 m0, s63, 81664\n"
    "s_add_u32 s63, s51, 4\n"
    "s_cmp_lt_u32 s63, s57\n"
    "s_cbranch_scc0 .Lp1_111\n"
    "buffer_load_dwordx4 v218, s[36:39], s62 offen offset:256 lds\n"
    ".Lp1_111:\n"
    "s_setprio 1\n"
    "v_exp_f32 v96, v96\n"
    "v_exp_f32 v97, v97\n"
    "v_exp_f32 v98, v98\n"
    "v_exp_f32 v99, v99\n"
    "v_exp_f32 v100, v100\n"
    "v_mfma_f32_32x32x16_bf16 v[112:127], v[4:7], v[144:147], 0\n"
    "v_exp_f32 v101, v101\n"
    "v_exp_f32 v102, v102\n"
    "v_exp_f32 v103, v103\n"
    "v_exp_f32 v104, v104\n"
    "v_exp_f32 v105, v105\n"
    "v_smfmac_f32_32x32x32_bf16 v[128:143], v[32:35], v[96:103], v232\n"
    "v_exp_f32 v106, v106\n"
    "v_exp_f32 v107, v107\n"
    "v_exp_f32 v108, v108\n"
    "v_exp_f32 v109, v109\n"
    "v_exp_f32 v110, v110\n"
    "v_exp_f32 v111, v111\n"
    "s_waitcnt vmcnt(11)\n"
    "s_mov_b64 exec, s[88:89]\n"
    "ds_read_b128 v[48:51], v249 offset:6144\n"
    "ds_read_b128 v[52:55], v249 offset:6400\n"
    "ds_read_b128 v[56:59], v249 offset:7168\n"
    "ds_read_b128 v[60:63], v249 offset:7424\n"
    "s_mov_b64 exec, -1\n"
    "v_exp_f32 v112, v112\n"
    "v_exp_f32 v113, v113\n"
    "v_smfmac_f32_32x32x32_bf16 v[128:143], v[36:39], v[104:111], v232\n"
    ".Lgc_113:\n"
    "s_waitcnt lgkmcnt(2)\n"
    "v_max3_u32 v239, v8, v48, v52\n"
    "s_waitcnt lgkmcnt(1)\n"
    "v_max3_u32 v239, v12, v56, v239\n"
    "s_waitcnt lgkmcnt(0)\n"
    "v_max_u32 v239, v60, v239\n"
    "v_cmp_eq_u32 vcc, -1, v239\n"
    "s_nop 0\n"
    "s_cbranch_vccnz .Lgr_114\n"
    ".Lgo_115:\n"
    "v_exp_f32 v114, v114\n"
    "v_exp_f32 v115, v115\n"
    "v_exp_f32 v116, v116\n"
    "v_mfma_f32_32x32x16_bf16 v[96:111], v[8:11], v[144:147], 0\n"
    "v_exp_f32 v117, v117\n"
    "v_exp_f32 v118, v118\n"
    "v_exp_f32 v119, v119\n"
    "v_exp_f32 v120, v120\n"
    "v_exp_f32 v121, v121\n"
    "v_smfmac_f32_32x32x32_bf16 v[128:143], v[40:43], v[112:119], v232\n"
    "v_exp_f32 v122, v122\n"
    "v_exp_f32 v123, v123\n"
    "v_exp_f32 v124, v124\n"
    "v_exp_f32 v125, v125\n"
    "v_exp_f32 v126, v126\n"
    "v_exp_f32 v127, v127\n"
    "v_exp_f32 v96, v96\n"
    "v_exp_f32 v97, v97\n"
    "v_smfmac_f32_32x32x32_bf16 v[128:143], v[44:47], v[120:127], v232\n"
    "v_exp_f32 v98, v98\n"
    "v_exp_f32 v99, v99\n"
    "v_exp_f32 v100, v100\n"
    "v_mfma_f32_32x32x16_bf16 v[112:127], v[12:15], v[144:147], 0\n"
    "v_exp_f32 v101, v101\n"
    "v_exp_f32 v102, v102\n"
    "v_exp_f32 v103, v103\n"
    "v_exp_f32 v104, v104\n"
    "v_exp_f32 v105, v105\n"
    "v_smfmac_f32_32x32x32_bf16 v[128:143], v[48:51], v[96:103], v232\n"
    ".Lgc_116:\n"
    "s_waitcnt vmcnt(2)\n"
    "v_cmp_ne_u32 vcc, s53, v211\n"
    "s_nop 0\n"
    "s_cbranch_vccnz .Lgr_117\n"
    ".Lgo_118:\n"
    "s_xor_b32 s72, s71, 1024\n"
    "buffer_load_dwordx4 v[148:151], v241, s[12:15], s72 offen sc1\n"
    "v_exp_f32 v106, v106\n"
    "v_exp_f32 v107, v107\n"
    "v_exp_f32 v108, v108\n"
    "v_exp_f32 v109, v109\n"
    "v_exp_f32 v110, v110\n"
    "v_exp_f32 v111, v111\n"
    "s_mov_b64 exec, s[88:89]\n"
    "ds_read_b128 v[64:67], v249 offset:8192\n"
    "ds_read_b128 v[68:71], v249 offset:8448\n"
    "ds_read_b128 v[72:75], v249 offset:9216\n"
    "ds_read_b128 v[76:79], v249 offset:9472\n"
    "s_mov_b64 exec, -1\n"
    "v_exp_f32 v112, v112\n"
    "v_exp_f32 v113, v113\n"
    "v_smfmac_f32_32x32x32_bf16 v[128:143], v[52:55], v[104:111], v232\n"
    ".Lgc_119:\n"
    "s_waitcnt lgkmcnt(2)\n"
    "v_max3_u32 v239, v16, v64, v68\n"
    "s_waitcnt lgkmcnt(1)\n"
    "v_max3_u32 v239, v20, v72, v239\n"
    "s_waitcnt lgkmcnt(0)\n"
    "v_max_u32 v239, v76, v239\n"
    "v_cmp_eq_u32 vcc, -1, v239\n"
    "s_nop 0\n"
    "s_cbranch_vccnz .Lgr_120\n"
    ".Lgo_121:\n"
    "v_exp_f32 v114, v114\n"
    "v_exp_f32 v115, v115\n"
    "v_exp_f32 v116, v116\n"
    "v_mfma_f32_32x32x16_bf16 v[96:111], v[16:19], v[144:147], 0\n"
    "v_exp_f32 v117, v117\n"
    "v_exp_f32 v118, v118\n"
    "v_exp_f32 v119, v119\n"
    "v_exp_f32 v120, v120\n"
    "v_exp_f32 v121, v121\n"
    "v_smfmac_f32_32x32x32_bf16 v[128:143], v[56:59], v[112:119], v232\n"
    "v_exp_f32 v122, v122\n"
    "v_exp_f32 v123, v123\n"
    "v_exp_f32 v124, v124\n"
    "v_exp_f32 v125, v125\n"
    "v_exp_f32 v126, v126\n"
    "v_exp_f32 v127, v127\n"
    "s_barrier\n"
    "ds_read_b128 v[184:187], v242\n"
    "ds_read_b128 v[188:191], v242 offset:1024\n"
    "ds_read_b32 v192, v243\n"
    "s_setprio 3\n"
    "s_cmp_eq_u32 s48, 5\n"
    "s_cbranch_scc0 .Lw_122\n"
    "v_and_b32 v204, 7, v240\n"
    "v_lshlrev_b32 v204, 14, v204\n"
    "v_and_b32 v205, 48, v240\n"
    "v_or_b32 v204, v204, v205\n"
    "s_lshl_b32 s62, s55, 6\n"
    "s_add_u32 s62, s62, 262144\n"
    "buffer_load_dwordx3 v[200:202], v204, s[12:15], s62 offen sc1\n"
    ".Lw_122:\n"
    "v_bfe_u32 v168, v240, 2, 2\n"
    "v_lshlrev_b32 v168, 6, v168\n"
    "v_bfe_u32 v169, v240, 4, 1\n"
    "v_lshl_or_b32 v168, v169, 5, v168\n"
    "v_and_b32 v169, 3, v240\n"
    "v_lshl_or_b32 v168, v169, 3, v168\n"
    "s_mul_i32 s63, s51, 3072\n"
    "s_add_u32 s63, s63, 69632\n"
    "v_add_u32 v168, s63, v168\n"
    "v_lshrrev_b32 v170, 5, v240\n"
    "v_lshl_add_u32 v169, v170, 9, v168\n"
    "v_lshl_add_u32 v170, v170, 8, v168\n"
    "s_lshl_b32 s63, s51, 11\n"
    "v_subrev_u32 v168, s63, v169\n"
    "s_waitcnt vmcnt(1)\n"
    "ds_read_b64_tr_b16 v[216:217], v169 offset:0\n"
    "ds_read_b64_tr_b16 v[218:219], v169 offset:256\n"
    "ds_read_b64_tr_b16 v[220:221], v170 offset:1024\n"
    "ds_read_b64_tr_b16 v[222:223], v170 offset:1536\n"
    "ds_read_b64_tr_b16 v[224:225], v168 offset:12288\n"
    "ds_read_b64_tr_b16 v[226:227], v168 offset:12544\n"
    "ds_read_b64_tr_b16 v[228:229], v170 offset:2048\n"
    "ds_read_b64_tr_b16 v[230:231], v170 offset:2560\n"
    "s_setprio 1\n"
    "s_add_u32 s62, s56, s51\n"
    "s_lshl_b32 s62, s62, 15\n"
    "s_lshl_b32 s63, s49, 10\n"
    "s_add_u32 s62, s62, s63\n"
    "s_add_u32 s62, s62, 3145728\n"
    "s_mul_i32 s64, s50, 43\n"
    "s_add_u32 s64, s64, s49\n"
    "s_lshl2_add_u32 s64, s64, s51\n"
    "s_lshl_b32 s64, s64, 10\n"
    "s_add_u32 s64, s64, 4718592\n"
    "s_waitcnt lgkmcnt(6)\n"
    "buffer_store_dwordx4 v[216:219], v241, s[12:15], s62 offen sc1\n"
    "s_waitcnt lgkmcnt(4)\n"
    "buffer_store_dwordx4 v[220:223], v241, s[12:15], s64 offen sc1\n"
    "s_add_u32 s62, s62, 131072\n"
    "s_add_u32 s63, s51, 4\n"
    "s_cmp_lt_u32 s63, s57\n"
    "s_cbranch_scc0 .Lp1_123\n"
    "s_waitcnt lgkmcnt(2)\n"
    "buffer_store_dwordx4 v[224:227], v241, s[12:15], s62 offen sc1\n"
    ".Lp1_123:\n"
    "s_add_u32 s64, s64, 131072\n"
    "s_cmp_lt_u32 s49, 11\n"
    "s_cbranch_scc0 .Lp2_124\n"
    "s_waitcnt lgkmcnt(0)\n"
    "buffer_store_dwordx4 v[228:231], v241, s[12:15], s64 offen sc1\n"
    ".Lp2_124:\n"
    "s_lshl2_add_u32 s62, s55, s51\n"
    "s_lshl_b32 s62, s62, 11\n"
    "s_add_u32 s62, s62, 6291456\n"
    "buffer_store_dwordx4 v[212:215], v241, s[12:15], s62 offen offset:0 sc1\n"
    "buffer_store_dwordx4 v[212:215], v241, s[12:15], s62 offen offset:1024 sc1\n"
    "v_exp_f32 v96, v96\n"
    "v_exp_f32 v97, v97\n"
    "v_smfmac_f32_32x32x32_bf16 v[128:143], v[60:63], v[120:127], v232\n"
    "v_exp_f32 v98, v98\n"
    "v_exp_f32 v99, v99\n"
    "v_exp_f32 v100, v100\n"
    "v_mfma_f32_32x32x16_bf16 v[112:127], v[20:23], v[144:147], 0\n"
    "v_exp_f32 v101, v101\n"
    "v_exp_f32 v102, v102\n"
    "v_exp_f32 v103, v103\n"
    "v_exp_f32 v104, v104\n"
    "v_exp_f32 v105, v105\n"
    "v_smfmac_f32_32x32x32_bf16 v[128:143], v[64:67], v[96:103], v232\n"
    "v_exp_f32 v106, v106\n"
    "v_exp_f32 v107, v107\n"
    "v_exp_f32 v108, v108\n"
    "v_exp_f32 v109, v109\n"
    "v_exp_f32 v110, v110\n"
    "v_exp_f32 v111, v111\n"
    "s_mov_b64 exec, s[88:89]\n"
    "ds_read_b128 v[80:83], v249 offset:10240\n"
    "ds_read_b128 v[84:87], v249 offset:10496\n"
    "ds_read_b128 v[88:91], v249 offset:11264\n"
    "ds_read_b128 v[92:95], v249 offset:11520\n"
    "s_mov_b64 exec, -1\n"
    "v_exp_f32 v112, v112\n"
    "v_exp_f32 v113, v113\n"
    "v_smfmac_f32_32x32x32_bf16 v[128:143], v[68:71], v[104:111], v232\n"
    ".Lgc_125:\n"
    "s_waitcnt lgkmcnt(2)\n"
    "v_max3_u32 v239, v24, v80, v84\n"
    "s_waitcnt lgkmcnt(1)\n"
    "v_max3_u32 v239, v28, v88, v239\n"
    "s_waitcnt lgkmcnt(0)\n"
    "v_max_u32 v239, v92, v239\n"
    "v_cmp_eq_u32 vcc, -1, v239\n"
    "s_nop 0\n"
    "s_cbranch_vccnz .Lgr_126\n"
    ".Lgo_127:\n"
    "v_exp_f32 v114, v114\n"
    "v_exp_f32 v115, v115\n"
    "v_exp_f32 v116, v116\n"
    "v_mfma_f32_32x32x16_bf16 v[96:111], v[24:27], v[144:147], 0\n"
    "v_exp_f32 v117, v117\n"
    "v_exp_f32 v118, v118\n"
    "v_exp_f32 v119, v119\n"
    "v_exp_f32 v120, v120\n"
    "v_exp_f32 v121, v121\n"
    "v_smfmac_f32_32x32x32_bf16 v[128:143], v[72:75], v[112:119], v232\n"
    "v_exp_f32 v122, v122\n"
    "v_exp_f32 v123, v123\n"
    "v_exp_f32 v124, v124\n"
    "v_exp_f32 v125, v125\n"
    "v_exp_f32 v126, v126\n"
    "v_exp_f32 v127, v127\n"
    "v_exp_f32 v96, v96\n"
    "v_exp_f32 v97, v97\n"
    "v_smfmac_f32_32x32x32_bf16 v[128:143], v[76:79], v[120:127], v232\n"
    "v_exp_f32 v98, v98\n"
    "v_exp_f32 v99, v99\n"
    "v_exp_f32 v100, v100\n"
    "v_mfma_f32_32x32x16_bf16 v[112:127], v[28:31], v[144:147], 0\n"
    "v_exp_f32 v101, v101\n"
    "v_exp_f32 v102, v102\n"
    "v_exp_f32 v103, v103\n"
    "v_exp_f32 v104, v104\n"
    "v_exp_f32 v105, v105\n"
    "v_smfmac_f32_32x32x32_bf16 v[128:143], v[80:83], v[96:103], v232\n"
    "v_exp_f32 v106, v106\n"
    "v_exp_f32 v107, v107\n"
    "v_exp_f32 v108, v108\n"
    "v_exp_f32 v109, v109\n"
    "v_exp_f32 v110, v110\n"
    "v_exp_f32 v111, v111\n"
    "v_exp_f32 v112, v112\n"
    "v_exp_f32 v113, v113\n"
    "v_smfmac_f32_32x32x32_bf16 v[128:143], v[84:87], v[104:111], v232\n"
    "v_exp_f32 v114, v114\n"
    "v_exp_f32 v115, v115\n"
    "v_exp_f32 v116, v116\n"
    ".Lgc_128:\n"
    "s_waitcnt vmcnt(4)\n"
    "v_cmp_eq_u32 vcc, -1, v148\n"
    "s_nop 0\n"
    "s_cbranch_vccnz .Lgr_129\n"
    ".Lgo_130:\n"
    "v_mfma_f32_32x32x16_bf16 v[96:111], v[0:3], v[148:151], 0\n"
    "v_exp_f32 v117, v117\n"
    "v_exp_f32 v118, v118\n"
    "v_exp_f32 v119, v119\n"
    "v_exp_f32 v120, v120\n"
    "v_exp_f32 v121, v121\n"
    "v_smfmac_f32_32x32x32_bf16 v[128:143], v[88:91], v[112:119], v232\n"
    "buffer_store_dwordx4 v[212:215], v241, s[12:15], s86 offen offset:0 sc1\n"
    "v_exp_f32 v122, v122\n"
    "v_exp_f32 v123, v123\n"
    "v_exp_f32 v124, v124\n"
    "v_exp_f32 v125, v125\n"
    "v_exp_f32 v126, v126\n"
    "v_exp_f32 v127, v127\n"
    "v_exp_f32 v96, v96\n"
    "v_exp_f32 v97, v97\n"
    "v_smfmac_f32_32x32x32_bf16 v[128:143], v[92:95], v[120:127], v232\n"
    "v_exp_f32 v98, v98\n"
    "v_exp_f32 v99, v99\n"
    "v_exp_f32 v100, v100\n"
    "v_mfma_f32_32x32x16_bf16 v[112:127], v[4:7], v[148:151], 0\n"
    "v_exp_f32 v101, v101\n"
    "v_exp_f32 v102, v102\n"
    "v_exp_f32 v103, v103\n"
    "v_exp_f32 v104, v104\n"
    "v_exp_f32 v105, v105\n"
    "v_exp_f32 v106, v106\n"
    "v_exp_f32 v107, v107\n"
    "v_exp_f32 v108, v108\n"
    "v_add_f32 v128, v128, v184\n"
    "v_add_f32 v129, v129, v185\n"
    "v_add_f32 v130, v130, v186\n"
    "v_add_f32 v131, v131, v187\n"
    "v_add_f32 v132, v132, v188\n"
    "v_add_f32 v133, v133, v189\n"
    "v_add_f32 v134, v134, v190\n"
    "v_add_f32 v135, v135, v191\n"
    "v_add_f32 v136, v136, v192\n"
    "ds_write_b128 v244, v[128:131] offset:0\n"
    "ds_write_b128 v244, v[132:135] offset:1024\n"
    "ds_write_b32 v245, v136 offset:0\n"
    "v_exp_f32 v109, v109\n"
    "v_exp_f32 v110, v110\n"
    "v_exp_f32 v111, v111\n"
    "s_waitcnt vmcnt(1) lgkmcnt(0)\n"
    "s_barrier\n"
    "ds_read_b128 v[128:131], v242 offset:9216\n"
    "ds_read_b128 v[132:135], v242 offset:10240\n"
    "ds_read_b32 v136, v243 offset:9216\n"
    "s_xor_b32 s72, s71, 2048\n"
    "buffer_load_dwordx4 v[144:147], v241, s[12:15], s72 offen sc1\n"
    "s_setprio 3\n"
    "s_cmp_eq_u32 s48, 4\n"
    "s_cbranch_scc0 .Lw_131\n"
    "s_lshl_b32 s62, s50, 5\n"
    "s_add_u32 s62, s62, s49\n"
    "s_lshl_b32 s62, s62, 6\n"
    "s_add_u32 s62, s62, 65536\n"
    "v_mov_b32 v210, s53\n"
    "s_mov_b64 exec, 1\n"
    "buffer_store_dword v210, off, s[12:15], s62 sc1\n"
    "s_mov_b64 exec, -1\n"
    ".Lw_131:\n"
    "s_cmp_eq_u32 s48, 5\n"
    "s_cbranch_scc0 .Lw_132\n"
    ".Lgc_133:\n"
    "v_cmp_ne_u32 vcc, s53, v202\n"
    "s_nop 0\n"
    "s_cbranch_vccnz .Lgr_134\n"
    ".Lgo_135:\n"
    "v_add_f32_dpp v200, v200, v200 row_ror:4 row_mask:0xf bank_mask:0xf\n"
    "v_add_f32_dpp v201, v201, v201 row_ror:4 row_mask:0xf bank_mask:0xf\n"
    "s_nop 0\n"
    "v_add_f32_dpp v200, v200, v200 row_ror:2 row_mask:0xf bank_mask:0xf\n"
    "v_add_f32_dpp v201, v201, v201 row_ror:2 row_mask:0xf bank_mask:0xf\n"
    "s_nop 0\n"
    "v_add_f32_dpp v200, v200, v200 row_ror:1 row_mask:0xf bank_mask:0xf\n"
    "v_add_f32_dpp v201, v201, v201 row_ror:1 row_mask:0xf bank_mask:0xf\n"
    "s_lshr_b32 s62, s59, 1\n"
    "s_cmp_eq_u32 s62, 2\n"
    "s_cselect_b32 s62, 1.0, 0\n"
    "v_mov_b32 v203, v202\n"
    "v_cvt_pk_bf16_f32 v202, v200, v201\n"
    "v_lshlrev_b32 v200, 16, v202\n"
    "v_and_b32 v201, 0xffff0000, v202\n"
    "v_add_f32 v200, s62, v200\n"
    "v_add_f32 v201, s62, v201\n"
    "v_cvt_pk_bf16_f32 v202, v200, v201\n"
    "v_lshrrev_b32 v200, 1, v240\n"
    "s_lshl_b32 s62, s55, 5\n"
    "s_add_u32 s62, s62, 36864\n"
    "s_mov_b32 exec_lo, 0x00010001\n"
    "s_mov_b32 exec_hi, 0x00010001\n"
    "buffer_store_dwordx2 v[202:203], v200, s[12:15], s62 offen sc1\n"
    "s_mov_b64 exec, -1\n"
    ".Lw_132:\n"
    "s_setprio 1\n"
    "s_xor_b32 s63, s75, 462848\n"
    "buffer_load_dwordx4 v[216:219], v241, s[12:15], s63 offen offset:0 sc1\n"
    "buffer_load_dwordx4 v[220:223], v241, s[12:15], s63 offen offset:1024 sc1\n"
    "buffer_load_dwordx4 v[224:227], v241, s[12:15], s63 offen offset:2048 sc1\n"
    "buffer_load_dwordx4 v[228:231], v241, s[12:15], s63 offen offset:3072 sc1\n"
    "s_waitcnt lgkmcnt(0)\n"
    "v_smfmac_f32_32x32x32_bf16 v[128:143], v[32:35], v[96:103], v232\n"
    "v_exp_f32 v112, v112\n"
    "v_exp_f32 v113, v113\n"
    "v_smfmac_f32_32x32x32_bf16 v[128:143], v[36:39], v[104:111], v232\n"
    "v_exp_f32 v114, v114\n"
    "v_exp_f32 v115, v115\n"
    "v_exp_f32 v116, v116\n"
    "s_nop 0\n"
    ".Lpad5632:\n"
    "s_cbranch_execz .Lpad8685\n"
    "v_mfma_f32_32x32x16_bf16 v[96:111], v[8:11], v[148:151], 0\n"
    "v_exp_f32 v117, v117\n"
    "v_exp_f32 v118, v118\n"
    "v_exp_f32 v119, v119\n"
    "v_exp_f32 v120, v120\n"
    "v_exp_f32 v121, v121\n"
    "v_smfmac_f32_32x32x32_bf16 v[128:143], v[40:43], v[112:119], v232\n"
    "v_exp_f32 v122, v122\n"
    "v_exp_f32 v123, v123\n"
    "v_exp_f32 v124, v124\n"
    "v_exp_f32 v125, v125\n"
    "v_exp_f32 v126, v126\n"
    "v_exp_f32 v127, v127\n"
    "v_exp_f32 v96, v96\n"
    "v_exp_f32 v97, v97\n"
    "v_smfmac_f32_32x32x32_bf16 v[128:143], v[44:47], v[120:127], v232\n"
    "v_exp_f32 v98, v98\n"
    "v_exp_f32 v99, v99\n"
    "v_exp_f32 v100, v100\n"
    "v_mfma_f32_32x32x16_bf16 v[112:127], v[12:15], v[148:151], 0\n"
    "v_exp_f32 v101, v101\n"
    "v_exp_f32 v102, v102\n"
    "v_exp_f32 v103, v103\n"
    "v_exp_f32 v104, v104\n"
    "v_exp_f32 v105, v105\n"
    "v_smfmac_f32_32x32x32_bf16 v[128:143], v[48:51], v[96:103], v232\n"
    "s_xor_b32 s73, s75, 0\n"
    "s_xor_b32 s74, s75, 4096\n"
    "v_exp_f32 v106, v106\n"
    "v_exp_f32 v107, v107\n"
    "v_exp_f32 v108, v108\n"
    "v_exp_f32 v109, v109\n"
    "v_exp_f32 v110, v110\n"
    "v_exp_f32 v111, v111\n"
    "buffer_load_dwordx4 v[168:171], v241, s[12:15], s73 offen offset:0 sc1\n"
    "v_exp_f32 v112, v112\n"
    "v_exp_f32 v113, v113\n"
    "v_smfmac_f32_32x32x32_bf16 v[128:143], v[52:55], v[104:111], v232\n"
    "buffer_load_dwordx4 v[172:175], v241, s[12:15], s73 offen offset:1024 sc1\n"
    "v_exp_f32 v114, v114\n"
    "v_exp_f32 v115, v115\n"
    "v_exp_f32 v116, v116\n"
    "v_mfma_f32_32x32x16_bf16 v[96:111], v[16:19], v[148:151], 0\n"
    "v_exp_f32 v117, v117\n"
    "v_exp_f32 v118, v118\n"
    "v_exp_f32 v119, v119\n"
    "v_exp_f32 v120, v120\n"
    "v_exp_f32 v121, v121\n"
    "v_smfmac_f32_32x32x32_bf16 v[128:143], v[56:59], v[112:119], v232\n"
    "v_exp_f32 v122, v122\n"
    "v_exp_f32 v123, v123\n"
    "v_exp_f32 v124, v124\n"
    "v_exp_f32 v125, v125\n"
    "v_exp_f32 v126, v126\n"
    "v_exp_f32 v127, v127\n"
    "buffer_load_dwordx4 v[176:179], v241, s[12:15], s73 offen offset:2048 sc1\n"
    "v_exp_f32 v96, v96\n"
    "v_exp_f32 v97, v97\n"
    "v_smfmac_f32_32x32x32_bf16 v[128:143], v[60:63], v[120:127], v232\n"
    "buffer_load_dwordx4 v[180:183], v241, s[12:15], s73 offen offset:3072 sc1\n"
    "v_exp_f32 v98, v98\n"
    "v_exp_f32 v99, v99\n"
    "v_exp_f32 v100, v100\n"
    "v_mfma_f32_32x32x16_bf16 v[112:127], v[20:23], v[148:151], 0\n"
    "v_exp_f32 v101, v101\n"
    "v_exp_f32 v102, v102\n"
    "v_exp_f32 v103, v103\n"
    "v_exp_f32 v104, v104\n"
    "v_exp_f32 v105, v105\n"
    "v_smfmac_f32_32x32x32_bf16 v[128:143], v[64:67], v[96:103], v232\n"
    "v_exp_f32 v106, v106\n"
    "v_exp_f32 v107, v107\n"
    "v_exp_f32 v108, v108\n"
    "v_exp_f32 v109, v109\n"
    "v_exp_f32 v110, v110\n"
    "v_exp_f32 v111, v111\n"
    "buffer_load_dwordx4 v[184:187], v241, s[12:15], s74 offen offset:0 sc1\n"
    "v_exp_f32 v112, v112\n"
    "v_exp_f32 v113, v113\n"
    "v_smfmac_f32_32x32x32_bf16 v[128:143], v[68:71], v[104:111], v232\n"
    "buffer_load_dwordx4 v[188:191], v241, s[12:15], s74 offen offset:1024 sc1\n"
    "v_exp_f32 v114, v114\n"
    "v_exp_f32 v115, v115\n"
    "v_exp_f32 v116, v116\n"
    "v_mfma_f32_32x32x16_bf16 v[96:111], v[24:27], v[148:151], 0\n"
    "v_exp_f32 v117, v117\n"
    "v_exp_f32 v118, v118\n"
    "v_exp_f32 v119, v119\n"
    "v_exp_f32 v120, v120\n"
    "v_exp_f32 v121, v121\n"
    "v_smfmac_f32_32x32x32_bf16 v[128:143], v[72:75], v[112:119], v232\n"
    "v_exp_f32 v122, v122\n"
    "v_exp_f32 v123, v123\n"
    "v_exp_f32 v124, v124\n"
    "v_exp_f32 v125, v125\n"
    "v_exp_f32 v126, v126\n"
    "v_exp_f32 v127, v127\n"
    "buffer_load_dwordx4 v[192:195], v241, s[12:15], s74 offen offset:2048 sc1\n"
    "v_exp_f32 v96, v96\n"
    "v_exp_f32 v97, v97\n"
    "v_smfmac_f32_32x32x32_bf16 v[128:143], v[76:79], v[120:127], v232\n"
    "buffer_load_dwordx4 v[196:199], v241, s[12:15], s74 offen offset:3072 sc1\n"
    "v_exp_f32 v98, v98\n"
    "v_exp_f32 v99, v99\n"
    "v_exp_f32 v100, v100\n"
    "v_mfma_f32_32x32x16_bf16 v[112:127], v[28:31], v[148:151], 0\n"
    "v_exp_f32 v101, v101\n"
    "v_exp_f32 v102, v102\n"
    "v_exp_f32 v103, v103\n"
    "v_exp_f32 v104, v104\n"
    "v_exp_f32 v105, v105\n"
    "v_smfmac_f32_32x32x32_bf16 v[128:143], v[80:83], v[96:103], v232\n"
    "s_waitcnt vmcnt(12)\n"
    "v_cmp_eq_u32 vcc, -1, v144\n"
    "v_exp_f32 v106, v106\n"
    "v_exp_f32 v107, v107\n"
    "v_exp_f32 v108, v108\n"
    "v_exp_f32 v109, v109\n"
    "v_exp_f32 v110, v110\n"
    "v_exp_f32 v111, v111\n"
    "v_exp_f32 v112, v112\n"
    "v_exp_f32 v113, v113\n"
    "v_smfmac_f32_32x32x32_bf16 v[128:143], v[84:87], v[104:111], v232\n"
    "s_cbranch_vccnz .Lqr_99\n"
    ".Lqo_100:\n"
    "v_exp_f32 v114, v114\n"
    "v_exp_f32 v115, v115\n"
    "v_exp_f32 v116, v116\n"
    "v_mfma_f32_32x32x16_bf16 v[96:111], v[0:3], v[144:147], 0\n"
    "v_exp_f32 v117, v117\n"
    "v_exp_f32 v118, v118\n"
    "v_exp_f32 v119, v119\n"
    "v_exp_f32 v120, v120\n"
    "v_exp_f32 v121, v121\n"
    "v_smfmac_f32_32x32x32_bf16 v[128:143], v[88:91], v[112:119], v232\n"
    "buffer_store_dwordx4 v[212:215], v241, s[12:15], s86 offen offset:1024 sc1\n"
    "v_exp_f32 v122, v122\n"
    "v_exp_f32 v123, v123\n"
    "v_exp_f32 v124, v124\n"
    "v_exp_f32 v125, v125\n"
    "v_exp_f32 v126, v126\n"
    "v_exp_f32 v127, v127\n"
    "v_exp_f32 v96, v96\n"
    "v_exp_f32 v97, v97\n"
    "v_smfmac_f32_32x32x32_bf16 v[128:143], v[92:95], v[120:127], v232\n"
    "v_exp_f32 v98, v98\n"
    "v_exp_f32 v99, v99\n"
    "v_exp_f32 v100, v100\n"
    "v_mfma_f32_32x32x16_bf16 v[112:127], v[4:7], v[144:147], 0\n"
    "v_exp_f32 v101, v101\n"
    "v_exp_f32 v102, v102\n"
    "v_exp_f32 v103, v103\n"
    "v_exp_f32 v104, v104\n"
    "v_exp_f32 v105, v105\n"
    "v_exp_f32 v106, v106\n"
    "v_exp_f32 v107, v107\n"
    "v_exp_f32 v108, v108\n"
    "ds_write_b128 v244, v[128:131] offset:8192\n"
    "ds_write_b128 v244, v[132:135] offset:9216\n"
    "ds_write_b32 v245, v136 offset:512\n"
    "v_exp_f32 v109, v109\n"
    "v_exp_f32 v110, v110\n"
    "v_exp_f32 v111, v111\n"
    "s_waitcnt lgkmcnt(0)\n"
    "s_barrier\n"
    "ds_read_b128 v[128:131], v242 offset:0\n"
    "ds_read_b128 v[132:135], v242 offset:1024\n"
    "ds_read_b32 v136, v243 offset:0\n"
    "ds_read_b128 v[250:253], v247 offset:0\n"
    "s_xor_b32 s72, s71, 3072\n"
    "buffer_load_dwordx4 v[148:151], v241, s[12:15], s72 offen sc1\n"
    "s_setprio 3\n"
    "v_lshl_add_u32 v211, s50, 5, v240\n"
    "v_and_b32 v210, 7, v240\n"
    "v_lshl_add_u32 v210, v210, 5, s49\n"
    "v_cmp_gt_u32 vcc, 32, v240\n"
    "s_mov_b32 s66, 65536\n"
    "s_nop 0\n"
    "v_cndmask_b32 v211, v210, v211, vcc\n"
    "v_lshlrev_b32 v211, 6, v211\n"
    "buffer_load_dword v210, v211, s[12:15], s66 offen sc1\n"
    ".Lgc_136:\n"
    "s_waitcnt vmcnt(0)\n"
    "v_cmp_ne_u32 vcc, s53, v210\n"
    "s_nop 0\n"
    "s_cbranch_vccnz .Lgr_137\n"
    ".Lgo_138:\n"
    "s_add_u32 s67, s56, 4\n"
    "s_lshl_b32 s67, s67, 15\n"
    "s_lshl_b32 s68, s51, 14\n"
    "s_add_u32 s67, s67, s68\n"
    "s_add_u32 s67, s67, 3145728\n"
    "s_add_u32 m0, s68, 0\n"
    "s_lshr_b32 s69, s51, 1\n"
    "s_add_u32 s69, s69, 4\n"
    "s_setprio 1\n"
    "s_cmp_lt_u32 s69, s57\n"
    "s_cbranch_scc0 .Ls5_83\n"
    "buffer_load_dwordx4 v241, s[12:15], s67 offen offset:0 sc1 lds\n"
    ".Ls5_83:\n"
    "s_cmp_lt_u32 s69, s57\n"
    "s_cbranch_scc0 .Ls5_84\n"
    "buffer_load_dwordx4 v241, s[12:15], s67 offen offset:1024 sc1 lds\n"
    ".Ls5_84:\n"
    "s_cmp_lt_u32 s69, s57\n"
    "s_cbranch_scc0 .Ls5_85\n"
    "buffer_load_dwordx4 v241, s[12:15], s67 offen offset:2048 sc1 lds\n"
    ".Ls5_85:\n"
    "s_cmp_lt_u32 s69, s57\n"
    "s_cbranch_scc0 .Ls5_86\n"
    "buffer_load_dwordx4 v241, s[12:15], s67 offen offset:3072 sc1 lds\n"
    ".Ls5_86:\n"
    "s_waitcnt lgkmcnt(1)\n"
    "v_smfmac_f32_32x32x32_bf16 v[128:143], v[32:35], v[96:103], v232\n"
    "v_exp_f32 v112, v112\n"
    "v_exp_f32 v113, v113\n"
    "v_smfmac_f32_32x32x32_bf16 v[128:143], v[36:39], v[104:111], v232\n"
    "v_exp_f32 v114, v114\n"
    "v_exp_f32 v115, v115\n"
    "v_exp_f32 v116, v116\n"
    "v_mfma_f32_32x32x16_bf16 v[96:111], v[8:11], v[144:147], 0\n"
    "s_add_u32 s85, s56, s51\n"
    "s_lshl_b32 s85, s85, 15\n"
    "s_and_b32 s84, s49, 3\n"
    "s_mul_i32 s62, s84, 5120\n"
    "s_add_u32 s85, s85, s62\n"
    "s_add_u32 s85, s85, 3158016\n"
    "buffer_load_dwordx4 v[236:239], v241, s[12:15], s85 offen offset:0 sc1\n"
    "v_exp_f32 v117, v117\n"
    "v_exp_f32 v118, v118\n"
    "v_exp_f32 v119, v119\n"
    "v_exp_f32 v120, v120\n"
    "v_exp_f32 v121, v121\n"
    "v_smfmac_f32_32x32x32_bf16 v[128:143], v[40:43], v[112:119], v232\n"
    "v_exp_f32 v122, v122\n"
    "v_exp_f32 v123, v123\n"
    "v_exp_f32 v124, v124\n"
    "v_exp_f32 v125, v125\n"
    "v_exp_f32 v126, v126\n"
    "v_exp_f32 v127, v127\n"
    "v_exp_f32 v96, v96\n"
    "v_exp_f32 v97, v97\n"
    "v_smfmac_f32_32x32x32_bf16 v[128:143], v[44:47], v[120:127], v232\n"
    "v_exp_f32 v98, v98\n"
    "v_exp_f32 v99, v99\n"
    "v_exp_f32 v100, v100\n"
    "v_mfma_f32_32x32x16_bf16 v[112:127], v[12:15], v[144:147], 0\n"
    "v_exp_f32 v101, v101\n"
    "v_exp_f32 v102, v102\n"
    "v_exp_f32 v103, v103\n"
    "v_exp_f32 v104, v104\n"
    "v_exp_f32 v105, v105\n"
    "v_smfmac_f32_32x32x32_bf16 v[128:143], v[48:51], v[96:103], v232\n"
    "s_xor_b32 s73, s75, 65536\n"
    "s_xor_b32 s74, s75, 69632\n"
    "v_exp_f32 v106, v106\n"
    "v_exp_f32 v107, v107\n"
    "v_exp_f32 v108, v108\n"
    "v_exp_f32 v109, v109\n"
    "v_exp_f32 v110, v110\n"
    "v_exp_f32 v111, v111\n"
    "s_waitcnt lgkmcnt(0)\n"
    "v_smfmac_f32_16x16x64_bf16 v[152:155], v[250:253], v[168:175], v233\n"
    "buffer_load_dwordx4 v[168:171], v241, s[12:15], s73 offen offset:0 sc1\n"
    "v_exp_f32 v112, v112\n"
    "v_exp_f32 v113, v113\n"
    "v_smfmac_f32_32x32x32_bf16 v[128:143], v[52:55], v[104:111], v232\n"
    "buffer_load_dwordx4 v[172:175], v241, s[12:15], s73 offen offset:1024 sc1\n"
    "v_exp_f32 v114, v114\n"
    "v_exp_f32 v115, v115\n"
    "v_exp_f32 v116, v116\n"
    "v_mfma_f32_32x32x16_bf16 v[96:111], v[16:19], v[144:147], 0\n"
    "buffer_load_dwordx4 v[236:239], v241, s[12:15], s85 offen offset:1024 sc1\n"
    "v_exp_f32 v117, v117\n"
    "v_exp_f32 v118, v118\n"
    "v_exp_f32 v119, v119\n"
    "v_exp_f32 v120, v120\n"
    "v_exp_f32 v121, v121\n"
    "v_smfmac_f32_32x32x32_bf16 v[128:143], v[56:59], v[112:119], v232\n"
    "v_exp_f32 v122, v122\n"
    "v_exp_f32 v123, v123\n"
    "v_exp_f32 v124, v124\n"
    "v_exp_f32 v125, v125\n"
    "v_exp_f32 v126, v126\n"
    "v_exp_f32 v127, v127\n"
    "v_smfmac_f32_16x16x64_bf16 v[156:159], v[250:253], v[176:183], v233\n"
    "buffer_load_dwordx4 v[176:179], v241, s[12:15], s73 offen offset:2048 sc1\n"
    "v_exp_f32 v96, v96\n"
    "v_exp_f32 v97, v97\n"
    "v_smfmac_f32_32x32x32_bf16 v[128:143], v[60:63], v[120:127], v232\n"
    "buffer_load_dwordx4 v[180:183], v241, s[12:15], s73 offen offset:3072 sc1\n"
    "v_exp_f32 v98, v98\n"
    "v_exp_f32 v99, v99\n"
    "v_exp_f32 v100, v100\n"
    "v_mfma_f32_32x32x16_bf16 v[112:127], v[20:23], v[144:147], 0\n"
    "v_exp_f32 v101, v101\n"
    "v_exp_f32 v102, v102\n"
    "v_exp_f32 v103, v103\n"
    "v_exp_f32 v104, v104\n"
    "v_exp_f32 v105, v105\n"
    "v_smfmac_f32_32x32x32_bf16 v[128:143], v[64:67], v[96:103], v232\n"
    "v_exp_f32 v106, v106\n"
    "v_exp_f32 v107, v107\n"
    "v_exp_f32 v108, v108\n"
    "v_exp_f32 v109, v109\n"
    "v_exp_f32 v110, v110\n"
    "v_exp_f32 v111, v111\n"
    "v_smfmac_f32_16x16x64_bf16 v[160:163], v[250:253], v[184:191], v233\n"
    "buffer_load_dwordx4 v[184:187], v241, s[12:15], s74 offen offset:0 sc1\n"
    "v_exp_f32 v112, v112\n"
    "v_exp_f32 v113, v113\n"
    "v_smfmac_f32_32x32x32_bf16 v[128:143], v[68:71], v[104:111], v232\n"
    "buffer_load_dwordx4 v[188:191], v241, s[12:15], s74 offen offset:1024 sc1\n"
    "v_exp_f32 v114, v114\n"
    "v_exp_f32 v115, v115\n"
    "v_exp_f32 v116, v116\n"
    "v_mfma_f32_32x32x16_bf16 v[96:111], v[24:27], v[144:147], 0\n"
    "buffer_load_dwordx4 v[236:239], v241, s[12:15], s85 offen offset:2048 sc1\n"
    "v_exp_f32 v117, v117\n"
    "v_exp_f32 v118, v118\n"
    "v_exp_f32 v119, v119\n"
    "v_exp_f32 v120, v120\n"
    "v_exp_f32 v121, v121\n"
    "v_smfmac_f32_32x32x32_bf16 v[128:143], v[72:75], v[112:119], v232\n"
    "v_exp_f32 v122, v122\n"
    "v_exp_f32 v123, v123\n"
    "v_exp_f32 v124, v124\n"
    "v_exp_f32 v125, v125\n"
    "v_exp_f32 v126, v126\n"
    "v_exp_f32 v127, v127\n"
    "v_smfmac_f32_16x16x64_bf16 v[164:167], v[250:253], v[192:199], v233\n"
    "buffer_load_dwordx4 v[192:195], v241, s[12:15], s74 offen offset:2048 sc1\n"
    "v_exp_f32 v96, v96\n"
    "v_exp_f32 v97, v97\n"
    "v_smfmac_f32_32x32x32_bf16 v[128:143], v[76:79], v[120:127], v232\n"
    "buffer_load_dwordx4 v[196:199], v241, s[12:15], s74 offen offset:3072 sc1\n"
    "v_exp_f32 v98, v98\n"
    "v_exp_f32 v99, v99\n"
    "v_exp_f32 v100, v100\n"
    "v_mfma_f32_32x32x16_bf16 v[112:127], v[28:31], v[144:147], 0\n"
    "v_exp_f32 v101, v101\n"
    "v_exp_f32 v102, v102\n"
    "v_exp_f32 v103, v103\n"
    "v_exp_f32 v104, v104\n"
    "v_exp_f32 v105, v105\n"
    "v_smfmac_f32_32x32x32_bf16 v[128:143], v[80:83], v[96:103], v232\n"
    "v_cmp_eq_u32 vcc, -1, v148\n"
    "v_exp_f32 v106, v106\n"
    "v_exp_f32 v107, v107\n"
    "v_exp_f32 v108, v108\n"
    "v_exp_f32 v109, v109\n"
    "v_exp_f32 v110, v110\n"
    "v_exp_f32 v111, v111\n"
    "v_exp_f32 v112, v112\n"
    "v_exp_f32 v113, v113\n"
    "v_smfmac_f32_32x32x32_bf16 v[128:143], v[84:87], v[104:111], v232\n"
    "s_cbranch_vccnz .Lqr_101\n"
    ".Lqo_102:\n"
    "v_exp_f32 v114, v114\n"
    "v_exp_f32 v115, v115\n"
    "v_exp_f32 v116, v116\n"
    "v_mfma_f32_32x32x16_bf16 v[96:111], v[0:3], v[148:151], 0\n"
    "buffer_load_dwordx4 v[236:239], v241, s[12:15], s85 offen offset:3072 sc1\n"
    "v_exp_f32 v117, v117\n"
    "v_exp_f32 v118, v118\n"
    "v_exp_f32 v119, v119\n"
    "v_exp_f32 v120, v120\n"
    "v_exp_f32 v121, v121\n"
    "v_smfmac_f32_32x32x32_bf16 v[128:143], v[88:91], v[112:119], v232\n"
    "buffer_store_dwordx4 v[212:215], v241, s[12:15], s86 offen offset:2048 sc1\n"
    "v_exp_f32 v122, v122\n"
    "v_exp_f32 v123, v123\n"
    "v_exp_f32 v124, v124\n"
    "v_exp_f32 v125, v125\n"
    "v_exp_f32 v126, v126\n"
    "v_exp_f32 v127, v127\n"
    "v_exp_f32 v96, v96\n"
    "v_exp_f32 v97, v97\n"
    "v_smfmac_f32_32x32x32_bf16 v[128:143], v[92:95], v[120:127], v232\n"
    "v_exp_f32 v98, v98\n"
    "v_exp_f32 v99, v99\n"
    "v_exp_f32 v100, v100\n"
    "v_mfma_f32_32x32x16_bf16 v[112:127], v[4:7], v[148:151], 0\n"
    "v_exp_f32 v101, v101\n"
    "v_exp_f32 v102, v102\n"
    "v_exp_f32 v103, v103\n"
    "v_exp_f32 v104, v104\n"
    "v_exp_f32 v105, v105\n"
    "v_exp_f32 v106, v106\n"
    "v_exp_f32 v107, v107\n"
    "v_exp_f32 v108, v108\n"
    "ds_write_b128 v244, v[128:131] offset:0\n"
    "ds_write_b128 v244, v[132:135] offset:1024\n"
    "ds_write_b32 v245, v136 offset:0\n"
    "v_exp_f32 v109, v109\n"
    "v_exp_f32 v110, v110\n"
    "v_exp_f32 v111, v111\n"
    "s_waitcnt lgkmcnt(0)\n"
    "s_barrier\n"
    "ds_read_b128 v[128:131], v242 offset:9216\n"
    "ds_read_b128 v[132:135], v242 offset:10240\n"
    "ds_read_b32 v136, v243 offset:9216\n"
    "ds_read_b128 v[250:253], v247 offset:1024\n"
    "s_xor_b32 s72, s71, 4096\n"
    "buffer_load_dwordx4 v[144:147], v241, s[12:15], s72 offen sc1\n"
    "s_lshl_b32 s66, s51, 10\n"
    "s_add_u32 s66, s66, 36864\n"
    "s_add_u32 s65, s66, 4096\n"
    "buffer_load_dwordx4 v[200:203], v241, s[12:15], s66 offen sc1\n"
    "buffer_load_dwordx4 v[204:207], v241, s[12:15], s65 offen sc1\n"
    "s_add_u32 s67, s67, 4096\n"
    "s_add_u32 m0, m0, 4096\n"
    "s_cmp_lt_u32 s69, s57\n"
    "s_cbranch_scc0 .Ls5_87\n"
    "buffer_load_dwordx4 v241, s[12:15], s67 offen offset:0 sc1 lds\n"
    ".Ls5_87:\n"
    "s_cmp_lt_u32 s69, s57\n"
    "s_cbranch_scc0 .Ls5_88\n"
    "buffer_load_dwordx4 v241, s[12:15], s67 offen offset:1024 sc1 lds\n"
    ".Ls5_88:\n"
    "s_cmp_lt_u32 s69, s57\n"
    "s_cbranch_scc0 .Ls5_89\n"
    "buffer_load_dwordx4 v241, s[12:15], s67 offen offset:2048 sc1 lds\n"
    ".Ls5_89:\n"
    "s_cmp_lt_u32 s69, s57\n"
    "s_cbranch_scc0 .Ls5_90\n"
    "buffer_load_dwordx4 v241, s[12:15], s67 offen offset:3072 sc1 lds\n"
    ".Ls5_90:\n"
    "s_waitcnt lgkmcnt(1)\n"
    "v_smfmac_f32_32x32x32_bf16 v[128:143], v[32:35], v[96:103], v232\n"
    "v_exp_f32 v112, v112\n"
    "v_exp_f32 v113, v113\n"
    "v_smfmac_f32_32x32x32_bf16 v[128:143], v[36:39], v[104:111], v232\n"
    "v_exp_f32 v114, v114\n"
    "v_exp_f32 v115, v115\n"
    "v_exp_f32 v116, v116\n"
    "v_mfma_f32_32x32x16_bf16 v[96:111], v[8:11], v[148:151], 0\n"
    "s_add_u32 s85, s85, 4096\n"
    "buffer_load_dwordx4 v[236:239], v241, s[12:15], s85 offen offset:0 sc1\n"
    "v_exp_f32 v117, v117\n"
    "v_exp_f32 v118, v118\n"
    "v_exp_f32 v119, v119\n"
    "v_exp_f32 v120, v120\n"
    "v_exp_f32 v121, v121\n"
    "v_smfmac_f32_32x32x32_bf16 v[128:143], v[40:43], v[112:119], v232\n"
    "v_exp_f32 v122, v122\n"
    "v_exp_f32 v123, v123\n"
    "v_exp_f32 v124, v124\n"
    "v_exp_f32 v125, v125\n"
    "v_exp_f32 v126, v126\n"
    "v_exp_f32 v127, v127\n"
    "v_exp_f32 v96, v96\n"
    "v_exp_f32 v97, v97\n"
    "v_smfmac_f32_32x32x32_bf16 v[128:143], v[44:47], v[120:127], v232\n"
    "v_exp_f32 v98, v98\n"
    "v_exp_f32 v99, v99\n"
    "v_exp_f32 v100, v100\n"
    "v_mfma_f32_32x32x16_bf16 v[112:127], v[12:15], v[148:151], 0\n"
    "v_exp_f32 v101, v101\n"
    "v_exp_f32 v102, v102\n"
    "v_exp_f32 v103, v103\n"
    "v_exp_f32 v104, v104\n"
    "v_exp_f32 v105, v105\n"
    "v_smfmac_f32_32x32x32_bf16 v[128:143], v[48:51], v[96:103], v232\n"
    "s_xor_b32 s73, s75, 131072\n"
    "s_xor_b32 s74, s75, 135168\n"
    "v_exp_f32 v106, v106\n"
    "v_exp_f32 v107, v107\n"
    "v_exp_f32 v108, v108\n"
    "v_exp_f32 v109, v109\n"
    "v_exp_f32 v110, v110\n"
    "v_exp_f32 v111, v111\n"
    "s_waitcnt vmcnt(14) lgkmcnt(0)\n"
    "v_smfmac_f32_16x16x64_bf16 v[152:155], v[250:253], v[168:175], v233\n"
    "buffer_load_dwordx4 v[168:171], v241, s[12:15], s73 offen offset:0 sc1\n"
    "v_exp_f32 v112, v112\n"
    "v_exp_f32 v113, v113\n"
    "v_smfmac_f32_32x32x32_bf16 v[128:143], v[52:55], v[104:111], v232\n"
    "buffer_load_dwordx4 v[172:175], v241, s[12:15], s73 offen offset:1024 sc1\n"
    "v_exp_f32 v114, v114\n"
    "v_exp_f32 v115, v115\n"
    "v_exp_f32 v116, v116\n"
    "v_mfma_f32_32x32x16_bf16 v[96:111], v[16:19], v[148:151], 0\n"
    "s_and_b32 s84, s49, 3\n"
    "s_lshl2_add_u32 s62, s84, s51\n"
    "s_mul_i32 s62, s62, 11264\n"
    "s_mul_i32 s85, s50, 176128\n"
    "s_add_u32 s85, s85, s62\n"
    "s_add_u32 s85, s85, 4718592\n"
    "buffer_load_dwordx4 v[236:239], v241, s[12:15], s85 offen offset:0 sc1\n"
    "v_exp_f32 v117, v117\n"
    "v_exp_f32 v118, v118\n"
    "v_exp_f32 v119, v119\n"
    "v_exp_f32 v120, v120\n"
    "v_exp_f32 v121, v121\n"
    "v_smfmac_f32_32x32x32_bf16 v[128:143], v[56:59], v[112:119], v232\n"
    "v_exp_f32 v122, v122\n"
    "v_exp_f32 v123, v123\n"
    "v_exp_f32 v124, v124\n"
    "v_exp_f32 v125, v125\n"
    "v_exp_f32 v126, v126\n"
    "v_exp_f32 v127, v127\n"
    "s_waitcnt vmcnt(14)\n"
    "v_smfmac_f32_16x16x64_bf16 v[156:159], v[250:253], v[176:183], v233\n"
    "buffer_load_dwordx4 v[176:179], v241, s[12:15], s73 offen offset:2048 sc1\n"
    "v_exp_f32 v96, v96\n"
    "v_exp_f32 v97, v97\n"
    "v_smfmac_f32_32x32x32_bf16 v[128:143], v[60:63], v[120:127], v232\n"
    "buffer_load_dwordx4 v[180:183], v241, s[12:15], s73 offen offset:3072 sc1\n"
    "v_exp_f32 v98, v98\n"
    "v_exp_f32 v99, v99\n"
    "v_exp_f32 v100, v100\n"
    "v_mfma_f32_32x32x16_bf16 v[112:127], v[20:23], v[148:151], 0\n"
    "v_exp_f32 v101, v101\n"
    "v_exp_f32 v102, v102\n"
    "v_exp_f32 v103, v103\n"
    "v_exp_f32 v104, v104\n"
    "v_exp_f32 v105, v105\n"
    "v_smfmac_f32_32x32x32_bf16 v[128:143], v[64:67], v[96:103], v232\n"
    "v_exp_f32 v106, v106\n"
    "v_exp_f32 v107, v107\n"
    "v_exp_f32 v108, v108\n"
    "v_exp_f32 v109, v109\n"
    "v_exp_f32 v110, v110\n"
    "v_exp_f32 v111, v111\n"
    "s_waitcnt vmcnt(14)\n"
    "v_smfmac_f32_16x16x64_bf16 v[160:163], v[250:253], v[184:191], v233\n"
    "buffer_load_dwordx4 v[184:187], v241, s[12:15], s74 offen offset:0 sc1\n"
    "v_exp_f32 v112, v112\n"
    "v_exp_f32 v113, v113\n"
    "v_smfmac_f32_32x32x32_bf16 v[128:143], v[68:71], v[104:111], v232\n"
    "buffer_load_dwordx4 v[188:191], v241, s[12:15], s74 offen offset:1024 sc1\n"
    "v_exp_f32 v114, v114\n"
    "v_exp_f32 v115, v115\n"
    "v_exp_f32 v116, v116\n"
    "v_mfma_f32_32x32x16_bf16 v[96:111], v[24:27], v[148:151], 0\n"
    "buffer_load_dwordx4 v[236:239], v241, s[12:15], s85 offen offset:1024 sc1\n"
    "v_exp_f32 v117, v117\n"
    "v_exp_f32 v118, v118\n"
    "v_exp_f32 v119, v119\n"
    "v_exp_f32 v120, v120\n"
    "v_exp_f32 v121, v121\n"
    "v_smfmac_f32_32x32x32_bf16 v[128:143], v[72:75], v[112:119], v232\n"
    "v_exp_f32 v122, v122\n"
    "v_exp_f32 v123, v123\n"
    "v_exp_f32 v124, v124\n"
    "v_exp_f32 v125, v125\n"
    "v_exp_f32 v126, v126\n"
    "v_exp_f32 v127, v127\n"
    "s_waitcnt vmcnt(14)\n"
    "v_smfmac_f32_16x16x64_bf16 v[164:167], v[250:253], v[192:199], v233\n"
    "buffer_load_dwordx4 v[192:195], v241, s[12:15], s74 offen offset:2048 sc1\n"
    "v_exp_f32 v96, v96\n"
    "v_exp_f32 v97, v97\n"
    "v_smfmac_f32_32x32x32_bf16 v[128:143], v[76:79], v[120:127], v232\n"
    "buffer_load_dwordx4 v[196:199], v241, s[12:15], s74 offen offset:3072 sc1\n"
    "v_exp_f32 v98, v98\n"
    "v_exp_f32 v99, v99\n"
    "v_exp_f32 v100, v100\n"
    "v_mfma_f32_32x32x16_bf16 v[112:127], v[28:31], v[148:151], 0\n"
    "v_exp_f32 v101, v101\n"
    "v_exp_f32 v102, v102\n"
    "v_exp_f32 v103, v103\n"
    "v_exp_f32 v104, v104\n"
    "v_exp_f32 v105, v105\n"
    "v_smfmac_f32_32x32x32_bf16 v[128:143], v[80:83], v[96:103], v232\n"
    "s_waitcnt vmcnt(13)\n"
    "v_cmp_eq_u32 vcc, -1, v144\n"
    "v_exp_f32 v106, v106\n"
    "v_exp_f32 v107, v107\n"
    "v_exp_f32 v108, v108\n"
    "v_exp_f32 v109, v109\n"
    "v_exp_f32 v110, v110\n"
    "v_exp_f32 v111, v111\n"
    "v_exp_f32 v112, v112\n"
    "v_exp_f32 v113, v113\n"
    "v_smfmac_f32_32x32x32_bf16 v[128:143], v[84:87], v[104:111], v232\n"
    "s_cbranch_vccnz .Lqr_103\n"
    ".Lqo_104:\n"
    "v_exp_f32 v114, v114\n"
    "v_exp_f32 v115, v115\n"
    "v_exp_f32 v116, v116\n"
    "v_mfma_f32_32x32x16_bf16 v[96:111], v[0:3], v[144:147], 0\n"
    "buffer_load_dwordx4 v[236:239], v241, s[12:15], s85 offen offset:2048 sc1\n"
    "v_exp_f32 v117, v117\n"
    "v_exp_f32 v118, v118\n"
    "v_exp_f32 v119, v119\n"
    "v_exp_f32 v120, v120\n"
    "v_exp_f32 v121, v121\n"
    "v_smfmac_f32_32x32x32_bf16 v[128:143], v[88:91], v[112:119], v232\n"
    "buffer_store_dwordx4 v[212:215], v241, s[12:15], s87 offen offset:0 sc1\n"
    "v_exp_f32 v122, v122\n"
    "v_exp_f32 v123, v123\n"
    "v_exp_f32 v124, v124\n"
    "v_exp_f32 v125, v125\n"
    "v_exp_f32 v126, v126\n"
    "v_exp_f32 v127, v127\n"
    "v_exp_f32 v96, v96\n"
    "v_exp_f32 v97, v97\n"
    "v_smfmac_f32_32x32x32_bf16 v[128:143], v[92:95], v[120:127], v232\n"
    "v_exp_f32 v98, v98\n"
    "v_exp_f32 v99, v99\n"
    "v_exp_f32 v100, v100\n"
    "v_mfma_f32_32x32x16_bf16 v[112:127], v[4:7], v[144:147], 0\n"
    "v_exp_f32 v101, v101\n"
    "v_exp_f32 v102, v102\n"
    "v_exp_f32 v103, v103\n"
    "v_exp_f32 v104, v104\n"
    "v_exp_f32 v105, v105\n"
    "v_exp_f32 v106, v106\n"
    "v_exp_f32 v107, v107\n"
    "v_exp_f32 v108, v108\n"
    "ds_write_b128 v244, v[128:131] offset:8192\n"
    "ds_write_b128 v244, v[132:135] offset:9216\n"
    "ds_write_b32 v245, v136 offset:512\n"
    "v_exp_f32 v109, v109\n"
    "v_exp_f32 v110, v110\n"
    "v_exp_f32 v111, v111\n"
    "s_waitcnt lgkmcnt(0)\n"
    "s_barrier\n"
    "ds_read_b128 v[128:131], v242 offset:0\n"
    "ds_read_b128 v[132:135], v242 offset:1024\n"
    "ds_read_b32 v136, v243 offset:0\n"
    "ds_read_b128 v[250:253], v247 offset:0\n"
    "s_xor_b32 s72, s71, 5120\n"
    "buffer_load_dwordx4 v[148:151], v241, s[12:15], s72 offen sc1\n"
    "s_setprio 3\n"
    ".Lgc_139:\n"
    "s_waitcnt vmcnt(15)\n"
    "v_cmp_ne_u32 s[76:77], s53, v201\n"
    "v_cmp_ne_u32 s[78:79], s53, v203\n"
    "s_waitcnt vmcnt(14)\n"
    "v_cmp_ne_u32 vcc, s53, v205\n"
    "s_or_b64 s[76:77], s[76:77], s[78:79]\n"
    "v_cmp_ne_u32 s[78:79], s53, v207\n"
    "s_or_b64 vcc, vcc, s[76:77]\n"
    "s_or_b64 vcc, vcc, s[78:79]\n"
    "s_cbranch_vccnz .Lgr_140\n"
    ".Lgo_141:\n"
    "v_and_b32 v201, 0xffff0000, v200\n"
    "v_lshlrev_b32 v200, 16, v200\n"
    "v_and_b32 v203, 0xffff0000, v202\n"
    "v_lshlrev_b32 v202, 16, v202\n"
    "v_and_b32 v205, 0xffff0000, v204\n"
    "v_lshlrev_b32 v204, 16, v204\n"
    "v_and_b32 v207, 0xffff0000, v206\n"
    "v_lshlrev_b32 v206, 16, v206\n"
    "v_and_b32 v208, 1, v240\n"
    "v_lshrrev_b32 v209, 1, v240\n"
    "v_lshlrev_b32 v209, 4, v209\n"
    "v_lshl_or_b32 v208, v208, 10, v209\n"
    "s_lshr_b32 s62, s51, 1\n"
    "s_lshl_b32 s62, s62, 11\n"
    "s_and_b32 s63, s51, 1\n"
    "s_lshl_b32 s63, s63, 9\n"
    "s_add_u32 s62, s62, s63\n"
    "s_add_u32 s62, s62, 152576\n"
    "v_add_u32 v208, s62, v208\n"
    "ds_write_b128 v208, v[200:203]\n"
    "ds_write_b128 v208, v[204:207] offset:4096\n"
    "s_setprio 1\n"
    "s_add_u32 s67, s67, 4096\n"
    "s_add_u32 m0, m0, 4096\n"
    "s_cmp_lt_u32 s69, s57\n"
    "s_cbranch_scc0 .Ls5_91\n"
    "buffer_load_dwordx4 v241, s[12:15], s67 offen offset:0 sc1 lds\n"
    ".Ls5_91:\n"
    "s_cmp_lt_u32 s69, s57\n"
    "s_cbranch_scc0 .Ls5_92\n"
    "buffer_load_dwordx4 v241, s[12:15], s67 offen offset:1024 sc1 lds\n"
    ".Ls5_92:\n"
    "s_cmp_lt_u32 s69, s57\n"
    "s_cbranch_scc0 .Ls5_93\n"
    "buffer_load_dwordx4 v241, s[12:15], s67 offen offset:2048 sc1 lds\n"
    ".Ls5_93:\n"
    "s_cmp_lt_u32 s69, s57\n"
    "s_cbranch_scc0 .Ls5_94\n"
    "buffer_load_dwordx4 v241, s[12:15], s67 offen offset:3072 sc1 lds\n"
    ".Ls5_94:\n"
    "s_waitcnt lgkmcnt(3)\n"
    "v_smfmac_f32_32x32x32_bf16 v[128:143], v[32:35], v[96:103], v232\n"
    "v_exp_f32 v112, v112\n"
    "v_exp_f32 v113, v113\n"
    "v_smfmac_f32_32x32x32_bf16 v[128:143], v[36:39], v[104:111], v232\n"
    "v_exp_f32 v114, v114\n"
    "v_exp_f32 v115, v115\n"
    "v_exp_f32 v116, v116\n"
    "v_mfma_f32_32x32x16_bf16 v[96:111], v[8:11], v[144:147], 0\n"
    "buffer_load_dwordx4 v[236:239], v241, s[12:15], s85 offen offset:3072 sc1\n"
    "v_exp_f32 v117, v117\n"
    "v_exp_f32 v118, v118\n"
    "v_exp_f32 v119, v119\n"
    "v_exp_f32 v120, v120\n"
    "v_exp_f32 v121, v121\n"
    "v_smfmac_f32_32x32x32_bf16 v[128:143], v[40:43], v[112:119], v232\n"
    "v_exp_f32 v122, v122\n"
    "v_exp_f32 v123, v123\n"
    "v_exp_f32 v124, v124\n"
    "v_exp_f32 v125, v125\n"
    "v_exp_f32 v126, v126\n"
    "v_exp_f32 v127, v127\n"
    "v_exp_f32 v96, v96\n"
    "v_exp_f32 v97, v97\n"
    "v_smfmac_f32_32x32x32_bf16 v[128:143], v[44:47], v[120:127], v232\n"
    "v_exp_f32 v98, v98\n"
    "v_exp_f32 v99, v99\n"
    "v_exp_f32 v100, v100\n"
    "v_mfma_f32_32x32x16_bf16 v[112:127], v[12:15], v[144:147], 0\n"
    "v_exp_f32 v101, v101\n"
    "v_exp_f32 v102, v102\n"
    "v_exp_f32 v103, v103\n"
    "v_exp_f32 v104, v104\n"
    "v_exp_f32 v105, v105\n"
    "v_smfmac_f32_32x32x32_bf16 v[128:143], v[48:51], v[96:103], v232\n"
    "s_xor_b32 s73, s75, 196608\n"
    "s_xor_b32 s74, s75, 200704\n"
    "v_exp_f32 v106, v106\n"
    "v_exp_f32 v107, v107\n"
    "v_exp_f32 v108, v108\n"
    "v_exp_f32 v109, v109\n"
    "v_exp_f32 v110, v110\n"
    "v_exp_f32 v111, v111\n"
    "s_waitcnt vmcnt(12) lgkmcnt(2)\n"
    "v_smfmac_f32_16x16x64_bf16 v[152:155], v[250:253], v[168:175], v233\n"
    "buffer_load_dwordx4 v[168:171], v241, s[12:15], s73 offen offset:0 sc1\n"
    "v_exp_f32 v112, v112\n"
    "v_exp_f32 v113, v113\n"
    "v_smfmac_f32_32x32x32_bf16 v[128:143], v[52:55], v[104:111], v232\n"
    "buffer_load_dwordx4 v[172:175], v241, s[12:15], s73 offen offset:1024 sc1\n"
    "v_exp_f32 v114, v114\n"
    "v_exp_f32 v115, v115\n"
    "v_exp_f32 v116, v116\n"
    "v_mfma_f32_32x32x16_bf16 v[96:111], v[16:19], v[144:147], 0\n"
    "s_add_u32 s85, s85, 4096\n"
    "buffer_load_dwordx4 v[236:239], v241, s[12:15], s85 offen offset:0 sc1\n"
    "v_exp_f32 v117, v117\n"
    "v_exp_f32 v118, v118\n"
    "v_exp_f32 v119, v119\n"
    "v_exp_f32 v120, v120\n"
    "v_exp_f32 v121, v121\n"
    "v_smfmac_f32_32x32x32_bf16 v[128:143], v[56:59], v[112:119], v232\n"
    "v_exp_f32 v122, v122\n"
    "v_exp_f32 v123, v123\n"
    "v_exp_f32 v124, v124\n"
    "v_exp_f32 v125, v125\n"
    "v_exp_f32 v126, v126\n"
    "v_exp_f32 v127, v127\n"
    "s_waitcnt vmcnt(12)\n"
    "v_smfmac_f32_16x16x64_bf16 v[156:159], v[250:253], v[176:183], v233\n"
    "buffer_load_dwordx4 v[176:179], v241, s[12:15], s73 offen offset:2048 sc1\n"
    "v_exp_f32 v96, v96\n"
    "v_exp_f32 v97, v97\n"
    "v_smfmac_f32_32x32x32_bf16 v[128:143], v[60:63], v[120:127], v232\n"
    "buffer_load_dwordx4 v[180:183], v241, s[12:15], s73 offen offset:3072 sc1\n"
    "v_exp_f32 v98, v98\n"
    "v_exp_f32 v99, v99\n"
    "v_exp_f32 v100, v100\n"
    "v_mfma_f32_32x32x16_bf16 v[112:127], v[20:23], v[144:147], 0\n"
    "v_exp_f32 v101, v101\n"
    "v_exp_f32 v102, v102\n"
    "v_exp_f32 v103, v103\n"
    "v_exp_f32 v104, v104\n"
    "v_exp_f32 v105, v105\n"
    "v_smfmac_f32_32x32x32_bf16 v[128:143], v[64:67], v[96:103], v232\n"
    "v_exp_f32 v106, v106\n"
    "v_exp_f32 v107, v107\n"
    "v_exp_f32 v108, v108\n"
    "v_exp_f32 v109, v109\n"
    "v_exp_f32 v110, v110\n"
    "v_exp_f32 v111, v111\n"
    "s_waitcnt vmcnt(12)\n"
    "v_smfmac_f32_16x16x64_bf16 v[160:163], v[250:253], v[184:191], v233\n"
    "buffer_load_dwordx4 v[184:187], v241, s[12:15], s74 offen offset:0 sc1\n"
    "v_exp_f32 v112, v112\n"
    "v_exp_f32 v113, v113\n"
    "v_smfmac_f32_32x32x32_bf16 v[128:143], v[68:71], v[104:111], v232\n"
    "buffer_load_dwordx4 v[188:191], v241, s[12:15], s74 offen offset:1024 sc1\n"
    "v_exp_f32 v114, v114\n"
    "v_exp_f32 v115, v115\n"
    "v_exp_f32 v116, v116\n"
    "v_mfma_f32_32x32x16_bf16 v[96:111], v[24:27], v[144:147], 0\n"
    "buffer_load_dwordx4 v[236:239], v241, s[12:15], s85 offen offset:1024 sc1\n"
    "v_exp_f32 v117, v117\n"
    "v_exp_f32 v118, v118\n"
    "v_exp_f32 v119, v119\n"
    "v_exp_f32 v120, v120\n"
    "v_exp_f32 v121, v121\n"
    "v_smfmac_f32_32x32x32_bf16 v[128:143], v[72:75], v[112:119], v232\n"
    "v_exp_f32 v122, v122\n"
    "v_exp_f32 v123, v123\n"
    "v_exp_f32 v124, v124\n"
    "v_exp_f32 v125, v125\n"
    "v_exp_f32 v126, v126\n"
    "v_exp_f32 v127, v127\n"
    "s_waitcnt vmcnt(12)\n"
    "v_smfmac_f32_16x16x64_bf16 v[164:167], v[250:253], v[192:199], v233\n"
    "buffer_load_dwordx4 v[192:195], v241, s[12:15], s74 offen offset:2048 sc1\n"
    "v_exp_f32 v96, v96\n"
    "v_exp_f32 v97, v97\n"
    "v_smfmac_f32_32x32x32_bf16 v[128:143], v[76:79], v[120:127], v232\n"
    "buffer_load_dwordx4 v[196:199], v241, s[12:15], s74 offen offset:3072 sc1\n"
    "v_exp_f32 v98, v98\n"
    "v_exp_f32 v99, v99\n"
    "v_exp_f32 v100, v100\n"
    "v_mfma_f32_32x32x16_bf16 v[112:127], v[28:31], v[144:147], 0\n"
    "v_exp_f32 v101, v101\n"
    "v_exp_f32 v102, v102\n"
    "v_exp_f32 v103, v103\n"
    "v_exp_f32 v104, v104\n"
    "v_exp_f32 v105, v105\n"
    "v_smfmac_f32_32x32x32_bf16 v[128:143], v[80:83], v[96:103], v232\n"
    "s_waitcnt vmcnt(11)\n"
    "v_cmp_eq_u32 vcc, -1, v148\n"
    "v_exp_f32 v106, v106\n"
    "v_exp_f32 v107, v107\n"
    "v_exp_f32 v108, v108\n"
    "v_exp_f32 v109, v109\n"
    "v_exp_f32 v110, v110\n"
    ".Lpad6451:\n"
    "s_cbranch_execz .Lwr1_6\n"
    "v_exp_f32 v111, v111\n"
    "v_exp_f32 v112, v112\n"
    "v_exp_f32 v113, v113\n"
    "v_smfmac_f32_32x32x32_bf16 v[128:143], v[84:87], v[104:111], v232\n"
    "s_cbranch_vccnz .Lqr_105\n"
    ".Lqo_106:\n"
    "v_exp_f32 v114, v114\n"
    "v_exp_f32 v115, v115\n"
    "v_exp_f32 v116, v116\n"
    "v_mfma_f32_32x32x16_bf16 v[96:111], v[0:3], v[148:151], 0\n"
    "buffer_load_dwordx4 v[236:239], v241, s[12:15], s85 offen offset:2048 sc1\n"
    "v_exp_f32 v117, v117\n"
    "v_exp_f32 v118, v118\n"
    "v_exp_f32 v119, v119\n"
    "v_exp_f32 v120, v120\n"
    "v_exp_f32 v121, v121\n"
    "v_smfmac_f32_32x32x32_bf16 v[128:143], v[88:91], v[112:119], v232\n"
    "buffer_store_dwordx4 v[212:215], v241, s[12:15], s87 offen offset:1024 sc1\n"
    "v_exp_f32 v122, v122\n"
    "v_exp_f32 v123, v123\n"
    "v_exp_f32 v124, v124\n"
    "v_exp_f32 v125, v125\n"
    "v_exp_f32 v126, v126\n"
    "v_exp_f32 v127, v127\n"
    "v_exp_f32 v96, v96\n"
    "v_exp_f32 v97, v97\n"
    "v_smfmac_f32_32x32x32_bf16 v[128:143], v[92:95], v[120:127], v232\n"
    "v_exp_f32 v98, v98\n"
    "v_exp_f32 v99, v99\n"
    "v_exp_f32 v100, v100\n"
    "v_mfma_f32_32x32x16_bf16 v[112:127], v[4:7], v[148:151], 0\n"
    "v_exp_f32 v101, v101\n"
    "v_exp_f32 v102, v102\n"
    "v_exp_f32 v103, v103\n"
    "v_exp_f32 v104, v104\n"
    "v_exp_f32 v105, v105\n"
    "v_exp_f32 v106, v106\n"
    "v_exp_f32 v107, v107\n"
    "v_exp_f32 v108, v108\n"
    "ds_write_b128 v244, v[128:131] offset:0\n"
    "ds_write_b128 v244, v[132:135] offset:1024\n"
    "ds_write_b32 v245, v136 offset:0\n"
    "v_exp_f32 v109, v109\n"
    "v_exp_f32 v110, v110\n"
    "v_exp_f32 v111, v111\n"
    "s_waitcnt lgkmcnt(0)\n"
    "s_barrier\n"
    "ds_read_b128 v[128:131], v242 offset:9216\n"
    "ds_read_b128 v[132:135], v242 offset:10240\n"
    "ds_read_b32 v136, v243 offset:9216\n"
    "ds_read_b128 v[250:253], v247 offset:1024\n"
    "s_xor_b32 s72, s71, 6144\n"
    "buffer_load_dwordx4 v[144:147], v241, s[12:15], s72 offen sc1\n"
    "s_add_u32 s67, s67, 4096\n"
    "s_add_u32 m0, m0, 4096\n"
    "s_cmp_lt_u32 s69, s57\n"
    "s_cbranch_scc0 .Ls5_95\n"
    "buffer_load_dwordx4 v241, s[12:15], s67 offen offset:0 sc1 lds\n"
    ".Ls5_95:\n"
    "s_cmp_lt_u32 s69, s57\n"
    "s_cbranch_scc0 .Ls5_96\n"
    "buffer_load_dwordx4 v241, s[12:15], s67 offen offset:1024 sc1 lds\n"
    ".Ls5_96:\n"
    "s_cmp_lt_u32 s69, s57\n"
    "s_cbranch_scc0 .Ls5_97\n"
    "buffer_load_dwordx4 v241, s[12:15], s67 offen offset:2048 sc1 lds\n"
    ".Ls5_97:\n"
    "s_cmp_lt_u32 s69, s57\n"
    "s_cbranch_scc0 .Ls5_98\n"
    "buffer_load_dwordx4 v241, s[12:15], s67 offen offset:3072 sc1 lds\n"
    ".Ls5_98:\n"
    "s_waitcnt lgkmcnt(1)\n"
    "v_smfmac_f32_32x32x32_bf16 v[128:143], v[32:35], v[96:103], v232\n"
    "v_exp_f32 v112, v112\n"
    "v_exp_f32 v113, v113\n"
    "v_smfmac_f32_32x32x32_bf16 v[128:143], v[36:39], v[104:111], v232\n"
    "v_exp_f32 v114, v114\n"
    "v_exp_f32 v115, v115\n"
    "v_exp_f32 v116, v116\n"
    "v_mfma_f32_32x32x16_bf16 v[96:111], v[8:11], v[148:151], 0\n"
    "buffer_load_dwordx4 v[236:239], v241, s[12:15], s85 offen offset:3072 sc1\n"
    "v_exp_f32 v117, v117\n"
    "v_exp_f32 v118, v118\n"
    "v_exp_f32 v119, v119\n"
    "v_exp_f32 v120, v120\n"
    "v_exp_f32 v121, v121\n"
    "v_smfmac_f32_32x32x32_bf16 v[128:143], v[40:43], v[112:119], v232\n"
    "v_exp_f32 v122, v122\n"
    "v_exp_f32 v123, v123\n"
    "v_exp_f32 v124, v124\n"
    "v_exp_f32 v125, v125\n"
    "v_exp_f32 v126, v126\n"
    "v_exp_f32 v127, v127\n"
    "v_exp_f32 v96, v96\n"
    "v_exp_f32 v97, v97\n"
    "v_smfmac_f32_32x32x32_bf16 v[128:143], v[44:47], v[120:127], v232\n"
    "v_exp_f32 v98, v98\n"
    "v_exp_f32 v99, v99\n"
    "v_exp_f32 v100, v100\n"
    "v_mfma_f32_32x32x16_bf16 v[112:127], v[12:15], v[148:151], 0\n"
    "v_exp_f32 v101, v101\n"
    "v_exp_f32 v102, v102\n"
    "v_exp_f32 v103, v103\n"
    "v_exp_f32 v104, v104\n"
    "v_exp_f32 v105, v105\n"
    "v_smfmac_f32_32x32x32_bf16 v[128:143], v[48:51], v[96:103], v232\n"
    "s_xor_b32 s73, s75, 262144\n"
    "s_xor_b32 s74, s75, 266240\n"
    "v_exp_f32 v106, v106\n"
    "v_exp_f32 v107, v107\n"
    "v_exp_f32 v108, v108\n"
    "v_exp_f32 v109, v109\n"
    "v_exp_f32 v110, v110\n"
    "v_exp_f32 v111, v111\n"
    "s_waitcnt vmcnt(12) lgkmcnt(0)\n"
    "v_smfmac_f32_16x16x64_bf16 v[152:155], v[250:253], v[168:175], v233\n"
    "buffer_load_dwordx4 v[168:171], v241, s[12:15], s73 offen offset:0 sc1\n"
    "v_exp_f32 v112, v112\n"
    "v_exp_f32 v113, v113\n"
    "v_smfmac_f32_32x32x32_bf16 v[128:143], v[52:55], v[104:111], v232\n"
    "buffer_load_dwordx4 v[172:175], v241, s[12:15], s73 offen offset:1024 sc1\n"
    "v_exp_f32 v114, v114\n"
    "v_exp_f32 v115, v115\n"
    "v_exp_f32 v116, v116\n"
    "v_mfma_f32_32x32x16_bf16 v[96:111], v[16:19], v[148:151], 0\n"
    "s_add_u32 s85, s85, 4096\n"
    "buffer_load_dwordx4 v[236:239], v241, s[12:15], s85 offen offset:0 sc1\n"
    "v_exp_f32 v117, v117\n"
    "v_exp_f32 v118, v118\n"
    "v_exp_f32 v119, v119\n"
    "v_exp_f32 v120, v120\n"
    "v_exp_f32 v121, v121\n"
    "v_smfmac_f32_32x32x32_bf16 v[128:143], v[56:59], v[112:119], v232\n"
    "v_exp_f32 v122, v122\n"
    "v_exp_f32 v123, v123\n"
    "v_exp_f32 v124, v124\n"
    "v_exp_f32 v125, v125\n"
    "v_exp_f32 v126, v126\n"
    "v_exp_f32 v127, v127\n"
    "s_waitcnt vmcnt(12)\n"
    "v_smfmac_f32_16x16x64_bf16 v[156:159], v[250:253], v[176:183], v233\n"
    "buffer_load_dwordx4 v[176:179], v241, s[12:15], s73 offen offset:2048 sc1\n"
    "v_exp_f32 v96, v96\n"
    "v_exp_f32 v97, v97\n"
    "v_smfmac_f32_32x32x32_bf16 v[128:143], v[60:63], v[120:127], v232\n"
    "buffer_load_dwordx4 v[180:183], v241, s[12:15], s73 offen offset:3072 sc1\n"
    "v_exp_f32 v98, v98\n"
    "v_exp_f32 v99, v99\n"
    "v_exp_f32 v100, v100\n"
    "v_mfma_f32_32x32x16_bf16 v[112:127], v[20:23], v[148:151], 0\n"
    "v_exp_f32 v101, v101\n"
    "v_exp_f32 v102, v102\n"
    "v_exp_f32 v103, v103\n"
    "v_exp_f32 v104, v104\n"
    "v_exp_f32 v105, v105\n"
    "v_smfmac_f32_32x32x32_bf16 v[128:143], v[64:67], v[96:103], v232\n"
    "v_exp_f32 v106, v106\n"
    "v_exp_f32 v107, v107\n"
    "v_exp_f32 v108, v108\n"
    "v_exp_f32 v109, v109\n"
    "v_exp_f32 v110, v110\n"
    "v_exp_f32 v111, v111\n"
    "s_waitcnt vmcnt(12)\n"
    "v_smfmac_f32_16x16x64_bf16 v[160:163], v[250:253], v[184:191], v233\n"
    "buffer_load_dwordx4 v[184:187], v241, s[12:15], s74 offen offset:0 sc1\n"
    "v_exp_f32 v112, v112\n"
    "v_exp_f32 v113, v113\n"
    "v_smfmac_f32_32x32x32_bf16 v[128:143], v[68:71], v[104:111], v232\n"
    "buffer_load_dwordx4 v[188:191], v241, s[12:15], s74 offen offset:1024 sc1\n"
    "v_exp_f32 v114, v114\n"
    "v_exp_f32 v115, v115\n"
    "v_exp_f32 v116, v116\n"
    "v_mfma_f32_32x32x16_bf16 v[96:111], v[24:27], v[148:151], 0\n"
    "buffer_load_dwordx4 v[236:239], v241, s[12:15], s85 offen offset:1024 sc1\n"
    "v_exp_f32 v117, v117\n"
    "v_exp_f32 v118, v118\n"
    "v_exp_f32 v119, v119\n"
    "v_exp_f32 v120, v120\n"
    "v_exp_f32 v121, v121\n"
    "v_smfmac_f32_32x32x32_bf16 v[128:143], v[72:75], v[112:119], v232\n"
    "v_exp_f32 v122, v122\n"
    "v_exp_f32 v123, v123\n"
    "v_exp_f32 v124, v124\n"
    "v_exp_f32 v125, v125\n"
    "v_exp_f32 v126, v126\n"
    "v_exp_f32 v127, v127\n"
    "s_waitcnt vmcnt(12)\n"
    "v_smfmac_f32_16x16x64_bf16 v[164:167], v[250:253], v[192:199], v233\n"
    "buffer_load_dwordx4 v[192:195], v241, s[12:15], s74 offen offset:2048 sc1\n"
    "v_exp_f32 v96, v96\n"
    "v_exp_f32 v97, v97\n"
    "v_smfmac_f32_32x32x32_bf16 v[128:143], v[76:79], v[120:127], v232\n"
    "buffer_load_dwordx4 v[196:199], v241, s[12:15], s74 offen offset:3072 sc1\n"
    "v_exp_f32 v98, v98\n"
    "v_exp_f32 v99, v99\n"
    "v_exp_f32 v100, v100\n"
    "v_mfma_f32_32x32x16_bf16 v[112:127], v[28:31], v[148:151], 0\n"
    "v_exp_f32 v101, v101\n"
    "v_exp_f32 v102, v102\n"
    "v_exp_f32 v103, v103\n"
    "v_exp_f32 v104, v104\n"
    "v_exp_f32 v105, v105\n"
    "v_smfmac_f32_32x32x32_bf16 v[128:143], v[80:83], v[96:103], v232\n"
    "s_waitcnt vmcnt(11)\n"
    "v_cmp_eq_u32 vcc, -1, v144\n"
    "v_exp_f32 v106, v106\n"
    "v_exp_f32 v107, v107\n"
    "v_exp_f32 v108, v108\n"
    "v_exp_f32 v109, v109\n"
    "v_exp_f32 v110, v110\n"
    "v_exp_f32 v111, v111\n"
    "v_exp_f32 v112, v112\n"
    "v_exp_f32 v113, v113\n"
    "v_smfmac_f32_32x32x32_bf16 v[128:143], v[84:87], v[104:111], v232\n"
    "s_cbranch_vccnz .Lqr_107\n"
    ".Lqo_108:\n"
    "v_exp_f32 v114, v114\n"
    "v_exp_f32 v115, v115\n"
    "v_exp_f32 v116, v116\n"
    "v_mfma_f32_32x32x16_bf16 v[96:111], v[0:3], v[144:147], 0\n"
    "buffer_load_dwordx4 v[236:239], v241, s[12:15], s85 offen offset:2048 sc1\n"
    "v_exp_f32 v117, v117\n"
    "v_exp_f32 v118, v118\n"
    "v_exp_f32 v119, v119\n"
    "v_exp_f32 v120, v120\n"
    "v_exp_f32 v121, v121\n"
    "v_smfmac_f32_32x32x32_bf16 v[128:143], v[88:91], v[112:119], v232\n"
    "buffer_store_dwordx4 v[212:215], v241, s[12:15], s87 offen offset:2048 sc1\n"
    "v_exp_f32 v122, v122\n"
    "v_exp_f32 v123, v123\n"
    "v_exp_f32 v124, v124\n"
    "v_exp_f32 v125, v125\n"
    "v_exp_f32 v126, v126\n"
    "v_exp_f32 v127, v127\n"
    "v_exp_f32 v96, v96\n"
    "v_exp_f32 v97, v97\n"
    "v_smfmac_f32_32x32x32_bf16 v[128:143], v[92:95], v[120:127], v232\n"
    "v_exp_f32 v98, v98\n"
    "v_exp_f32 v99, v99\n"
    "v_exp_f32 v100, v100\n"
    "v_mfma_f32_32x32x16_bf16 v[112:127], v[4:7], v[144:147], 0\n"
    "v_exp_f32 v101, v101\n"
    "v_exp_f32 v102, v102\n"
    "v_exp_f32 v103, v103\n"
    "v_exp_f32 v104, v104\n"
    "v_exp_f32 v105, v105\n"
    "v_exp_f32 v106, v106\n"
    "v_exp_f32 v107, v107\n"
    "v_exp_f32 v108, v108\n"
    "ds_write_b128 v244, v[128:131] offset:8192\n"
    "ds_write_b128 v244, v[132:135] offset:9216\n"
    "ds_write_b32 v245, v136 offset:512\n"
    "v_exp_f32 v109, v109\n"
    "v_exp_f32 v110, v110\n"
    "v_exp_f32 v111, v111\n"
    "s_waitcnt lgkmcnt(0)\n"
    "s_barrier\n"
    "ds_read_b128 v[128:131], v242 offset:0\n"
    "ds_read_b128 v[132:135], v242 offset:1024\n"
    "ds_read_b32 v136, v243 offset:0\n"
    "ds_read_b128 v[250:253], v247 offset:0\n"
    "s_xor_b32 s72, s71, 7168\n"
    "buffer_load_dwordx4 v[148:151], v241, s[12:15], s72 offen sc1\n"
    "s_xor_b32 s62, s75, 458752\n"
    "buffer_load_dwordx4 v[200:203], v241, s[12:15], s62 offen offset:0 sc1\n"
    "buffer_load_dwordx4 v[204:207], v241, s[12:15], s62 offen offset:1024 sc1\n"
    "buffer_load_dwordx4 v[208:211], v241, s[12:15], s62 offen offset:2048 sc1\n"
    "buffer_load_dwordx4 v[212:215], v241, s[12:15], s62 offen offset:3072 sc1\n"
    "s_waitcnt lgkmcnt(1)\n"
    "v_smfmac_f32_32x32x32_bf16 v[128:143], v[32:35], v[96:103], v232\n"
    "v_exp_f32 v112, v112\n"
    "v_exp_f32 v113, v113\n"
    "v_smfmac_f32_32x32x32_bf16 v[128:143], v[36:39], v[104:111], v232\n"
    "v_exp_f32 v114, v114\n"
    "v_exp_f32 v115, v115\n"
    "v_exp_f32 v116, v116\n"
    "v_mfma_f32_32x32x16_bf16 v[96:111], v[8:11], v[144:147], 0\n"
    "v_exp_f32 v117, v117\n"
    "v_exp_f32 v118, v118\n"
    "v_exp_f32 v119, v119\n"
    "v_exp_f32 v120, v120\n"
    "v_exp_f32 v121, v121\n"
    "v_smfmac_f32_32x32x32_bf16 v[128:143], v[40:43], v[112:119], v232\n"
    "v_exp_f32 v122, v122\n"
    "v_exp_f32 v123, v123\n"
    "v_exp_f32 v124, v124\n"
    "v_exp_f32 v125, v125\n"
    "v_exp_f32 v126, v126\n"
    "v_exp_f32 v127, v127\n"
    "v_exp_f32 v96, v96\n"
    "v_exp_f32 v97, v97\n"
    "v_smfmac_f32_32x32x32_bf16 v[128:143], v[44:47], v[120:127], v232\n"
    "v_exp_f32 v98, v98\n"
    "v_exp_f32 v99, v99\n"
    "v_exp_f32 v100, v100\n"
    "v_mfma_f32_32x32x16_bf16 v[112:127], v[12:15], v[144:147], 0\n"
    "v_exp_f32 v101, v101\n"
    "v_exp_f32 v102, v102\n"
    "v_exp_f32 v103, v103\n"
    "v_exp_f32 v104, v104\n"
    "v_exp_f32 v105, v105\n"
    "v_smfmac_f32_32x32x32_bf16 v[128:143], v[48:51], v[96:103], v232\n"
    "s_xor_b32 s73, s75, 327680\n"
    "s_xor_b32 s74, s75, 331776\n"
    "v_exp_f32 v106, v106\n"
    "v_exp_f32 v107, v107\n"
    "v_exp_f32 v108, v108\n"
    "v_exp_f32 v109, v109\n"
    "v_exp_f32 v110, v110\n"
    "v_exp_f32 v111, v111\n"
    "s_waitcnt vmcnt(15) lgkmcnt(0)\n"
    "v_smfmac_f32_16x16x64_bf16 v[152:155], v[250:253], v[168:175], v233\n"
    "buffer_load_dwordx4 v[168:171], v241, s[12:15], s73 offen offset:0 sc1\n"
    "v_exp_f32 v112, v112\n"
    "v_exp_f32 v113, v113\n"
    "v_smfmac_f32_32x32x32_bf16 v[128:143], v[52:55], v[104:111], v232\n"
    "buffer_load_dwordx4 v[172:175], v241, s[12:15], s73 offen offset:1024 sc1\n"
    "v_exp_f32 v114, v114\n"
    "v_exp_f32 v115, v115\n"
    "v_exp_f32 v116, v116\n"
    "v_mfma_f32_32x32x16_bf16 v[96:111], v[16:19], v[144:147], 0\n"
    "v_exp_f32 v117, v117\n"
    "v_exp_f32 v118, v118\n"
    "v_exp_f32 v119, v119\n"
    "v_exp_f32 v120, v120\n"
    "v_exp_f32 v121, v121\n"
    "v_smfmac_f32_32x32x32_bf16 v[128:143], v[56:59], v[112:119], v232\n"
    "v_exp_f32 v122, v122\n"
    "v_exp_f32 v123, v123\n"
    "v_exp_f32 v124, v124\n"
    "v_exp_f32 v125, v125\n"
    "v_exp_f32 v126, v126\n"
    "v_exp_f32 v127, v127\n"
    "s_waitcnt vmcnt(14)\n"
    "v_smfmac_f32_16x16x64_bf16 v[156:159], v[250:253], v[176:183], v233\n"
    "buffer_load_dwordx4 v[176:179], v241, s[12:15], s73 offen offset:2048 sc1\n"
    "v_exp_f32 v96, v96\n"
    "v_exp_f32 v97, v97\n"
    "v_smfmac_f32_32x32x32_bf16 v[128:143], v[60:63], v[120:127], v232\n"
    "buffer_load_dwordx4 v[180:183], v241, s[12:15], s73 offen offset:3072 sc1\n"
    "v_exp_f32 v98, v98\n"
    "v_exp_f32 v99, v99\n"
    "v_exp_f32 v100, v100\n"
    "v_mfma_f32_32x32x16_bf16 v[112:127], v[20:23], v[144:147], 0\n"
    "v_exp_f32 v101, v101\n"
    "v_exp_f32 v102, v102\n"
    "v_exp_f32 v103, v103\n"
    "v_exp_f32 v104, v104\n"
    "v_exp_f32 v105, v105\n"
    "v_smfmac_f32_32x32x32_bf16 v[128:143], v[64:67], v[96:103], v232\n"
    "v_exp_f32 v106, v106\n"
    "v_exp_f32 v107, v107\n"
    "v_exp_f32 v108, v108\n"
    "v_exp_f32 v109, v109\n"
    "v_exp_f32 v110, v110\n"
    "v_exp_f32 v111, v111\n"
    "s_waitcnt vmcnt(14)\n"
    "v_smfmac_f32_16x16x64_bf16 v[160:163], v[250:253], v[184:191], v233\n"
    "buffer_load_dwordx4 v[184:187], v241, s[12:15], s74 offen offset:0 sc1\n"
    "v_exp_f32 v112, v112\n"
    "v_exp_f32 v113, v113\n"
    "v_smfmac_f32_32x32x32_bf16 v[128:143], v[68:71], v[104:111], v232\n"
    "buffer_load_dwordx4 v[188:191], v241, s[12:15], s74 offen offset:1024 sc1\n"
    "v_exp_f32 v114, v114\n"
    "v_exp_f32 v115, v115\n"
    "v_exp_f32 v116, v116\n"
    "v_mfma_f32_32x32x16_bf16 v[96:111], v[24:27], v[144:147], 0\n"
    "v_exp_f32 v117, v117\n"
    "v_exp_f32 v118, v118\n"
    "v_exp_f32 v119, v119\n"
    "v_exp_f32 v120, v120\n"
    "v_exp_f32 v121, v121\n"
    "v_smfmac_f32_32x32x32_bf16 v[128:143], v[72:75], v[112:119], v232\n"
    "v_exp_f32 v122, v122\n"
    "v_exp_f32 v123, v123\n"
    "v_exp_f32 v124, v124\n"
    "v_exp_f32 v125, v125\n"
    "v_exp_f32 v126, v126\n"
    "v_exp_f32 v127, v127\n"
    "s_waitcnt vmcnt(13)\n"
    "v_smfmac_f32_16x16x64_bf16 v[164:167], v[250:253], v[192:199], v233\n"
    "buffer_load_dwordx4 v[192:195], v241, s[12:15], s74 offen offset:2048 sc1\n"
    "v_exp_f32 v96, v96\n"
    "v_exp_f32 v97, v97\n"
    "v_smfmac_f32_32x32x32_bf16 v[128:143], v[76:79], v[120:127], v232\n"
    "buffer_load_dwordx4 v[196:199], v241, s[12:15], s74 offen offset:3072 sc1\n"
    "v_exp_f32 v98, v98\n"
    "v_exp_f32 v99, v99\n"
    "v_exp_f32 v100, v100\n"
    "v_mfma_f32_32x32x16_bf16 v[112:127], v[28:31], v[144:147], 0\n"
    "v_exp_f32 v101, v101\n"
    "v_exp_f32 v102, v102\n"
    "v_exp_f32 v103, v103\n"
    "v_exp_f32 v104, v104\n"
    "v_exp_f32 v105, v105\n"
    "v_smfmac_f32_32x32x32_bf16 v[128:143], v[80:83], v[96:103], v232\n"
    "s_waitcnt vmcnt(12)\n"
    "v_cmp_eq_u32 vcc, -1, v148\n"
    "v_exp_f32 v106, v106\n"
    "v_exp_f32 v107, v107\n"
    "v_exp_f32 v108, v108\n"
    "v_exp_f32 v109, v109\n"
    "v_exp_f32 v110, v110\n"
    "v_exp_f32 v111, v111\n"
    "v_exp_f32 v112, v112\n"
    "v_exp_f32 v113, v113\n"
    "v_smfmac_f32_32x32x32_bf16 v[128:143], v[84:87], v[104:111], v232\n"
    "s_cbranch_vccnz .Lqr_109\n"
    ".Lqo_110:\n"
    "v_exp_f32 v114, v114\n"
    "v_exp_f32 v115, v115\n"
    "v_exp_f32 v116, v116\n"
    "v_mfma_f32_32x32x16_bf16 v[96:111], v[0:3], v[148:151], 0\n"
    "v_exp_f32 v117, v117\n"
    "v_exp_f32 v118, v118\n"
    "v_exp_f32 v119, v119\n"
    "v_exp_f32 v120, v120\n"
    "v_exp_f32 v121, v121\n"
    "v_smfmac_f32_32x32x32_bf16 v[128:143], v[88:91], v[112:119], v232\n"
    "v_exp_f32 v122, v122\n"
    "v_exp_f32 v123, v123\n"
    "v_exp_f32 v124, v124\n"
    "v_exp_f32 v125, v125\n"
    "v_exp_f32 v126, v126\n"
    "v_exp_f32 v127, v127\n"
    "v_exp_f32 v96, v96\n"
    "v_exp_f32 v97, v97\n"
    "v_smfmac_f32_32x32x32_bf16 v[128:143], v[92:95], v[120:127], v232\n"
    "v_exp_f32 v98, v98\n"
    "v_exp_f32 v99, v99\n"
    "v_exp_f32 v100, v100\n"
    "v_mfma_f32_32x32x16_bf16 v[112:127], v[4:7], v[148:151], 0\n"
    "v_exp_f32 v101, v101\n"
    "v_exp_f32 v102, v102\n"
    "v_exp_f32 v103, v103\n"
    "v_exp_f32 v104, v104\n"
    "v_exp_f32 v105, v105\n"
    "v_exp_f32 v106, v106\n"
    "v_exp_f32 v107, v107\n"
    "v_exp_f32 v108, v108\n"
    "ds_write_b128 v244, v[128:131] offset:0\n"
    "ds_write_b128 v244, v[132:135] offset:1024\n"
    "ds_write_b32 v245, v136 offset:0\n"
    "v_mfma_f32_32x32x8_bf16 v[128:143], v[234:235], v[234:235], 0\n"
    "v_exp_f32 v109, v109\n"
    "v_exp_f32 v110, v110\n"
    "v_exp_f32 v111, v111\n"
    "v_smfmac_f32_32x32x32_bf16 v[128:143], v[32:35], v[96:103], v232\n"
    "v_exp_f32 v112, v112\n"
    "v_exp_f32 v113, v113\n"
    "v_smfmac_f32_32x32x32_bf16 v[128:143], v[36:39], v[104:111], v232\n"
    "v_exp_f32 v114, v114\n"
    "v_exp_f32 v115, v115\n"
    "v_exp_f32 v116, v116\n"
    "v_mfma_f32_32x32x16_bf16 v[96:111], v[8:11], v[148:151], 0\n"
    "v_exp_f32 v117, v117\n"
    "v_exp_f32 v118, v118\n"
    "v_exp_f32 v119, v119\n"
    "v_exp_f32 v120, v120\n"
    "v_exp_f32 v121, v121\n"
    "v_smfmac_f32_32x32x32_bf16 v[128:143], v[40:43], v[112:119], v232\n"
    "s_waitcnt lgkmcnt(0)\n"
    "s_barrier\n"
    "ds_read_b128 v[250:253], v247 offset:1024\n"
    "v_exp_f32 v122, v122\n"
    "v_exp_f32 v123, v123\n"
    "v_exp_f32 v124, v124\n"
    "v_exp_f32 v125, v125\n"
    "v_exp_f32 v126, v126\n"
    "v_exp_f32 v127, v127\n"
    "v_exp_f32 v96, v96\n"
    "v_exp_f32 v97, v97\n"
    "v_smfmac_f32_32x32x32_bf16 v[128:143], v[44:47], v[120:127], v232\n"
    "v_exp_f32 v98, v98\n"
    "v_exp_f32 v99, v99\n"
    "v_exp_f32 v100, v100\n"
    "v_mfma_f32_32x32x16_bf16 v[112:127], v[12:15], v[148:151], 0\n"
    "v_exp_f32 v101, v101\n"
    "v_exp_f32 v102, v102\n"
    "v_exp_f32 v103, v103\n"
    "v_exp_f32 v104, v104\n"
    "v_exp_f32 v105, v105\n"
    "v_smfmac_f32_32x32x32_bf16 v[128:143], v[48:51], v[96:103], v232\n"
    "s_xor_b32 s73, s75, 393216\n"
    "s_xor_b32 s74, s75, 397312\n"
    "v_exp_f32 v106, v106\n"
    "v_exp_f32 v107, v107\n"
    "v_exp_f32 v108, v108\n"
    "v_exp_f32 v109, v109\n"
    "v_exp_f32 v110, v110\n"
    "v_exp_f32 v111, v111\n"
    "s_waitcnt vmcnt(6) lgkmcnt(0)\n"
    "v_smfmac_f32_16x16x64_bf16 v[152:155], v[250:253], v[168:175], v233\n"
    "buffer_load_dwordx4 v[168:171], v241, s[12:15], s73 offen offset:0 sc1\n"
    "v_exp_f32 v112, v112\n"
    "v_exp_f32 v113, v113\n"
    "v_smfmac_f32_32x32x32_bf16 v[128:143], v[52:55], v[104:111], v232\n"
    "buffer_load_dwordx4 v[172:175], v241, s[12:15], s73 offen offset:1024 sc1\n"
    "v_exp_f32 v114, v114\n"
    "v_exp_f32 v115, v115\n"
    "v_exp_f32 v116, v116\n"
    "v_mfma_f32_32x32x16_bf16 v[96:111], v[16:19], v[148:151], 0\n"
    "v_exp_f32 v117, v117\n"
    "v_exp_f32 v118, v118\n"
    "v_exp_f32 v119, v119\n"
    "v_exp_f32 v120, v120\n"
    "v_exp_f32 v121, v121\n"
    "v_smfmac_f32_32x32x32_bf16 v[128:143], v[56:59], v[112:119], v232\n"
    "v_exp_f32 v122, v122\n"
    "v_exp_f32 v123, v123\n"
    "v_exp_f32 v124, v124\n"
    "v_exp_f32 v125, v125\n"
    "v_exp_f32 v126, v126\n"
    "v_exp_f32 v127, v127\n"
    "s_waitcnt vmcnt(6)\n"
    "v_smfmac_f32_16x16x64_bf16 v[156:159], v[250:253], v[176:183], v233\n"
    "buffer_load_dwordx4 v[176:179], v241, s[12:15], s73 offen offset:2048 sc1\n"
    "v_exp_f32 v96, v96\n"
    "v_exp_f32 v97, v97\n"
    "v_smfmac_f32_32x32x32_bf16 v[128:143], v[60:63], v[120:127], v232\n"
    "buffer_load_dwordx4 v[180:183], v241, s[12:15], s73 offen offset:3072 sc1\n"
    "v_exp_f32 v98, v98\n"
    "v_exp_f32 v99, v99\n"
    "v_exp_f32 v100, v100\n"
    "v_mfma_f32_32x32x16_bf16 v[112:127], v[20:23], v[148:151], 0\n"
    "v_exp_f32 v101, v101\n"
    "v_exp_f32 v102, v102\n"
    "v_exp_f32 v103, v103\n"
    "v_exp_f32 v104, v104\n"
    "v_exp_f32 v105, v105\n"
    "v_smfmac_f32_32x32x32_bf16 v[128:143], v[64:67], v[96:103], v232\n"
    "v_exp_f32 v106, v106\n"
    "v_exp_f32 v107, v107\n"
    "v_exp_f32 v108, v108\n"
    "v_exp_f32 v109, v109\n"
    "v_exp_f32 v110, v110\n"
    "v_exp_f32 v111, v111\n"
    "s_waitcnt vmcnt(6)\n"
    "v_smfmac_f32_16x16x64_bf16 v[160:163], v[250:253], v[184:191], v233\n"
    "buffer_load_dwordx4 v[184:187], v241, s[12:15], s74 offen offset:0 sc1\n"
    "v_exp_f32 v112, v112\n"
    "v_exp_f32 v113, v113\n"
    "v_smfmac_f32_32x32x32_bf16 v[128:143], v[68:71], v[104:111], v232\n"
    "buffer_load_dwordx4 v[188:191], v241, s[12:15], s74 offen offset:1024 sc1\n"
    "v_exp_f32 v114, v114\n"
    "v_exp_f32 v115, v115\n"
    "v_exp_f32 v116, v116\n"
    "v_mfma_f32_32x32x16_bf16 v[96:111], v[24:27], v[148:151], 0\n"
    "v_exp_f32 v117, v117\n"
    "v_exp_f32 v118, v118\n"
    "v_exp_f32 v119, v119\n"
    "v_exp_f32 v120, v120\n"
    "v_exp_f32 v121, v121\n"
    "v_smfmac_f32_32x32x32_bf16 v[128:143], v[72:75], v[112:119], v232\n"
    "v_exp_f32 v122, v122\n"
    "v_exp_f32 v123, v123\n"
    "v_exp_f32 v124, v124\n"
    "v_exp_f32 v125, v125\n"
    "v_exp_f32 v126, v126\n"
    "v_exp_f32 v127, v127\n"
    "s_waitcnt vmcnt(6)\n"
    "v_smfmac_f32_16x16x64_bf16 v[164:167], v[250:253], v[192:199], v233\n"
    "buffer_load_dwordx4 v[192:195], v241, s[12:15], s74 offen offset:2048 sc1\n"
    "v_exp_f32 v96, v96\n"
    "v_exp_f32 v97, v97\n"
    "v_smfmac_f32_32x32x32_bf16 v[128:143], v[76:79], v[120:127], v232\n"
    "buffer_load_dwordx4 v[196:199], v241, s[12:15], s74 offen offset:3072 sc1\n"
    "v_exp_f32 v98, v98\n"
    "v_exp_f32 v99, v99\n"
    "v_exp_f32 v100, v100\n"
    "v_mfma_f32_32x32x16_bf16 v[112:127], v[28:31], v[148:151], 0\n"
    "v_exp_f32 v101, v101\n"
    "v_exp_f32 v102, v102\n"
    "v_exp_f32 v103, v103\n"
    "v_exp_f32 v104, v104\n"
    "v_exp_f32 v105, v105\n"
    "v_smfmac_f32_32x32x32_bf16 v[128:143], v[80:83], v[96:103], v232\n"
    "s_lshl3_add_u32 s62, s55, s48\n"
    "s_lshl_b32 s62, s62, 10\n"
    "buffer_load_dwordx4 v[0:3], v241, s[16:19], s62 offen\n"
    "v_exp_f32 v106, v106\n"
    "v_exp_f32 v107, v107\n"
    "v_exp_f32 v108, v108\n"
    "v_exp_f32 v109, v109\n"
    "v_exp_f32 v110, v110\n"
    "v_exp_f32 v111, v111\n"
    "v_exp_f32 v112, v112\n"
    "v_exp_f32 v113, v113\n"
    "v_smfmac_f32_32x32x32_bf16 v[128:143], v[84:87], v[104:111], v232\n"
    "v_exp_f32 v114, v114\n"
    "v_exp_f32 v115, v115\n"
    "v_exp_f32 v116, v116\n"
    "v_exp_f32 v117, v117\n"
    "v_exp_f32 v118, v118\n"
    "v_exp_f32 v119, v119\n"
    "v_exp_f32 v120, v120\n"
    "v_exp_f32 v121, v121\n"
    "v_smfmac_f32_32x32x32_bf16 v[128:143], v[88:91], v[112:119], v232\n"
    "v_exp_f32 v122, v122\n"
    "v_exp_f32 v123, v123\n"
    "v_exp_f32 v124, v124\n"
    "v_exp_f32 v125, v125\n"
    "v_exp_f32 v126, v126\n"
    "v_exp_f32 v127, v127\n"
    "s_nop 1\n"
    "v_smfmac_f32_32x32x32_bf16 v[128:143], v[92:95], v[120:127], v232\n"
    "s_nop 11\n"
    "ds_write_b128 v244, v[128:131] offset:8192\n"
    "ds_write_b128 v244, v[132:135] offset:9216\n"
    "ds_write_b32 v245, v136 offset:512\n"
    "s_waitcnt lgkmcnt(0)\n"
    "s_barrier\n"
    "s_setprio 1\n"
    "ds_read_b128 v[250:253], v247 offset:0\n"
    "s_and_b32 s61, s50, 4\n"
    "s_xor_b32 s61, s61, s48\n"
    "s_min_u32 s66, s61, 3\n"
    "s_mul_i32 s67, s61, 5\n"
    "s_add_u32 s66, s66, s67\n"
    "s_mul_i32 s67, s50, 43\n"
    "s_add_u32 s67, s67, s66\n"
    "s_lshl_b32 s67, s67, 12\n"
    "s_add_u32 s85, s67, 4718592\n"
    "v_bfe_u32 v242, v240, 4, 1\n"
    "v_lshrrev_b32 v243, 5, v240\n"
    "v_lshl_or_b32 v242, v242, 1, v243\n"
    "v_lshlrev_b32 v242, 12, v242\n"
    "v_and_b32 v243, 15, v240\n"
    "v_lshl_or_b32 v242, v243, 4, v242\n"
    "s_lshl_b32 s62, s48, 8\n"
    "s_add_u32 s62, s62, 131072\n"
    "v_add_u32 v242, s62, v242\n"
    "s_lshl_b32 s62, s48, 11\n"
    "s_add_u32 s62, s62, 131072\n"
    "v_lshl_add_u32 v243, v240, 5, s62\n"
    "v_mov_b32 v249, 161792\n"
    "s_waitcnt vmcnt(0)\n"
    "v_lshlrev_b32 v144, 16, v0\n"
    "v_and_b32 v145, 0xffff0000, v0\n"
    "v_lshlrev_b32 v146, 16, v1\n"
    "v_and_b32 v147, 0xffff0000, v1\n"
    "v_lshlrev_b32 v148, 16, v2\n"
    "v_and_b32 v149, 0xffff0000, v2\n"
    "v_lshlrev_b32 v150, 16, v3\n"
    "v_and_b32 v151, 0xffff0000, v3\n"
    "s_waitcnt lgkmcnt(0)\n"
    "v_smfmac_f32_16x16x64_bf16 v[152:155], v[250:253], v[168:175], v233\n"
    "v_smfmac_f32_16x16x64_bf16 v[156:159], v[250:253], v[176:183], v233\n"
    "v_smfmac_f32_16x16x64_bf16 v[160:163], v[250:253], v[184:191], v233\n"
    "v_smfmac_f32_16x16x64_bf16 v[164:167], v[250:253], v[192:199], v233\n"
    "ds_read_b128 v[168:171], v247 offset:2048\n"
    "ds_read_b128 v[172:175], v247 offset:3072\n"
    "ds_read_b128 v[176:179], v247 offset:4096\n"
    "ds_read_b128 v[180:183], v247 offset:5120\n"
    "ds_read_b128 v[184:187], v247 offset:6144\n"
    "ds_read_b128 v[188:191], v247 offset:7168\n"
    "buffer_load_dwordx4 v[16:19], v241, s[12:15], s85 offen offset:0 sc1\n"
    "buffer_load_dwordx4 v[20:23], v241, s[12:15], s85 offen offset:1024 sc1\n"
    "buffer_load_dwordx4 v[24:27], v241, s[12:15], s85 offen offset:2048 sc1\n"
    "buffer_load_dwordx4 v[28:31], v241, s[12:15], s85 offen offset:3072 sc1\n"
    "s_barrier\n"
    "ds_read_b128 v[250:253], v247 offset:1024\n"
    "s_waitcnt lgkmcnt(0)\n"
    "v_smfmac_f32_16x16x64_bf16 v[152:155], v[250:253], v[200:207], v233\n"
    "v_smfmac_f32_16x16x64_bf16 v[156:159], v[250:253], v[208:215], v233\n"
    "v_smfmac_f32_16x16x64_bf16 v[160:163], v[250:253], v[216:223], v233\n"
    "v_smfmac_f32_16x16x64_bf16 v[164:167], v[250:253], v[224:231], v233\n"
    ".LjoinB:\n"
    "s_lshl3_add_u32 s62, s55, s48\n"
    "s_lshl_b32 s62, s62, 10\n"
    "s_add_u32 s86, s62, 8388608\n"
    "s_add_u32 s62, s62, 6291456\n"
    "s_nop 0\n"
    "v_permlane32_swap_b32 v152, v154\n"
    "v_permlane32_swap_b32 v156, v158\n"
    "v_permlane32_swap_b32 v160, v162\n"
    "v_permlane32_swap_b32 v164, v166\n"
    "v_add_f32 v200, v152, v154\n"
    "v_add_f32 v201, v156, v158\n"
    "v_add_f32 v202, v160, v162\n"
    "v_add_f32 v203, v164, v166\n"
    "ds_write_b128 v242, v[200:203] offset:0\n"
    "v_permlane32_swap_b32 v153, v155\n"
    "v_permlane32_swap_b32 v157, v159\n"
    "v_permlane32_swap_b32 v161, v163\n"
    "v_permlane32_swap_b32 v165, v167\n"
    "v_add_f32 v204, v153, v155\n"
    "v_add_f32 v205, v157, v159\n"
    "v_add_f32 v206, v161, v163\n"
    "v_add_f32 v207, v165, v167\n"
    "ds_write_b128 v242, v[204:207] offset:2048\n"
    "s_waitcnt lgkmcnt(0)\n"
    "s_barrier\n"
    "ds_read_b128 v[128:131], v243\n"
    "ds_read_b128 v[132:135], v243 offset:16\n"
    "ds_read_b128 v[192:195], v249\n"
    "s_waitcnt lgkmcnt(2)\n"
    "v_pk_fma_f32 v[128:129], v[128:129], v[168:169], v[144:145]\n"
    "v_pk_fma_f32 v[130:131], v[130:131], v[170:171], v[146:147]\n"
    "s_waitcnt lgkmcnt(1)\n"
    "v_pk_fma_f32 v[132:133], v[132:133], v[172:173], v[148:149]\n"
    "v_pk_fma_f32 v[134:135], v[134:135], v[174:175], v[150:151]\n"
    "v_pk_add_f32 v[200:201], v[128:129], v[130:131]\n"
    "v_pk_mul_f32 v[202:203], v[128:129], v[128:129]\n"
    "v_pk_add_f32 v[200:201], v[200:201], v[132:133]\n"
    "v_pk_fma_f32 v[202:203], v[130:131], v[130:131], v[202:203]\n"
    "v_pk_add_f32 v[200:201], v[200:201], v[134:135]\n"
    "v_pk_fma_f32 v[202:203], v[132:133], v[132:133], v[202:203]\n"
    "v_add_f32 v200, v200, v201\n"
    "v_pk_fma_f32 v[202:203], v[134:135], v[134:135], v[202:203]\n"
    "s_waitcnt lgkmcnt(0)\n"
    "v_or3_b32 v192, v192, v193, v194\n"
    "v_add_f32 v202, v202, v203\n"
    "v_add_f32_dpp v200, v200, v200 quad_perm:[1,0,3,2] row_mask:0xf bank_mask:0xf\n"
    "v_or_b32 v192, v192, v195\n"
    "v_add_f32_dpp v202, v202, v202 quad_perm:[1,0,3,2] row_mask:0xf bank_mask:0xf\n"
    "v_add_f32_dpp v200, v200, v200 quad_perm:[2,3,0,1] row_mask:0xf bank_mask:0xf\n"
    "v_cmp_ne_u32 vcc, 0, v192\n"
    "v_add_f32_dpp v202, v202, v202 quad_perm:[2,3,0,1] row_mask:0xf bank_mask:0xf\n"
    "v_add_f32_dpp v200, v200, v200 row_half_mirror row_mask:0xf bank_mask:0xf\n"
    "v_pk_mul_f32 v[136:137], v[128:129], v[184:185]\n"
    "v_add_f32_dpp v202, v202, v202 row_half_mirror row_mask:0xf bank_mask:0xf\n"
    "v_add_f32_dpp v200, v200, v200 row_mirror row_mask:0xf bank_mask:0xf\n"
    "v_pk_mul_f32 v[138:139], v[130:131], v[186:187]\n"
    "v_add_f32_dpp v202, v202, v202 row_mirror row_mask:0xf bank_mask:0xf\n"
    "v_add_f32_dpp v200, v200, v200 row_bcast:15 row_mask:0xa bank_mask:0xf\n"
    "v_pk_mul_f32 v[140:141], v[132:133], v[188:189]\n"
    "v_add_f32_dpp v202, v202, v202 row_bcast:15 row_mask:0xa bank_mask:0xf\n"
    "v_add_f32_dpp v200, v200, v200 row_bcast:31 row_mask:0xc bank_mask:0xf\n"
    "v_pk_mul_f32 v[142:143], v[134:135], v[190:191]\n"
    "v_add_f32_dpp v202, v202, v202 row_bcast:31 row_mask:0xc bank_mask:0xf\n"
    "v_mul_f32 v200, 0xbb000000, v200\n"
    "s_cbranch_vccnz .Lslow\n"
    "v_mul_f32 v202, 0x3b000000, v202\n"
    "v_readlane_b32 s76, v200, 63\n"
    "v_fma_f32 v202, -v200, v200, v202\n"
    "v_max_f32 v202, 0, v202\n"
    "v_add_f32 v202, 0x3727c5ac, v202\n"
    "v_rsq_f32 v202, v202\n"
    "v_pk_fma_f32 v[136:137], v[184:185], s[76:77], v[136:137] op_sel_hi:[1,0,1]\n"
    "v_readlane_b32 s78, v202, 63\n"
    "v_pk_fma_f32 v[138:139], v[186:187], s[76:77], v[138:139] op_sel_hi:[1,0,1]\n"
    "v_pk_fma_f32 v[140:141], v[188:189], s[76:77], v[140:141] op_sel_hi:[1,0,1]\n"
    "v_pk_fma_f32 v[142:143], v[190:191], s[76:77], v[142:143] op_sel_hi:[1,0,1]\n"
    "v_pk_fma_f32 v[136:137], v[136:137], s[78:79], v[176:177] op_sel_hi:[1,0,1]\n"
    "v_pk_fma_f32 v[138:139], v[138:139], s[78:79], v[178:179] op_sel_hi:[1,0,1]\n"
    "v_pk_fma_f32 v[140:141], v[140:141], s[78:79], v[180:181] op_sel_hi:[1,0,1]\n"
    "v_pk_fma_f32 v[142:143], v[142:143], s[78:79], v[182:183] op_sel_hi:[1,0,1]\n"
    "v_cvt_pk_bf16_f32 v200, v136, v137\n"
    "v_cvt_pk_bf16_f32 v201, v138, v139\n"
    "v_cvt_pk_bf16_f32 v202, v140, v141\n"
    "v_cvt_pk_bf16_f32 v203, v142, v143\n"
    "s_setprio 3\n"
    "buffer_store_dwordx4 v[200:203], v241, s[12:15], s62 offen sc1\n"
    "s_setprio 0\n"
    "v_cvt_pk_bf16_f32 v136, v128, v129\n"
    "v_cvt_pk_bf16_f32 v137, v130, v131\n"
    "v_cvt_pk_bf16_f32 v138, v132, v133\n"
    "v_cvt_pk_bf16_f32 v139, v134, v135\n"
    "buffer_store_dwordx4 v[136:139], v241, s[12:15], s86 offen sc1\n"
    "s_add_u32 s85, s85, 4096\n"
    "buffer_load_dwordx4 v[32:35], v241, s[12:15], s85 offen offset:0 sc1\n"
    "buffer_load_dwordx4 v[36:39], v241, s[12:15], s85 offen offset:1024 sc1\n"
    "buffer_load_dwordx4 v[40:43], v241, s[12:15], s85 offen offset:2048 sc1\n"
    "buffer_load_dwordx4 v[44:47], v241, s[12:15], s85 offen offset:3072 sc1\n"
    "s_add_u32 s85, s85, 4096\n"
    "buffer_load_dwordx4 v[48:51], v241, s[12:15], s85 offen offset:0 sc1\n"
    "buffer_load_dwordx4 v[52:55], v241, s[12:15], s85 offen offset:1024 sc1\n"
    "v_and_b32 v170, 1, v240\n"
    ".Lpad7221:\n"
    "s_cbranch_execz .Lwr1_7\n"
    "v_bfe_u32 v171, v240, 1, 3\n"
    "v_lshlrev_b32 v171, 4, v171\n"
    "v_lshl_or_b32 v170, v170, 10, v171\n"
    "s_lshl_b32 s62, s50, 7\n"
    "s_add_u32 s62, s62, 158720\n"
    "v_add_u32 v170, s62, v170\n"
    "ds_read_b128 v[250:253], v170\n"
    "s_and_b32 s62, s48, 3\n"
    "s_lshl_b32 s62, s62, 6\n"
    "v_xor_b32 v161, s62, v241\n"
    "v_xor_b32 v162, 16, v161\n"
    "v_xor_b32 v163, 32, v161\n"
    "v_xor_b32 v164, 48, v161\n"
    "s_lshl4_add_u32 s62, s49, s48\n"
    "s_lshl_b32 s62, s62, 12\n"
    "s_add_u32 s62, s62, 6291456\n"
    "s_lshl_b32 s64, s48, 12\n"
    "s_add_u32 s64, s64, 65536\n"
    "s_and_b32 s65, s50, 0\n"
    "s_lshl_b32 s65, s65, 15\n"
    "s_xor_b32 s62, s62, s65\n"
    "s_xor_b32 s64, s64, s65\n"
    "s_xor_b32 s63, s62, 32768\n"
    "s_xor_b32 s65, s64, 32768\n"
    "v_add_u32 v165, s64, v241\n"
    "v_add_u32 v166, s65, v241\n"
    "v_and_b32 v170, 31, v240\n"
    "v_lshrrev_b32 v171, 5, v240\n"
    "v_and_b32 v172, 15, v240\n"
    "v_xor_b32 v171, v171, v172\n"
    "v_lshlrev_b32 v171, 4, v171\n"
    "v_lshl_or_b32 v171, v170, 10, v171\n"
    "s_lshr_b32 s67, s51, 1\n"
    "s_lshl_b32 s67, s67, 15\n"
    "s_add_u32 s67, s67, 65536\n"
    "v_add_u32 v242, s67, v171\n"
    "s_and_b32 s67, s51, 1\n"
    "s_lshl_b32 s67, s67, 15\n"
    "v_add_u32 v12, s67, v241\n"
    "v_xor_b32 v243, 32, v242\n"
    "v_xor_b32 v244, 64, v242\n"
    "v_xor_b32 v245, 96, v242\n"
    "v_xor_b32 v246, 128, v242\n"
    "v_xor_b32 v247, 160, v242\n"
    "v_xor_b32 v248, 192, v242\n"
    "v_xor_b32 v249, 224, v242\n"
    "s_lshl_b32 s66, s66, 12\n"
    "s_mul_i32 s67, s49, 180224\n"
    "s_add_u32 s67, s67, 10485760\n"
    "s_add_u32 s71, s66, s67\n"
    "s_add_u32 s80, s71, 4096\n"
    "s_add_u32 s81, s71, 8192\n"
    "s_add_u32 s82, s71, 12288\n"
    "s_add_u32 s83, s71, 16384\n"
    "s_add_u32 s84, s71, 20480\n"
    "s_and_b32 s68, s51, 1\n"
    "s_add_u32 s68, s68, 4\n"
    "s_add_u32 s72, s56, s68\n"
    "s_lshl_b32 s72, s72, 12\n"
    "s_add_u32 s72, s72, s67\n"
    "s_lshr_b32 s66, s51, 1\n"
    "s_lshl_b32 s66, s66, 11\n"
    "s_add_u32 s72, s72, s66\n"
    "s_lshl_b32 s66, s56, 12\n"
    "s_add_u32 s66, s66, s67\n"
    "s_sub_u32 s66, s72, s66\n"
    "s_add_u32 s66, s66, 139264\n"
    "v_add_u32 v13, s66, v241\n"
    "s_waitcnt vmcnt(7)\n"
    "s_barrier\n"
    "s_setprio 1\n"
    "s_setprio 2\n"
    "s_mov_b32 m0, s64\n"
    "s_nop 0\n"
    "buffer_load_dwordx4 v161, s[12:15], s62 offen offset:0 sc1 lds\n"
    "buffer_load_dwordx4 v162, s[12:15], s62 offen offset:1024 sc1 lds\n"
    "buffer_load_dwordx4 v163, s[12:15], s62 offen offset:2048 sc1 lds\n"
    "buffer_load_dwordx4 v164, s[12:15], s62 offen offset:3072 sc1 lds\n"
    "s_setprio 0\n"
    "s_waitcnt vmcnt(3)\n"
    "ds_read_b32 v112, v165 offset:0\n"
    "s_waitcnt vmcnt(2)\n"
    "ds_read_b32 v113, v165 offset:1024\n"
    "s_waitcnt vmcnt(1)\n"
    "ds_read_b32 v114, v165 offset:2048\n"
    "s_waitcnt vmcnt(0)\n"
    "ds_read_b32 v115, v165 offset:3072\n"
    "s_mov_b32 m0, s65\n"
    "s_nop 0\n"
    "buffer_load_dwordx4 v161, s[12:15], s63 offen offset:0 sc1 lds\n"
    "buffer_load_dwordx4 v162, s[12:15], s63 offen offset:1024 sc1 lds\n"
    "buffer_load_dwordx4 v163, s[12:15], s63 offen offset:2048 sc1 lds\n"
    "buffer_load_dwordx4 v164, s[12:15], s63 offen offset:3072 sc1 lds\n"
    ".Lgc_142:\n"
    "s_waitcnt lgkmcnt(1)\n"
    "v_max3_u32 v112, v112, v113, v114\n"
    "s_waitcnt lgkmcnt(0)\n"
    "v_max_u32 v112, v112, v115\n"
    "v_cmp_eq_u32 vcc, -1, v112\n"
    "s_nop 0\n"
    "s_cbranch_vccnz .Lgr_143\n"
    ".Lgo_144:\n"
    "s_barrier\n"
    "s_mov_b32 s76, 0xbfb8aa3b\n"
    "buffer_load_dwordx4 v[56:59], v241, s[12:15], s85 offen offset:2048 sc1\n"
    "buffer_load_dwordx4 v[60:63], v241, s[12:15], s85 offen offset:3072 sc1\n"
    "s_add_u32 s85, s85, 4096\n"
    "buffer_load_dwordx4 v[64:67], v241, s[12:15], s85 offen offset:0 sc1\n"
    "buffer_load_dwordx4 v[68:71], v241, s[12:15], s85 offen offset:1024 sc1\n"
    "buffer_load_dwordx4 v[72:75], v241, s[12:15], s85 offen offset:2048 sc1\n"
    "buffer_load_dwordx4 v[76:79], v241, s[12:15], s85 offen offset:3072 sc1\n"
    "s_add_u32 s85, s85, 4096\n"
    "buffer_load_dwordx4 v[80:83], v241, s[12:15], s85 offen offset:0 sc1\n"
    "buffer_load_dwordx4 v[84:87], v241, s[12:15], s85 offen offset:1024 sc1\n"
    "buffer_load_dwordx4 v[88:91], v241, s[12:15], s85 offen offset:2048 sc1\n"
    "buffer_load_dwordx4 v[92:95], v241, s[12:15], s85 offen offset:3072 sc1\n"
    "s_waitcnt vmcnt(13)\n"
    "ds_read_b32 v112, v166 offset:0\n"
    "s_waitcnt vmcnt(12)\n"
    "ds_read_b32 v113, v166 offset:1024\n"
    "s_waitcnt vmcnt(11)\n"
    "ds_read_b32 v114, v166 offset:2048\n"
    "s_waitcnt vmcnt(10)\n"
    "ds_read_b32 v115, v166 offset:3072\n"
    ".Lgc_145:\n"
    "s_waitcnt lgkmcnt(1)\n"
    "v_max3_u32 v112, v112, v113, v114\n"
    "s_waitcnt lgkmcnt(0)\n"
    "v_max_u32 v112, v112, v115\n"
    "v_cmp_eq_u32 vcc, -1, v112\n"
    "s_nop 0\n"
    "s_cbranch_vccnz .Lgr_146\n"
    ".Lgo_147:\n"
    "s_barrier\n"
    "s_cmp_lt_u32 s61, 3\n"
    "s_cbranch_scc0 .Lu5_148\n"
    "s_waitcnt vmcnt(3)\n"
    "s_add_u32 s85, s85, 4096\n"
    "buffer_load_dwordx4 v[96:99], v241, s[12:15], s85 offen offset:0 sc1\n"
    "s_waitcnt vmcnt(3)\n"
    "buffer_load_dwordx4 v[100:103], v241, s[12:15], s85 offen offset:1024 sc1\n"
    "s_waitcnt vmcnt(3)\n"
    "buffer_load_dwordx4 v[104:107], v241, s[12:15], s85 offen offset:2048 sc1\n"
    "s_waitcnt vmcnt(3)\n"
    "buffer_load_dwordx4 v[108:111], v241, s[12:15], s85 offen offset:3072 sc1\n"
    ".Lu5_148:\n"
    "s_setprio 2\n"
    "s_cmp_lt_u32 s68, s57\n"
    "s_cbranch_scc1 .Litem_150\n"
    "s_branch .Lnoitem_149\n"
    ".Litem_150:\n"
    "ds_read_b128 v[136:139], v12 offset:0\n"
    "ds_read_b128 v[112:115], v242 offset:0\n"
    "ds_read_b128 v[140:143], v12 offset:1024\n"
    "ds_read_b128 v[116:119], v243 offset:0\n"
    "ds_read_b128 v[144:147], v12 offset:2048\n"
    "ds_read_b128 v[120:123], v244 offset:0\n"
    "ds_read_b128 v[148:151], v12 offset:3072\n"
    "ds_read_b128 v[124:127], v245 offset:0\n"
    "ds_read_b128 v[152:155], v12 offset:4096\n"
    "ds_read_b128 v[128:131], v246 offset:0\n"
    "ds_read_b128 v[156:159], v12 offset:5120\n"
    "ds_read_b128 v[132:135], v247 offset:0\n"
    "s_waitcnt lgkmcnt(10)\n"
    "v_mfma_f32_32x32x16_bf16 v[160:175], v[136:139], v[112:115], 0\n"
    "ds_read_b128 v[136:139], v12 offset:6144\n"
    "ds_read_b128 v[112:115], v248 offset:0\n"
    "s_waitcnt lgkmcnt(10)\n"
    "v_mfma_f32_32x32x16_bf16 v[160:175], v[140:143], v[116:119], v[160:175]\n"
    "ds_read_b128 v[140:143], v12 offset:7168\n"
    "ds_read_b128 v[116:119], v249 offset:0\n"
    "s_waitcnt lgkmcnt(10)\n"
    "v_mfma_f32_32x32x16_bf16 v[160:175], v[144:147], v[120:123], v[160:175]\n"
    "ds_read_b128 v[144:147], v12 offset:8192\n"
    "ds_read_b128 v[120:123], v242 offset:256\n"
    "s_waitcnt lgkmcnt(10)\n"
    "v_mfma_f32_32x32x16_bf16 v[160:175], v[148:151], v[124:127], v[160:175]\n"
    "ds_read_b128 v[148:151], v12 offset:9216\n"
    "ds_read_b128 v[124:127], v243 offset:256\n"
    "s_waitcnt lgkmcnt(10)\n"
    "v_mfma_f32_32x32x16_bf16 v[160:175], v[152:155], v[128:131], v[160:175]\n"
    "ds_read_b128 v[152:155], v12 offset:10240\n"
    "ds_read_b128 v[128:131], v244 offset:256\n"
    "s_waitcnt lgkmcnt(10)\n"
    "v_mfma_f32_32x32x16_bf16 v[160:175], v[156:159], v[132:135], v[160:175]\n"
    "ds_read_b128 v[156:159], v12 offset:11264\n"
    "ds_read_b128 v[132:135], v245 offset:256\n"
    "s_waitcnt lgkmcnt(10)\n"
    "v_mfma_f32_32x32x16_bf16 v[160:175], v[136:139], v[112:115], v[160:175]\n"
    "ds_read_b128 v[136:139], v12 offset:12288\n"
    "ds_read_b128 v[112:115], v246 offset:256\n"
    "s_waitcnt lgkmcnt(10)\n"
    "v_mfma_f32_32x32x16_bf16 v[160:175], v[140:143], v[116:119], v[160:175]\n"
    "ds_read_b128 v[140:143], v12 offset:13312\n"
    "ds_read_b128 v[116:119], v247 offset:256\n"
    "s_waitcnt lgkmcnt(10)\n"
    "v_mfma_f32_32x32x16_bf16 v[160:175], v[144:147], v[120:123], v[160:175]\n"
    "ds_read_b128 v[144:147], v12 offset:14336\n"
    "ds_read_b128 v[120:123], v248 offset:256\n"
    "s_waitcnt lgkmcnt(10)\n"
    "v_mfma_f32_32x32x16_bf16 v[160:175], v[148:151], v[124:127], v[160:175]\n"
    "ds_read_b128 v[148:151], v12 offset:15360\n"
    "ds_read_b128 v[124:127], v249 offset:256\n"
    "s_waitcnt lgkmcnt(10)\n"
    "v_mfma_f32_32x32x16_bf16 v[160:175], v[152:155], v[128:131], v[160:175]\n"
    "ds_read_b128 v[152:155], v12 offset:16384\n"
    "ds_read_b128 v[128:131], v242 offset:512\n"
    "s_waitcnt lgkmcnt(10)\n"
    "v_mfma_f32_32x32x16_bf16 v[160:175], v[156:159], v[132:135], v[160:175]\n"
    "ds_read_b128 v[156:159], v12 offset:17408\n"
    "ds_read_b128 v[132:135], v243 offset:512\n"
    "s_waitcnt lgkmcnt(10)\n"
    "v_mfma_f32_32x32x16_bf16 v[160:175], v[136:139], v[112:115], v[160:175]\n"
    "ds_read_b128 v[136:139], v12 offset:18432\n"
    "ds_read_b128 v[112:115], v244 offset:512\n"
    "s_waitcnt lgkmcnt(10)\n"
    "v_mfma_f32_32x32x16_bf16 v[160:175], v[140:143], v[116:119], v[160:175]\n"
    "ds_read_b128 v[140:143], v12 offset:19456\n"
    "ds_read_b128 v[116:119], v245 offset:512\n"
    "s_waitcnt lgkmcnt(10)\n"
    "v_mfma_f32_32x32x16_bf16 v[160:175], v[144:147], v[120:123], v[160:175]\n"
    "ds_read_b128 v[144:147], v12 offset:20480\n"
    "ds_read_b128 v[120:123], v246 offset:512\n"
    "s_waitcnt lgkmcnt(10)\n"
    "v_mfma_f32_32x32x16_bf16 v[160:175], v[148:151], v[124:127], v[160:175]\n"
    "ds_read_b128 v[148:151], v12 offset:21504\n"
    "ds_read_b128 v[124:127], v247 offset:512\n"
    "s_waitcnt lgkmcnt(10)\n"
    "v_mfma_f32_32x32x16_bf16 v[160:175], v[152:155], v[128:131], v[160:175]\n"
    "ds_read_b128 v[152:155], v12 offset:22528\n"
    "ds_read_b128 v[128:131], v248 offset:512\n"
    "s_waitcnt lgkmcnt(10)\n"
    "v_mfma_f32_32x32x16_bf16 v[160:175], v[156:159], v[132:135], v[160:175]\n"
    "ds_read_b128 v[156:159], v12 offset:23552\n"
    "ds_read_b128 v[132:135], v249 offset:512\n"
    "s_waitcnt lgkmcnt(10)\n"
    "v_mfma_f32_32x32x16_bf16 v[160:175], v[136:139], v[112:115], v[160:175]\n"
    "ds_read_b128 v[136:139], v12 offset:24576\n"
    "ds_read_b128 v[112:115], v242 offset:768\n"
    "s_waitcnt lgkmcnt(10)\n"
    "v_mfma_f32_32x32x16_bf16 v[160:175], v[140:143], v[116:119], v[160:175]\n"
    "ds_read_b128 v[140:143], v12 offset:25600\n"
    "ds_read_b128 v[116:119], v243 offset:768\n"
    "s_waitcnt lgkmcnt(10)\n"
    "v_mfma_f32_32x32x16_bf16 v[160:175], v[144:147], v[120:123], v[160:175]\n"
    "ds_read_b128 v[144:147], v12 offset:26624\n"
    "ds_read_b128 v[120:123], v244 offset:768\n"
    "s_waitcnt lgkmcnt(10)\n"
    "v_mfma_f32_32x32x16_bf16 v[160:175], v[148:151], v[124:127], v[160:175]\n"
    "ds_read_b128 v[148:151], v12 offset:27648\n"
    "ds_read_b128 v[124:127], v245 offset:768\n"
    "s_waitcnt lgkmcnt(10)\n"
    "v_mfma_f32_32x32x16_bf16 v[160:175], v[152:155], v[128:131], v[160:175]\n"
    "ds_read_b128 v[152:155], v12 offset:28672\n"
    "ds_read_b128 v[128:131], v246 offset:768\n"
    "s_waitcnt lgkmcnt(10)\n"
    "v_mfma_f32_32x32x16_bf16 v[160:175], v[156:159], v[132:135], v[160:175]\n"
    "ds_read_b128 v[156:159], v12 offset:29696\n"
    "ds_read_b128 v[132:135], v247 offset:768\n"
    "s_waitcnt lgkmcnt(10)\n"
    "v_mfma_f32_32x32x16_bf16 v[160:175], v[136:139], v[112:115], v[160:175]\n"
    "ds_read_b128 v[136:139], v12 offset:30720\n"
    "ds_read_b128 v[112:115], v248 offset:768\n"
    "s_waitcnt lgkmcnt(10)\n"
    "v_mfma_f32_32x32x16_bf16 v[160:175], v[140:143], v[116:119], v[160:175]\n"
    "ds_read_b128 v[140:143], v12 offset:31744\n"
    "ds_read_b128 v[116:119], v249 offset:768\n"
    "s_waitcnt lgkmcnt(10)\n"
    "v_mfma_f32_32x32x16_bf16 v[160:175], v[144:147], v[120:123], v[160:175]\n"
    "s_waitcnt lgkmcnt(8)\n"
    "v_mfma_f32_32x32x16_bf16 v[160:175], v[148:151], v[124:127], v[160:175]\n"
    "s_waitcnt lgkmcnt(6)\n"
    "v_mfma_f32_32x32x16_bf16 v[160:175], v[152:155], v[128:131], v[160:175]\n"
    "s_waitcnt lgkmcnt(4)\n"
    "v_mfma_f32_32x32x16_bf16 v[160:175], v[156:159], v[132:135], v[160:175]\n"
    "s_waitcnt lgkmcnt(2)\n"
    "v_mfma_f32_32x32x16_bf16 v[160:175], v[136:139], v[112:115], v[160:175]\n"
    "s_waitcnt lgkmcnt(0)\n"
    "v_mfma_f32_32x32x16_bf16 v[160:175], v[140:143], v[116:119], v[160:175]\n"
    "s_setprio 0\n"
    "s_nop 10\n"
    "v_mul_f32 v4, s76, v160\n"
    "v_mul_f32 v5, s76, v161\n"
    "v_mul_f32 v6, s76, v162\n"
    "v_mul_f32 v7, s76, v163\n"
    "v_mul_f32 v8, s76, v164\n"
    "v_mul_f32 v9, s76, v165\n"
    "v_mul_f32 v10, s76, v166\n"
    "v_mul_f32 v11, s76, v167\n"
    "v_exp_f32 v4, v4\n"
    "v_exp_f32 v5, v5\n"
    "v_exp_f32 v6, v6\n"
    "v_exp_f32 v7, v7\n"
    "v_exp_f32 v8, v8\n"
    "v_exp_f32 v9, v9\n"
    "v_exp_f32 v10, v10\n"
    "v_exp_f32 v11, v11\n"
    "v_add_f32 v4, 1.0, v4\n"
    "v_add_f32 v5, 1.0, v5\n"
    "v_add_f32 v6, 1.0, v6\n"
    "v_add_f32 v7, 1.0, v7\n"
    "v_add_f32 v8, 1.0, v8\n"
    "v_add_f32 v9, 1.0, v9\n"
    "v_add_f32 v10, 1.0, v10\n"
    "v_add_f32 v11, 1.0, v11\n"
    "v_rcp_f32 v4, v4\n"
    "v_rcp_f32 v5, v5\n"
    "v_rcp_f32 v6, v6\n"
    "v_rcp_f32 v7, v7\n"
    "v_rcp_f32 v8, v8\n"
    "v_rcp_f32 v9, v9\n"
    "v_rcp_f32 v10, v10\n"
    "v_rcp_f32 v11, v11\n"
    "v_mul_f32 v4, v4, v160\n"
    "v_mul_f32 v5, v5, v161\n"
    "v_mul_f32 v6, v6, v162\n"
    "v_mul_f32 v7, v7, v163\n"
    "v_mul_f32 v8, v8, v164\n"
    "v_mul_f32 v9, v9, v165\n"
    "v_mul_f32 v10, v10, v166\n"
    "v_mul_f32 v11, v11, v167\n"
    "v_cvt_pk_bf16_f32 v160, v4, v5\n"
    "v_cvt_pk_bf16_f32 v161, v6, v7\n"
    "v_cvt_pk_bf16_f32 v162, v8, v9\n"
    "v_cvt_pk_bf16_f32 v163, v10, v11\n"
    "buffer_store_dwordx4 v[160:163], v241, s[12:15], s72 offen offset:0 sc1\n"
    "ds_write_b128 v13, v[160:163] offset:0\n"
    "v_mul_f32 v4, s76, v168\n"
    "v_mul_f32 v5, s76, v169\n"
    "v_mul_f32 v6, s76, v170\n"
    "v_mul_f32 v7, s76, v171\n"
    "v_mul_f32 v8, s76, v172\n"
    "v_mul_f32 v9, s76, v173\n"
    "v_mul_f32 v10, s76, v174\n"
    "v_mul_f32 v11, s76, v175\n"
    "v_exp_f32 v4, v4\n"
    "v_exp_f32 v5, v5\n"
    "v_exp_f32 v6, v6\n"
    "v_exp_f32 v7, v7\n"
    "v_exp_f32 v8, v8\n"
    "v_exp_f32 v9, v9\n"
    "v_exp_f32 v10, v10\n"
    "v_exp_f32 v11, v11\n"
    "v_add_f32 v4, 1.0, v4\n"
    "v_add_f32 v5, 1.0, v5\n"
    "v_add_f32 v6, 1.0, v6\n"
    "v_add_f32 v7, 1.0, v7\n"
    "v_add_f32 v8, 1.0, v8\n"
    "v_add_f32 v9, 1.0, v9\n"
    "v_add_f32 v10, 1.0, v10\n"
    "v_add_f32 v11, 1.0, v11\n"
    "v_rcp_f32 v4, v4\n"
    "v_rcp_f32 v5, v5\n"
    "v_rcp_f32 v6, v6\n"
    "v_rcp_f32 v7, v7\n"
    "v_rcp_f32 v8, v8\n"
    "v_rcp_f32 v9, v9\n"
    "v_rcp_f32 v10, v10\n"
    "v_rcp_f32 v11, v11\n"
    "v_mul_f32 v4, v4, v168\n"
    "v_mul_f32 v5, v5, v169\n"
    "v_mul_f32 v6, v6, v170\n"
    "v_mul_f32 v7, v7, v171\n"
    "v_mul_f32 v8, v8, v172\n"
    "v_mul_f32 v9, v9, v173\n"
    "v_mul_f32 v10, v10, v174\n"
    "v_mul_f32 v11, v11, v175\n"
    "v_cvt_pk_bf16_f32 v168, v4, v5\n"
    "v_cvt_pk_bf16_f32 v169, v6, v7\n"
    "v_cvt_pk_bf16_f32 v170, v8, v9\n"
    "v_cvt_pk_bf16_f32 v171, v10, v11\n"
    "buffer_store_dwordx4 v[168:171], v241, s[12:15], s72 offen offset:1024 sc1\n"
    "ds_write_b128 v13, v[168:171] offset:1024\n"
    ".Lnoitem_149:\n"
    "s_setprio 0\n"
    "s_waitcnt lgkmcnt(0)\n"
    "s_barrier\n"
    "s_cmp_lt_u32 s61, 3\n"
    "s_cbranch_scc1 .Lv_B0\n"
    "buffer_load_dwordx4 v[144:147], v241, s[12:15], s71 offen offset:0 sc1\n"
    "buffer_load_dwordx4 v[148:151], v241, s[12:15], s71 offen offset:1024 sc1\n"
    "buffer_load_dwordx4 v[152:155], v241, s[12:15], s80 offen offset:0 sc1\n"
    "buffer_load_dwordx4 v[156:159], v241, s[12:15], s80 offen offset:1024 sc1\n"
    "buffer_load_dwordx4 v[160:163], v241, s[12:15], s81 offen offset:0 sc1\n"
    "buffer_load_dwordx4 v[164:167], v241, s[12:15], s81 offen offset:1024 sc1\n"
    "buffer_load_dwordx4 v[168:171], v241, s[12:15], s82 offen offset:0 sc1\n"
    "buffer_load_dwordx4 v[172:175], v241, s[12:15], s82 offen offset:1024 sc1\n"
    "v_lshrrev_b32 v8, 5, v240\n"
    "v_lshlrev_b32 v15, 4, v8\n"
    "v_xor_b32 v15, v15, v241\n"
    "s_lshl_b32 s67, s48, 13\n"
    "v_add_u32 v0, s67, v15\n"
    "v_xor_b32 v1, 32, v0\n"
    "v_xor_b32 v2, 64, v0\n"
    "v_xor_b32 v3, 96, v0\n"
    "v_xor_b32 v4, 128, v0\n"
    "v_xor_b32 v5, 160, v0\n"
    "v_xor_b32 v6, 192, v0\n"
    "v_xor_b32 v7, 224, v0\n"
    "v_bfe_u32 v15, v240, 4, 1\n"
    "v_lshl_or_b32 v8, v15, 1, v8\n"
    "v_lshl_add_u32 v8, v8, 3, s48\n"
    "v_and_b32 v15, 15, v240\n"
    "v_lshlrev_b32 v9, 3, v15\n"
    "v_lshl_or_b32 v9, v8, 10, v9\n"
    "v_xor_b32 v8, v8, v15\n"
    "v_lshlrev_b32 v8, 4, v8\n"
    "v_lshl_or_b32 v8, v15, 9, v8\n"
    "s_lshl_b32 s68, s49, 16\n"
    "s_lshl_b32 s69, s50, 7\n"
    "s_add_u32 s68, s68, s69\n"
    "s_add_u32 s69, s68, 32768\n"
    "v_lshrrev_b32 v10, 10, v9\n"
    "v_lshlrev_b32 v15, 3, v15\n"
    "v_lshl_or_b32 v15, v10, 7, v15\n"
    "v_add_u32 v15, 131072, v15\n"
    "v_lshrrev_b32 v10, 3, v240\n"
    "v_and_b32 v11, 7, v240\n"
    "v_lshlrev_b32 v11, 4, v11\n"
    "v_lshl_or_b32 v10, v10, 10, v11\n"
    "s_lshl_b32 s62, s61, 13\n"
    "s_add_u32 s62, s62, s68\n"
    "s_add_u32 s62, s62, 8388608\n"
    "s_lshl_b32 s63, s61, 10\n"
    "s_add_u32 m0, s63, 131072\n"
    "s_and_b32 s67, s51, 1\n"
    "s_add_u32 s67, s67, 4\n"
    "s_cmp_lt_u32 s67, s57\n"
    "s_cbranch_scc0 .Lnt_151\n"
    ".Lnt_151:\n"
    ".Lgc_152:\n"
    "s_waitcnt vmcnt(6)\n"
    "v_max_u32 v14, v144, v148\n"
    "s_waitcnt vmcnt(4)\n"
    "v_max3_u32 v14, v152, v156, v14\n"
    "s_waitcnt vmcnt(2)\n"
    "v_max3_u32 v14, v160, v164, v14\n"
    "s_waitcnt vmcnt(0)\n"
    "v_max3_u32 v14, v168, v172, v14\n"
    "v_cmp_eq_u32 vcc, -1, v14\n"
    "s_nop 0\n"
    "s_cbranch_vccnz .Lgr_153\n"
    ".Lgo_154:\n"
    "s_setprio 2\n"
    "v_mfma_f32_32x32x16_bf16 v[112:127], v[16:19], v[144:147], 0\n"
    "v_mfma_f32_32x32x16_bf16 v[128:143], v[20:23], v[144:147], 0\n"
    "v_mfma_f32_32x32x16_bf16 v[112:127], v[24:27], v[148:151], v[112:127]\n"
    "v_mfma_f32_32x32x16_bf16 v[128:143], v[28:31], v[148:151], v[128:143]\n"
    "v_mfma_f32_32x32x16_bf16 v[112:127], v[32:35], v[152:155], v[112:127]\n"
    "v_mfma_f32_32x32x16_bf16 v[128:143], v[36:39], v[152:155], v[128:143]\n"
    "v_mfma_f32_32x32x16_bf16 v[112:127], v[40:43], v[156:159], v[112:127]\n"
    "v_mfma_f32_32x32x16_bf16 v[128:143], v[44:47], v[156:159], v[128:143]\n"
    "v_mfma_f32_32x32x16_bf16 v[112:127], v[48:51], v[160:163], v[112:127]\n"
    "v_mfma_f32_32x32x16_bf16 v[128:143], v[52:55], v[160:163], v[128:143]\n"
    "v_mfma_f32_32x32x16_bf16 v[112:127], v[56:59], v[164:167], v[112:127]\n"
    "v_mfma_f32_32x32x16_bf16 v[128:143], v[60:63], v[164:167], v[128:143]\n"
    "v_mfma_f32_32x32x16_bf16 v[112:127], v[64:67], v[168:171], v[112:127]\n"
    "buffer_load_dwordx4 v[176:179], v241, s[12:15], s83 offen offset:0 sc1\n"
    "v_mfma_f32_32x32x16_bf16 v[128:143], v[68:71], v[168:171], v[128:143]\n"
    "buffer_load_dwordx4 v[180:183], v241, s[12:15], s83 offen offset:1024 sc1\n"
    "v_mfma_f32_32x32x16_bf16 v[112:127], v[72:75], v[172:175], v[112:127]\n"
    "buffer_load_dwordx4 v[224:227], v241, s[12:15], s83 offen offset:2048 sc1\n"
    "v_mfma_f32_32x32x16_bf16 v[128:143], v[76:79], v[172:175], v[128:143]\n"
    "buffer_load_dwordx4 v[228:231], v241, s[12:15], s83 offen offset:3072 sc1\n"
    "buffer_load_dwordx4 v10, s[12:15], s62 offen sc1 lds\n"
    ".Lgc_155:\n"
    "s_waitcnt vmcnt(3)\n"
    "v_max_u32 v14, v176, v180\n"
    "s_waitcnt vmcnt(1)\n"
    "v_max3_u32 v14, v224, v228, v14\n"
    "v_cmp_eq_u32 vcc, -1, v14\n"
    "s_nop 0\n"
    "s_cbranch_vccnz .Lgr_156\n"
    ".Lgo_157:\n"
    "v_mfma_f32_32x32x16_bf16 v[112:127], v[80:83], v[176:179], v[112:127]\n"
    "buffer_load_dwordx4 v[192:195], v241, s[12:15], s71 offen offset:2048 sc1\n"
    "v_mfma_f32_32x32x16_bf16 v[128:143], v[84:87], v[176:179], v[128:143]\n"
    "buffer_load_dwordx4 v[200:203], v241, s[12:15], s80 offen offset:2048 sc1\n"
    "v_mfma_f32_32x32x16_bf16 v[112:127], v[88:91], v[180:183], v[112:127]\n"
    "buffer_load_dwordx4 v[208:211], v241, s[12:15], s81 offen offset:2048 sc1\n"
    "v_mfma_f32_32x32x16_bf16 v[128:143], v[92:95], v[180:183], v[128:143]\n"
    "buffer_load_dwordx4 v[216:219], v241, s[12:15], s82 offen offset:2048 sc1\n"
    "buffer_load_dwordx4 v[196:199], v241, s[12:15], s71 offen offset:3072 sc1\n"
    "buffer_load_dwordx4 v[204:207], v241, s[12:15], s80 offen offset:3072 sc1\n"
    "buffer_load_dwordx4 v[212:215], v241, s[12:15], s81 offen offset:3072 sc1\n"
    "buffer_load_dwordx4 v[220:223], v241, s[12:15], s82 offen offset:3072 sc1\n"
    "v_mfma_f32_32x32x16_bf16 v[144:159], v[80:83], v[224:227], 0\n"
    "v_mfma_f32_32x32x16_bf16 v[160:175], v[84:87], v[224:227], 0\n"
    "v_mfma_f32_32x32x16_bf16 v[144:159], v[88:91], v[228:231], v[144:159]\n"
    "v_mfma_f32_32x32x16_bf16 v[160:175], v[92:95], v[228:231], v[160:175]\n"
    "s_nop 0\n"
    "ds_write_b128 v0, v[112:115] offset:0\n"
    "v_add_u32 v0, 0x10000, v0\n"
    "ds_write_b128 v1, v[116:119] offset:1024\n"
    "v_add_u32 v1, 0x10000, v1\n"
    "ds_write_b128 v2, v[120:123] offset:2048\n"
    "v_add_u32 v2, 0x10000, v2\n"
    "ds_write_b128 v3, v[124:127] offset:3072\n"
    "v_add_u32 v3, 0x10000, v3\n"
    "ds_write_b128 v4, v[128:131] offset:4096\n"
    "v_add_u32 v4, 0x10000, v4\n"
    "ds_write_b128 v5, v[132:135] offset:5120\n"
    "v_add_u32 v5, 0x10000, v5\n"
    "ds_write_b128 v6, v[136:139] offset:6144\n"
    "v_add_u32 v6, 0x10000, v6\n"
    "ds_write_b128 v7, v[140:143] offset:7168\n"
    "v_add_u32 v7, 0x10000, v7\n"
    "s_setprio 0\n"
    "s_waitcnt vmcnt(8) lgkmcnt(0)\n"
    "s_barrier\n"
    "ds_read_b128 v[112:115], v8 offset:0\n"
    "ds_read_b128 v[116:119], v8 offset:8192\n"
    "ds_read_b128 v[120:123], v8 offset:16384\n"
    "ds_read_b128 v[124:127], v8 offset:24576\n"
    "ds_read_b128 v[128:131], v8 offset:32768\n"
    "ds_read_b128 v[132:135], v8 offset:40960\n"
    "ds_read_b128 v[136:139], v8 offset:49152\n"
    "ds_read_b128 v[140:143], v8 offset:57344\n"
    "ds_read_b64 v[10:11], v15 offset:0\n"
    "ds_read_b64 v[12:13], v15 offset:4096\n"
    "v_add_u32 v8, 0x10000, v8\n"
    ".Lgc_158:\n"
    "s_waitcnt vmcnt(6)\n"
    "v_max_u32 v14, v192, v200\n"
    "s_waitcnt vmcnt(4)\n"
    "v_max3_u32 v14, v208, v216, v14\n"
    "s_waitcnt vmcnt(2)\n"
    "v_max3_u32 v14, v196, v204, v14\n"
    "s_waitcnt vmcnt(0)\n"
    "v_max3_u32 v14, v212, v220, v14\n"
    "v_cmp_eq_u32 vcc, -1, v14\n"
    "s_nop 0\n"
    "s_cbranch_vccnz .Lgr_159\n"
    ".Lgo_160:\n"
    "v_mfma_f32_32x32x16_bf16 v[144:159], v[16:19], v[192:195], v[144:159]\n"
    "s_waitcnt lgkmcnt(8)\n"
    "v_add_f32 v112, v112, v116\n"
    "v_add_f32 v113, v113, v117\n"
    "v_add_f32 v114, v114, v118\n"
    "v_add_f32 v115, v115, v119\n"
    "s_waitcnt lgkmcnt(6)\n"
    "v_add_f32 v120, v120, v124\n"
    "v_mfma_f32_32x32x16_bf16 v[144:159], v[32:35], v[200:203], v[144:159]\n"
    "v_add_f32 v121, v121, v125\n"
    "v_add_f32 v122, v122, v126\n"
    "v_add_f32 v123, v123, v127\n"
    "s_waitcnt lgkmcnt(4)\n"
    "v_add_f32 v128, v128, v132\n"
    "v_add_f32 v129, v129, v133\n"
    "v_mfma_f32_32x32x16_bf16 v[144:159], v[48:51], v[208:211], v[144:159]\n"
    "v_add_f32 v130, v130, v134\n"
    "v_add_f32 v131, v131, v135\n"
    "s_waitcnt lgkmcnt(2)\n"
    "v_add_f32 v136, v136, v140\n"
    "v_add_f32 v137, v137, v141\n"
    "v_add_f32 v138, v138, v142\n"
    "v_mfma_f32_32x32x16_bf16 v[144:159], v[64:67], v[216:219], v[144:159]\n"
    "v_add_f32 v139, v139, v143\n"
    "v_add_f32 v112, v112, v120\n"
    "v_add_f32 v113, v113, v121\n"
    "v_add_f32 v114, v114, v122\n"
    "v_add_f32 v115, v115, v123\n"
    "v_mfma_f32_32x32x16_bf16 v[144:159], v[24:27], v[196:199], v[144:159]\n"
    "v_add_f32 v128, v128, v136\n"
    "v_add_f32 v129, v129, v137\n"
    "v_add_f32 v130, v130, v138\n"
    "v_add_f32 v131, v131, v139\n"
    "v_add_f32 v112, v112, v128\n"
    "v_mfma_f32_32x32x16_bf16 v[144:159], v[40:43], v[204:207], v[144:159]\n"
    "v_add_f32 v113, v113, v129\n"
    "v_add_f32 v114, v114, v130\n"
    "v_add_f32 v115, v115, v131\n"
    "s_waitcnt lgkmcnt(1)\n"
    "v_lshlrev_b32 v176, 16, v10\n"
    "v_and_b32 v177, 0xffff0000, v10\n"
    "v_mfma_f32_32x32x16_bf16 v[144:159], v[56:59], v[212:215], v[144:159]\n"
    "v_lshlrev_b32 v178, 16, v11\n"
    "v_and_b32 v179, 0xffff0000, v11\n"
    "v_fma_f32 v112, v112, v250, v176\n"
    "v_fma_f32 v113, v113, v251, v177\n"
    "v_fma_f32 v114, v114, v252, v178\n"
    "v_mfma_f32_32x32x16_bf16 v[144:159], v[72:75], v[220:223], v[144:159]\n"
    "v_fma_f32 v115, v115, v253, v179\n"
    "v_cvt_pk_bf16_f32 v116, v112, v113\n"
    "v_cvt_pk_bf16_f32 v117, v114, v115\n"
    "buffer_store_dwordx2 v[116:117], v9, s[44:47], s68 offen sc0 sc1\n"
    "s_waitcnt lgkmcnt(0)\n"
    "v_lshlrev_b32 v176, 16, v12\n"
    "v_mfma_f32_32x32x16_bf16 v[160:175], v[20:23], v[192:195], v[160:175]\n"
    "v_and_b32 v177, 0xffff0000, v12\n"
    "v_lshlrev_b32 v178, 16, v13\n"
    "v_and_b32 v179, 0xffff0000, v13\n"
    "v_mfma_f32_32x32x16_bf16 v[160:175], v[36:39], v[200:203], v[160:175]\n"
    "v_mfma_f32_32x32x16_bf16 v[160:175], v[52:55], v[208:211], v[160:175]\n"
    "v_mfma_f32_32x32x16_bf16 v[160:175], v[68:71], v[216:219], v[160:175]\n"
    "ds_write_b128 v0, v[144:147] offset:0\n"
    "v_mfma_f32_32x32x16_bf16 v[160:175], v[28:31], v[196:199], v[160:175]\n"
    "ds_write_b128 v1, v[148:151] offset:1024\n"
    "v_mfma_f32_32x32x16_bf16 v[160:175], v[44:47], v[204:207], v[160:175]\n"
    "ds_write_b128 v2, v[152:155] offset:2048\n"
    "v_mfma_f32_32x32x16_bf16 v[160:175], v[60:63], v[212:215], v[160:175]\n"
    "ds_write_b128 v3, v[156:159] offset:3072\n"
    "v_mfma_f32_32x32x16_bf16 v[160:175], v[76:79], v[220:223], v[160:175]\n"
    "s_nop 11\n"
    "ds_write_b128 v4, v[160:163] offset:4096\n"
    "ds_write_b128 v5, v[164:167] offset:5120\n"
    "ds_write_b128 v6, v[168:171] offset:6144\n"
    "ds_write_b128 v7, v[172:175] offset:7168\n"
    "s_waitcnt lgkmcnt(0)\n"
    "s_barrier\n"
    "ds_read_b128 v[112:115], v8 offset:0\n"
    "ds_read_b128 v[116:119], v8 offset:8192\n"
    "ds_read_b128 v[120:123], v8 offset:16384\n"
    "ds_read_b128 v[124:127], v8 offset:24576\n"
    "ds_read_b128 v[128:131], v8 offset:32768\n"
    "ds_read_b128 v[132:135], v8 offset:40960\n"
    "ds_read_b128 v[136:139], v8 offset:49152\n"
    "ds_read_b128 v[140:143], v8 offset:57344\n"
    "s_waitcnt lgkmcnt(6)\n"
    "v_pk_add_f32 v[112:113], v[112:113], v[116:117]\n"
    "v_pk_add_f32 v[114:115], v[114:115], v[118:119]\n"
    "s_waitcnt lgkmcnt(4)\n"
    "v_pk_add_f32 v[120:121], v[120:121], v[124:125]\n"
    "v_pk_add_f32 v[122:123], v[122:123], v[126:127]\n"
    "s_waitcnt lgkmcnt(2)\n"
    "v_pk_add_f32 v[128:129], v[128:129], v[132:133]\n"
    "v_pk_add_f32 v[130:131], v[130:131], v[134:135]\n"
    "s_waitcnt lgkmcnt(0)\n"
    "v_pk_add_f32 v[136:137], v[136:137], v[140:141]\n"
    "v_pk_add_f32 v[138:139], v[138:139], v[142:143]\n"
    "v_pk_add_f32 v[112:113], v[112:113], v[120:121]\n"
    "v_pk_add_f32 v[114:115], v[114:115], v[122:123]\n"
    "v_pk_add_f32 v[128:129], v[128:129], v[136:137]\n"
    "v_pk_add_f32 v[130:131], v[130:131], v[138:139]\n"
    "v_pk_add_f32 v[112:113], v[112:113], v[128:129]\n"
    "v_pk_add_f32 v[114:115], v[114:115], v[130:131]\n"
    "v_fma_f32 v112, v112, v250, v176\n"
    "v_fma_f32 v113, v113, v251, v177\n"
    "v_fma_f32 v114, v114, v252, v178\n"
    "v_fma_f32 v115, v115, v253, v179\n"
    "v_cvt_pk_bf16_f32 v116, v112, v113\n"
    "v_cvt_pk_bf16_f32 v117, v114, v115\n"
    "buffer_store_dwordx2 v[116:117], v9, s[44:47], s69 offen sc0 sc1\n"
    "s_branch .Lfin\n"
    ".Lv_B0:\n"
    ".Lpad7867:\n"
    "s_cbranch_execz .Lwr1_1\n"
    "buffer_load_dwordx4 v[144:147], v241, s[12:15], s71 offen offset:0 sc1\n"
    "buffer_load_dwordx4 v[148:151], v241, s[12:15], s71 offen offset:1024 sc1\n"
    "buffer_load_dwordx4 v[152:155], v241, s[12:15], s80 offen offset:0 sc1\n"
    "buffer_load_dwordx4 v[156:159], v241, s[12:15], s80 offen offset:1024 sc1\n"
    "buffer_load_dwordx4 v[160:163], v241, s[12:15], s81 offen offset:0 sc1\n"
    "buffer_load_dwordx4 v[164:167], v241, s[12:15], s81 offen offset:1024 sc1\n"
    "buffer_load_dwordx4 v[168:171], v241, s[12:15], s82 offen offset:0 sc1\n"
    "buffer_load_dwordx4 v[172:175], v241, s[12:15], s82 offen offset:1024 sc1\n"
    "v_lshrrev_b32 v8, 5, v240\n"
    "v_lshlrev_b32 v15, 4, v8\n"
    "v_xor_b32 v15, v15, v241\n"
    "s_lshl_b32 s67, s48, 13\n"
    "v_add_u32 v0, s67, v15\n"
    "v_xor_b32 v1, 32, v0\n"
    "v_xor_b32 v2, 64, v0\n"
    "v_xor_b32 v3, 96, v0\n"
    "v_xor_b32 v4, 128, v0\n"
    "v_xor_b32 v5, 160, v0\n"
    "v_xor_b32 v6, 192, v0\n"
    "v_xor_b32 v7, 224, v0\n"
    "v_bfe_u32 v15, v240, 4, 1\n"
    "v_lshl_or_b32 v8, v15, 1, v8\n"
    "v_lshl_add_u32 v8, v8, 3, s48\n"
    "v_and_b32 v15, 15, v240\n"
    "v_lshlrev_b32 v9, 3, v15\n"
    "v_lshl_or_b32 v9, v8, 10, v9\n"
    "v_xor_b32 v8, v8, v15\n"
    "v_lshlrev_b32 v8, 4, v8\n"
    "v_lshl_or_b32 v8, v15, 9, v8\n"
    "s_lshl_b32 s68, s49, 16\n"
    "s_lshl_b32 s69, s50, 7\n"
    "s_add_u32 s68, s68, s69\n"
    "s_add_u32 s69, s68, 32768\n"
    "v_lshrrev_b32 v10, 10, v9\n"
    "v_lshlrev_b32 v15, 3, v15\n"
    "v_lshl_or_b32 v15, v10, 7, v15\n"
    "v_add_u32 v15, 131072, v15\n"
    "v_lshrrev_b32 v10, 3, v240\n"
    "v_and_b32 v11, 7, v240\n"
    "v_lshlrev_b32 v11, 4, v11\n"
    "v_lshl_or_b32 v10, v10, 10, v11\n"
    "s_lshl_b32 s62, s61, 13\n"
    "s_add_u32 s62, s62, s68\n"
    "s_nop 0\n"
    ".Lpad7912:\n"
    "s_cbranch_execz .Lwr1_2\n"
    "s_add_u32 s62, s62, 8388608\n"
    "s_lshl_b32 s63, s61, 10\n"
    "s_add_u32 m0, s63, 131072\n"
    "s_and_b32 s67, s51, 1\n"
    "s_add_u32 s67, s67, 4\n"
    "s_cmp_lt_u32 s67, s57\n"
    "s_cbranch_scc0 .Lnt_161\n"
    ".Lnt_161:\n"
    ".Lgc_162:\n"
    "s_waitcnt vmcnt(6)\n"
    "v_max_u32 v14, v144, v148\n"
    "s_waitcnt vmcnt(4)\n"
    "v_max3_u32 v14, v152, v156, v14\n"
    "s_waitcnt vmcnt(2)\n"
    "v_max3_u32 v14, v160, v164, v14\n"
    "s_waitcnt vmcnt(0)\n"
    "v_max3_u32 v14, v168, v172, v14\n"
    "v_cmp_eq_u32 vcc, -1, v14\n"
    "s_nop 0\n"
    "s_cbranch_vccnz .Lgr_163\n"
    ".Lgo_164:\n"
    "s_setprio 2\n"
    "v_mfma_f32_32x32x16_bf16 v[112:127], v[16:19], v[144:147], 0\n"
    "v_mfma_f32_32x32x16_bf16 v[128:143], v[20:23], v[144:147], 0\n"
    "v_mfma_f32_32x32x16_bf16 v[112:127], v[24:27], v[148:151], v[112:127]\n"
    "v_mfma_f32_32x32x16_bf16 v[128:143], v[28:31], v[148:151], v[128:143]\n"
    "v_mfma_f32_32x32x16_bf16 v[112:127], v[32:35], v[152:155], v[112:127]\n"
    "v_mfma_f32_32x32x16_bf16 v[128:143], v[36:39], v[152:155], v[128:143]\n"
    "v_mfma_f32_32x32x16_bf16 v[112:127], v[40:43], v[156:159], v[112:127]\n"
    "v_mfma_f32_32x32x16_bf16 v[128:143], v[44:47], v[156:159], v[128:143]\n"
    "v_mfma_f32_32x32x16_bf16 v[112:127], v[48:51], v[160:163], v[112:127]\n"
    "v_mfma_f32_32x32x16_bf16 v[128:143], v[52:55], v[160:163], v[128:143]\n"
    "v_mfma_f32_32x32x16_bf16 v[112:127], v[56:59], v[164:167], v[112:127]\n"
    "v_mfma_f32_32x32x16_bf16 v[128:143], v[60:63], v[164:167], v[128:143]\n"
    "v_mfma_f32_32x32x16_bf16 v[112:127], v[64:67], v[168:171], v[112:127]\n"
    "buffer_load_dwordx4 v[176:179], v241, s[12:15], s83 offen offset:0 sc1\n"
    "v_mfma_f32_32x32x16_bf16 v[128:143], v[68:71], v[168:171], v[128:143]\n"
    "buffer_load_dwordx4 v[180:183], v241, s[12:15], s83 offen offset:1024 sc1\n"
    "v_mfma_f32_32x32x16_bf16 v[112:127], v[72:75], v[172:175], v[112:127]\n"
    "buffer_load_dwordx4 v[184:187], v241, s[12:15], s84 offen offset:0 sc1\n"
    "v_mfma_f32_32x32x16_bf16 v[128:143], v[76:79], v[172:175], v[128:143]\n"
    "buffer_load_dwordx4 v[188:191], v241, s[12:15], s84 offen offset:1024 sc1\n"
    "buffer_load_dwordx4 v[224:227], v241, s[12:15], s83 offen offset:2048 sc1\n"
    "buffer_load_dwordx4 v[228:231], v241, s[12:15], s83 offen offset:3072 sc1\n"
    "buffer_load_dwordx4 v[232:235], v241, s[12:15], s84 offen offset:2048 sc1\n"
    "buffer_load_dwordx4 v[236:239], v241, s[12:15], s84 offen offset:3072 sc1\n"
    "buffer_load_dwordx4 v10, s[12:15], s62 offen sc1 lds\n"
    ".Lgc_165:\n"
    "s_waitcnt vmcnt(7)\n"
    "v_max_u32 v14, v176, v180\n"
    "s_waitcnt vmcnt(5)\n"
    "v_max3_u32 v14, v184, v188, v14\n"
    "s_waitcnt vmcnt(3)\n"
    "v_max3_u32 v14, v224, v228, v14\n"
    "s_waitcnt vmcnt(1)\n"
    "v_max3_u32 v14, v232, v236, v14\n"
    "v_cmp_eq_u32 vcc, -1, v14\n"
    "s_nop 0\n"
    "s_cbranch_vccnz .Lgr_166\n"
    ".Lgo_167:\n"
    "v_mfma_f32_32x32x16_bf16 v[112:127], v[80:83], v[176:179], v[112:127]\n"
    "buffer_load_dwordx4 v[192:195], v241, s[12:15], s71 offen offset:2048 sc1\n"
    "v_mfma_f32_32x32x16_bf16 v[128:143], v[84:87], v[176:179], v[128:143]\n"
    "buffer_load_dwordx4 v[200:203], v241, s[12:15], s80 offen offset:2048 sc1\n"
    "v_mfma_f32_32x32x16_bf16 v[112:127], v[88:91], v[180:183], v[112:127]\n"
    "buffer_load_dwordx4 v[208:211], v241, s[12:15], s81 offen offset:2048 sc1\n"
    "v_mfma_f32_32x32x16_bf16 v[128:143], v[92:95], v[180:183], v[128:143]\n"
    "buffer_load_dwordx4 v[216:219], v241, s[12:15], s82 offen offset:2048 sc1\n"
    "v_mfma_f32_32x32x16_bf16 v[112:127], v[96:99], v[184:187], v[112:127]\n"
    "buffer_load_dwordx4 v[196:199], v241, s[12:15], s71 offen offset:3072 sc1\n"
    "v_mfma_f32_32x32x16_bf16 v[128:143], v[100:103], v[184:187], v[128:143]\n"
    "buffer_load_dwordx4 v[204:207], v241, s[12:15], s80 offen offset:3072 sc1\n"
    "v_mfma_f32_32x32x16_bf16 v[112:127], v[104:107], v[188:191], v[112:127]\n"
    "buffer_load_dwordx4 v[212:215], v241, s[12:15], s81 offen offset:3072 sc1\n"
    "v_mfma_f32_32x32x16_bf16 v[128:143], v[108:111], v[188:191], v[128:143]\n"
    "buffer_load_dwordx4 v[220:223], v241, s[12:15], s82 offen offset:3072 sc1\n"
    "v_mfma_f32_32x32x16_bf16 v[144:159], v[80:83], v[224:227], 0\n"
    "v_mfma_f32_32x32x16_bf16 v[160:175], v[84:87], v[224:227], 0\n"
    "v_mfma_f32_32x32x16_bf16 v[144:159], v[88:91], v[228:231], v[144:159]\n"
    "v_mfma_f32_32x32x16_bf16 v[160:175], v[92:95], v[228:231], v[160:175]\n"
    "s_nop 4\n"
    "ds_write_b128 v0, v[112:115] offset:0\n"
    "v_add_u32 v0, 0x10000, v0\n"
    "v_mfma_f32_32x32x16_bf16 v[144:159], v[96:99], v[232:235], v[144:159]\n"
    "ds_write_b128 v1, v[116:119] offset:1024\n"
    "v_add_u32 v1, 0x10000, v1\n"
    "v_mfma_f32_32x32x16_bf16 v[160:175], v[100:103], v[232:235], v[160:175]\n"
    "ds_write_b128 v2, v[120:123] offset:2048\n"
    "v_add_u32 v2, 0x10000, v2\n"
    "v_mfma_f32_32x32x16_bf16 v[144:159], v[104:107], v[236:239], v[144:159]\n"
    "ds_write_b128 v3, v[124:127] offset:3072\n"
    "v_add_u32 v3, 0x10000, v3\n"
    "v_mfma_f32_32x32x16_bf16 v[160:175], v[108:111], v[236:239], v[160:175]\n"
    "ds_write_b128 v4, v[128:131] offset:4096\n"
    "v_add_u32 v4, 0x10000, v4\n"
    "ds_write_b128 v5, v[132:135] offset:5120\n"
    "v_add_u32 v5, 0x10000, v5\n"
    "ds_write_b128 v6, v[136:139] offset:6144\n"
    "v_add_u32 v6, 0x10000, v6\n"
    "ds_write_b128 v7, v[140:143] offset:7168\n"
    "v_add_u32 v7, 0x10000, v7\n"
    "s_setprio 0\n"
    "s_waitcnt vmcnt(8) lgkmcnt(0)\n"
    "s_barrier\n"
    "ds_read_b128 v[112:115], v8 offset:0\n"
    "ds_read_b128 v[116:119], v8 offset:8192\n"
    "ds_read_b128 v[120:123], v8 offset:16384\n"
    "ds_read_b128 v[124:127], v8 offset:24576\n"
    "ds_read_b128 v[128:131], v8 offset:32768\n"
    "ds_read_b128 v[132:135], v8 offset:40960\n"
    "ds_read_b128 v[136:139], v8 offset:49152\n"
    "ds_read_b128 v[140:143], v8 offset:57344\n"
    "ds_read_b64 v[10:11], v15 offset:0\n"
    "ds_read_b64 v[12:13], v15 offset:4096\n"
    "v_add_u32 v8, 0x10000, v8\n"
    ".Lgc_168:\n"
    "s_waitcnt vmcnt(6)\n"
    "v_max_u32 v14, v192, v200\n"
    "s_waitcnt vmcnt(4)\n"
    "v_max3_u32 v14, v208, v216, v14\n"
    "s_waitcnt vmcnt(2)\n"
    "v_max3_u32 v14, v196, v204, v14\n"
    "s_waitcnt vmcnt(0)\n"
    "v_max3_u32 v14, v212, v220, v14\n"
    "v_cmp_eq_u32 vcc, -1, v14\n"
    "s_nop 0\n"
    "s_cbranch_vccnz .Lgr_169\n"
    ".Lgo_170:\n"
    "v_mfma_f32_32x32x16_bf16 v[144:159], v[16:19], v[192:195], v[144:159]\n"
    "s_waitcnt lgkmcnt(8)\n"
    "v_add_f32 v112, v112, v116\n"
    "v_add_f32 v113, v113, v117\n"
    "v_add_f32 v114, v114, v118\n"
    "v_add_f32 v115, v115, v119\n"
    "s_waitcnt lgkmcnt(6)\n"
    "v_add_f32 v120, v120, v124\n"
    "v_mfma_f32_32x32x16_bf16 v[144:159], v[32:35], v[200:203], v[144:159]\n"
    "v_add_f32 v121, v121, v125\n"
    "v_add_f32 v122, v122, v126\n"
    "v_add_f32 v123, v123, v127\n"
    "s_waitcnt lgkmcnt(4)\n"
    "v_add_f32 v128, v128, v132\n"
    "v_add_f32 v129, v129, v133\n"
    "v_mfma_f32_32x32x16_bf16 v[144:159], v[48:51], v[208:211], v[144:159]\n"
    "v_add_f32 v130, v130, v134\n"
    "v_add_f32 v131, v131, v135\n"
    "s_waitcnt lgkmcnt(2)\n"
    "v_add_f32 v136, v136, v140\n"
    "v_add_f32 v137, v137, v141\n"
    "v_add_f32 v138, v138, v142\n"
    "v_mfma_f32_32x32x16_bf16 v[144:159], v[64:67], v[216:219], v[144:159]\n"
    "v_add_f32 v139, v139, v143\n"
    "v_add_f32 v112, v112, v120\n"
    "v_add_f32 v113, v113, v121\n"
    "v_add_f32 v114, v114, v122\n"
    "v_add_f32 v115, v115, v123\n"
    "v_mfma_f32_32x32x16_bf16 v[144:159], v[24:27], v[196:199], v[144:159]\n"
    "v_add_f32 v128, v128, v136\n"
    "v_add_f32 v129, v129, v137\n"
    "v_add_f32 v130, v130, v138\n"
    "v_add_f32 v131, v131, v139\n"
    "v_add_f32 v112, v112, v128\n"
    "v_mfma_f32_32x32x16_bf16 v[144:159], v[40:43], v[204:207], v[144:159]\n"
    "v_add_f32 v113, v113, v129\n"
    "v_add_f32 v114, v114, v130\n"
    "v_add_f32 v115, v115, v131\n"
    "s_waitcnt lgkmcnt(1)\n"
    "v_lshlrev_b32 v176, 16, v10\n"
    "v_and_b32 v177, 0xffff0000, v10\n"
    "v_mfma_f32_32x32x16_bf16 v[144:159], v[56:59], v[212:215], v[144:159]\n"
    "v_lshlrev_b32 v178, 16, v11\n"
    "v_and_b32 v179, 0xffff0000, v11\n"
    "v_fma_f32 v112, v112, v250, v176\n"
    "v_fma_f32 v113, v113, v251, v177\n"
    "v_fma_f32 v114, v114, v252, v178\n"
    "v_mfma_f32_32x32x16_bf16 v[144:159], v[72:75], v[220:223], v[144:159]\n"
    "v_fma_f32 v115, v115, v253, v179\n"
    "v_cvt_pk_bf16_f32 v116, v112, v113\n"
    "v_cvt_pk_bf16_f32 v117, v114, v115\n"
    "buffer_store_dwordx2 v[116:117], v9, s[44:47], s68 offen sc0 sc1\n"
    "s_waitcnt lgkmcnt(0)\n"
    "v_lshlrev_b32 v176, 16, v12\n"
    "v_mfma_f32_32x32x16_bf16 v[160:175], v[20:23], v[192:195], v[160:175]\n"
    "v_and_b32 v177, 0xffff0000, v12\n"
    "v_lshlrev_b32 v178, 16, v13\n"
    "v_and_b32 v179, 0xffff0000, v13\n"
    "v_mfma_f32_32x32x16_bf16 v[160:175], v[36:39], v[200:203], v[160:175]\n"
    "v_mfma_f32_32x32x16_bf16 v[160:175], v[52:55], v[208:211], v[160:175]\n"
    "v_mfma_f32_32x32x16_bf16 v[160:175], v[68:71], v[216:219], v[160:175]\n"
    "ds_write_b128 v0, v[144:147] offset:0\n"
    "v_mfma_f32_32x32x16_bf16 v[160:175], v[28:31], v[196:199], v[160:175]\n"
    "ds_write_b128 v1, v[148:151] offset:1024\n"
    "v_mfma_f32_32x32x16_bf16 v[160:175], v[44:47], v[204:207], v[160:175]\n"
    "ds_write_b128 v2, v[152:155] offset:2048\n"
    "v_mfma_f32_32x32x16_bf16 v[160:175], v[60:63], v[212:215], v[160:175]\n"
    "ds_write_b128 v3, v[156:159] offset:3072\n"
    "v_mfma_f32_32x32x16_bf16 v[160:175], v[76:79], v[220:223], v[160:175]\n"
    "s_nop 11\n"
    "ds_write_b128 v4, v[160:163] offset:4096\n"
    "ds_write_b128 v5, v[164:167] offset:5120\n"
    "ds_write_b128 v6, v[168:171] offset:6144\n"
    "ds_write_b128 v7, v[172:175] offset:7168\n"
    "s_waitcnt lgkmcnt(0)\n"
    "s_barrier\n"
    "ds_read_b128 v[112:115], v8 offset:0\n"
    "ds_read_b128 v[116:119], v8 offset:8192\n"
    "ds_read_b128 v[120:123], v8 offset:16384\n"
    "ds_read_b128 v[124:127], v8 offset:24576\n"
    "ds_read_b128 v[128:131], v8 offset:32768\n"
    "ds_read_b128 v[132:135], v8 offset:40960\n"
    "ds_read_b128 v[136:139], v8 offset:49152\n"
    "ds_read_b128 v[140:143], v8 offset:57344\n"
    "s_waitcnt lgkmcnt(6)\n"
    "v_pk_add_f32 v[112:113], v[112:113], v[116:117]\n"
    "v_pk_add_f32 v[114:115], v[114:115], v[118:119]\n"
    "s_waitcnt lgkmcnt(4)\n"
    "v_pk_add_f32 v[120:121], v[120:121], v[124:125]\n"
    "v_pk_add_f32 v[122:123], v[122:123], v[126:127]\n"
    "s_waitcnt lgkmcnt(2)\n"
    "v_pk_add_f32 v[128:129], v[128:129], v[132:133]\n"
    "v_pk_add_f32 v[130:131], v[130:131], v[134:135]\n"
    "s_waitcnt lgkmcnt(0)\n"
    "v_pk_add_f32 v[136:137], v[136:137], v[140:141]\n"
    "v_pk_add_f32 v[138:139], v[138:139], v[142:143]\n"
    "v_pk_add_f32 v[112:113], v[112:113], v[120:121]\n"
    "v_pk_add_f32 v[114:115], v[114:115], v[122:123]\n"
    "v_pk_add_f32 v[128:129], v[128:129], v[136:137]\n"
    "v_pk_add_f32 v[130:131], v[130:131], v[138:139]\n"
    "v_pk_add_f32 v[112:113], v[112:113], v[128:129]\n"
    "v_pk_add_f32 v[114:115], v[114:115], v[130:131]\n"
    "v_fma_f32 v112, v112, v250, v176\n"
    "v_fma_f32 v113, v113, v251, v177\n"
    "v_fma_f32 v114, v114, v252, v178\n"
    "v_fma_f32 v115, v115, v253, v179\n"
    "v_cvt_pk_bf16_f32 v116, v112, v113\n"
    "v_cvt_pk_bf16_f32 v117, v114, v115\n"
    "buffer_store_dwordx2 v[116:117], v9, s[44:47], s69 offen sc0 sc1\n"
    ".Lfin:\n"
    ".Lpad8151:\n"
    "s_cbranch_execz .Lwr1_3\n"
    "s_or_b32 s62, s2, s48\n"
    "s_cbranch_scc1 .Lnoep_171\n"
    "v_mov_b32 v0, s53\n"
    "buffer_store_dword v0, off, s[12:15], 0 sc1\n"
    ".Lnoep_171:\n"
    "s_endpgm\n"
    ".Lcold:\n"
    ".Lpad8159:\n"
    "s_cbranch_execz .Lwr1_4\n"
    ".Lgr_13:\n"
    "s_sub_u32 s58, s58, 1\n"
    "s_cbranch_scc0 .Lgg_172\n"
    "s_mov_b32 s58, 0\n"
    "s_branch .Lgo_14\n"
    ".Lgg_172:\n"
    "s_sleep 1\n"
    "buffer_load_dwordx3 v[192:194], v227, s[12:15], s72 offen sc1\n"
    "buffer_load_dwordx3 v[196:198], v227, s[12:15], s73 offen sc1\n"
    "buffer_load_dwordx3 v[200:202], v227, s[12:15], s74 offen sc1\n"
    "buffer_load_dwordx3 v[204:206], v227, s[12:15], s75 offen sc1\n"
    "s_waitcnt vmcnt(0) lgkmcnt(0)\n"
    "s_branch .Lgc_12\n"
    ".Lgr_21:\n"
    "s_sub_u32 s58, s58, 1\n"
    "s_cbranch_scc0 .Lgg_173\n"
    "s_mov_b32 s58, 0\n"
    "s_branch .Lgo_22\n"
    ".Lgg_173:\n"
    "s_sleep 1\n"
    "s_add_u32 m0, s85, 130048\n"
    "buffer_load_dwordx4 v[0:3], v241, s[12:15], s70 offen offset:0 sc1\n"
    "buffer_load_dwordx4 v241, s[12:15], s70 offen offset:1024 sc1 lds\n"
    "s_add_u32 m0, s85, 129024\n"
    "buffer_load_dwordx4 v[4:7], v241, s[12:15], s70 offen offset:2048 sc1\n"
    "buffer_load_dwordx4 v241, s[12:15], s70 offen offset:3072 sc1 lds\n"
    "s_waitcnt vmcnt(0)\n"
    "s_mov_b64 exec, s[88:89]\n"
    "ds_read_b128 v[32:35], v248 offset:0\n"
    "ds_read_b128 v[36:39], v248 offset:256\n"
    "ds_read_b128 v[40:43], v248 offset:1024\n"
    "ds_read_b128 v[44:47], v248 offset:1280\n"
    "s_mov_b64 exec, -1\n"
    "s_add_u32 m0, s85, 132096\n"
    "buffer_load_dwordx4 v[8:11], v241, s[12:15], s80 offen offset:0 sc1\n"
    "buffer_load_dwordx4 v241, s[12:15], s80 offen offset:1024 sc1 lds\n"
    "s_add_u32 m0, s85, 131072\n"
    "buffer_load_dwordx4 v[12:15], v241, s[12:15], s80 offen offset:2048 sc1\n"
    "buffer_load_dwordx4 v241, s[12:15], s80 offen offset:3072 sc1 lds\n"
    "s_waitcnt vmcnt(0)\n"
    "s_mov_b64 exec, s[88:89]\n"
    "ds_read_b128 v[48:51], v248 offset:2048\n"
    "ds_read_b128 v[52:55], v248 offset:2304\n"
    "ds_read_b128 v[56:59], v248 offset:3072\n"
    "ds_read_b128 v[60:63], v248 offset:3328\n"
    "s_mov_b64 exec, -1\n"
    "s_waitcnt vmcnt(0) lgkmcnt(0)\n"
    "s_branch .Lgc_20\n"
    ".Lqr_23:\n"
    "s_xor_b32 s72, s71, 2048\n"
    "buffer_load_dwordx4 v[144:147], v241, s[12:15], s72 offen sc1\n"
    "s_waitcnt vmcnt(0)\n"
    "v_cmp_eq_u32 vcc, -1, v144\n"
    "s_nop 0\n"
    "s_cbranch_vccz .Lok_174\n"
    "s_sleep 1\n"
    "s_sub_u32 s58, s58, 1\n"
    "s_cbranch_scc0 .Lqr_23\n"
    "s_mov_b32 s58, 0\n"
    ".Lok_174:\n"
    "s_branch .Lqo_24\n"
    ".Lqr_25:\n"
    "s_xor_b32 s72, s71, 3072\n"
    "buffer_load_dwordx4 v[148:151], v241, s[12:15], s72 offen sc1\n"
    "s_waitcnt vmcnt(0)\n"
    "v_cmp_eq_u32 vcc, -1, v148\n"
    "s_nop 0\n"
    "s_cbranch_vccz .Lok_175\n"
    "s_sleep 1\n"
    "s_sub_u32 s58, s58, 1\n"
    "s_cbranch_scc0 .Lqr_25\n"
    "s_mov_b32 s58, 0\n"
    ".Lok_175:\n"
    "s_branch .Lqo_26\n"
    ".Lqr_27:\n"
    "s_xor_b32 s72, s71, 4096\n"
    "buffer_load_dwordx4 v[144:147], v241, s[12:15], s72 offen sc1\n"
    "s_waitcnt vmcnt(0)\n"
    "v_cmp_eq_u32 vcc, -1, v144\n"
    "s_nop 0\n"
    "s_cbranch_vccz .Lok_176\n"
    "s_sleep 1\n"
    "s_sub_u32 s58, s58, 1\n"
    "s_cbranch_scc0 .Lqr_27\n"
    "s_mov_b32 s58, 0\n"
    ".Lok_176:\n"
    "s_branch .Lqo_28\n"
    ".Lqr_29:\n"
    "s_xor_b32 s72, s71, 5120\n"
    "buffer_load_dwordx4 v[148:151], v241, s[12:15], s72 offen sc1\n"
    "s_waitcnt vmcnt(0)\n"
    "v_cmp_eq_u32 vcc, -1, v148\n"
    "s_nop 0\n"
    "s_cbranch_vccz .Lok_177\n"
    "s_sleep 1\n"
    "s_sub_u32 s58, s58, 1\n"
    "s_cbranch_scc0 .Lqr_29\n"
    "s_mov_b32 s58, 0\n"
    ".Lok_177:\n"
    "s_branch .Lqo_30\n"
    ".Lqr_31:\n"
    "s_xor_b32 s72, s71, 6144\n"
    "buffer_load_dwordx4 v[144:147], v241, s[12:15], s72 offen sc1\n"
    "s_waitcnt vmcnt(0)\n"
    "v_cmp_eq_u32 vcc, -1, v144\n"
    "s_nop 0\n"
    "s_cbranch_vccz .Lok_178\n"
    "s_sleep 1\n"
    "s_sub_u32 s58, s58, 1\n"
    "s_cbranch_scc0 .Lqr_31\n"
    "s_mov_b32 s58, 0\n"
    ".Lok_178:\n"
    "s_branch .Lqo_32\n"
    ".Lqr_33:\n"
    "s_xor_b32 s72, s71, 7168\n"
    "buffer_load_dwordx4 v[148:151], v241, s[12:15], s72 offen sc1\n"
    "s_waitcnt vmcnt(0)\n"
    "v_cmp_eq_u32 vcc, -1, v148\n"
    "s_nop 0\n"
    "s_cbranch_vccz .Lok_179\n"
    "s_sleep 1\n"
    "s_sub_u32 s58, s58, 1\n"
    "s_cbranch_scc0 .Lqr_33\n"
    "s_mov_b32 s58, 0\n"
    ".Lok_179:\n"
    "s_branch .Lqo_34\n"
    ".Lgr_36:\n"
    "s_sub_u32 s58, s58, 1\n"
    "s_cbranch_scc0 .Lgg_180\n"
    "s_mov_b32 s58, 0\n"
    "s_branch .Lgo_37\n"
    ".Lgg_180:\n"
    "s_sleep 1\n"
    "buffer_load_dword v211, v210, s[12:15], s84 offen sc1\n"
    "s_waitcnt vmcnt(0) lgkmcnt(0)\n"
    "s_branch .Lgc_35\n"
    ".Lgr_39:\n"
    "s_sub_u32 s58, s58, 1\n"
    "s_cbranch_scc0 .Lgg_181\n"
    "s_mov_b32 s58, 0\n"
    "s_branch .Lgo_40\n"
    ".Lgg_181:\n"
    "s_sleep 1\n"
    "s_add_u32 m0, s83, 7168\n"
    "buffer_load_dwordx4 v[16:19], v241, s[12:15], s81 offen offset:0 sc1\n"
    "buffer_load_dwordx4 v241, s[12:15], s81 offen offset:1024 sc1 lds\n"
    "s_add_u32 m0, s83, 6144\n"
    "buffer_load_dwordx4 v[20:23], v241, s[12:15], s81 offen offset:2048 sc1\n"
    "buffer_load_dwordx4 v241, s[12:15], s81 offen offset:3072 sc1 lds\n"
    "s_waitcnt vmcnt(0)\n"
    "s_mov_b64 exec, s[88:89]\n"
    "ds_read_b128 v[64:67], v249 offset:8192\n"
    "ds_read_b128 v[68:71], v249 offset:8448\n"
    "ds_read_b128 v[72:75], v249 offset:9216\n"
    "ds_read_b128 v[76:79], v249 offset:9472\n"
    "s_mov_b64 exec, -1\n"
    "s_waitcnt vmcnt(0) lgkmcnt(0)\n"
    "s_branch .Lgc_38\n"
    ".Lgr_42:\n"
    "s_sub_u32 s58, s58, 1\n"
    "s_cbranch_scc0 .Lgg_182\n"
    "s_mov_b32 s58, 0\n"
    "s_branch .Lgo_43\n"
    ".Lgg_182:\n"
    "s_sleep 1\n"
    "s_add_u32 m0, s83, 9216\n"
    "buffer_load_dwordx4 v[24:27], v241, s[12:15], s82 offen offset:0 sc1\n"
    "buffer_load_dwordx4 v241, s[12:15], s82 offen offset:1024 sc1 lds\n"
    "s_add_u32 m0, s83, 8192\n"
    "buffer_load_dwordx4 v[28:31], v241, s[12:15], s82 offen offset:2048 sc1\n"
    "buffer_load_dwordx4 v241, s[12:15], s82 offen offset:3072 sc1 lds\n"
    "s_waitcnt vmcnt(0)\n"
    "s_mov_b64 exec, s[88:89]\n"
    "ds_read_b128 v[80:83], v249 offset:10240\n"
    "ds_read_b128 v[84:87], v249 offset:10496\n"
    "ds_read_b128 v[88:91], v249 offset:11264\n"
    "ds_read_b128 v[92:95], v249 offset:11520\n"
    "s_mov_b64 exec, -1\n"
    "s_waitcnt vmcnt(0) lgkmcnt(0)\n"
    "s_branch .Lgc_41\n"
    ".Lgr_45:\n"
    "s_sub_u32 s58, s58, 1\n"
    "s_cbranch_scc0 .Lgg_183\n"
    "s_mov_b32 s58, 0\n"
    "s_branch .Lgo_46\n"
    ".Lgg_183:\n"
    "s_sleep 1\n"
    "s_xor_b32 s72, s71, 1024\n"
    "buffer_load_dwordx4 v[148:151], v241, s[12:15], s72 offen sc1\n"
    "s_waitcnt vmcnt(0) lgkmcnt(0)\n"
    "s_branch .Lgc_44\n"
    ".Lgr_48:\n"
    "s_sub_u32 s58, s58, 1\n"
    "s_cbranch_scc0 .Lgg_184\n"
    "s_mov_b32 s58, 0\n"
    "s_branch .Lgo_49\n"
    ".Lgg_184:\n"
    "s_sleep 1\n"
    "s_mov_b32 m0, s64\n"
    "s_nop 0\n"
    "buffer_load_dwordx4 v227, s[12:15], s62 offen offset:0 sc1 lds\n"
    "buffer_load_dwordx4 v228, s[12:15], s62 offen offset:1024 sc1 lds\n"
    "buffer_load_dwordx4 v229, s[12:15], s62 offen offset:2048 sc1 lds\n"
    "buffer_load_dwordx4 v230, s[12:15], s62 offen offset:3072 sc1 lds\n"
    "s_waitcnt vmcnt(3)\n"
    "ds_read_b32 v168, v231 offset:0\n"
    "s_waitcnt vmcnt(2)\n"
    "ds_read_b32 v169, v231 offset:1024\n"
    "s_waitcnt vmcnt(1)\n"
    "ds_read_b32 v170, v231 offset:2048\n"
    "s_waitcnt vmcnt(0)\n"
    "ds_read_b32 v171, v231 offset:3072\n"
    "s_waitcnt vmcnt(0) lgkmcnt(0)\n"
    "s_branch .Lgc_47\n"
    ".Lgr_51:\n"
    "s_sub_u32 s58, s58, 1\n"
    "s_cbranch_scc0 .Lgg_185\n"
    "s_mov_b32 s58, 0\n"
    "s_branch .Lgo_52\n"
    ".Lgg_185:\n"
    "s_sleep 1\n"
    "s_mov_b32 m0, s65\n"
    "s_nop 0\n"
    "buffer_load_dwordx4 v227, s[12:15], s63 offen offset:0 sc1 lds\n"
    "buffer_load_dwordx4 v228, s[12:15], s63 offen offset:1024 sc1 lds\n"
    "buffer_load_dwordx4 v229, s[12:15], s63 offen offset:2048 sc1 lds\n"
    "buffer_load_dwordx4 v230, s[12:15], s63 offen offset:3072 sc1 lds\n"
    "s_waitcnt vmcnt(3)\n"
    "ds_read_b32 v168, v232 offset:0\n"
    "s_waitcnt vmcnt(2)\n"
    "ds_read_b32 v169, v232 offset:1024\n"
    "s_waitcnt vmcnt(1)\n"
    "ds_read_b32 v170, v232 offset:2048\n"
    "s_waitcnt vmcnt(0)\n"
    "ds_read_b32 v171, v232 offset:3072\n"
    "s_waitcnt vmcnt(0) lgkmcnt(0)\n"
    "s_branch .Lgc_50\n"
    ".Lgr_59:\n"
    "s_sub_u32 s58, s58, 1\n"
    "s_cbranch_scc0 .Lgg_186\n"
    "s_mov_b32 s58, 0\n"
    "s_branch .Lgo_60\n"
    ".Lgg_186:\n"
    "s_sleep 1\n"
    "s_sleep 3\n"
    "buffer_load_dwordx4 v[32:35], v241, s[12:15], s71 offen offset:0 sc1\n"
    "buffer_load_dwordx4 v[36:39], v241, s[12:15], s71 offen offset:1024 sc1\n"
    "buffer_load_dwordx4 v[40:43], v241, s[12:15], s80 offen offset:0 sc1\n"
    "buffer_load_dwordx4 v[44:47], v241, s[12:15], s80 offen offset:1024 sc1\n"
    "buffer_load_dwordx4 v[48:51], v241, s[12:15], s81 offen offset:0 sc1\n"
    "buffer_load_dwordx4 v[52:55], v241, s[12:15], s81 offen offset:1024 sc1\n"
    "buffer_load_dwordx4 v[56:59], v241, s[12:15], s82 offen offset:0 sc1\n"
    "buffer_load_dwordx4 v[60:63], v241, s[12:15], s82 offen offset:1024 sc1\n"
    "s_waitcnt vmcnt(0) lgkmcnt(0)\n"
    "s_branch .Lgc_58\n"
    ".Lgr_62:\n"
    "s_sub_u32 s58, s58, 1\n"
    "s_cbranch_scc0 .Lgg_187\n"
    "s_mov_b32 s58, 0\n"
    "s_branch .Lgo_63\n"
    ".Lgg_187:\n"
    "s_sleep 1\n"
    "s_sleep 3\n"
    "buffer_load_dwordx4 v[64:67], v241, s[12:15], s83 offen offset:0 sc1\n"
    "buffer_load_dwordx4 v[68:71], v241, s[12:15], s83 offen offset:1024 sc1\n"
    "buffer_load_dwordx4 v[112:115], v241, s[12:15], s83 offen offset:2048 sc1\n"
    "buffer_load_dwordx4 v[116:119], v241, s[12:15], s83 offen offset:3072 sc1\n"
    "s_waitcnt vmcnt(0) lgkmcnt(0)\n"
    "s_branch .Lgc_61\n"
    ".Lgr_65:\n"
    "s_sub_u32 s58, s58, 1\n"
    "s_cbranch_scc0 .Lgg_188\n"
    "s_mov_b32 s58, 0\n"
    "s_branch .Lgo_66\n"
    ".Lgg_188:\n"
    "s_sleep 1\n"
    "s_sleep 3\n"
    "buffer_load_dwordx4 v[80:83], v241, s[12:15], s71 offen offset:2048 sc1\n"
    "buffer_load_dwordx4 v[88:91], v241, s[12:15], s80 offen offset:2048 sc1\n"
    "buffer_load_dwordx4 v[96:99], v241, s[12:15], s81 offen offset:2048 sc1\n"
    "buffer_load_dwordx4 v[104:107], v241, s[12:15], s82 offen offset:2048 sc1\n"
    "buffer_load_dwordx4 v[84:87], v241, s[12:15], s71 offen offset:3072 sc1\n"
    "buffer_load_dwordx4 v[92:95], v241, s[12:15], s80 offen offset:3072 sc1\n"
    "buffer_load_dwordx4 v[100:103], v241, s[12:15], s81 offen offset:3072 sc1\n"
    "buffer_load_dwordx4 v[108:111], v241, s[12:15], s82 offen offset:3072 sc1\n"
    "s_waitcnt vmcnt(0) lgkmcnt(0)\n"
    "s_branch .Lgc_64\n"
    ".Lgr_69:\n"
    "s_sub_u32 s58, s58, 1\n"
    "s_cbranch_scc0 .Lgg_189\n"
    "s_mov_b32 s58, 0\n"
    "s_branch .Lgo_70\n"
    ".Lgg_189:\n"
    "s_sleep 1\n"
    "s_sleep 3\n"
    "buffer_load_dwordx4 v[32:35], v241, s[12:15], s71 offen offset:0 sc1\n"
    "buffer_load_dwordx4 v[36:39], v241, s[12:15], s71 offen offset:1024 sc1\n"
    "buffer_load_dwordx4 v[40:43], v241, s[12:15], s80 offen offset:0 sc1\n"
    "buffer_load_dwordx4 v[44:47], v241, s[12:15], s80 offen offset:1024 sc1\n"
    "buffer_load_dwordx4 v[48:51], v241, s[12:15], s81 offen offset:0 sc1\n"
    "buffer_load_dwordx4 v[52:55], v241, s[12:15], s81 offen offset:1024 sc1\n"
    "buffer_load_dwordx4 v[56:59], v241, s[12:15], s82 offen offset:0 sc1\n"
    "buffer_load_dwordx4 v[60:63], v241, s[12:15], s82 offen offset:1024 sc1\n"
    "s_waitcnt vmcnt(0) lgkmcnt(0)\n"
    "s_branch .Lgc_68\n"
    ".Lgr_72:\n"
    "s_sub_u32 s58, s58, 1\n"
    "s_cbranch_scc0 .Lgg_190\n"
    "s_mov_b32 s58, 0\n"
    "s_branch .Lgo_73\n"
    ".Lgg_190:\n"
    "s_sleep 1\n"
    "s_sleep 3\n"
    "buffer_load_dwordx4 v[64:67], v241, s[12:15], s83 offen offset:0 sc1\n"
    "buffer_load_dwordx4 v[68:71], v241, s[12:15], s83 offen offset:1024 sc1\n"
    "buffer_load_dwordx4 v[72:75], v241, s[12:15], s84 offen offset:0 sc1\n"
    "buffer_load_dwordx4 v[76:79], v241, s[12:15], s84 offen offset:1024 sc1\n"
    "buffer_load_dwordx4 v[112:115], v241, s[12:15], s83 offen offset:2048 sc1\n"
    "buffer_load_dwordx4 v[116:119], v241, s[12:15], s83 offen offset:3072 sc1\n"
    "buffer_load_dwordx4 v[120:123], v241, s[12:15], s84 offen offset:2048 sc1\n"
    "buffer_load_dwordx4 v[124:127], v241, s[12:15], s84 offen offset:3072 sc1\n"
    "s_waitcnt vmcnt(0) lgkmcnt(0)\n"
    "s_branch .Lgc_71\n"
    ".Lgr_75:\n"
    "s_sub_u32 s58, s58, 1\n"
    "s_cbranch_scc0 .Lgg_191\n"
    "s_mov_b32 s58, 0\n"
    "s_branch .Lgo_76\n"
    ".Lgg_191:\n"
    "s_sleep 1\n"
    "s_sleep 3\n"
    "buffer_load_dwordx4 v[80:83], v241, s[12:15], s71 offen offset:2048 sc1\n"
    "buffer_load_dwordx4 v[88:91], v241, s[12:15], s80 offen offset:2048 sc1\n"
    "buffer_load_dwordx4 v[96:99], v241, s[12:15], s81 offen offset:2048 sc1\n"
    "buffer_load_dwordx4 v[104:107], v241, s[12:15], s82 offen offset:2048 sc1\n"
    "buffer_load_dwordx4 v[84:87], v241, s[12:15], s71 offen offset:3072 sc1\n"
    "buffer_load_dwordx4 v[92:95], v241, s[12:15], s80 offen offset:3072 sc1\n"
    "buffer_load_dwordx4 v[100:103], v241, s[12:15], s81 offen offset:3072 sc1\n"
    "buffer_load_dwordx4 v[108:111], v241, s[12:15], s82 offen offset:3072 sc1\n"
    "s_waitcnt vmcnt(0) lgkmcnt(0)\n"
    "s_branch .Lgc_74\n"
    ".Lgr_81:\n"
    "s_sub_u32 s58, s58, 1\n"
    "s_cbranch_scc0 .Lgg_192\n"
    "s_mov_b32 s58, 0\n"
    "s_branch .Lgo_82\n"
    ".Lgg_192:\n"
    "s_sleep 1\n"
    "s_add_u32 m0, s83, 3072\n"
    "buffer_load_dwordx4 v[0:3], v241, s[12:15], s70 offen offset:0 sc1\n"
    "buffer_load_dwordx4 v241, s[12:15], s70 offen offset:1024 sc1 lds\n"
    "s_add_u32 m0, s83, 2048\n"
    "buffer_load_dwordx4 v[4:7], v241, s[12:15], s70 offen offset:2048 sc1\n"
    "buffer_load_dwordx4 v241, s[12:15], s70 offen offset:3072 sc1 lds\n"
    "s_waitcnt vmcnt(0)\n"
    "s_mov_b64 exec, s[88:89]\n"
    "ds_read_b128 v[32:35], v249 offset:4096\n"
    "ds_read_b128 v[36:39], v249 offset:4352\n"
    "ds_read_b128 v[40:43], v249 offset:5120\n"
    "ds_read_b128 v[44:47], v249 offset:5376\n"
    "s_mov_b64 exec, -1\n"
    "s_waitcnt vmcnt(0) lgkmcnt(0)\n"
    "s_branch .Lgc_80\n"
    ".Lqr_99:\n"
    "s_xor_b32 s72, s71, 2048\n"
    "buffer_load_dwordx4 v[144:147], v241, s[12:15], s72 offen sc1\n"
    "s_waitcnt vmcnt(0)\n"
    "v_cmp_eq_u32 vcc, -1, v144\n"
    "s_nop 0\n"
    "s_cbranch_vccz .Lok_193\n"
    "s_sleep 1\n"
    "s_sub_u32 s58, s58, 1\n"
    "s_cbranch_scc0 .Lqr_99\n"
    "s_mov_b32 s58, 0\n"
    ".Lok_193:\n"
    "s_branch .Lqo_100\n"
    ".Lqr_101:\n"
    "s_xor_b32 s72, s71, 3072\n"
    "buffer_load_dwordx4 v[148:151], v241, s[12:15], s72 offen sc1\n"
    "s_waitcnt vmcnt(0)\n"
    "v_cmp_eq_u32 vcc, -1, v148\n"
    "s_nop 0\n"
    "s_cbranch_vccz .Lok_194\n"
    "s_sleep 1\n"
    "s_sub_u32 s58, s58, 1\n"
    "s_cbranch_scc0 .Lqr_101\n"
    "s_mov_b32 s58, 0\n"
    ".Lok_194:\n"
    "s_branch .Lqo_102\n"
    ".Lqr_103:\n"
    "s_xor_b32 s72, s71, 4096\n"
    "buffer_load_dwordx4 v[144:147], v241, s[12:15], s72 offen sc1\n"
    "s_waitcnt vmcnt(0)\n"
    "v_cmp_eq_u32 vcc, -1, v144\n"
    "s_nop 0\n"
    "s_cbranch_vccz .Lok_195\n"
    "s_sleep 1\n"
    "s_sub_u32 s58, s58, 1\n"
    "s_cbranch_scc0 .Lqr_103\n"
    "s_mov_b32 s58, 0\n"
    ".Lok_195:\n"
    "s_branch .Lqo_104\n"
    ".Lqr_105:\n"
    "s_xor_b32 s72, s71, 5120\n"
    "buffer_load_dwordx4 v[148:151], v241, s[12:15], s72 offen sc1\n"
    "s_waitcnt vmcnt(0)\n"
    "v_cmp_eq_u32 vcc, -1, v148\n"
    "s_nop 0\n"
    "s_cbranch_vccz .Lok_196\n"
    "s_sleep 1\n"
    "s_sub_u32 s58, s58, 1\n"
    "s_cbranch_scc0 .Lqr_105\n"
    "s_mov_b32 s58, 0\n"
    ".Lok_196:\n"
    "s_branch .Lqo_106\n"
    ".Lqr_107:\n"
    "s_xor_b32 s72, s71, 6144\n"
    "buffer_load_dwordx4 v[144:147], v241, s[12:15], s72 offen sc1\n"
    "s_waitcnt vmcnt(0)\n"
    "v_cmp_eq_u32 vcc, -1, v144\n"
    "s_nop 0\n"
    "s_cbranch_vccz .Lok_197\n"
    "s_sleep 1\n"
    "s_sub_u32 s58, s58, 1\n"
    "s_cbranch_scc0 .Lqr_107\n"
    "s_mov_b32 s58, 0\n"
    ".Lok_197:\n"
    "s_branch .Lqo_108\n"
    ".Lqr_109:\n"
    "s_xor_b32 s72, s71, 7168\n"
    "buffer_load_dwordx4 v[148:151], v241, s[12:15], s72 offen sc1\n"
    "s_waitcnt vmcnt(0)\n"
    "v_cmp_eq_u32 vcc, -1, v148\n"
    "s_nop 0\n"
    "s_cbranch_vccz .Lok_198\n"
    "s_sleep 1\n"
    "s_sub_u32 s58, s58, 1\n"
    "s_cbranch_scc0 .Lqr_109\n"
    "s_mov_b32 s58, 0\n"
    ".Lok_198:\n"
    "s_branch .Lqo_110\n"
    ".Lgr_114:\n"
    "s_sub_u32 s58, s58, 1\n"
    "s_cbranch_scc0 .Lgg_199\n"
    "s_mov_b32 s58, 0\n"
    "s_branch .Lgo_115\n"
    ".Lgg_199:\n"
    "s_sleep 1\n"
    "s_add_u32 m0, s83, 5120\n"
    "buffer_load_dwordx4 v[8:11], v241, s[12:15], s80 offen offset:0 sc1\n"
    "buffer_load_dwordx4 v241, s[12:15], s80 offen offset:1024 sc1 lds\n"
    "s_add_u32 m0, s83, 4096\n"
    "buffer_load_dwordx4 v[12:15], v241, s[12:15], s80 offen offset:2048 sc1\n"
    "buffer_load_dwordx4 v241, s[12:15], s80 offen offset:3072 sc1 lds\n"
    "s_waitcnt vmcnt(0)\n"
    "s_mov_b64 exec, s[88:89]\n"
    "ds_read_b128 v[48:51], v249 offset:6144\n"
    "ds_read_b128 v[52:55], v249 offset:6400\n"
    "ds_read_b128 v[56:59], v249 offset:7168\n"
    "ds_read_b128 v[60:63], v249 offset:7424\n"
    "s_mov_b64 exec, -1\n"
    "s_waitcnt vmcnt(0) lgkmcnt(0)\n"
    "s_branch .Lgc_113\n"
    ".Lgr_117:\n"
    "s_sub_u32 s58, s58, 1\n"
    "s_cbranch_scc0 .Lgg_200\n"
    "s_mov_b32 s58, 0\n"
    "s_branch .Lgo_118\n"
    ".Lgg_200:\n"
    "s_sleep 1\n"
    "buffer_load_dword v211, v210, s[12:15], s84 offen sc1\n"
    "s_waitcnt vmcnt(0) lgkmcnt(0)\n"
    "s_branch .Lgc_116\n"
    ".Lgr_120:\n"
    "s_sub_u32 s58, s58, 1\n"
    "s_cbranch_scc0 .Lgg_201\n"
    "s_mov_b32 s58, 0\n"
    "s_branch .Lgo_121\n"
    ".Lgg_201:\n"
    "s_sleep 1\n"
    "s_add_u32 m0, s83, 7168\n"
    "buffer_load_dwordx4 v[16:19], v241, s[12:15], s81 offen offset:0 sc1\n"
    "buffer_load_dwordx4 v241, s[12:15], s81 offen offset:1024 sc1 lds\n"
    "s_add_u32 m0, s83, 6144\n"
    "buffer_load_dwordx4 v[20:23], v241, s[12:15], s81 offen offset:2048 sc1\n"
    "buffer_load_dwordx4 v241, s[12:15], s81 offen offset:3072 sc1 lds\n"
    "s_waitcnt vmcnt(0)\n"
    "s_mov_b64 exec, s[88:89]\n"
    "ds_read_b128 v[64:67], v249 offset:8192\n"
    "ds_read_b128 v[68:71], v249 offset:8448\n"
    "ds_read_b128 v[72:75], v249 offset:9216\n"
    "ds_read_b128 v[76:79], v249 offset:9472\n"
    "s_mov_b64 exec, -1\n"
    "s_waitcnt vmcnt(0) lgkmcnt(0)\n"
    "s_branch .Lgc_119\n"
    ".Lgr_126:\n"
    "s_sub_u32 s58, s58, 1\n"
    "s_cbranch_scc0 .Lgg_202\n"
    "s_mov_b32 s58, 0\n"
    "s_branch .Lgo_127\n"
    ".Lgg_202:\n"
    "s_sleep 1\n"
    "s_add_u32 m0, s83, 9216\n"
    "buffer_load_dwordx4 v[24:27], v241, s[12:15], s82 offen offset:0 sc1\n"
    "buffer_load_dwordx4 v241, s[12:15], s82 offen offset:1024 sc1 lds\n"
    "s_add_u32 m0, s83, 8192\n"
    "buffer_load_dwordx4 v[28:31], v241, s[12:15], s82 offen offset:2048 sc1\n"
    "buffer_load_dwordx4 v241, s[12:15], s82 offen offset:3072 sc1 lds\n"
    "s_waitcnt vmcnt(0)\n"
    "s_mov_b64 exec, s[88:89]\n"
    "ds_read_b128 v[80:83], v249 offset:10240\n"
    "ds_read_b128 v[84:87], v249 offset:10496\n"
    "ds_read_b128 v[88:91], v249 offset:11264\n"
    "ds_read_b128 v[92:95], v249 offset:11520\n"
    "s_mov_b64 exec, -1\n"
    "s_waitcnt vmcnt(0) lgkmcnt(0)\n"
    "s_branch .Lgc_125\n"
    ".Lgr_129:\n"
    "s_sub_u32 s58, s58, 1\n"
    "s_cbranch_scc0 .Lgg_203\n"
    "s_mov_b32 s58, 0\n"
    "s_branch .Lgo_130\n"
    ".Lgg_203:\n"
    "s_sleep 1\n"
    "s_xor_b32 s72, s71, 1024\n"
    ".Lpad8685:\n"
    "s_cbranch_execz .Lwr1_5\n"
    "buffer_load_dwordx4 v[148:151], v241, s[12:15], s72 offen sc1\n"
    "s_waitcnt vmcnt(0) lgkmcnt(0)\n"
    "s_branch .Lgc_128\n"
    ".Lgr_134:\n"
    "s_sub_u32 s58, s58, 1\n"
    "s_cbranch_scc0 .Lgg_204\n"
    "s_mov_b32 s58, 0\n"
    "s_branch .Lgo_135\n"
    ".Lgg_204:\n"
    "s_sleep 1\n"
    "v_and_b32 v204, 7, v240\n"
    "v_lshlrev_b32 v204, 14, v204\n"
    "v_and_b32 v205, 48, v240\n"
    "v_or_b32 v204, v204, v205\n"
    "s_lshl_b32 s62, s55, 6\n"
    "s_add_u32 s62, s62, 262144\n"
    "buffer_load_dwordx3 v[200:202], v204, s[12:15], s62 offen sc1\n"
    "s_waitcnt vmcnt(0) lgkmcnt(0)\n"
    "s_branch .Lgc_133\n"
    ".Lgr_137:\n"
    "s_sub_u32 s58, s58, 1\n"
    "s_cbranch_scc0 .Lgg_205\n"
    "s_mov_b32 s58, 0\n"
    "s_branch .Lgo_138\n"
    ".Lgg_205:\n"
    "s_sleep 1\n"
    "v_lshl_add_u32 v211, s50, 5, v240\n"
    "v_and_b32 v210, 7, v240\n"
    "v_lshl_add_u32 v210, v210, 5, s49\n"
    "v_cmp_gt_u32 vcc, 32, v240\n"
    "s_mov_b32 s66, 65536\n"
    "s_nop 0\n"
    "v_cndmask_b32 v211, v210, v211, vcc\n"
    "v_lshlrev_b32 v211, 6, v211\n"
    "buffer_load_dword v210, v211, s[12:15], s66 offen sc1\n"
    "s_waitcnt vmcnt(0) lgkmcnt(0)\n"
    "s_branch .Lgc_136\n"
    ".Lgr_140:\n"
    "s_sub_u32 s58, s58, 1\n"
    "s_cbranch_scc0 .Lgg_206\n"
    "s_mov_b32 s58, 0\n"
    "s_branch .Lgo_141\n"
    ".Lgg_206:\n"
    "s_sleep 1\n"
    "s_lshl_b32 s66, s51, 10\n"
    "s_add_u32 s66, s66, 36864\n"
    "s_add_u32 s65, s66, 4096\n"
    "buffer_load_dwordx4 v[200:203], v241, s[12:15], s66 offen sc1\n"
    "buffer_load_dwordx4 v[204:207], v241, s[12:15], s65 offen sc1\n"
    "s_waitcnt vmcnt(0) lgkmcnt(0)\n"
    "s_branch .Lgc_139\n"
    ".Lgr_143:\n"
    "s_sub_u32 s58, s58, 1\n"
    "s_cbranch_scc0 .Lgg_207\n"
    "s_mov_b32 s58, 0\n"
    "s_branch .Lgo_144\n"
    ".Lgg_207:\n"
    "s_sleep 1\n"
    "s_mov_b32 m0, s64\n"
    "s_nop 0\n"
    "buffer_load_dwordx4 v161, s[12:15], s62 offen offset:0 sc1 lds\n"
    "buffer_load_dwordx4 v162, s[12:15], s62 offen offset:1024 sc1 lds\n"
    "buffer_load_dwordx4 v163, s[12:15], s62 offen offset:2048 sc1 lds\n"
    "buffer_load_dwordx4 v164, s[12:15], s62 offen offset:3072 sc1 lds\n"
    "s_waitcnt vmcnt(3)\n"
    "ds_read_b32 v112, v165 offset:0\n"
    "s_waitcnt vmcnt(2)\n"
    "ds_read_b32 v113, v165 offset:1024\n"
    "s_waitcnt vmcnt(1)\n"
    "ds_read_b32 v114, v165 offset:2048\n"
    "s_waitcnt vmcnt(0)\n"
    "ds_read_b32 v115, v165 offset:3072\n"
    "s_waitcnt vmcnt(0) lgkmcnt(0)\n"
    "s_branch .Lgc_142\n"
    ".Lgr_146:\n"
    "s_sub_u32 s58, s58, 1\n"
    "s_cbranch_scc0 .Lgg_208\n"
    "s_mov_b32 s58, 0\n"
    "s_branch .Lgo_147\n"
    ".Lgg_208:\n"
    "s_sleep 1\n"
    "s_mov_b32 m0, s65\n"
    "s_nop 0\n"
    "buffer_load_dwordx4 v161, s[12:15], s63 offen offset:0 sc1 lds\n"
    "buffer_load_dwordx4 v162, s[12:15], s63 offen offset:1024 sc1 lds\n"
    "buffer_load_dwordx4 v163, s[12:15], s63 offen offset:2048 sc1 lds\n"
    "buffer_load_dwordx4 v164, s[12:15], s63 offen offset:3072 sc1 lds\n"
    "s_waitcnt vmcnt(3)\n"
    "ds_read_b32 v112, v166 offset:0\n"
    "s_waitcnt vmcnt(2)\n"
    "ds_read_b32 v113, v166 offset:1024\n"
    "s_waitcnt vmcnt(1)\n"
    "ds_read_b32 v114, v166 offset:2048\n"
    "s_waitcnt vmcnt(0)\n"
    "ds_read_b32 v115, v166 offset:3072\n"
    "s_waitcnt vmcnt(0) lgkmcnt(0)\n"
    "s_branch .Lgc_145\n"
    ".Lgr_153:\n"
    "s_sub_u32 s58, s58, 1\n"
    "s_cbranch_scc0 .Lgg_209\n"
    "s_mov_b32 s58, 0\n"
    "s_branch .Lgo_154\n"
    ".Lgg_209:\n"
    "s_sleep 1\n"
    "s_sleep 3\n"
    "buffer_load_dwordx4 v[144:147], v241, s[12:15], s71 offen offset:0 sc1\n"
    "buffer_load_dwordx4 v[148:151], v241, s[12:15], s71 offen offset:1024 sc1\n"
    "buffer_load_dwordx4 v[152:155], v241, s[12:15], s80 offen offset:0 sc1\n"
    "buffer_load_dwordx4 v[156:159], v241, s[12:15], s80 offen offset:1024 sc1\n"
    "buffer_load_dwordx4 v[160:163], v241, s[12:15], s81 offen offset:0 sc1\n"
    "buffer_load_dwordx4 v[164:167], v241, s[12:15], s81 offen offset:1024 sc1\n"
    "buffer_load_dwordx4 v[168:171], v241, s[12:15], s82 offen offset:0 sc1\n"
    "buffer_load_dwordx4 v[172:175], v241, s[12:15], s82 offen offset:1024 sc1\n"
    "s_waitcnt vmcnt(0) lgkmcnt(0)\n"
    "s_branch .Lgc_152\n"
    ".Lgr_156:\n"
    "s_sub_u32 s58, s58, 1\n"
    "s_cbranch_scc0 .Lgg_210\n"
    "s_mov_b32 s58, 0\n"
    "s_branch .Lgo_157\n"
    ".Lgg_210:\n"
    "s_sleep 1\n"
    "s_sleep 3\n"
    "buffer_load_dwordx4 v[176:179], v241, s[12:15], s83 offen offset:0 sc1\n"
    "buffer_load_dwordx4 v[180:183], v241, s[12:15], s83 offen offset:1024 sc1\n"
    "buffer_load_dwordx4 v[224:227], v241, s[12:15], s83 offen offset:2048 sc1\n"
    "buffer_load_dwordx4 v[228:231], v241, s[12:15], s83 offen offset:3072 sc1\n"
    "s_waitcnt vmcnt(0) lgkmcnt(0)\n"
    "s_branch .Lgc_155\n"
    ".Lgr_159:\n"
    "s_sub_u32 s58, s58, 1\n"
    "s_cbranch_scc0 .Lgg_211\n"
    "s_mov_b32 s58, 0\n"
    "s_branch .Lgo_160\n"
    ".Lgg_211:\n"
    "s_sleep 1\n"
    "s_sleep 3\n"
    "buffer_load_dwordx4 v[192:195], v241, s[12:15], s71 offen offset:2048 sc1\n"
    "buffer_load_dwordx4 v[200:203], v241, s[12:15], s80 offen offset:2048 sc1\n"
    "buffer_load_dwordx4 v[208:211], v241, s[12:15], s81 offen offset:2048 sc1\n"
    "buffer_load_dwordx4 v[216:219], v241, s[12:15], s82 offen offset:2048 sc1\n"
    "buffer_load_dwordx4 v[196:199], v241, s[12:15], s71 offen offset:3072 sc1\n"
    "buffer_load_dwordx4 v[204:207], v241, s[12:15], s80 offen offset:3072 sc1\n"
    "buffer_load_dwordx4 v[212:215], v241, s[12:15], s81 offen offset:3072 sc1\n"
    "buffer_load_dwordx4 v[220:223], v241, s[12:15], s82 offen offset:3072 sc1\n"
    "s_waitcnt vmcnt(0) lgkmcnt(0)\n"
    "s_branch .Lgc_158\n"
    ".Lgr_163:\n"
    "s_sub_u32 s58, s58, 1\n"
    "s_cbranch_scc0 .Lgg_212\n"
    "s_mov_b32 s58, 0\n"
    "s_branch .Lgo_164\n"
    ".Lgg_212:\n"
    "s_sleep 1\n"
    "s_sleep 3\n"
    "buffer_load_dwordx4 v[144:147], v241, s[12:15], s71 offen offset:0 sc1\n"
    "buffer_load_dwordx4 v[148:151], v241, s[12:15], s71 offen offset:1024 sc1\n"
    "buffer_load_dwordx4 v[152:155], v241, s[12:15], s80 offen offset:0 sc1\n"
    "buffer_load_dwordx4 v[156:159], v241, s[12:15], s80 offen offset:1024 sc1\n"
    "buffer_load_dwordx4 v[160:163], v241, s[12:15], s81 offen offset:0 sc1\n"
    "buffer_load_dwordx4 v[164:167], v241, s[12:15], s81 offen offset:1024 sc1\n"
    "buffer_load_dwordx4 v[168:171], v241, s[12:15], s82 offen offset:0 sc1\n"
    "buffer_load_dwordx4 v[172:175], v241, s[12:15], s82 offen offset:1024 sc1\n"
    "s_waitcnt vmcnt(0) lgkmcnt(0)\n"
    "s_branch .Lgc_162\n"
    ".Lgr_166:\n"
    "s_sub_u32 s58, s58, 1\n"
    "s_cbranch_scc0 .Lgg_213\n"
    "s_mov_b32 s58, 0\n"
    "s_branch .Lgo_167\n"
    ".Lgg_213:\n"
    "s_sleep 1\n"
    "s_sleep 3\n"
    "buffer_load_dwordx4 v[176:179], v241, s[12:15], s83 offen offset:0 sc1\n"
    "buffer_load_dwordx4 v[180:183], v241, s[12:15], s83 offen offset:1024 sc1\n"
    "buffer_load_dwordx4 v[184:187], v241, s[12:15], s84 offen offset:0 sc1\n"
    "buffer_load_dwordx4 v[188:191], v241, s[12:15], s84 offen offset:1024 sc1\n"
    "buffer_load_dwordx4 v[224:227], v241, s[12:15], s83 offen offset:2048 sc1\n"
    "buffer_load_dwordx4 v[228:231], v241, s[12:15], s83 offen offset:3072 sc1\n"
    "buffer_load_dwordx4 v[232:235], v241, s[12:15], s84 offen offset:2048 sc1\n"
    "buffer_load_dwordx4 v[236:239], v241, s[12:15], s84 offen offset:3072 sc1\n"
    "s_waitcnt vmcnt(0) lgkmcnt(0)\n"
    "s_branch .Lgc_165\n"
    ".Lgr_169:\n"
    "s_sub_u32 s58, s58, 1\n"
    "s_cbranch_scc0 .Lgg_214\n"
    "s_mov_b32 s58, 0\n"
    "s_branch .Lgo_170\n"
    ".Lgg_214:\n"
    "s_sleep 1\n"
    "s_sleep 3\n"
    "buffer_load_dwordx4 v[192:195], v241, s[12:15], s71 offen offset:2048 sc1\n"
    "buffer_load_dwordx4 v[200:203], v241, s[12:15], s80 offen offset:2048 sc1\n"
    "buffer_load_dwordx4 v[208:211], v241, s[12:15], s81 offen offset:2048 sc1\n"
    "buffer_load_dwordx4 v[216:219], v241, s[12:15], s82 offen offset:2048 sc1\n"
    "buffer_load_dwordx4 v[196:199], v241, s[12:15], s71 offen offset:3072 sc1\n"
    "buffer_load_dwordx4 v[204:207], v241, s[12:15], s80 offen offset:3072 sc1\n"
    "buffer_load_dwordx4 v[212:215], v241, s[12:15], s81 offen offset:3072 sc1\n"
    "buffer_load_dwordx4 v[220:223], v241, s[12:15], s82 offen offset:3072 sc1\n"
    "s_waitcnt vmcnt(0) lgkmcnt(0)\n"
    "s_branch .Lgc_168\n"
    ".Lhot_end:\n"
    ".Lslow:\n"
    "s_waitcnt vmcnt(0) lgkmcnt(0)\n"
    "s_barrier\n"
    "v_and_b32 v250, 11, v240\n"
    "v_cmp_eq_u32 vcc, 0, v250\n"
    "v_mov_b32 v250, 0x3f803f80\n"
    "v_mov_b64 v[234:235], 0\n"
    "v_cndmask_b32 v250, 0, v250, vcc\n"
    "v_mov_b32 v251, v250\n"
    "v_mov_b64 v[36:37], v[250:251]\n"
    "v_mov_b64 v[38:39], v[250:251]\n"
    "v_mov_b64 v[44:45], v[250:251]\n"
    "v_mov_b64 v[46:47], v[250:251]\n"
    "v_mov_b64 v[52:53], v[250:251]\n"
    "v_mov_b64 v[54:55], v[250:251]\n"
    "v_mov_b64 v[60:61], v[250:251]\n"
    "v_mov_b64 v[62:63], v[250:251]\n"
    "v_mov_b64 v[68:69], v[250:251]\n"
    "v_mov_b64 v[70:71], v[250:251]\n"
    "v_mov_b64 v[76:77], v[250:251]\n"
    "v_mov_b64 v[78:79], v[250:251]\n"
    "v_mov_b64 v[84:85], v[250:251]\n"
    "v_mov_b64 v[86:87], v[250:251]\n"
    "v_mov_b64 v[92:93], v[250:251]\n"
    "v_mov_b64 v[94:95], v[250:251]\n"
    "v_mov_b64 v[152:153], 0\n"
    "v_mov_b64 v[128:129], 0\n"
    "v_mov_b64 v[154:155], 0\n"
    "v_mov_b64 v[130:131], 0\n"
    "v_mov_b64 v[156:157], 0\n"
    "v_mov_b64 v[132:133], 0\n"
    "v_mov_b64 v[158:159], 0\n"
    "v_mov_b64 v[134:135], 0\n"
    "v_mov_b64 v[160:161], 0\n"
    "v_mov_b64 v[136:137], 0\n"
    "v_mov_b64 v[162:163], 0\n"
    "v_mov_b64 v[138:139], 0\n"
    "v_mov_b64 v[164:165], 0\n"
    "v_mov_b64 v[140:141], 0\n"
    "v_mov_b64 v[166:167], 0\n"
    "v_mov_b64 v[142:143], 0\n"
    "v_mov_b32 v236, 0\n"
    "v_mov_b32 v238, 1.0\n"
    "s_lshl3_add_u32 s75, s50, s48\n"
    "s_lshl_b32 s75, s75, 13\n"
    "s_add_u32 s75, s75, 524288\n"
    "buffer_load_dwordx4 v[0:3], v241, s[12:15], s70 offen offset:0 sc1\n"
    "buffer_load_dwordx4 v[32:35], v241, s[12:15], s70 offen offset:1024 sc1\n"
    "buffer_load_dwordx4 v[4:7], v241, s[12:15], s70 offen offset:2048 sc1\n"
    "buffer_load_dwordx4 v[40:43], v241, s[12:15], s70 offen offset:3072 sc1\n"
    "buffer_load_dwordx4 v[8:11], v241, s[12:15], s80 offen offset:0 sc1\n"
    "buffer_load_dwordx4 v[48:51], v241, s[12:15], s80 offen offset:1024 sc1\n"
    "buffer_load_dwordx4 v[12:15], v241, s[12:15], s80 offen offset:2048 sc1\n"
    "buffer_load_dwordx4 v[56:59], v241, s[12:15], s80 offen offset:3072 sc1\n"
    "buffer_load_dwordx4 v[16:19], v241, s[12:15], s81 offen offset:0 sc1\n"
    "buffer_load_dwordx4 v[64:67], v241, s[12:15], s81 offen offset:1024 sc1\n"
    "buffer_load_dwordx4 v[20:23], v241, s[12:15], s81 offen offset:2048 sc1\n"
    "buffer_load_dwordx4 v[72:75], v241, s[12:15], s81 offen offset:3072 sc1\n"
    "buffer_load_dwordx4 v[24:27], v241, s[12:15], s82 offen offset:0 sc1\n"
    "buffer_load_dwordx4 v[80:83], v241, s[12:15], s82 offen offset:1024 sc1\n"
    "buffer_load_dwordx4 v[28:31], v241, s[12:15], s82 offen offset:2048 sc1\n"
    "buffer_load_dwordx4 v[88:91], v241, s[12:15], s82 offen offset:3072 sc1\n"
    "s_waitcnt vmcnt(14)\n"
    "v_permlane16_swap_b32 v32, v36\n"
    "v_permlane16_swap_b32 v33, v37\n"
    "v_permlane16_swap_b32 v34, v38\n"
    "v_permlane16_swap_b32 v35, v39\n"
    "s_waitcnt vmcnt(12)\n"
    "v_permlane16_swap_b32 v40, v44\n"
    "v_permlane16_swap_b32 v41, v45\n"
    "v_permlane16_swap_b32 v42, v46\n"
    "v_permlane16_swap_b32 v43, v47\n"
    "s_waitcnt vmcnt(10)\n"
    "v_permlane16_swap_b32 v48, v52\n"
    "v_permlane16_swap_b32 v49, v53\n"
    "v_permlane16_swap_b32 v50, v54\n"
    "v_permlane16_swap_b32 v51, v55\n"
    "s_waitcnt vmcnt(8)\n"
    "v_permlane16_swap_b32 v56, v60\n"
    "v_permlane16_swap_b32 v57, v61\n"
    "v_permlane16_swap_b32 v58, v62\n"
    "v_permlane16_swap_b32 v59, v63\n"
    "s_waitcnt vmcnt(6)\n"
    "v_permlane16_swap_b32 v64, v68\n"
    "v_permlane16_swap_b32 v65, v69\n"
    "v_permlane16_swap_b32 v66, v70\n"
    "v_permlane16_swap_b32 v67, v71\n"
    "s_waitcnt vmcnt(4)\n"
    "v_permlane16_swap_b32 v72, v76\n"
    "v_permlane16_swap_b32 v73, v77\n"
    "v_permlane16_swap_b32 v74, v78\n"
    "v_permlane16_swap_b32 v75, v79\n"
    "s_waitcnt vmcnt(2)\n"
    "v_permlane16_swap_b32 v80, v84\n"
    "v_permlane16_swap_b32 v81, v85\n"
    "v_permlane16_swap_b32 v82, v86\n"
    "v_permlane16_swap_b32 v83, v87\n"
    "s_waitcnt vmcnt(0)\n"
    "v_permlane16_swap_b32 v88, v92\n"
    "v_permlane16_swap_b32 v89, v93\n"
    "v_permlane16_swap_b32 v90, v94\n"
    "v_permlane16_swap_b32 v91, v95\n"
    "v_and_b32 v211, 31, v240\n"
    "v_lshlrev_b32 v211, 5, v211\n"
    "s_lshl2_add_u32 s62, s48, 114688\n"
    "v_add_u32 v210, s62, v211\n"
    ".Lsq_215:\n"
    "s_xor_b32 s72, s71, 0\n"
    "buffer_load_dwordx4 v[200:203], v241, s[12:15], s72 offen sc1\n"
    "s_waitcnt vmcnt(0)\n"
    "v_cmp_eq_u32 vcc, -1, v200\n"
    "s_nop 0\n"
    "s_cbranch_vccz .Lok_216\n"
    "s_sleep 1\n"
    "s_sub_u32 s58, s58, 1\n"
    "s_cbranch_scc0 .Lsq_215\n"
    "s_mov_b32 s58, 0\n"
    ".Lok_216:\n"
    "v_mfma_f32_32x32x16_bf16 v[96:111], v[0:3], v[200:203], 0\n"
    "s_nop 11\n"
    "v_max_f32 v208, v96, v97\n"
    "v_max3_f32 v208, v98, v99, v208\n"
    "v_max3_f32 v208, v100, v101, v208\n"
    "v_max3_f32 v208, v102, v103, v208\n"
    "v_max3_f32 v208, v104, v105, v208\n"
    "v_max3_f32 v208, v106, v107, v208\n"
    "v_max3_f32 v208, v108, v109, v208\n"
    "v_max3_f32 v208, v110, v111, v208\n"
    "v_mfma_f32_32x32x16_bf16 v[96:111], v[4:7], v[200:203], 0\n"
    "s_nop 11\n"
    "v_max3_f32 v208, v96, v97, v208\n"
    "v_max3_f32 v208, v98, v99, v208\n"
    "v_max3_f32 v208, v100, v101, v208\n"
    "v_max3_f32 v208, v102, v103, v208\n"
    "v_max3_f32 v208, v104, v105, v208\n"
    "v_max3_f32 v208, v106, v107, v208\n"
    "v_max3_f32 v208, v108, v109, v208\n"
    "v_max3_f32 v208, v110, v111, v208\n"
    "v_mfma_f32_32x32x16_bf16 v[96:111], v[8:11], v[200:203], 0\n"
    "s_nop 11\n"
    "v_max3_f32 v208, v96, v97, v208\n"
    "v_max3_f32 v208, v98, v99, v208\n"
    "v_max3_f32 v208, v100, v101, v208\n"
    "v_max3_f32 v208, v102, v103, v208\n"
    "v_max3_f32 v208, v104, v105, v208\n"
    "v_max3_f32 v208, v106, v107, v208\n"
    "v_max3_f32 v208, v108, v109, v208\n"
    "v_max3_f32 v208, v110, v111, v208\n"
    "v_mfma_f32_32x32x16_bf16 v[96:111], v[12:15], v[200:203], 0\n"
    "s_nop 11\n"
    "v_max3_f32 v208, v96, v97, v208\n"
    "v_max3_f32 v208, v98, v99, v208\n"
    "v_max3_f32 v208, v100, v101, v208\n"
    "v_max3_f32 v208, v102, v103, v208\n"
    "v_max3_f32 v208, v104, v105, v208\n"
    "v_max3_f32 v208, v106, v107, v208\n"
    "v_max3_f32 v208, v108, v109, v208\n"
    "v_max3_f32 v208, v110, v111, v208\n"
    "v_mfma_f32_32x32x16_bf16 v[96:111], v[16:19], v[200:203], 0\n"
    "s_nop 11\n"
    "v_max3_f32 v208, v96, v97, v208\n"
    "v_max3_f32 v208, v98, v99, v208\n"
    "v_max3_f32 v208, v100, v101, v208\n"
    "v_max3_f32 v208, v102, v103, v208\n"
    "v_max3_f32 v208, v104, v105, v208\n"
    "v_max3_f32 v208, v106, v107, v208\n"
    "v_max3_f32 v208, v108, v109, v208\n"
    "v_max3_f32 v208, v110, v111, v208\n"
    "v_mfma_f32_32x32x16_bf16 v[96:111], v[20:23], v[200:203], 0\n"
    "s_nop 11\n"
    "v_max3_f32 v208, v96, v97, v208\n"
    "v_max3_f32 v208, v98, v99, v208\n"
    "v_max3_f32 v208, v100, v101, v208\n"
    "v_max3_f32 v208, v102, v103, v208\n"
    "v_max3_f32 v208, v104, v105, v208\n"
    "v_max3_f32 v208, v106, v107, v208\n"
    "v_max3_f32 v208, v108, v109, v208\n"
    "v_max3_f32 v208, v110, v111, v208\n"
    "v_mfma_f32_32x32x16_bf16 v[96:111], v[24:27], v[200:203], 0\n"
    "s_nop 11\n"
    "v_max3_f32 v208, v96, v97, v208\n"
    "v_max3_f32 v208, v98, v99, v208\n"
    "v_max3_f32 v208, v100, v101, v208\n"
    "v_max3_f32 v208, v102, v103, v208\n"
    "v_max3_f32 v208, v104, v105, v208\n"
    "v_max3_f32 v208, v106, v107, v208\n"
    "v_max3_f32 v208, v108, v109, v208\n"
    "v_max3_f32 v208, v110, v111, v208\n"
    "v_mfma_f32_32x32x16_bf16 v[96:111], v[28:31], v[200:203], 0\n"
    "s_nop 11\n"
    "v_max3_f32 v208, v96, v97, v208\n"
    "v_max3_f32 v208, v98, v99, v208\n"
    "v_max3_f32 v208, v100, v101, v208\n"
    "v_max3_f32 v208, v102, v103, v208\n"
    "v_max3_f32 v208, v104, v105, v208\n"
    "v_max3_f32 v208, v106, v107, v208\n"
    "v_max3_f32 v208, v108, v109, v208\n"
    "v_max3_f32 v208, v110, v111, v208\n"
    "v_mov_b32 v209, v208\n"
    "s_nop 1\n"
    "v_permlane32_swap_b32 v208, v209\n"
    "v_max_f32 v208, v208, v209\n"
    "ds_write_b32 v210, v208 offset:0\n"
    ".Lsq_217:\n"
    "s_xor_b32 s72, s71, 1024\n"
    "buffer_load_dwordx4 v[200:203], v241, s[12:15], s72 offen sc1\n"
    "s_waitcnt vmcnt(0)\n"
    "v_cmp_eq_u32 vcc, -1, v200\n"
    "s_nop 0\n"
    "s_cbranch_vccz .Lok_218\n"
    "s_sleep 1\n"
    "s_sub_u32 s58, s58, 1\n"
    "s_cbranch_scc0 .Lsq_217\n"
    "s_mov_b32 s58, 0\n"
    ".Lok_218:\n"
    "v_mfma_f32_32x32x16_bf16 v[96:111], v[0:3], v[200:203], 0\n"
    "s_nop 11\n"
    "v_max_f32 v208, v96, v97\n"
    "v_max3_f32 v208, v98, v99, v208\n"
    "v_max3_f32 v208, v100, v101, v208\n"
    "v_max3_f32 v208, v102, v103, v208\n"
    "v_max3_f32 v208, v104, v105, v208\n"
    "v_max3_f32 v208, v106, v107, v208\n"
    "v_max3_f32 v208, v108, v109, v208\n"
    "v_max3_f32 v208, v110, v111, v208\n"
    "v_mfma_f32_32x32x16_bf16 v[96:111], v[4:7], v[200:203], 0\n"
    "s_nop 11\n"
    "v_max3_f32 v208, v96, v97, v208\n"
    "v_max3_f32 v208, v98, v99, v208\n"
    "v_max3_f32 v208, v100, v101, v208\n"
    "v_max3_f32 v208, v102, v103, v208\n"
    "v_max3_f32 v208, v104, v105, v208\n"
    "v_max3_f32 v208, v106, v107, v208\n"
    "v_max3_f32 v208, v108, v109, v208\n"
    "v_max3_f32 v208, v110, v111, v208\n"
    "v_mfma_f32_32x32x16_bf16 v[96:111], v[8:11], v[200:203], 0\n"
    "s_nop 11\n"
    "v_max3_f32 v208, v96, v97, v208\n"
    "v_max3_f32 v208, v98, v99, v208\n"
    "v_max3_f32 v208, v100, v101, v208\n"
    "v_max3_f32 v208, v102, v103, v208\n"
    "v_max3_f32 v208, v104, v105, v208\n"
    "v_max3_f32 v208, v106, v107, v208\n"
    "v_max3_f32 v208, v108, v109, v208\n"
    "v_max3_f32 v208, v110, v111, v208\n"
    "v_mfma_f32_32x32x16_bf16 v[96:111], v[12:15], v[200:203], 0\n"
    "s_nop 11\n"
    "v_max3_f32 v208, v96, v97, v208\n"
    "v_max3_f32 v208, v98, v99, v208\n"
    "v_max3_f32 v208, v100, v101, v208\n"
    "v_max3_f32 v208, v102, v103, v208\n"
    "v_max3_f32 v208, v104, v105, v208\n"
    "v_max3_f32 v208, v106, v107, v208\n"
    "v_max3_f32 v208, v108, v109, v208\n"
    "v_max3_f32 v208, v110, v111, v208\n"
    "v_mfma_f32_32x32x16_bf16 v[96:111], v[16:19], v[200:203], 0\n"
    "s_nop 11\n"
    "v_max3_f32 v208, v96, v97, v208\n"
    "v_max3_f32 v208, v98, v99, v208\n"
    "v_max3_f32 v208, v100, v101, v208\n"
    "v_max3_f32 v208, v102, v103, v208\n"
    "v_max3_f32 v208, v104, v105, v208\n"
    "v_max3_f32 v208, v106, v107, v208\n"
    "v_max3_f32 v208, v108, v109, v208\n"
    "v_max3_f32 v208, v110, v111, v208\n"
    "v_mfma_f32_32x32x16_bf16 v[96:111], v[20:23], v[200:203], 0\n"
    "s_nop 11\n"
    "v_max3_f32 v208, v96, v97, v208\n"
    "v_max3_f32 v208, v98, v99, v208\n"
    "v_max3_f32 v208, v100, v101, v208\n"
    "v_max3_f32 v208, v102, v103, v208\n"
    "v_max3_f32 v208, v104, v105, v208\n"
    "v_max3_f32 v208, v106, v107, v208\n"
    "v_max3_f32 v208, v108, v109, v208\n"
    "v_max3_f32 v208, v110, v111, v208\n"
    "v_mfma_f32_32x32x16_bf16 v[96:111], v[24:27], v[200:203], 0\n"
    "s_nop 11\n"
    "v_max3_f32 v208, v96, v97, v208\n"
    "v_max3_f32 v208, v98, v99, v208\n"
    "v_max3_f32 v208, v100, v101, v208\n"
    "v_max3_f32 v208, v102, v103, v208\n"
    "v_max3_f32 v208, v104, v105, v208\n"
    "v_max3_f32 v208, v106, v107, v208\n"
    "v_max3_f32 v208, v108, v109, v208\n"
    "v_max3_f32 v208, v110, v111, v208\n"
    "v_mfma_f32_32x32x16_bf16 v[96:111], v[28:31], v[200:203], 0\n"
    "s_nop 11\n"
    "v_max3_f32 v208, v96, v97, v208\n"
    "v_max3_f32 v208, v98, v99, v208\n"
    "v_max3_f32 v208, v100, v101, v208\n"
    "v_max3_f32 v208, v102, v103, v208\n"
    "v_max3_f32 v208, v104, v105, v208\n"
    "v_max3_f32 v208, v106, v107, v208\n"
    "v_max3_f32 v208, v108, v109, v208\n"
    "v_max3_f32 v208, v110, v111, v208\n"
    "v_mov_b32 v209, v208\n"
    "s_nop 1\n"
    "v_permlane32_swap_b32 v208, v209\n"
    "v_max_f32 v208, v208, v209\n"
    "ds_write_b32 v210, v208 offset:1024\n"
    ".Lsq_219:\n"
    "s_xor_b32 s72, s71, 2048\n"
    "buffer_load_dwordx4 v[200:203], v241, s[12:15], s72 offen sc1\n"
    "s_waitcnt vmcnt(0)\n"
    "v_cmp_eq_u32 vcc, -1, v200\n"
    "s_nop 0\n"
    "s_cbranch_vccz .Lok_220\n"
    "s_sleep 1\n"
    "s_sub_u32 s58, s58, 1\n"
    "s_cbranch_scc0 .Lsq_219\n"
    "s_mov_b32 s58, 0\n"
    ".Lok_220:\n"
    "v_mfma_f32_32x32x16_bf16 v[96:111], v[0:3], v[200:203], 0\n"
    "s_nop 11\n"
    "v_max_f32 v208, v96, v97\n"
    "v_max3_f32 v208, v98, v99, v208\n"
    "v_max3_f32 v208, v100, v101, v208\n"
    "v_max3_f32 v208, v102, v103, v208\n"
    "v_max3_f32 v208, v104, v105, v208\n"
    "v_max3_f32 v208, v106, v107, v208\n"
    "v_max3_f32 v208, v108, v109, v208\n"
    "v_max3_f32 v208, v110, v111, v208\n"
    "v_mfma_f32_32x32x16_bf16 v[96:111], v[4:7], v[200:203], 0\n"
    "s_nop 11\n"
    "v_max3_f32 v208, v96, v97, v208\n"
    "v_max3_f32 v208, v98, v99, v208\n"
    "v_max3_f32 v208, v100, v101, v208\n"
    "v_max3_f32 v208, v102, v103, v208\n"
    "v_max3_f32 v208, v104, v105, v208\n"
    "v_max3_f32 v208, v106, v107, v208\n"
    "v_max3_f32 v208, v108, v109, v208\n"
    "v_max3_f32 v208, v110, v111, v208\n"
    "v_mfma_f32_32x32x16_bf16 v[96:111], v[8:11], v[200:203], 0\n"
    "s_nop 11\n"
    "v_max3_f32 v208, v96, v97, v208\n"
    "v_max3_f32 v208, v98, v99, v208\n"
    "v_max3_f32 v208, v100, v101, v208\n"
    "v_max3_f32 v208, v102, v103, v208\n"
    "v_max3_f32 v208, v104, v105, v208\n"
    "v_max3_f32 v208, v106, v107, v208\n"
    "v_max3_f32 v208, v108, v109, v208\n"
    "v_max3_f32 v208, v110, v111, v208\n"
    "v_mfma_f32_32x32x16_bf16 v[96:111], v[12:15], v[200:203], 0\n"
    "s_nop 11\n"
    "v_max3_f32 v208, v96, v97, v208\n"
    "v_max3_f32 v208, v98, v99, v208\n"
    "v_max3_f32 v208, v100, v101, v208\n"
    "v_max3_f32 v208, v102, v103, v208\n"
    "v_max3_f32 v208, v104, v105, v208\n"
    "v_max3_f32 v208, v106, v107, v208\n"
    "v_max3_f32 v208, v108, v109, v208\n"
    "v_max3_f32 v208, v110, v111, v208\n"
    "v_mfma_f32_32x32x16_bf16 v[96:111], v[16:19], v[200:203], 0\n"
    "s_nop 11\n"
    "v_max3_f32 v208, v96, v97, v208\n"
    "v_max3_f32 v208, v98, v99, v208\n"
    "v_max3_f32 v208, v100, v101, v208\n"
    "v_max3_f32 v208, v102, v103, v208\n"
    "v_max3_f32 v208, v104, v105, v208\n"
    "v_max3_f32 v208, v106, v107, v208\n"
    "v_max3_f32 v208, v108, v109, v208\n"
    "v_max3_f32 v208, v110, v111, v208\n"
    "v_mfma_f32_32x32x16_bf16 v[96:111], v[20:23], v[200:203], 0\n"
    "s_nop 11\n"
    "v_max3_f32 v208, v96, v97, v208\n"
    "v_max3_f32 v208, v98, v99, v208\n"
    "v_max3_f32 v208, v100, v101, v208\n"
    "v_max3_f32 v208, v102, v103, v208\n"
    "v_max3_f32 v208, v104, v105, v208\n"
    "v_max3_f32 v208, v106, v107, v208\n"
    "v_max3_f32 v208, v108, v109, v208\n"
    "v_max3_f32 v208, v110, v111, v208\n"
    "v_mfma_f32_32x32x16_bf16 v[96:111], v[24:27], v[200:203], 0\n"
    "s_nop 11\n"
    "v_max3_f32 v208, v96, v97, v208\n"
    "v_max3_f32 v208, v98, v99, v208\n"
    "v_max3_f32 v208, v100, v101, v208\n"
    "v_max3_f32 v208, v102, v103, v208\n"
    "v_max3_f32 v208, v104, v105, v208\n"
    "v_max3_f32 v208, v106, v107, v208\n"
    "v_max3_f32 v208, v108, v109, v208\n"
    "v_max3_f32 v208, v110, v111, v208\n"
    "v_mfma_f32_32x32x16_bf16 v[96:111], v[28:31], v[200:203], 0\n"
    "s_nop 11\n"
    "v_max3_f32 v208, v96, v97, v208\n"
    "v_max3_f32 v208, v98, v99, v208\n"
    "v_max3_f32 v208, v100, v101, v208\n"
    "v_max3_f32 v208, v102, v103, v208\n"
    "v_max3_f32 v208, v104, v105, v208\n"
    "v_max3_f32 v208, v106, v107, v208\n"
    "v_max3_f32 v208, v108, v109, v208\n"
    "v_max3_f32 v208, v110, v111, v208\n"
    "v_mov_b32 v209, v208\n"
    "s_nop 1\n"
    "v_permlane32_swap_b32 v208, v209\n"
    "v_max_f32 v208, v208, v209\n"
    "ds_write_b32 v210, v208 offset:2048\n"
    ".Lsq_221:\n"
    "s_xor_b32 s72, s71, 3072\n"
    "buffer_load_dwordx4 v[200:203], v241, s[12:15], s72 offen sc1\n"
    "s_waitcnt vmcnt(0)\n"
    "v_cmp_eq_u32 vcc, -1, v200\n"
    "s_nop 0\n"
    "s_cbranch_vccz .Lok_222\n"
    "s_sleep 1\n"
    "s_sub_u32 s58, s58, 1\n"
    "s_cbranch_scc0 .Lsq_221\n"
    "s_mov_b32 s58, 0\n"
    ".Lok_222:\n"
    "v_mfma_f32_32x32x16_bf16 v[96:111], v[0:3], v[200:203], 0\n"
    "s_nop 11\n"
    "v_max_f32 v208, v96, v97\n"
    "v_max3_f32 v208, v98, v99, v208\n"
    "v_max3_f32 v208, v100, v101, v208\n"
    "v_max3_f32 v208, v102, v103, v208\n"
    "v_max3_f32 v208, v104, v105, v208\n"
    "v_max3_f32 v208, v106, v107, v208\n"
    "v_max3_f32 v208, v108, v109, v208\n"
    "v_max3_f32 v208, v110, v111, v208\n"
    "v_mfma_f32_32x32x16_bf16 v[96:111], v[4:7], v[200:203], 0\n"
    "s_nop 11\n"
    "v_max3_f32 v208, v96, v97, v208\n"
    "v_max3_f32 v208, v98, v99, v208\n"
    "v_max3_f32 v208, v100, v101, v208\n"
    "v_max3_f32 v208, v102, v103, v208\n"
    "v_max3_f32 v208, v104, v105, v208\n"
    "v_max3_f32 v208, v106, v107, v208\n"
    "v_max3_f32 v208, v108, v109, v208\n"
    "v_max3_f32 v208, v110, v111, v208\n"
    "v_mfma_f32_32x32x16_bf16 v[96:111], v[8:11], v[200:203], 0\n"
    "s_nop 11\n"
    "v_max3_f32 v208, v96, v97, v208\n"
    "v_max3_f32 v208, v98, v99, v208\n"
    "v_max3_f32 v208, v100, v101, v208\n"
    "v_max3_f32 v208, v102, v103, v208\n"
    "v_max3_f32 v208, v104, v105, v208\n"
    "v_max3_f32 v208, v106, v107, v208\n"
    "v_max3_f32 v208, v108, v109, v208\n"
    "v_max3_f32 v208, v110, v111, v208\n"
    "v_mfma_f32_32x32x16_bf16 v[96:111], v[12:15], v[200:203], 0\n"
    "s_nop 11\n"
    "v_max3_f32 v208, v96, v97, v208\n"
    "v_max3_f32 v208, v98, v99, v208\n"
    "v_max3_f32 v208, v100, v101, v208\n"
    "v_max3_f32 v208, v102, v103, v208\n"
    "v_max3_f32 v208, v104, v105, v208\n"
    "v_max3_f32 v208, v106, v107, v208\n"
    "v_max3_f32 v208, v108, v109, v208\n"
    "v_max3_f32 v208, v110, v111, v208\n"
    "v_mfma_f32_32x32x16_bf16 v[96:111], v[16:19], v[200:203], 0\n"
    "s_nop 11\n"
    "v_max3_f32 v208, v96, v97, v208\n"
    "v_max3_f32 v208, v98, v99, v208\n"
    "v_max3_f32 v208, v100, v101, v208\n"
    "v_max3_f32 v208, v102, v103, v208\n"
    "v_max3_f32 v208, v104, v105, v208\n"
    "v_max3_f32 v208, v106, v107, v208\n"
    "v_max3_f32 v208, v108, v109, v208\n"
    "v_max3_f32 v208, v110, v111, v208\n"
    "v_mfma_f32_32x32x16_bf16 v[96:111], v[20:23], v[200:203], 0\n"
    "s_nop 11\n"
    "v_max3_f32 v208, v96, v97, v208\n"
    "v_max3_f32 v208, v98, v99, v208\n"
    "v_max3_f32 v208, v100, v101, v208\n"
    "v_max3_f32 v208, v102, v103, v208\n"
    "v_max3_f32 v208, v104, v105, v208\n"
    "v_max3_f32 v208, v106, v107, v208\n"
    "v_max3_f32 v208, v108, v109, v208\n"
    "v_max3_f32 v208, v110, v111, v208\n"
    "v_mfma_f32_32x32x16_bf16 v[96:111], v[24:27], v[200:203], 0\n"
    "s_nop 11\n"
    "v_max3_f32 v208, v96, v97, v208\n"
    "v_max3_f32 v208, v98, v99, v208\n"
    "v_max3_f32 v208, v100, v101, v208\n"
    "v_max3_f32 v208, v102, v103, v208\n"
    "v_max3_f32 v208, v104, v105, v208\n"
    "v_max3_f32 v208, v106, v107, v208\n"
    "v_max3_f32 v208, v108, v109, v208\n"
    "v_max3_f32 v208, v110, v111, v208\n"
    "v_mfma_f32_32x32x16_bf16 v[96:111], v[28:31], v[200:203], 0\n"
    "s_nop 11\n"
    "v_max3_f32 v208, v96, v97, v208\n"
    "v_max3_f32 v208, v98, v99, v208\n"
    "v_max3_f32 v208, v100, v101, v208\n"
    "v_max3_f32 v208, v102, v103, v208\n"
    "v_max3_f32 v208, v104, v105, v208\n"
    "v_max3_f32 v208, v106, v107, v208\n"
    "v_max3_f32 v208, v108, v109, v208\n"
    "v_max3_f32 v208, v110, v111, v208\n"
    "v_mov_b32 v209, v208\n"
    "s_nop 1\n"
    "v_permlane32_swap_b32 v208, v209\n"
    "v_max_f32 v208, v208, v209\n"
    "ds_write_b32 v210, v208 offset:3072\n"
    ".Lsq_223:\n"
    "s_xor_b32 s72, s71, 4096\n"
    "buffer_load_dwordx4 v[200:203], v241, s[12:15], s72 offen sc1\n"
    "s_waitcnt vmcnt(0)\n"
    "v_cmp_eq_u32 vcc, -1, v200\n"
    "s_nop 0\n"
    "s_cbranch_vccz .Lok_224\n"
    "s_sleep 1\n"
    "s_sub_u32 s58, s58, 1\n"
    "s_cbranch_scc0 .Lsq_223\n"
    "s_mov_b32 s58, 0\n"
    ".Lok_224:\n"
    "v_mfma_f32_32x32x16_bf16 v[96:111], v[0:3], v[200:203], 0\n"
    "s_nop 11\n"
    "v_max_f32 v208, v96, v97\n"
    "v_max3_f32 v208, v98, v99, v208\n"
    "v_max3_f32 v208, v100, v101, v208\n"
    "v_max3_f32 v208, v102, v103, v208\n"
    "v_max3_f32 v208, v104, v105, v208\n"
    "v_max3_f32 v208, v106, v107, v208\n"
    "v_max3_f32 v208, v108, v109, v208\n"
    "v_max3_f32 v208, v110, v111, v208\n"
    "v_mfma_f32_32x32x16_bf16 v[96:111], v[4:7], v[200:203], 0\n"
    "s_nop 11\n"
    "v_max3_f32 v208, v96, v97, v208\n"
    "v_max3_f32 v208, v98, v99, v208\n"
    "v_max3_f32 v208, v100, v101, v208\n"
    "v_max3_f32 v208, v102, v103, v208\n"
    "v_max3_f32 v208, v104, v105, v208\n"
    "v_max3_f32 v208, v106, v107, v208\n"
    "v_max3_f32 v208, v108, v109, v208\n"
    "v_max3_f32 v208, v110, v111, v208\n"
    "v_mfma_f32_32x32x16_bf16 v[96:111], v[8:11], v[200:203], 0\n"
    "s_nop 11\n"
    "v_max3_f32 v208, v96, v97, v208\n"
    "v_max3_f32 v208, v98, v99, v208\n"
    "v_max3_f32 v208, v100, v101, v208\n"
    "v_max3_f32 v208, v102, v103, v208\n"
    "v_max3_f32 v208, v104, v105, v208\n"
    "v_max3_f32 v208, v106, v107, v208\n"
    "v_max3_f32 v208, v108, v109, v208\n"
    "v_max3_f32 v208, v110, v111, v208\n"
    "v_mfma_f32_32x32x16_bf16 v[96:111], v[12:15], v[200:203], 0\n"
    "s_nop 11\n"
    "v_max3_f32 v208, v96, v97, v208\n"
    "v_max3_f32 v208, v98, v99, v208\n"
    "v_max3_f32 v208, v100, v101, v208\n"
    "v_max3_f32 v208, v102, v103, v208\n"
    "v_max3_f32 v208, v104, v105, v208\n"
    "v_max3_f32 v208, v106, v107, v208\n"
    "v_max3_f32 v208, v108, v109, v208\n"
    "v_max3_f32 v208, v110, v111, v208\n"
    "v_mfma_f32_32x32x16_bf16 v[96:111], v[16:19], v[200:203], 0\n"
    "s_nop 11\n"
    "v_max3_f32 v208, v96, v97, v208\n"
    "v_max3_f32 v208, v98, v99, v208\n"
    "v_max3_f32 v208, v100, v101, v208\n"
    "v_max3_f32 v208, v102, v103, v208\n"
    "v_max3_f32 v208, v104, v105, v208\n"
    "v_max3_f32 v208, v106, v107, v208\n"
    "v_max3_f32 v208, v108, v109, v208\n"
    "v_max3_f32 v208, v110, v111, v208\n"
    "v_mfma_f32_32x32x16_bf16 v[96:111], v[20:23], v[200:203], 0\n"
    "s_nop 11\n"
    "v_max3_f32 v208, v96, v97, v208\n"
    "v_max3_f32 v208, v98, v99, v208\n"
    "v_max3_f32 v208, v100, v101, v208\n"
    "v_max3_f32 v208, v102, v103, v208\n"
    "v_max3_f32 v208, v104, v105, v208\n"
    "v_max3_f32 v208, v106, v107, v208\n"
    "v_max3_f32 v208, v108, v109, v208\n"
    "v_max3_f32 v208, v110, v111, v208\n"
    "v_mfma_f32_32x32x16_bf16 v[96:111], v[24:27], v[200:203], 0\n"
    "s_nop 11\n"
    "v_max3_f32 v208, v96, v97, v208\n"
    "v_max3_f32 v208, v98, v99, v208\n"
    "v_max3_f32 v208, v100, v101, v208\n"
    "v_max3_f32 v208, v102, v103, v208\n"
    "v_max3_f32 v208, v104, v105, v208\n"
    "v_max3_f32 v208, v106, v107, v208\n"
    "v_max3_f32 v208, v108, v109, v208\n"
    "v_max3_f32 v208, v110, v111, v208\n"
    "v_mfma_f32_32x32x16_bf16 v[96:111], v[28:31], v[200:203], 0\n"
    "s_nop 11\n"
    "v_max3_f32 v208, v96, v97, v208\n"
    "v_max3_f32 v208, v98, v99, v208\n"
    "v_max3_f32 v208, v100, v101, v208\n"
    "v_max3_f32 v208, v102, v103, v208\n"
    "v_max3_f32 v208, v104, v105, v208\n"
    "v_max3_f32 v208, v106, v107, v208\n"
    "v_max3_f32 v208, v108, v109, v208\n"
    "v_max3_f32 v208, v110, v111, v208\n"
    "v_mov_b32 v209, v208\n"
    "s_nop 1\n"
    "v_permlane32_swap_b32 v208, v209\n"
    "v_max_f32 v208, v208, v209\n"
    "ds_write_b32 v210, v208 offset:4096\n"
    ".Lsq_225:\n"
    "s_xor_b32 s72, s71, 5120\n"
    "buffer_load_dwordx4 v[200:203], v241, s[12:15], s72 offen sc1\n"
    "s_waitcnt vmcnt(0)\n"
    "v_cmp_eq_u32 vcc, -1, v200\n"
    "s_nop 0\n"
    "s_cbranch_vccz .Lok_226\n"
    "s_sleep 1\n"
    "s_sub_u32 s58, s58, 1\n"
    "s_cbranch_scc0 .Lsq_225\n"
    "s_mov_b32 s58, 0\n"
    ".Lok_226:\n"
    "v_mfma_f32_32x32x16_bf16 v[96:111], v[0:3], v[200:203], 0\n"
    "s_nop 11\n"
    "v_max_f32 v208, v96, v97\n"
    "v_max3_f32 v208, v98, v99, v208\n"
    "v_max3_f32 v208, v100, v101, v208\n"
    "v_max3_f32 v208, v102, v103, v208\n"
    "v_max3_f32 v208, v104, v105, v208\n"
    "v_max3_f32 v208, v106, v107, v208\n"
    "v_max3_f32 v208, v108, v109, v208\n"
    "v_max3_f32 v208, v110, v111, v208\n"
    "v_mfma_f32_32x32x16_bf16 v[96:111], v[4:7], v[200:203], 0\n"
    "s_nop 11\n"
    "v_max3_f32 v208, v96, v97, v208\n"
    "v_max3_f32 v208, v98, v99, v208\n"
    "v_max3_f32 v208, v100, v101, v208\n"
    "v_max3_f32 v208, v102, v103, v208\n"
    "v_max3_f32 v208, v104, v105, v208\n"
    "v_max3_f32 v208, v106, v107, v208\n"
    "v_max3_f32 v208, v108, v109, v208\n"
    "v_max3_f32 v208, v110, v111, v208\n"
    "v_mfma_f32_32x32x16_bf16 v[96:111], v[8:11], v[200:203], 0\n"
    "s_nop 11\n"
    "v_max3_f32 v208, v96, v97, v208\n"
    "v_max3_f32 v208, v98, v99, v208\n"
    "v_max3_f32 v208, v100, v101, v208\n"
    "v_max3_f32 v208, v102, v103, v208\n"
    "v_max3_f32 v208, v104, v105, v208\n"
    "v_max3_f32 v208, v106, v107, v208\n"
    "v_max3_f32 v208, v108, v109, v208\n"
    "v_max3_f32 v208, v110, v111, v208\n"
    "v_mfma_f32_32x32x16_bf16 v[96:111], v[12:15], v[200:203], 0\n"
    "s_nop 11\n"
    "v_max3_f32 v208, v96, v97, v208\n"
    "v_max3_f32 v208, v98, v99, v208\n"
    "v_max3_f32 v208, v100, v101, v208\n"
    "v_max3_f32 v208, v102, v103, v208\n"
    "v_max3_f32 v208, v104, v105, v208\n"
    "v_max3_f32 v208, v106, v107, v208\n"
    "v_max3_f32 v208, v108, v109, v208\n"
    "v_max3_f32 v208, v110, v111, v208\n"
    "v_mfma_f32_32x32x16_bf16 v[96:111], v[16:19], v[200:203], 0\n"
    "s_nop 11\n"
    "v_max3_f32 v208, v96, v97, v208\n"
    "v_max3_f32 v208, v98, v99, v208\n"
    "v_max3_f32 v208, v100, v101, v208\n"
    "v_max3_f32 v208, v102, v103, v208\n"
    "v_max3_f32 v208, v104, v105, v208\n"
    "v_max3_f32 v208, v106, v107, v208\n"
    "v_max3_f32 v208, v108, v109, v208\n"
    "v_max3_f32 v208, v110, v111, v208\n"
    "v_mfma_f32_32x32x16_bf16 v[96:111], v[20:23], v[200:203], 0\n"
    "s_nop 11\n"
    "v_max3_f32 v208, v96, v97, v208\n"
    "v_max3_f32 v208, v98, v99, v208\n"
    "v_max3_f32 v208, v100, v101, v208\n"
    "v_max3_f32 v208, v102, v103, v208\n"
    "v_max3_f32 v208, v104, v105, v208\n"
    "v_max3_f32 v208, v106, v107, v208\n"
    "v_max3_f32 v208, v108, v109, v208\n"
    "v_max3_f32 v208, v110, v111, v208\n"
    "v_mfma_f32_32x32x16_bf16 v[96:111], v[24:27], v[200:203], 0\n"
    "s_nop 11\n"
    "v_max3_f32 v208, v96, v97, v208\n"
    "v_max3_f32 v208, v98, v99, v208\n"
    "v_max3_f32 v208, v100, v101, v208\n"
    "v_max3_f32 v208, v102, v103, v208\n"
    "v_max3_f32 v208, v104, v105, v208\n"
    "v_max3_f32 v208, v106, v107, v208\n"
    "v_max3_f32 v208, v108, v109, v208\n"
    "v_max3_f32 v208, v110, v111, v208\n"
    "v_mfma_f32_32x32x16_bf16 v[96:111], v[28:31], v[200:203], 0\n"
    "s_nop 11\n"
    "v_max3_f32 v208, v96, v97, v208\n"
    "v_max3_f32 v208, v98, v99, v208\n"
    "v_max3_f32 v208, v100, v101, v208\n"
    "v_max3_f32 v208, v102, v103, v208\n"
    "v_max3_f32 v208, v104, v105, v208\n"
    "v_max3_f32 v208, v106, v107, v208\n"
    "v_max3_f32 v208, v108, v109, v208\n"
    "v_max3_f32 v208, v110, v111, v208\n"
    "v_mov_b32 v209, v208\n"
    "s_nop 1\n"
    "v_permlane32_swap_b32 v208, v209\n"
    "v_max_f32 v208, v208, v209\n"
    "ds_write_b32 v210, v208 offset:5120\n"
    ".Lsq_227:\n"
    "s_xor_b32 s72, s71, 6144\n"
    "buffer_load_dwordx4 v[200:203], v241, s[12:15], s72 offen sc1\n"
    "s_waitcnt vmcnt(0)\n"
    "v_cmp_eq_u32 vcc, -1, v200\n"
    "s_nop 0\n"
    "s_cbranch_vccz .Lok_228\n"
    "s_sleep 1\n"
    "s_sub_u32 s58, s58, 1\n"
    "s_cbranch_scc0 .Lsq_227\n"
    "s_mov_b32 s58, 0\n"
    ".Lok_228:\n"
    "v_mfma_f32_32x32x16_bf16 v[96:111], v[0:3], v[200:203], 0\n"
    "s_nop 11\n"
    "v_max_f32 v208, v96, v97\n"
    "v_max3_f32 v208, v98, v99, v208\n"
    "v_max3_f32 v208, v100, v101, v208\n"
    "v_max3_f32 v208, v102, v103, v208\n"
    "v_max3_f32 v208, v104, v105, v208\n"
    "v_max3_f32 v208, v106, v107, v208\n"
    "v_max3_f32 v208, v108, v109, v208\n"
    "v_max3_f32 v208, v110, v111, v208\n"
    "v_mfma_f32_32x32x16_bf16 v[96:111], v[4:7], v[200:203], 0\n"
    "s_nop 11\n"
    "v_max3_f32 v208, v96, v97, v208\n"
    "v_max3_f32 v208, v98, v99, v208\n"
    "v_max3_f32 v208, v100, v101, v208\n"
    "v_max3_f32 v208, v102, v103, v208\n"
    "v_max3_f32 v208, v104, v105, v208\n"
    "v_max3_f32 v208, v106, v107, v208\n"
    "v_max3_f32 v208, v108, v109, v208\n"
    "v_max3_f32 v208, v110, v111, v208\n"
    "v_mfma_f32_32x32x16_bf16 v[96:111], v[8:11], v[200:203], 0\n"
    "s_nop 11\n"
    "v_max3_f32 v208, v96, v97, v208\n"
    "v_max3_f32 v208, v98, v99, v208\n"
    "v_max3_f32 v208, v100, v101, v208\n"
    "v_max3_f32 v208, v102, v103, v208\n"
    "v_max3_f32 v208, v104, v105, v208\n"
    "v_max3_f32 v208, v106, v107, v208\n"
    "v_max3_f32 v208, v108, v109, v208\n"
    "v_max3_f32 v208, v110, v111, v208\n"
    "v_mfma_f32_32x32x16_bf16 v[96:111], v[12:15], v[200:203], 0\n"
    "s_nop 11\n"
    "v_max3_f32 v208, v96, v97, v208\n"
    "v_max3_f32 v208, v98, v99, v208\n"
    "v_max3_f32 v208, v100, v101, v208\n"
    "v_max3_f32 v208, v102, v103, v208\n"
    "v_max3_f32 v208, v104, v105, v208\n"
    "v_max3_f32 v208, v106, v107, v208\n"
    "v_max3_f32 v208, v108, v109, v208\n"
    "v_max3_f32 v208, v110, v111, v208\n"
    "v_mfma_f32_32x32x16_bf16 v[96:111], v[16:19], v[200:203], 0\n"
    "s_nop 11\n"
    "v_max3_f32 v208, v96, v97, v208\n"
    "v_max3_f32 v208, v98, v99, v208\n"
    "v_max3_f32 v208, v100, v101, v208\n"
    "v_max3_f32 v208, v102, v103, v208\n"
    "v_max3_f32 v208, v104, v105, v208\n"
    "v_max3_f32 v208, v106, v107, v208\n"
    "v_max3_f32 v208, v108, v109, v208\n"
    "v_max3_f32 v208, v110, v111, v208\n"
    "v_mfma_f32_32x32x16_bf16 v[96:111], v[20:23], v[200:203], 0\n"
    "s_nop 11\n"
    "v_max3_f32 v208, v96, v97, v208\n"
    "v_max3_f32 v208, v98, v99, v208\n"
    "v_max3_f32 v208, v100, v101, v208\n"
    "v_max3_f32 v208, v102, v103, v208\n"
    "v_max3_f32 v208, v104, v105, v208\n"
    "v_max3_f32 v208, v106, v107, v208\n"
    "v_max3_f32 v208, v108, v109, v208\n"
    "v_max3_f32 v208, v110, v111, v208\n"
    "v_mfma_f32_32x32x16_bf16 v[96:111], v[24:27], v[200:203], 0\n"
    "s_nop 11\n"
    "v_max3_f32 v208, v96, v97, v208\n"
    "v_max3_f32 v208, v98, v99, v208\n"
    "v_max3_f32 v208, v100, v101, v208\n"
    "v_max3_f32 v208, v102, v103, v208\n"
    "v_max3_f32 v208, v104, v105, v208\n"
    "v_max3_f32 v208, v106, v107, v208\n"
    "v_max3_f32 v208, v108, v109, v208\n"
    "v_max3_f32 v208, v110, v111, v208\n"
    "v_mfma_f32_32x32x16_bf16 v[96:111], v[28:31], v[200:203], 0\n"
    "s_nop 11\n"
    "v_max3_f32 v208, v96, v97, v208\n"
    "v_max3_f32 v208, v98, v99, v208\n"
    "v_max3_f32 v208, v100, v101, v208\n"
    "v_max3_f32 v208, v102, v103, v208\n"
    "v_max3_f32 v208, v104, v105, v208\n"
    "v_max3_f32 v208, v106, v107, v208\n"
    "v_max3_f32 v208, v108, v109, v208\n"
    "v_max3_f32 v208, v110, v111, v208\n"
    "v_mov_b32 v209, v208\n"
    "s_nop 1\n"
    "v_permlane32_swap_b32 v208, v209\n"
    "v_max_f32 v208, v208, v209\n"
    "ds_write_b32 v210, v208 offset:6144\n"
    ".Lsq_229:\n"
    "s_xor_b32 s72, s71, 7168\n"
    "buffer_load_dwordx4 v[200:203], v241, s[12:15], s72 offen sc1\n"
    "s_waitcnt vmcnt(0)\n"
    "v_cmp_eq_u32 vcc, -1, v200\n"
    "s_nop 0\n"
    "s_cbranch_vccz .Lok_230\n"
    "s_sleep 1\n"
    "s_sub_u32 s58, s58, 1\n"
    "s_cbranch_scc0 .Lsq_229\n"
    "s_mov_b32 s58, 0\n"
    ".Lok_230:\n"
    "v_mfma_f32_32x32x16_bf16 v[96:111], v[0:3], v[200:203], 0\n"
    "s_nop 11\n"
    "v_max_f32 v208, v96, v97\n"
    "v_max3_f32 v208, v98, v99, v208\n"
    "v_max3_f32 v208, v100, v101, v208\n"
    "v_max3_f32 v208, v102, v103, v208\n"
    "v_max3_f32 v208, v104, v105, v208\n"
    "v_max3_f32 v208, v106, v107, v208\n"
    "v_max3_f32 v208, v108, v109, v208\n"
    "v_max3_f32 v208, v110, v111, v208\n"
    "v_mfma_f32_32x32x16_bf16 v[96:111], v[4:7], v[200:203], 0\n"
    "s_nop 11\n"
    "v_max3_f32 v208, v96, v97, v208\n"
    "v_max3_f32 v208, v98, v99, v208\n"
    "v_max3_f32 v208, v100, v101, v208\n"
    "v_max3_f32 v208, v102, v103, v208\n"
    "v_max3_f32 v208, v104, v105, v208\n"
    "v_max3_f32 v208, v106, v107, v208\n"
    "v_max3_f32 v208, v108, v109, v208\n"
    "v_max3_f32 v208, v110, v111, v208\n"
    "v_mfma_f32_32x32x16_bf16 v[96:111], v[8:11], v[200:203], 0\n"
    "s_nop 11\n"
    "v_max3_f32 v208, v96, v97, v208\n"
    "v_max3_f32 v208, v98, v99, v208\n"
    "v_max3_f32 v208, v100, v101, v208\n"
    "v_max3_f32 v208, v102, v103, v208\n"
    "v_max3_f32 v208, v104, v105, v208\n"
    "v_max3_f32 v208, v106, v107, v208\n"
    "v_max3_f32 v208, v108, v109, v208\n"
    "v_max3_f32 v208, v110, v111, v208\n"
    "v_mfma_f32_32x32x16_bf16 v[96:111], v[12:15], v[200:203], 0\n"
    "s_nop 11\n"
    "v_max3_f32 v208, v96, v97, v208\n"
    "v_max3_f32 v208, v98, v99, v208\n"
    "v_max3_f32 v208, v100, v101, v208\n"
    "v_max3_f32 v208, v102, v103, v208\n"
    "v_max3_f32 v208, v104, v105, v208\n"
    "v_max3_f32 v208, v106, v107, v208\n"
    "v_max3_f32 v208, v108, v109, v208\n"
    "v_max3_f32 v208, v110, v111, v208\n"
    "v_mfma_f32_32x32x16_bf16 v[96:111], v[16:19], v[200:203], 0\n"
    "s_nop 11\n"
    "v_max3_f32 v208, v96, v97, v208\n"
    "v_max3_f32 v208, v98, v99, v208\n"
    "v_max3_f32 v208, v100, v101, v208\n"
    "v_max3_f32 v208, v102, v103, v208\n"
    "v_max3_f32 v208, v104, v105, v208\n"
    "v_max3_f32 v208, v106, v107, v208\n"
    "v_max3_f32 v208, v108, v109, v208\n"
    "v_max3_f32 v208, v110, v111, v208\n"
    "v_mfma_f32_32x32x16_bf16 v[96:111], v[20:23], v[200:203], 0\n"
    "s_nop 11\n"
    "v_max3_f32 v208, v96, v97, v208\n"
    "v_max3_f32 v208, v98, v99, v208\n"
    "v_max3_f32 v208, v100, v101, v208\n"
    "v_max3_f32 v208, v102, v103, v208\n"
    "v_max3_f32 v208, v104, v105, v208\n"
    "v_max3_f32 v208, v106, v107, v208\n"
    "v_max3_f32 v208, v108, v109, v208\n"
    "v_max3_f32 v208, v110, v111, v208\n"
    "v_mfma_f32_32x32x16_bf16 v[96:111], v[24:27], v[200:203], 0\n"
    "s_nop 11\n"
    "v_max3_f32 v208, v96, v97, v208\n"
    "v_max3_f32 v208, v98, v99, v208\n"
    "v_max3_f32 v208, v100, v101, v208\n"
    "v_max3_f32 v208, v102, v103, v208\n"
    "v_max3_f32 v208, v104, v105, v208\n"
    "v_max3_f32 v208, v106, v107, v208\n"
    "v_max3_f32 v208, v108, v109, v208\n"
    "v_max3_f32 v208, v110, v111, v208\n"
    "v_mfma_f32_32x32x16_bf16 v[96:111], v[28:31], v[200:203], 0\n"
    "s_nop 11\n"
    "v_max3_f32 v208, v96, v97, v208\n"
    "v_max3_f32 v208, v98, v99, v208\n"
    "v_max3_f32 v208, v100, v101, v208\n"
    "v_max3_f32 v208, v102, v103, v208\n"
    "v_max3_f32 v208, v104, v105, v208\n"
    "v_max3_f32 v208, v106, v107, v208\n"
    "v_max3_f32 v208, v108, v109, v208\n"
    "v_max3_f32 v208, v110, v111, v208\n"
    "v_mov_b32 v209, v208\n"
    "s_nop 1\n"
    "v_permlane32_swap_b32 v208, v209\n"
    "v_max_f32 v208, v208, v209\n"
    "ds_write_b32 v210, v208 offset:7168\n"
    "s_xor_b32 s72, s71, 0\n"
    "buffer_load_dwordx4 v[144:147], v241, s[12:15], s72 offen sc1\n"
    "s_xor_b32 s72, s71, 1024\n"
    "buffer_load_dwordx4 v[148:151], v241, s[12:15], s72 offen sc1\n"
    "s_waitcnt lgkmcnt(0)\n"
    "s_barrier\n"
    "s_lshl_b32 s62, s48, 10\n"
    "s_add_u32 s62, s62, 114688\n"
    "v_add_u32 v211, s62, v211\n"
    "ds_read_b128 v[200:203], v211\n"
    "ds_read_b128 v[204:207], v211 offset:16\n"
    "s_waitcnt lgkmcnt(1)\n"
    "v_max3_f32 v208, v200, v201, v202\n"
    "s_waitcnt lgkmcnt(0)\n"
    "v_max3_f32 v208, v203, v204, v208\n"
    "v_max3_f32 v208, v205, v206, v208\n"
    "v_max_f32 v208, v207, v208\n"
    "ds_write_b32 v210, v208 offset:46080\n"
    "s_waitcnt lgkmcnt(0)\n"
    "s_barrier\n"
    "s_cmp_ge_u32 s48, 4\n"
    "s_cbranch_scc1 .LslowB\n"
    "s_and_b32 s62, s60, 4\n"
    "s_xor_b32 s62, s62, s48\n"
    "s_lshl_b32 s62, s62, 2\n"
    "s_and_b32 s63, s60, 0\n"
    "s_or_b32 s62, s62, s63\n"
    "s_lshl_b32 s62, s62, 12\n"
    "s_add_u32 s70, s62, 131072\n"
    "s_xor_b32 s80, s70, 4096\n"
    "s_xor_b32 s81, s70, 8192\n"
    "s_xor_b32 s82, s70, 12288\n"
    "s_lshl_b32 s71, s55, 13\n"
    "s_lshl_b32 s62, s50, 10\n"
    "s_add_u32 s71, s71, s62\n"
    "s_add_u32 s71, s71, 1048576\n"
    "s_lshl_b32 s83, s48, 13\n"
    "v_add_u32 v249, s83, v241\n"
    "s_mov_b32 s88, 0xffff\n"
    "s_mov_b32 s89, 0xffff\n"
    "v_mov_b32 v232, 0xdddd\n"
    "v_bfe_u32 v200, v240, 3, 1\n"
    "v_mul_u32_u24 v200, 0xaaaa, v200\n"
    "v_add_u32 v233, 0x4444, v200\n"
    "v_add_u32 v247, 150528, v241\n"
    "s_mul_i32 s62, s51, 2304\n"
    "s_add_u32 s62, s62, 131072\n"
    "v_add_u32 v242, s62, v241\n"
    "s_add_u32 s62, s62, 2048\n"
    "v_lshl_add_u32 v243, v240, 2, s62\n"
    "v_and_b32 v210, 7, v240\n"
    "v_lshlrev_b32 v210, 7, v210\n"
    "s_lshl_b32 s84, s49, 10\n"
    "s_add_u32 s84, s84, 81984\n"
    "v_and_b32 v200, 31, v240\n"
    "s_lshr_b32 s62, s51, 1\n"
    "s_lshl_b32 s62, s62, 10\n"
    "s_and_b32 s63, s51, 1\n"
    "s_lshl3_add_u32 s62, s63, s62\n"
    "s_add_u32 s62, s62, 114688\n"
    "v_add_u32 v244, s62, v241\n"
    "v_lshlrev_b32 v200, 4, v200\n"
    "v_add_u32 v245, 149504, v200\n"
    "v_bfe_u32 v200, v240, 3, 2\n"
    "v_lshrrev_b32 v201, 5, v240\n"
    "v_lshl_or_b32 v200, v200, 1, v201\n"
    "v_and_b32 v201, 7, v240\n"
    "v_lshl_or_b32 v200, v200, 3, v201\n"
    "s_lshl2_add_u32 s62, s51, 150528\n"
    "v_lshl_add_u32 v246, v200, 4, s62\n"
    "v_and_b32 v248, 31, v240\n"
    "v_lshlrev_b32 v248, 5, v248\n"
    "v_add_u32 v248, 160768, v248\n"
    "ds_read_b32 v248, v248\n"
    "s_waitcnt vmcnt(1)\n"
    "v_mfma_f32_32x32x16_bf16 v[96:111], v[0:3], v[144:147], 0\n"
    "s_waitcnt lgkmcnt(0)\n"
    "s_nop 10\n"
    "v_sub_f32 v96, v96, v248\n"
    "v_exp_f32 v96, v96\n"
    "v_sub_f32 v97, v97, v248\n"
    "v_exp_f32 v97, v97\n"
    "v_sub_f32 v98, v98, v248\n"
    "v_exp_f32 v98, v98\n"
    "v_sub_f32 v99, v99, v248\n"
    "v_exp_f32 v99, v99\n"
    "v_sub_f32 v100, v100, v248\n"
    "v_exp_f32 v100, v100\n"
    "v_mfma_f32_32x32x16_bf16 v[112:127], v[4:7], v[144:147], 0\n"
    "v_sub_f32 v101, v101, v248\n"
    "v_exp_f32 v101, v101\n"
    "v_sub_f32 v102, v102, v248\n"
    "v_exp_f32 v102, v102\n"
    "v_sub_f32 v103, v103, v248\n"
    "v_exp_f32 v103, v103\n"
    "v_sub_f32 v104, v104, v248\n"
    "v_exp_f32 v104, v104\n"
    "v_sub_f32 v105, v105, v248\n"
    "v_exp_f32 v105, v105\n"
    "v_smfmac_f32_32x32x32_bf16 v[128:143], v[32:35], v[96:103], v232\n"
    "v_sub_f32 v106, v106, v248\n"
    "v_exp_f32 v106, v106\n"
    "v_sub_f32 v107, v107, v248\n"
    "v_exp_f32 v107, v107\n"
    "v_sub_f32 v108, v108, v248\n"
    "v_exp_f32 v108, v108\n"
    "v_sub_f32 v109, v109, v248\n"
    "v_exp_f32 v109, v109\n"
    "v_sub_f32 v110, v110, v248\n"
    "v_exp_f32 v110, v110\n"
    "v_sub_f32 v111, v111, v248\n"
    "v_exp_f32 v111, v111\n"
    "v_sub_f32 v112, v112, v248\n"
    "v_exp_f32 v112, v112\n"
    "v_sub_f32 v113, v113, v248\n"
    "v_exp_f32 v113, v113\n"
    "v_smfmac_f32_32x32x32_bf16 v[128:143], v[36:39], v[104:111], v232\n"
    "v_sub_f32 v114, v114, v248\n"
    "v_exp_f32 v114, v114\n"
    "v_sub_f32 v115, v115, v248\n"
    "v_exp_f32 v115, v115\n"
    "v_sub_f32 v116, v116, v248\n"
    "v_exp_f32 v116, v116\n"
    "v_mfma_f32_32x32x16_bf16 v[96:111], v[8:11], v[144:147], 0\n"
    "v_sub_f32 v117, v117, v248\n"
    "v_exp_f32 v117, v117\n"
    "v_sub_f32 v118, v118, v248\n"
    "v_exp_f32 v118, v118\n"
    "v_sub_f32 v119, v119, v248\n"
    "v_exp_f32 v119, v119\n"
    "v_sub_f32 v120, v120, v248\n"
    "v_exp_f32 v120, v120\n"
    "v_sub_f32 v121, v121, v248\n"
    "v_exp_f32 v121, v121\n"
    "v_smfmac_f32_32x32x32_bf16 v[128:143], v[40:43], v[112:119], v232\n"
    "v_sub_f32 v122, v122, v248\n"
    "v_exp_f32 v122, v122\n"
    "v_sub_f32 v123, v123, v248\n"
    "v_exp_f32 v123, v123\n"
    "v_sub_f32 v124, v124, v248\n"
    "v_exp_f32 v124, v124\n"
    "v_sub_f32 v125, v125, v248\n"
    "v_exp_f32 v125, v125\n"
    "v_sub_f32 v126, v126, v248\n"
    "v_exp_f32 v126, v126\n"
    "v_sub_f32 v127, v127, v248\n"
    "v_exp_f32 v127, v127\n"
    "v_sub_f32 v96, v96, v248\n"
    "v_exp_f32 v96, v96\n"
    "v_sub_f32 v97, v97, v248\n"
    "v_exp_f32 v97, v97\n"
    "v_smfmac_f32_32x32x32_bf16 v[128:143], v[44:47], v[120:127], v232\n"
    "v_sub_f32 v98, v98, v248\n"
    "v_exp_f32 v98, v98\n"
    "v_sub_f32 v99, v99, v248\n"
    "v_exp_f32 v99, v99\n"
    "v_sub_f32 v100, v100, v248\n"
    "v_exp_f32 v100, v100\n"
    "v_mfma_f32_32x32x16_bf16 v[112:127], v[12:15], v[144:147], 0\n"
    "v_sub_f32 v101, v101, v248\n"
    "v_exp_f32 v101, v101\n"
    "v_sub_f32 v102, v102, v248\n"
    "v_exp_f32 v102, v102\n"
    "v_sub_f32 v103, v103, v248\n"
    "v_exp_f32 v103, v103\n"
    "v_sub_f32 v104, v104, v248\n"
    "v_exp_f32 v104, v104\n"
    "v_sub_f32 v105, v105, v248\n"
    "v_exp_f32 v105, v105\n"
    "v_smfmac_f32_32x32x32_bf16 v[128:143], v[48:51], v[96:103], v232\n"
    "v_sub_f32 v106, v106, v248\n"
    "v_exp_f32 v106, v106\n"
    "v_sub_f32 v107, v107, v248\n"
    "v_exp_f32 v107, v107\n"
    "v_sub_f32 v108, v108, v248\n"
    "v_exp_f32 v108, v108\n"
    "v_sub_f32 v109, v109, v248\n"
    "v_exp_f32 v109, v109\n"
    "v_sub_f32 v110, v110, v248\n"
    "v_exp_f32 v110, v110\n"
    "v_sub_f32 v111, v111, v248\n"
    "v_exp_f32 v111, v111\n"
    "v_sub_f32 v112, v112, v248\n"
    "v_exp_f32 v112, v112\n"
    "v_sub_f32 v113, v113, v248\n"
    "v_exp_f32 v113, v113\n"
    "v_smfmac_f32_32x32x32_bf16 v[128:143], v[52:55], v[104:111], v232\n"
    "v_sub_f32 v114, v114, v248\n"
    "v_exp_f32 v114, v114\n"
    "v_sub_f32 v115, v115, v248\n"
    "v_exp_f32 v115, v115\n"
    "v_sub_f32 v116, v116, v248\n"
    "v_exp_f32 v116, v116\n"
    "v_mfma_f32_32x32x16_bf16 v[96:111], v[16:19], v[144:147], 0\n"
    "v_sub_f32 v117, v117, v248\n"
    "v_exp_f32 v117, v117\n"
    "v_sub_f32 v118, v118, v248\n"
    "v_exp_f32 v118, v118\n"
    "v_sub_f32 v119, v119, v248\n"
    "v_exp_f32 v119, v119\n"
    "v_sub_f32 v120, v120, v248\n"
    "v_exp_f32 v120, v120\n"
    "v_sub_f32 v121, v121, v248\n"
    "v_exp_f32 v121, v121\n"
    "v_smfmac_f32_32x32x32_bf16 v[128:143], v[56:59], v[112:119], v232\n"
    "v_sub_f32 v122, v122, v248\n"
    "v_exp_f32 v122, v122\n"
    "v_sub_f32 v123, v123, v248\n"
    "v_exp_f32 v123, v123\n"
    "v_sub_f32 v124, v124, v248\n"
    "v_exp_f32 v124, v124\n"
    "v_sub_f32 v125, v125, v248\n"
    "v_exp_f32 v125, v125\n"
    "v_sub_f32 v126, v126, v248\n"
    "v_exp_f32 v126, v126\n"
    "v_sub_f32 v127, v127, v248\n"
    "v_exp_f32 v127, v127\n"
    "v_sub_f32 v96, v96, v248\n"
    "v_exp_f32 v96, v96\n"
    "v_sub_f32 v97, v97, v248\n"
    "v_exp_f32 v97, v97\n"
    "v_smfmac_f32_32x32x32_bf16 v[128:143], v[60:63], v[120:127], v232\n"
    "v_sub_f32 v98, v98, v248\n"
    "v_exp_f32 v98, v98\n"
    "v_sub_f32 v99, v99, v248\n"
    "v_exp_f32 v99, v99\n"
    "v_sub_f32 v100, v100, v248\n"
    "v_exp_f32 v100, v100\n"
    "v_mfma_f32_32x32x16_bf16 v[112:127], v[20:23], v[144:147], 0\n"
    "v_sub_f32 v101, v101, v248\n"
    "v_exp_f32 v101, v101\n"
    "v_sub_f32 v102, v102, v248\n"
    "v_exp_f32 v102, v102\n"
    "v_sub_f32 v103, v103, v248\n"
    "v_exp_f32 v103, v103\n"
    "v_sub_f32 v104, v104, v248\n"
    "v_exp_f32 v104, v104\n"
    "v_sub_f32 v105, v105, v248\n"
    "v_exp_f32 v105, v105\n"
    "v_smfmac_f32_32x32x32_bf16 v[128:143], v[64:67], v[96:103], v232\n"
    "v_sub_f32 v106, v106, v248\n"
    "v_exp_f32 v106, v106\n"
    "v_sub_f32 v107, v107, v248\n"
    "v_exp_f32 v107, v107\n"
    "v_sub_f32 v108, v108, v248\n"
    "v_exp_f32 v108, v108\n"
    "v_sub_f32 v109, v109, v248\n"
    "v_exp_f32 v109, v109\n"
    "v_sub_f32 v110, v110, v248\n"
    "v_exp_f32 v110, v110\n"
    "v_sub_f32 v111, v111, v248\n"
    "v_exp_f32 v111, v111\n"
    "v_sub_f32 v112, v112, v248\n"
    "v_exp_f32 v112, v112\n"
    "v_sub_f32 v113, v113, v248\n"
    "v_exp_f32 v113, v113\n"
    "v_smfmac_f32_32x32x32_bf16 v[128:143], v[68:71], v[104:111], v232\n"
    "v_sub_f32 v114, v114, v248\n"
    "v_exp_f32 v114, v114\n"
    "v_sub_f32 v115, v115, v248\n"
    "v_exp_f32 v115, v115\n"
    "v_sub_f32 v116, v116, v248\n"
    "v_exp_f32 v116, v116\n"
    "v_mfma_f32_32x32x16_bf16 v[96:111], v[24:27], v[144:147], 0\n"
    "v_sub_f32 v117, v117, v248\n"
    "v_exp_f32 v117, v117\n"
    "v_sub_f32 v118, v118, v248\n"
    "v_exp_f32 v118, v118\n"
    "v_sub_f32 v119, v119, v248\n"
    "v_exp_f32 v119, v119\n"
    "v_sub_f32 v120, v120, v248\n"
    "v_exp_f32 v120, v120\n"
    "v_sub_f32 v121, v121, v248\n"
    "v_exp_f32 v121, v121\n"
    "v_smfmac_f32_32x32x32_bf16 v[128:143], v[72:75], v[112:119], v232\n"
    "v_sub_f32 v122, v122, v248\n"
    "v_exp_f32 v122, v122\n"
    "v_sub_f32 v123, v123, v248\n"
    "v_exp_f32 v123, v123\n"
    "v_sub_f32 v124, v124, v248\n"
    "v_exp_f32 v124, v124\n"
    "v_sub_f32 v125, v125, v248\n"
    "v_exp_f32 v125, v125\n"
    "v_sub_f32 v126, v126, v248\n"
    "v_exp_f32 v126, v126\n"
    "v_sub_f32 v127, v127, v248\n"
    "v_exp_f32 v127, v127\n"
    "v_sub_f32 v96, v96, v248\n"
    "v_exp_f32 v96, v96\n"
    "v_sub_f32 v97, v97, v248\n"
    "v_exp_f32 v97, v97\n"
    "v_smfmac_f32_32x32x32_bf16 v[128:143], v[76:79], v[120:127], v232\n"
    "v_sub_f32 v98, v98, v248\n"
    "v_exp_f32 v98, v98\n"
    "v_sub_f32 v99, v99, v248\n"
    "v_exp_f32 v99, v99\n"
    "v_sub_f32 v100, v100, v248\n"
    "v_exp_f32 v100, v100\n"
    "v_mfma_f32_32x32x16_bf16 v[112:127], v[28:31], v[144:147], 0\n"
    "v_sub_f32 v101, v101, v248\n"
    "v_exp_f32 v101, v101\n"
    "v_sub_f32 v102, v102, v248\n"
    "v_exp_f32 v102, v102\n"
    "v_sub_f32 v103, v103, v248\n"
    "v_exp_f32 v103, v103\n"
    "v_sub_f32 v104, v104, v248\n"
    "v_exp_f32 v104, v104\n"
    "v_sub_f32 v105, v105, v248\n"
    "v_exp_f32 v105, v105\n"
    "v_smfmac_f32_32x32x32_bf16 v[128:143], v[80:83], v[96:103], v232\n"
    "v_sub_f32 v106, v106, v248\n"
    "v_exp_f32 v106, v106\n"
    "v_sub_f32 v107, v107, v248\n"
    "v_exp_f32 v107, v107\n"
    "v_sub_f32 v108, v108, v248\n"
    "v_exp_f32 v108, v108\n"
    "v_sub_f32 v109, v109, v248\n"
    "v_exp_f32 v109, v109\n"
    "v_sub_f32 v110, v110, v248\n"
    "v_exp_f32 v110, v110\n"
    "v_sub_f32 v111, v111, v248\n"
    "v_exp_f32 v111, v111\n"
    "v_sub_f32 v112, v112, v248\n"
    "v_exp_f32 v112, v112\n"
    "v_sub_f32 v113, v113, v248\n"
    "v_exp_f32 v113, v113\n"
    "v_smfmac_f32_32x32x32_bf16 v[128:143], v[84:87], v[104:111], v232\n"
    "v_sub_f32 v114, v114, v248\n"
    "v_exp_f32 v114, v114\n"
    "v_sub_f32 v115, v115, v248\n"
    "v_exp_f32 v115, v115\n"
    "v_sub_f32 v116, v116, v248\n"
    "v_exp_f32 v116, v116\n"
    "s_waitcnt vmcnt(0)\n"
    "v_mfma_f32_32x32x16_bf16 v[96:111], v[0:3], v[148:151], 0\n"
    "v_sub_f32 v117, v117, v248\n"
    "v_exp_f32 v117, v117\n"
    "v_sub_f32 v118, v118, v248\n"
    "v_exp_f32 v118, v118\n"
    "v_sub_f32 v119, v119, v248\n"
    "v_exp_f32 v119, v119\n"
    "v_sub_f32 v120, v120, v248\n"
    "v_exp_f32 v120, v120\n"
    "v_sub_f32 v121, v121, v248\n"
    "v_exp_f32 v121, v121\n"
    "v_smfmac_f32_32x32x32_bf16 v[128:143], v[88:91], v[112:119], v232\n"
    "v_sub_f32 v122, v122, v248\n"
    "v_exp_f32 v122, v122\n"
    "v_sub_f32 v123, v123, v248\n"
    "v_exp_f32 v123, v123\n"
    "v_sub_f32 v124, v124, v248\n"
    "v_exp_f32 v124, v124\n"
    "v_sub_f32 v125, v125, v248\n"
    "v_exp_f32 v125, v125\n"
    "v_sub_f32 v126, v126, v248\n"
    "v_exp_f32 v126, v126\n"
    "v_sub_f32 v127, v127, v248\n"
    "v_exp_f32 v127, v127\n"
    "s_setprio 0\n"
    "v_and_b32 v248, 31, v240\n"
    "v_lshlrev_b32 v248, 5, v248\n"
    "v_add_u32 v248, 160772, v248\n"
    "ds_read_b32 v248, v248\n"
    "s_waitcnt lgkmcnt(0)\n"
    "v_sub_f32 v96, v96, v248\n"
    "v_exp_f32 v96, v96\n"
    "v_sub_f32 v97, v97, v248\n"
    "v_exp_f32 v97, v97\n"
    "v_smfmac_f32_32x32x32_bf16 v[128:143], v[92:95], v[120:127], v232\n"
    "s_xor_b32 s72, s71, 2048\n"
    "buffer_load_dwordx4 v[144:147], v241, s[12:15], s72 offen sc1\n"
    "v_sub_f32 v98, v98, v248\n"
    "v_exp_f32 v98, v98\n"
    "v_sub_f32 v99, v99, v248\n"
    "v_exp_f32 v99, v99\n"
    "v_sub_f32 v100, v100, v248\n"
    "v_exp_f32 v100, v100\n"
    "v_mfma_f32_32x32x16_bf16 v[112:127], v[4:7], v[148:151], 0\n"
    "v_sub_f32 v101, v101, v248\n"
    "v_exp_f32 v101, v101\n"
    "v_sub_f32 v102, v102, v248\n"
    "v_exp_f32 v102, v102\n"
    "v_sub_f32 v103, v103, v248\n"
    "v_exp_f32 v103, v103\n"
    "v_sub_f32 v104, v104, v248\n"
    "v_exp_f32 v104, v104\n"
    "v_sub_f32 v105, v105, v248\n"
    "v_exp_f32 v105, v105\n"
    "v_sub_f32 v106, v106, v248\n"
    "v_exp_f32 v106, v106\n"
    "v_sub_f32 v107, v107, v248\n"
    "v_exp_f32 v107, v107\n"
    "v_sub_f32 v108, v108, v248\n"
    "v_exp_f32 v108, v108\n"
    "ds_write_b128 v242, v[128:131] offset:0\n"
    "ds_write_b128 v242, v[132:135] offset:1024\n"
    "ds_write_b32 v243, v136 offset:0\n"
    "v_mfma_f32_32x32x8_bf16 v[128:143], v[234:235], v[234:235], 0\n"
    "v_sub_f32 v109, v109, v248\n"
    "v_exp_f32 v109, v109\n"
    "v_sub_f32 v110, v110, v248\n"
    "v_exp_f32 v110, v110\n"
    "v_sub_f32 v111, v111, v248\n"
    "v_exp_f32 v111, v111\n"
    "v_smfmac_f32_32x32x32_bf16 v[128:143], v[32:35], v[96:103], v232\n"
    "v_sub_f32 v112, v112, v248\n"
    "v_exp_f32 v112, v112\n"
    "v_sub_f32 v113, v113, v248\n"
    "v_exp_f32 v113, v113\n"
    "v_smfmac_f32_32x32x32_bf16 v[128:143], v[36:39], v[104:111], v232\n"
    "v_sub_f32 v114, v114, v248\n"
    "v_exp_f32 v114, v114\n"
    "v_sub_f32 v115, v115, v248\n"
    "v_exp_f32 v115, v115\n"
    "v_sub_f32 v116, v116, v248\n"
    "v_exp_f32 v116, v116\n"
    "v_mfma_f32_32x32x16_bf16 v[96:111], v[8:11], v[148:151], 0\n"
    "v_sub_f32 v117, v117, v248\n"
    "v_exp_f32 v117, v117\n"
    "v_sub_f32 v118, v118, v248\n"
    "v_exp_f32 v118, v118\n"
    "v_sub_f32 v119, v119, v248\n"
    "v_exp_f32 v119, v119\n"
    "v_sub_f32 v120, v120, v248\n"
    "v_exp_f32 v120, v120\n"
    "v_sub_f32 v121, v121, v248\n"
    "v_exp_f32 v121, v121\n"
    "v_smfmac_f32_32x32x32_bf16 v[128:143], v[40:43], v[112:119], v232\n"
    "v_sub_f32 v122, v122, v248\n"
    "v_exp_f32 v122, v122\n"
    "v_sub_f32 v123, v123, v248\n"
    "v_exp_f32 v123, v123\n"
    "v_sub_f32 v124, v124, v248\n"
    "v_exp_f32 v124, v124\n"
    "v_sub_f32 v125, v125, v248\n"
    "v_exp_f32 v125, v125\n"
    "v_sub_f32 v126, v126, v248\n"
    "v_exp_f32 v126, v126\n"
    "v_sub_f32 v127, v127, v248\n"
    "v_exp_f32 v127, v127\n"
    "s_waitcnt lgkmcnt(0)\n"
    "s_barrier\n"
    "s_setprio 2\n"
    "v_sub_f32 v96, v96, v248\n"
    "v_exp_f32 v96, v96\n"
    "v_sub_f32 v97, v97, v248\n"
    "v_exp_f32 v97, v97\n"
    "v_smfmac_f32_32x32x32_bf16 v[128:143], v[44:47], v[120:127], v232\n"
    "v_sub_f32 v98, v98, v248\n"
    "v_exp_f32 v98, v98\n"
    "v_sub_f32 v99, v99, v248\n"
    "v_exp_f32 v99, v99\n"
    "v_sub_f32 v100, v100, v248\n"
    "v_exp_f32 v100, v100\n"
    "v_mfma_f32_32x32x16_bf16 v[112:127], v[12:15], v[148:151], 0\n"
    "v_sub_f32 v101, v101, v248\n"
    "v_exp_f32 v101, v101\n"
    "v_sub_f32 v102, v102, v248\n"
    "v_exp_f32 v102, v102\n"
    "v_sub_f32 v103, v103, v248\n"
    "v_exp_f32 v103, v103\n"
    "v_sub_f32 v104, v104, v248\n"
    "v_exp_f32 v104, v104\n"
    "v_sub_f32 v105, v105, v248\n"
    "v_exp_f32 v105, v105\n"
    "v_smfmac_f32_32x32x32_bf16 v[128:143], v[48:51], v[96:103], v232\n"
    "v_sub_f32 v106, v106, v248\n"
    "v_exp_f32 v106, v106\n"
    "v_sub_f32 v107, v107, v248\n"
    "v_exp_f32 v107, v107\n"
    "v_sub_f32 v108, v108, v248\n"
    "v_exp_f32 v108, v108\n"
    "v_sub_f32 v109, v109, v248\n"
    "v_exp_f32 v109, v109\n"
    "v_sub_f32 v110, v110, v248\n"
    "v_exp_f32 v110, v110\n"
    "v_sub_f32 v111, v111, v248\n"
    "v_exp_f32 v111, v111\n"
    "v_sub_f32 v112, v112, v248\n"
    "v_exp_f32 v112, v112\n"
    "v_sub_f32 v113, v113, v248\n"
    "v_exp_f32 v113, v113\n"
    "v_smfmac_f32_32x32x32_bf16 v[128:143], v[52:55], v[104:111], v232\n"
    "v_sub_f32 v114, v114, v248\n"
    "v_exp_f32 v114, v114\n"
    "v_sub_f32 v115, v115, v248\n"
    "v_exp_f32 v115, v115\n"
    "v_sub_f32 v116, v116, v248\n"
    "v_exp_f32 v116, v116\n"
    "v_mfma_f32_32x32x16_bf16 v[96:111], v[16:19], v[148:151], 0\n"
    "v_sub_f32 v117, v117, v248\n"
    "v_exp_f32 v117, v117\n"
    "v_sub_f32 v118, v118, v248\n"
    "v_exp_f32 v118, v118\n"
    "v_sub_f32 v119, v119, v248\n"
    "v_exp_f32 v119, v119\n"
    "v_sub_f32 v120, v120, v248\n"
    "v_exp_f32 v120, v120\n"
    "v_sub_f32 v121, v121, v248\n"
    "v_exp_f32 v121, v121\n"
    "v_smfmac_f32_32x32x32_bf16 v[128:143], v[56:59], v[112:119], v232\n"
    "v_sub_f32 v122, v122, v248\n"
    "v_exp_f32 v122, v122\n"
    "v_sub_f32 v123, v123, v248\n"
    "v_exp_f32 v123, v123\n"
    "v_sub_f32 v124, v124, v248\n"
    "v_exp_f32 v124, v124\n"
    "v_sub_f32 v125, v125, v248\n"
    "v_exp_f32 v125, v125\n"
    "v_sub_f32 v126, v126, v248\n"
    "v_exp_f32 v126, v126\n"
    "v_sub_f32 v127, v127, v248\n"
    "v_exp_f32 v127, v127\n"
    "v_sub_f32 v96, v96, v248\n"
    "v_exp_f32 v96, v96\n"
    "v_sub_f32 v97, v97, v248\n"
    "v_exp_f32 v97, v97\n"
    "v_smfmac_f32_32x32x32_bf16 v[128:143], v[60:63], v[120:127], v232\n"
    "v_sub_f32 v98, v98, v248\n"
    "v_exp_f32 v98, v98\n"
    "v_sub_f32 v99, v99, v248\n"
    "v_exp_f32 v99, v99\n"
    "v_sub_f32 v100, v100, v248\n"
    "v_exp_f32 v100, v100\n"
    "v_mfma_f32_32x32x16_bf16 v[112:127], v[20:23], v[148:151], 0\n"
    "v_sub_f32 v101, v101, v248\n"
    "v_exp_f32 v101, v101\n"
    "v_sub_f32 v102, v102, v248\n"
    "v_exp_f32 v102, v102\n"
    "v_sub_f32 v103, v103, v248\n"
    "v_exp_f32 v103, v103\n"
    "v_sub_f32 v104, v104, v248\n"
    "v_exp_f32 v104, v104\n"
    "v_sub_f32 v105, v105, v248\n"
    "v_exp_f32 v105, v105\n"
    "v_smfmac_f32_32x32x32_bf16 v[128:143], v[64:67], v[96:103], v232\n"
    "v_sub_f32 v106, v106, v248\n"
    "v_exp_f32 v106, v106\n"
    "v_sub_f32 v107, v107, v248\n"
    "v_exp_f32 v107, v107\n"
    "v_sub_f32 v108, v108, v248\n"
    "v_exp_f32 v108, v108\n"
    "v_sub_f32 v109, v109, v248\n"
    "v_exp_f32 v109, v109\n"
    "v_sub_f32 v110, v110, v248\n"
    "v_exp_f32 v110, v110\n"
    "v_sub_f32 v111, v111, v248\n"
    "v_exp_f32 v111, v111\n"
    "v_sub_f32 v112, v112, v248\n"
    "v_exp_f32 v112, v112\n"
    "v_sub_f32 v113, v113, v248\n"
    "v_exp_f32 v113, v113\n"
    "v_smfmac_f32_32x32x32_bf16 v[128:143], v[68:71], v[104:111], v232\n"
    "v_sub_f32 v114, v114, v248\n"
    "v_exp_f32 v114, v114\n"
    "v_sub_f32 v115, v115, v248\n"
    "v_exp_f32 v115, v115\n"
    "v_sub_f32 v116, v116, v248\n"
    "v_exp_f32 v116, v116\n"
    "v_mfma_f32_32x32x16_bf16 v[96:111], v[24:27], v[148:151], 0\n"
    "v_sub_f32 v117, v117, v248\n"
    "v_exp_f32 v117, v117\n"
    "v_sub_f32 v118, v118, v248\n"
    "v_exp_f32 v118, v118\n"
    "v_sub_f32 v119, v119, v248\n"
    "v_exp_f32 v119, v119\n"
    "v_sub_f32 v120, v120, v248\n"
    "v_exp_f32 v120, v120\n"
    "v_sub_f32 v121, v121, v248\n"
    "v_exp_f32 v121, v121\n"
    "v_smfmac_f32_32x32x32_bf16 v[128:143], v[72:75], v[112:119], v232\n"
    "v_sub_f32 v122, v122, v248\n"
    "v_exp_f32 v122, v122\n"
    "v_sub_f32 v123, v123, v248\n"
    "v_exp_f32 v123, v123\n"
    "v_sub_f32 v124, v124, v248\n"
    "v_exp_f32 v124, v124\n"
    "v_sub_f32 v125, v125, v248\n"
    "v_exp_f32 v125, v125\n"
    "v_sub_f32 v126, v126, v248\n"
    "v_exp_f32 v126, v126\n"
    "v_sub_f32 v127, v127, v248\n"
    "v_exp_f32 v127, v127\n"
    "v_sub_f32 v96, v96, v248\n"
    "v_exp_f32 v96, v96\n"
    "v_sub_f32 v97, v97, v248\n"
    "v_exp_f32 v97, v97\n"
    "v_smfmac_f32_32x32x32_bf16 v[128:143], v[76:79], v[120:127], v232\n"
    "v_sub_f32 v98, v98, v248\n"
    "v_exp_f32 v98, v98\n"
    "v_sub_f32 v99, v99, v248\n"
    "v_exp_f32 v99, v99\n"
    "v_sub_f32 v100, v100, v248\n"
    "v_exp_f32 v100, v100\n"
    "v_mfma_f32_32x32x16_bf16 v[112:127], v[28:31], v[148:151], 0\n"
    "v_sub_f32 v101, v101, v248\n"
    "v_exp_f32 v101, v101\n"
    "v_sub_f32 v102, v102, v248\n"
    "v_exp_f32 v102, v102\n"
    "v_sub_f32 v103, v103, v248\n"
    "v_exp_f32 v103, v103\n"
    "v_sub_f32 v104, v104, v248\n"
    "v_exp_f32 v104, v104\n"
    "v_sub_f32 v105, v105, v248\n"
    "v_exp_f32 v105, v105\n"
    "v_smfmac_f32_32x32x32_bf16 v[128:143], v[80:83], v[96:103], v232\n"
    "v_sub_f32 v106, v106, v248\n"
    "v_exp_f32 v106, v106\n"
    "v_sub_f32 v107, v107, v248\n"
    "v_exp_f32 v107, v107\n"
    "v_sub_f32 v108, v108, v248\n"
    "v_exp_f32 v108, v108\n"
    "v_sub_f32 v109, v109, v248\n"
    "v_exp_f32 v109, v109\n"
    "v_sub_f32 v110, v110, v248\n"
    "v_exp_f32 v110, v110\n"
    "v_sub_f32 v111, v111, v248\n"
    "v_exp_f32 v111, v111\n"
    "v_sub_f32 v112, v112, v248\n"
    "v_exp_f32 v112, v112\n"
    "v_sub_f32 v113, v113, v248\n"
    "v_exp_f32 v113, v113\n"
    "v_smfmac_f32_32x32x32_bf16 v[128:143], v[84:87], v[104:111], v232\n"
    "v_sub_f32 v114, v114, v248\n"
    "v_exp_f32 v114, v114\n"
    "v_sub_f32 v115, v115, v248\n"
    "v_exp_f32 v115, v115\n"
    "v_sub_f32 v116, v116, v248\n"
    "v_exp_f32 v116, v116\n"
    "s_waitcnt vmcnt(0)\n"
    "v_mfma_f32_32x32x16_bf16 v[96:111], v[0:3], v[144:147], 0\n"
    "v_sub_f32 v117, v117, v248\n"
    "v_exp_f32 v117, v117\n"
    "v_sub_f32 v118, v118, v248\n"
    "v_exp_f32 v118, v118\n"
    "v_sub_f32 v119, v119, v248\n"
    "v_exp_f32 v119, v119\n"
    "v_sub_f32 v120, v120, v248\n"
    "v_exp_f32 v120, v120\n"
    "v_sub_f32 v121, v121, v248\n"
    "v_exp_f32 v121, v121\n"
    "v_smfmac_f32_32x32x32_bf16 v[128:143], v[88:91], v[112:119], v232\n"
    "v_sub_f32 v122, v122, v248\n"
    "v_exp_f32 v122, v122\n"
    "v_sub_f32 v123, v123, v248\n"
    "v_exp_f32 v123, v123\n"
    "v_sub_f32 v124, v124, v248\n"
    "v_exp_f32 v124, v124\n"
    "v_sub_f32 v125, v125, v248\n"
    "v_exp_f32 v125, v125\n"
    "v_sub_f32 v126, v126, v248\n"
    "v_exp_f32 v126, v126\n"
    "v_sub_f32 v127, v127, v248\n"
    "v_exp_f32 v127, v127\n"
    "s_setprio 0\n"
    "v_and_b32 v248, 31, v240\n"
    "v_lshlrev_b32 v248, 5, v248\n"
    "v_add_u32 v248, 160776, v248\n"
    "ds_read_b32 v248, v248\n"
    "s_waitcnt lgkmcnt(0)\n"
    "v_sub_f32 v96, v96, v248\n"
    "v_exp_f32 v96, v96\n"
    "v_sub_f32 v97, v97, v248\n"
    "v_exp_f32 v97, v97\n"
    "v_smfmac_f32_32x32x32_bf16 v[128:143], v[92:95], v[120:127], v232\n"
    "s_xor_b32 s72, s71, 3072\n"
    "buffer_load_dwordx4 v[148:151], v241, s[12:15], s72 offen sc1\n"
    "v_sub_f32 v98, v98, v248\n"
    "v_exp_f32 v98, v98\n"
    "v_sub_f32 v99, v99, v248\n"
    "v_exp_f32 v99, v99\n"
    "v_sub_f32 v100, v100, v248\n"
    "v_exp_f32 v100, v100\n"
    "v_mfma_f32_32x32x16_bf16 v[112:127], v[4:7], v[144:147], 0\n"
    "v_sub_f32 v101, v101, v248\n"
    "v_exp_f32 v101, v101\n"
    "v_sub_f32 v102, v102, v248\n"
    "v_exp_f32 v102, v102\n"
    "v_sub_f32 v103, v103, v248\n"
    "v_exp_f32 v103, v103\n"
    "v_sub_f32 v104, v104, v248\n"
    "v_exp_f32 v104, v104\n"
    "v_sub_f32 v105, v105, v248\n"
    "v_exp_f32 v105, v105\n"
    "v_sub_f32 v106, v106, v248\n"
    "v_exp_f32 v106, v106\n"
    "v_sub_f32 v107, v107, v248\n"
    "v_exp_f32 v107, v107\n"
    "v_sub_f32 v108, v108, v248\n"
    "v_exp_f32 v108, v108\n"
    "ds_write_b128 v242, v[128:131] offset:9216\n"
    "ds_write_b128 v242, v[132:135] offset:10240\n"
    "ds_write_b32 v243, v136 offset:9216\n"
    "v_mfma_f32_32x32x8_bf16 v[128:143], v[234:235], v[234:235], 0\n"
    "v_sub_f32 v109, v109, v248\n"
    "v_exp_f32 v109, v109\n"
    "v_sub_f32 v110, v110, v248\n"
    "v_exp_f32 v110, v110\n"
    "v_sub_f32 v111, v111, v248\n"
    "v_exp_f32 v111, v111\n"
    "v_smfmac_f32_32x32x32_bf16 v[128:143], v[32:35], v[96:103], v232\n"
    "v_sub_f32 v112, v112, v248\n"
    "v_exp_f32 v112, v112\n"
    "v_sub_f32 v113, v113, v248\n"
    "v_exp_f32 v113, v113\n"
    "v_smfmac_f32_32x32x32_bf16 v[128:143], v[36:39], v[104:111], v232\n"
    "v_sub_f32 v114, v114, v248\n"
    "v_exp_f32 v114, v114\n"
    "v_sub_f32 v115, v115, v248\n"
    "v_exp_f32 v115, v115\n"
    "v_sub_f32 v116, v116, v248\n"
    "v_exp_f32 v116, v116\n"
    "v_mfma_f32_32x32x16_bf16 v[96:111], v[8:11], v[144:147], 0\n"
    "v_sub_f32 v117, v117, v248\n"
    "v_exp_f32 v117, v117\n"
    "v_sub_f32 v118, v118, v248\n"
    "v_exp_f32 v118, v118\n"
    "v_sub_f32 v119, v119, v248\n"
    "v_exp_f32 v119, v119\n"
    "v_sub_f32 v120, v120, v248\n"
    "v_exp_f32 v120, v120\n"
    "v_sub_f32 v121, v121, v248\n"
    "v_exp_f32 v121, v121\n"
    "v_smfmac_f32_32x32x32_bf16 v[128:143], v[40:43], v[112:119], v232\n"
    "v_sub_f32 v122, v122, v248\n"
    "v_exp_f32 v122, v122\n"
    "v_sub_f32 v123, v123, v248\n"
    "v_exp_f32 v123, v123\n"
    "v_sub_f32 v124, v124, v248\n"
    "v_exp_f32 v124, v124\n"
    "v_sub_f32 v125, v125, v248\n"
    "v_exp_f32 v125, v125\n"
    "v_sub_f32 v126, v126, v248\n"
    "v_exp_f32 v126, v126\n"
    "v_sub_f32 v127, v127, v248\n"
    "v_exp_f32 v127, v127\n"
    "s_waitcnt lgkmcnt(0)\n"
    "s_barrier\n"
    "s_setprio 2\n"
    "v_sub_f32 v96, v96, v248\n"
    "v_exp_f32 v96, v96\n"
    "v_sub_f32 v97, v97, v248\n"
    "v_exp_f32 v97, v97\n"
    "v_smfmac_f32_32x32x32_bf16 v[128:143], v[44:47], v[120:127], v232\n"
    "v_sub_f32 v98, v98, v248\n"
    "v_exp_f32 v98, v98\n"
    "v_sub_f32 v99, v99, v248\n"
    "v_exp_f32 v99, v99\n"
    "v_sub_f32 v100, v100, v248\n"
    "v_exp_f32 v100, v100\n"
    "v_mfma_f32_32x32x16_bf16 v[112:127], v[12:15], v[144:147], 0\n"
    "ds_read_b128 v[208:211], v245 offset:0\n"
    "ds_read_b64 v[200:201], v244 offset:0\n"
    "ds_read_b64 v[202:203], v244 offset:2048\n"
    "ds_read_b64 v[204:205], v244 offset:4096\n"
    "ds_read_b64 v[206:207], v244 offset:6144\n"
    "v_sub_f32 v101, v101, v248\n"
    "v_exp_f32 v101, v101\n"
    "v_sub_f32 v102, v102, v248\n"
    "v_exp_f32 v102, v102\n"
    "v_sub_f32 v103, v103, v248\n"
    "v_exp_f32 v103, v103\n"
    "v_sub_f32 v104, v104, v248\n"
    "v_exp_f32 v104, v104\n"
    "v_sub_f32 v105, v105, v248\n"
    "v_exp_f32 v105, v105\n"
    "v_smfmac_f32_32x32x32_bf16 v[128:143], v[48:51], v[96:103], v232\n"
    "v_sub_f32 v106, v106, v248\n"
    "v_exp_f32 v106, v106\n"
    "v_sub_f32 v107, v107, v248\n"
    "v_exp_f32 v107, v107\n"
    "v_sub_f32 v108, v108, v248\n"
    "v_exp_f32 v108, v108\n"
    "v_sub_f32 v109, v109, v248\n"
    "v_exp_f32 v109, v109\n"
    "v_sub_f32 v110, v110, v248\n"
    "v_exp_f32 v110, v110\n"
    "v_sub_f32 v111, v111, v248\n"
    "v_exp_f32 v111, v111\n"
    "v_sub_f32 v112, v112, v248\n"
    "v_exp_f32 v112, v112\n"
    "v_sub_f32 v113, v113, v248\n"
    "v_exp_f32 v113, v113\n"
    "v_smfmac_f32_32x32x32_bf16 v[128:143], v[52:55], v[104:111], v232\n"
    "v_sub_f32 v114, v114, v248\n"
    "v_exp_f32 v114, v114\n"
    "v_sub_f32 v115, v115, v248\n"
    "v_exp_f32 v115, v115\n"
    "v_sub_f32 v116, v116, v248\n"
    "v_exp_f32 v116, v116\n"
    "v_mfma_f32_32x32x16_bf16 v[96:111], v[16:19], v[144:147], 0\n"
    "s_waitcnt lgkmcnt(4)\n"
    "v_add_f32 v208, v208, v209\n"
    "v_add_f32 v210, v210, v211\n"
    "s_waitcnt lgkmcnt(2)\n"
    "v_add_f32 v200, v200, v202\n"
    "v_add_f32 v208, v208, v210\n"
    "v_add_f32 v201, v201, v203\n"
    "v_rcp_f32 v209, v208\n"
    "s_xor_b32 s62, s75, 458752\n"
    "v_sub_f32 v117, v117, v248\n"
    "v_exp_f32 v117, v117\n"
    "v_sub_f32 v118, v118, v248\n"
    "v_exp_f32 v118, v118\n"
    "v_sub_f32 v119, v119, v248\n"
    "v_exp_f32 v119, v119\n"
    "v_sub_f32 v120, v120, v248\n"
    "v_exp_f32 v120, v120\n"
    "v_sub_f32 v121, v121, v248\n"
    "v_exp_f32 v121, v121\n"
    "v_smfmac_f32_32x32x32_bf16 v[128:143], v[56:59], v[112:119], v232\n"
    "s_waitcnt lgkmcnt(0)\n"
    "v_add_f32 v204, v204, v206\n"
    "v_add_f32 v205, v205, v207\n"
    "v_add_f32 v200, v200, v204\n"
    "v_add_f32 v201, v201, v205\n"
    "v_mul_f32 v200, v200, v209\n"
    "v_mul_f32 v201, v201, v209\n"
    "s_xor_b32 s73, s75, 0\n"
    "s_xor_b32 s74, s75, 4096\n"
    "v_sub_f32 v122, v122, v248\n"
    "v_exp_f32 v122, v122\n"
    "v_sub_f32 v123, v123, v248\n"
    "v_exp_f32 v123, v123\n"
    "v_sub_f32 v124, v124, v248\n"
    "v_exp_f32 v124, v124\n"
    "v_sub_f32 v125, v125, v248\n"
    "v_exp_f32 v125, v125\n"
    "v_sub_f32 v126, v126, v248\n"
    "v_exp_f32 v126, v126\n"
    "v_sub_f32 v127, v127, v248\n"
    "v_exp_f32 v127, v127\n"
    "buffer_load_dwordx4 v[168:171], v241, s[12:15], s73 offen offset:0 sc1\n"
    "v_sub_f32 v96, v96, v248\n"
    "v_exp_f32 v96, v96\n"
    "v_sub_f32 v97, v97, v248\n"
    "v_exp_f32 v97, v97\n"
    "v_smfmac_f32_32x32x32_bf16 v[128:143], v[60:63], v[120:127], v232\n"
    "buffer_load_dwordx4 v[172:175], v241, s[12:15], s73 offen offset:1024 sc1\n"
    "v_sub_f32 v98, v98, v248\n"
    "v_exp_f32 v98, v98\n"
    "v_sub_f32 v99, v99, v248\n"
    "v_exp_f32 v99, v99\n"
    "v_sub_f32 v100, v100, v248\n"
    "v_exp_f32 v100, v100\n"
    "v_mfma_f32_32x32x16_bf16 v[112:127], v[20:23], v[144:147], 0\n"
    "v_max3_f32 v238, v238, v208, v209\n"
    "v_cvt_pk_bf16_f32 v202, v200, v201\n"
    "ds_write_b32 v246, v202 offset:0\n"
    "v_fmac_f32 v236, v200, v201\n"
    "v_sub_f32 v101, v101, v248\n"
    "v_exp_f32 v101, v101\n"
    "v_sub_f32 v102, v102, v248\n"
    "v_exp_f32 v102, v102\n"
    "v_sub_f32 v103, v103, v248\n"
    "v_exp_f32 v103, v103\n"
    "v_sub_f32 v104, v104, v248\n"
    "v_exp_f32 v104, v104\n"
    "v_sub_f32 v105, v105, v248\n"
    "v_exp_f32 v105, v105\n"
    "v_smfmac_f32_32x32x32_bf16 v[128:143], v[64:67], v[96:103], v232\n"
    "buffer_load_dwordx4 v[212:215], v241, s[12:15], s62 offen offset:0 sc1\n"
    "v_sub_f32 v106, v106, v248\n"
    "v_exp_f32 v106, v106\n"
    "v_sub_f32 v107, v107, v248\n"
    "v_exp_f32 v107, v107\n"
    "v_sub_f32 v108, v108, v248\n"
    "v_exp_f32 v108, v108\n"
    "v_sub_f32 v109, v109, v248\n"
    "v_exp_f32 v109, v109\n"
    "v_sub_f32 v110, v110, v248\n"
    "v_exp_f32 v110, v110\n"
    "v_sub_f32 v111, v111, v248\n"
    "v_exp_f32 v111, v111\n"
    "buffer_load_dwordx4 v[176:179], v241, s[12:15], s73 offen offset:2048 sc1\n"
    "v_sub_f32 v112, v112, v248\n"
    "v_exp_f32 v112, v112\n"
    "v_sub_f32 v113, v113, v248\n"
    "v_exp_f32 v113, v113\n"
    "v_smfmac_f32_32x32x32_bf16 v[128:143], v[68:71], v[104:111], v232\n"
    "buffer_load_dwordx4 v[180:183], v241, s[12:15], s73 offen offset:3072 sc1\n"
    "v_sub_f32 v114, v114, v248\n"
    "v_exp_f32 v114, v114\n"
    "v_sub_f32 v115, v115, v248\n"
    "v_exp_f32 v115, v115\n"
    "v_sub_f32 v116, v116, v248\n"
    "v_exp_f32 v116, v116\n"
    "v_mfma_f32_32x32x16_bf16 v[96:111], v[24:27], v[144:147], 0\n"
    "v_sub_f32 v117, v117, v248\n"
    "v_exp_f32 v117, v117\n"
    "v_sub_f32 v118, v118, v248\n"
    "v_exp_f32 v118, v118\n"
    "v_sub_f32 v119, v119, v248\n"
    "v_exp_f32 v119, v119\n"
    "v_sub_f32 v120, v120, v248\n"
    "v_exp_f32 v120, v120\n"
    "v_sub_f32 v121, v121, v248\n"
    "v_exp_f32 v121, v121\n"
    "v_smfmac_f32_32x32x32_bf16 v[128:143], v[72:75], v[112:119], v232\n"
    "buffer_load_dwordx4 v[216:219], v241, s[12:15], s62 offen offset:1024 sc1\n"
    "v_sub_f32 v122, v122, v248\n"
    "v_exp_f32 v122, v122\n"
    "v_sub_f32 v123, v123, v248\n"
    "v_exp_f32 v123, v123\n"
    "v_sub_f32 v124, v124, v248\n"
    "v_exp_f32 v124, v124\n"
    "v_sub_f32 v125, v125, v248\n"
    "v_exp_f32 v125, v125\n"
    "v_sub_f32 v126, v126, v248\n"
    "v_exp_f32 v126, v126\n"
    "v_sub_f32 v127, v127, v248\n"
    "v_exp_f32 v127, v127\n"
    "buffer_load_dwordx4 v[184:187], v241, s[12:15], s74 offen offset:0 sc1\n"
    "v_sub_f32 v96, v96, v248\n"
    "v_exp_f32 v96, v96\n"
    "v_sub_f32 v97, v97, v248\n"
    "v_exp_f32 v97, v97\n"
    "v_smfmac_f32_32x32x32_bf16 v[128:143], v[76:79], v[120:127], v232\n"
    "buffer_load_dwordx4 v[188:191], v241, s[12:15], s74 offen offset:1024 sc1\n"
    "v_sub_f32 v98, v98, v248\n"
    "v_exp_f32 v98, v98\n"
    "v_sub_f32 v99, v99, v248\n"
    "v_exp_f32 v99, v99\n"
    "v_sub_f32 v100, v100, v248\n"
    "v_exp_f32 v100, v100\n"
    "v_mfma_f32_32x32x16_bf16 v[112:127], v[28:31], v[144:147], 0\n"
    "v_sub_f32 v101, v101, v248\n"
    "v_exp_f32 v101, v101\n"
    "v_sub_f32 v102, v102, v248\n"
    "v_exp_f32 v102, v102\n"
    "v_sub_f32 v103, v103, v248\n"
    "v_exp_f32 v103, v103\n"
    "v_sub_f32 v104, v104, v248\n"
    "v_exp_f32 v104, v104\n"
    "v_sub_f32 v105, v105, v248\n"
    "v_exp_f32 v105, v105\n"
    "v_smfmac_f32_32x32x32_bf16 v[128:143], v[80:83], v[96:103], v232\n"
    "buffer_load_dwordx4 v[220:223], v241, s[12:15], s62 offen offset:2048 sc1\n"
    "v_sub_f32 v106, v106, v248\n"
    "v_exp_f32 v106, v106\n"
    "v_sub_f32 v107, v107, v248\n"
    "v_exp_f32 v107, v107\n"
    "v_sub_f32 v108, v108, v248\n"
    "v_exp_f32 v108, v108\n"
    "v_sub_f32 v109, v109, v248\n"
    "v_exp_f32 v109, v109\n"
    "v_sub_f32 v110, v110, v248\n"
    "v_exp_f32 v110, v110\n"
    "v_sub_f32 v111, v111, v248\n"
    "v_exp_f32 v111, v111\n"
    "buffer_load_dwordx4 v[192:195], v241, s[12:15], s74 offen offset:2048 sc1\n"
    "v_sub_f32 v112, v112, v248\n"
    "v_exp_f32 v112, v112\n"
    "v_sub_f32 v113, v113, v248\n"
    "v_exp_f32 v113, v113\n"
    "v_smfmac_f32_32x32x32_bf16 v[128:143], v[84:87], v[104:111], v232\n"
    "buffer_load_dwordx4 v[196:199], v241, s[12:15], s74 offen offset:3072 sc1\n"
    "v_sub_f32 v114, v114, v248\n"
    "v_exp_f32 v114, v114\n"
    "v_sub_f32 v115, v115, v248\n"
    "v_exp_f32 v115, v115\n"
    "v_sub_f32 v116, v116, v248\n"
    "v_exp_f32 v116, v116\n"
    "s_waitcnt vmcnt(11)\n"
    "v_mfma_f32_32x32x16_bf16 v[96:111], v[0:3], v[148:151], 0\n"
    "v_sub_f32 v117, v117, v248\n"
    "v_exp_f32 v117, v117\n"
    "v_sub_f32 v118, v118, v248\n"
    "v_exp_f32 v118, v118\n"
    "v_sub_f32 v119, v119, v248\n"
    "v_exp_f32 v119, v119\n"
    "v_sub_f32 v120, v120, v248\n"
    "v_exp_f32 v120, v120\n"
    "v_sub_f32 v121, v121, v248\n"
    "v_exp_f32 v121, v121\n"
    "v_smfmac_f32_32x32x32_bf16 v[128:143], v[88:91], v[112:119], v232\n"
    "buffer_load_dwordx4 v[224:227], v241, s[12:15], s62 offen offset:3072 sc1\n"
    "v_sub_f32 v122, v122, v248\n"
    "v_exp_f32 v122, v122\n"
    "v_sub_f32 v123, v123, v248\n"
    "v_exp_f32 v123, v123\n"
    "v_sub_f32 v124, v124, v248\n"
    "v_exp_f32 v124, v124\n"
    "v_sub_f32 v125, v125, v248\n"
    "v_exp_f32 v125, v125\n"
    "v_sub_f32 v126, v126, v248\n"
    "v_exp_f32 v126, v126\n"
    "v_sub_f32 v127, v127, v248\n"
    "v_exp_f32 v127, v127\n"
    "s_setprio 0\n"
    "v_and_b32 v248, 31, v240\n"
    "v_lshlrev_b32 v248, 5, v248\n"
    "v_add_u32 v248, 160780, v248\n"
    "ds_read_b32 v248, v248\n"
    "s_waitcnt lgkmcnt(0)\n"
    "v_sub_f32 v96, v96, v248\n"
    "v_exp_f32 v96, v96\n"
    "v_sub_f32 v97, v97, v248\n"
    "v_exp_f32 v97, v97\n"
    "v_smfmac_f32_32x32x32_bf16 v[128:143], v[92:95], v[120:127], v232\n"
    "s_xor_b32 s72, s71, 4096\n"
    "buffer_load_dwordx4 v[144:147], v241, s[12:15], s72 offen sc1\n"
    "v_sub_f32 v98, v98, v248\n"
    "v_exp_f32 v98, v98\n"
    "v_sub_f32 v99, v99, v248\n"
    "v_exp_f32 v99, v99\n"
    "v_sub_f32 v100, v100, v248\n"
    "v_exp_f32 v100, v100\n"
    "v_mfma_f32_32x32x16_bf16 v[112:127], v[4:7], v[148:151], 0\n"
    "v_sub_f32 v101, v101, v248\n"
    "v_exp_f32 v101, v101\n"
    "v_sub_f32 v102, v102, v248\n"
    "v_exp_f32 v102, v102\n"
    "v_sub_f32 v103, v103, v248\n"
    "v_exp_f32 v103, v103\n"
    "v_sub_f32 v104, v104, v248\n"
    "v_exp_f32 v104, v104\n"
    "v_sub_f32 v105, v105, v248\n"
    "v_exp_f32 v105, v105\n"
    "v_sub_f32 v106, v106, v248\n"
    "v_exp_f32 v106, v106\n"
    "v_sub_f32 v107, v107, v248\n"
    "v_exp_f32 v107, v107\n"
    "v_sub_f32 v108, v108, v248\n"
    "v_exp_f32 v108, v108\n"
    "ds_write_b128 v242, v[128:131] offset:0\n"
    "ds_write_b128 v242, v[132:135] offset:1024\n"
    "ds_write_b32 v243, v136 offset:0\n"
    "v_mfma_f32_32x32x8_bf16 v[128:143], v[234:235], v[234:235], 0\n"
    "v_sub_f32 v109, v109, v248\n"
    "v_exp_f32 v109, v109\n"
    "v_sub_f32 v110, v110, v248\n"
    "v_exp_f32 v110, v110\n"
    "v_sub_f32 v111, v111, v248\n"
    "v_exp_f32 v111, v111\n"
    "v_smfmac_f32_32x32x32_bf16 v[128:143], v[32:35], v[96:103], v232\n"
    "v_sub_f32 v112, v112, v248\n"
    "v_exp_f32 v112, v112\n"
    "v_sub_f32 v113, v113, v248\n"
    "v_exp_f32 v113, v113\n"
    "v_smfmac_f32_32x32x32_bf16 v[128:143], v[36:39], v[104:111], v232\n"
    "v_sub_f32 v114, v114, v248\n"
    "v_exp_f32 v114, v114\n"
    "v_sub_f32 v115, v115, v248\n"
    "v_exp_f32 v115, v115\n"
    "v_sub_f32 v116, v116, v248\n"
    "v_exp_f32 v116, v116\n"
    "v_mfma_f32_32x32x16_bf16 v[96:111], v[8:11], v[148:151], 0\n"
    "v_sub_f32 v117, v117, v248\n"
    "v_exp_f32 v117, v117\n"
    "v_sub_f32 v118, v118, v248\n"
    "v_exp_f32 v118, v118\n"
    "v_sub_f32 v119, v119, v248\n"
    "v_exp_f32 v119, v119\n"
    "v_sub_f32 v120, v120, v248\n"
    "v_exp_f32 v120, v120\n"
    "v_sub_f32 v121, v121, v248\n"
    "v_exp_f32 v121, v121\n"
    "v_smfmac_f32_32x32x32_bf16 v[128:143], v[40:43], v[112:119], v232\n"
    "v_sub_f32 v122, v122, v248\n"
    "v_exp_f32 v122, v122\n"
    "v_sub_f32 v123, v123, v248\n"
    "v_exp_f32 v123, v123\n"
    "v_sub_f32 v124, v124, v248\n"
    "v_exp_f32 v124, v124\n"
    "v_sub_f32 v125, v125, v248\n"
    "v_exp_f32 v125, v125\n"
    "v_sub_f32 v126, v126, v248\n"
    "v_exp_f32 v126, v126\n"
    "v_sub_f32 v127, v127, v248\n"
    "v_exp_f32 v127, v127\n"
    "s_waitcnt lgkmcnt(0)\n"
    "s_barrier\n"
    "s_setprio 2\n"
    "v_sub_f32 v96, v96, v248\n"
    "v_exp_f32 v96, v96\n"
    "v_sub_f32 v97, v97, v248\n"
    "v_exp_f32 v97, v97\n"
    "v_smfmac_f32_32x32x32_bf16 v[128:143], v[44:47], v[120:127], v232\n"
    "ds_read_b128 v[250:253], v247 offset:0\n"
    "v_sub_f32 v98, v98, v248\n"
    "v_exp_f32 v98, v98\n"
    "v_sub_f32 v99, v99, v248\n"
    "v_exp_f32 v99, v99\n"
    "v_sub_f32 v100, v100, v248\n"
    "v_exp_f32 v100, v100\n"
    "v_mfma_f32_32x32x16_bf16 v[112:127], v[12:15], v[148:151], 0\n"
    "ds_read_b128 v[208:211], v245 offset:512\n"
    "ds_read_b64 v[200:201], v244 offset:8192\n"
    "ds_read_b64 v[202:203], v244 offset:10240\n"
    "ds_read_b64 v[204:205], v244 offset:12288\n"
    "ds_read_b64 v[206:207], v244 offset:14336\n"
    "v_sub_f32 v101, v101, v248\n"
    "v_exp_f32 v101, v101\n"
    "v_sub_f32 v102, v102, v248\n"
    "v_exp_f32 v102, v102\n"
    "v_sub_f32 v103, v103, v248\n"
    "v_exp_f32 v103, v103\n"
    "v_sub_f32 v104, v104, v248\n"
    "v_exp_f32 v104, v104\n"
    "v_sub_f32 v105, v105, v248\n"
    "v_exp_f32 v105, v105\n"
    "v_smfmac_f32_32x32x32_bf16 v[128:143], v[48:51], v[96:103], v232\n"
    "v_sub_f32 v106, v106, v248\n"
    "v_exp_f32 v106, v106\n"
    "v_sub_f32 v107, v107, v248\n"
    "v_exp_f32 v107, v107\n"
    "v_sub_f32 v108, v108, v248\n"
    "v_exp_f32 v108, v108\n"
    "v_sub_f32 v109, v109, v248\n"
    "v_exp_f32 v109, v109\n"
    "v_sub_f32 v110, v110, v248\n"
    "v_exp_f32 v110, v110\n"
    "v_sub_f32 v111, v111, v248\n"
    "v_exp_f32 v111, v111\n"
    "v_sub_f32 v112, v112, v248\n"
    "v_exp_f32 v112, v112\n"
    "v_sub_f32 v113, v113, v248\n"
    "v_exp_f32 v113, v113\n"
    "v_smfmac_f32_32x32x32_bf16 v[128:143], v[52:55], v[104:111], v232\n"
    "v_sub_f32 v114, v114, v248\n"
    "v_exp_f32 v114, v114\n"
    "v_sub_f32 v115, v115, v248\n"
    "v_exp_f32 v115, v115\n"
    "v_sub_f32 v116, v116, v248\n"
    "v_exp_f32 v116, v116\n"
    "v_mfma_f32_32x32x16_bf16 v[96:111], v[16:19], v[148:151], 0\n"
    "s_waitcnt lgkmcnt(4)\n"
    "v_add_f32 v208, v208, v209\n"
    "v_add_f32 v210, v210, v211\n"
    "s_waitcnt lgkmcnt(2)\n"
    "v_add_f32 v200, v200, v202\n"
    "v_add_f32 v208, v208, v210\n"
    "v_add_f32 v201, v201, v203\n"
    "v_rcp_f32 v209, v208\n"
    "v_sub_f32 v117, v117, v248\n"
    "v_exp_f32 v117, v117\n"
    "v_sub_f32 v118, v118, v248\n"
    "v_exp_f32 v118, v118\n"
    "v_sub_f32 v119, v119, v248\n"
    "v_exp_f32 v119, v119\n"
    "v_sub_f32 v120, v120, v248\n"
    "v_exp_f32 v120, v120\n"
    "v_sub_f32 v121, v121, v248\n"
    "v_exp_f32 v121, v121\n"
    "v_smfmac_f32_32x32x32_bf16 v[128:143], v[56:59], v[112:119], v232\n"
    "s_waitcnt lgkmcnt(0)\n"
    "v_add_f32 v204, v204, v206\n"
    "v_add_f32 v205, v205, v207\n"
    "v_add_f32 v200, v200, v204\n"
    "v_add_f32 v201, v201, v205\n"
    "v_mul_f32 v200, v200, v209\n"
    "v_mul_f32 v201, v201, v209\n"
    "s_xor_b32 s73, s75, 65536\n"
    "s_xor_b32 s74, s75, 69632\n"
    "v_sub_f32 v122, v122, v248\n"
    "v_exp_f32 v122, v122\n"
    "v_sub_f32 v123, v123, v248\n"
    "v_exp_f32 v123, v123\n"
    "v_sub_f32 v124, v124, v248\n"
    "v_exp_f32 v124, v124\n"
    "v_sub_f32 v125, v125, v248\n"
    "v_exp_f32 v125, v125\n"
    "v_sub_f32 v126, v126, v248\n"
    "v_exp_f32 v126, v126\n"
    "v_sub_f32 v127, v127, v248\n"
    "v_exp_f32 v127, v127\n"
    "s_waitcnt vmcnt(11)\n"
    "v_smfmac_f32_16x16x64_bf16 v[152:155], v[250:253], v[168:175], v233\n"
    "buffer_load_dwordx4 v[168:171], v241, s[12:15], s73 offen offset:0 sc1\n"
    "v_sub_f32 v96, v96, v248\n"
    "v_exp_f32 v96, v96\n"
    "v_sub_f32 v97, v97, v248\n"
    "v_exp_f32 v97, v97\n"
    "v_smfmac_f32_32x32x32_bf16 v[128:143], v[60:63], v[120:127], v232\n"
    "buffer_load_dwordx4 v[172:175], v241, s[12:15], s73 offen offset:1024 sc1\n"
    "v_sub_f32 v98, v98, v248\n"
    "v_exp_f32 v98, v98\n"
    "v_sub_f32 v99, v99, v248\n"
    "v_exp_f32 v99, v99\n"
    "v_sub_f32 v100, v100, v248\n"
    "v_exp_f32 v100, v100\n"
    "v_mfma_f32_32x32x16_bf16 v[112:127], v[20:23], v[148:151], 0\n"
    "v_max3_f32 v238, v238, v208, v209\n"
    "v_cvt_pk_bf16_f32 v202, v200, v201\n"
    "ds_write_b32 v246, v202 offset:1024\n"
    "v_fmac_f32 v236, v200, v201\n"
    "v_sub_f32 v101, v101, v248\n"
    "v_exp_f32 v101, v101\n"
    "v_sub_f32 v102, v102, v248\n"
    "v_exp_f32 v102, v102\n"
    "v_sub_f32 v103, v103, v248\n"
    "v_exp_f32 v103, v103\n"
    "v_sub_f32 v104, v104, v248\n"
    "v_exp_f32 v104, v104\n"
    "v_sub_f32 v105, v105, v248\n"
    "v_exp_f32 v105, v105\n"
    "v_smfmac_f32_32x32x32_bf16 v[128:143], v[64:67], v[96:103], v232\n"
    "v_sub_f32 v106, v106, v248\n"
    "v_exp_f32 v106, v106\n"
    "v_sub_f32 v107, v107, v248\n"
    "v_exp_f32 v107, v107\n"
    "v_sub_f32 v108, v108, v248\n"
    "v_exp_f32 v108, v108\n"
    "v_sub_f32 v109, v109, v248\n"
    "v_exp_f32 v109, v109\n"
    "v_sub_f32 v110, v110, v248\n"
    "v_exp_f32 v110, v110\n"
    "v_sub_f32 v111, v111, v248\n"
    "v_exp_f32 v111, v111\n"
    "s_waitcnt vmcnt(10)\n"
    "v_smfmac_f32_16x16x64_bf16 v[156:159], v[250:253], v[176:183], v233\n"
    "buffer_load_dwordx4 v[176:179], v241, s[12:15], s73 offen offset:2048 sc1\n"
    "v_sub_f32 v112, v112, v248\n"
    "v_exp_f32 v112, v112\n"
    "v_sub_f32 v113, v113, v248\n"
    "v_exp_f32 v113, v113\n"
    "v_smfmac_f32_32x32x32_bf16 v[128:143], v[68:71], v[104:111], v232\n"
    "buffer_load_dwordx4 v[180:183], v241, s[12:15], s73 offen offset:3072 sc1\n"
    "v_sub_f32 v114, v114, v248\n"
    "v_exp_f32 v114, v114\n"
    "v_sub_f32 v115, v115, v248\n"
    "v_exp_f32 v115, v115\n"
    "v_sub_f32 v116, v116, v248\n"
    "v_exp_f32 v116, v116\n"
    "v_mfma_f32_32x32x16_bf16 v[96:111], v[24:27], v[148:151], 0\n"
    "v_sub_f32 v117, v117, v248\n"
    "v_exp_f32 v117, v117\n"
    "v_sub_f32 v118, v118, v248\n"
    "v_exp_f32 v118, v118\n"
    "v_sub_f32 v119, v119, v248\n"
    "v_exp_f32 v119, v119\n"
    "v_sub_f32 v120, v120, v248\n"
    "v_exp_f32 v120, v120\n"
    "v_sub_f32 v121, v121, v248\n"
    "v_exp_f32 v121, v121\n"
    "v_smfmac_f32_32x32x32_bf16 v[128:143], v[72:75], v[112:119], v232\n"
    "v_sub_f32 v122, v122, v248\n"
    "v_exp_f32 v122, v122\n"
    "v_sub_f32 v123, v123, v248\n"
    "v_exp_f32 v123, v123\n"
    "v_sub_f32 v124, v124, v248\n"
    "v_exp_f32 v124, v124\n"
    "v_sub_f32 v125, v125, v248\n"
    "v_exp_f32 v125, v125\n"
    "v_sub_f32 v126, v126, v248\n"
    "v_exp_f32 v126, v126\n"
    "v_sub_f32 v127, v127, v248\n"
    "v_exp_f32 v127, v127\n"
    "s_waitcnt vmcnt(9)\n"
    "v_smfmac_f32_16x16x64_bf16 v[160:163], v[250:253], v[184:191], v233\n"
    "buffer_load_dwordx4 v[184:187], v241, s[12:15], s74 offen offset:0 sc1\n"
    "v_sub_f32 v96, v96, v248\n"
    "v_exp_f32 v96, v96\n"
    "v_sub_f32 v97, v97, v248\n"
    "v_exp_f32 v97, v97\n"
    "v_smfmac_f32_32x32x32_bf16 v[128:143], v[76:79], v[120:127], v232\n"
    "buffer_load_dwordx4 v[188:191], v241, s[12:15], s74 offen offset:1024 sc1\n"
    "v_sub_f32 v98, v98, v248\n"
    "v_exp_f32 v98, v98\n"
    "v_sub_f32 v99, v99, v248\n"
    "v_exp_f32 v99, v99\n"
    "v_sub_f32 v100, v100, v248\n"
    "v_exp_f32 v100, v100\n"
    "v_mfma_f32_32x32x16_bf16 v[112:127], v[28:31], v[148:151], 0\n"
    "v_sub_f32 v101, v101, v248\n"
    "v_exp_f32 v101, v101\n"
    "v_sub_f32 v102, v102, v248\n"
    "v_exp_f32 v102, v102\n"
    "v_sub_f32 v103, v103, v248\n"
    "v_exp_f32 v103, v103\n"
    "v_sub_f32 v104, v104, v248\n"
    "v_exp_f32 v104, v104\n"
    "v_sub_f32 v105, v105, v248\n"
    "v_exp_f32 v105, v105\n"
    "v_smfmac_f32_32x32x32_bf16 v[128:143], v[80:83], v[96:103], v232\n"
    "v_sub_f32 v106, v106, v248\n"
    "v_exp_f32 v106, v106\n"
    "v_sub_f32 v107, v107, v248\n"
    "v_exp_f32 v107, v107\n"
    "v_sub_f32 v108, v108, v248\n"
    "v_exp_f32 v108, v108\n"
    "v_sub_f32 v109, v109, v248\n"
    "v_exp_f32 v109, v109\n"
    "v_sub_f32 v110, v110, v248\n"
    "v_exp_f32 v110, v110\n"
    "v_sub_f32 v111, v111, v248\n"
    "v_exp_f32 v111, v111\n"
    "s_waitcnt vmcnt(8)\n"
    "v_smfmac_f32_16x16x64_bf16 v[164:167], v[250:253], v[192:199], v233\n"
    "buffer_load_dwordx4 v[192:195], v241, s[12:15], s74 offen offset:2048 sc1\n"
    "v_sub_f32 v112, v112, v248\n"
    "v_exp_f32 v112, v112\n"
    "v_sub_f32 v113, v113, v248\n"
    "v_exp_f32 v113, v113\n"
    "v_smfmac_f32_32x32x32_bf16 v[128:143], v[84:87], v[104:111], v232\n"
    "buffer_load_dwordx4 v[196:199], v241, s[12:15], s74 offen offset:3072 sc1\n"
    "v_sub_f32 v114, v114, v248\n"
    "v_exp_f32 v114, v114\n"
    "v_sub_f32 v115, v115, v248\n"
    "v_exp_f32 v115, v115\n"
    "v_sub_f32 v116, v116, v248\n"
    "v_exp_f32 v116, v116\n"
    "s_waitcnt vmcnt(8)\n"
    "v_mfma_f32_32x32x16_bf16 v[96:111], v[0:3], v[144:147], 0\n"
    "v_sub_f32 v117, v117, v248\n"
    "v_exp_f32 v117, v117\n"
    "v_sub_f32 v118, v118, v248\n"
    "v_exp_f32 v118, v118\n"
    "v_sub_f32 v119, v119, v248\n"
    "v_exp_f32 v119, v119\n"
    "v_sub_f32 v120, v120, v248\n"
    "v_exp_f32 v120, v120\n"
    "v_sub_f32 v121, v121, v248\n"
    "v_exp_f32 v121, v121\n"
    "v_smfmac_f32_32x32x32_bf16 v[128:143], v[88:91], v[112:119], v232\n"
    "v_sub_f32 v122, v122, v248\n"
    "v_exp_f32 v122, v122\n"
    "v_sub_f32 v123, v123, v248\n"
    "v_exp_f32 v123, v123\n"
    "v_sub_f32 v124, v124, v248\n"
    "v_exp_f32 v124, v124\n"
    "v_sub_f32 v125, v125, v248\n"
    "v_exp_f32 v125, v125\n"
    "v_sub_f32 v126, v126, v248\n"
    "v_exp_f32 v126, v126\n"
    "v_sub_f32 v127, v127, v248\n"
    "v_exp_f32 v127, v127\n"
    "s_setprio 0\n"
    "v_and_b32 v248, 31, v240\n"
    "v_lshlrev_b32 v248, 5, v248\n"
    "v_add_u32 v248, 160784, v248\n"
    "ds_read_b32 v248, v248\n"
    "s_waitcnt lgkmcnt(0)\n"
    "v_sub_f32 v96, v96, v248\n"
    "v_exp_f32 v96, v96\n"
    "v_sub_f32 v97, v97, v248\n"
    "v_exp_f32 v97, v97\n"
    "v_smfmac_f32_32x32x32_bf16 v[128:143], v[92:95], v[120:127], v232\n"
    "s_xor_b32 s72, s71, 5120\n"
    "buffer_load_dwordx4 v[148:151], v241, s[12:15], s72 offen sc1\n"
    "v_sub_f32 v98, v98, v248\n"
    "v_exp_f32 v98, v98\n"
    "v_sub_f32 v99, v99, v248\n"
    "v_exp_f32 v99, v99\n"
    "v_sub_f32 v100, v100, v248\n"
    "v_exp_f32 v100, v100\n"
    "v_mfma_f32_32x32x16_bf16 v[112:127], v[4:7], v[144:147], 0\n"
    "v_sub_f32 v101, v101, v248\n"
    "v_exp_f32 v101, v101\n"
    "v_sub_f32 v102, v102, v248\n"
    "v_exp_f32 v102, v102\n"
    "v_sub_f32 v103, v103, v248\n"
    "v_exp_f32 v103, v103\n"
    "v_sub_f32 v104, v104, v248\n"
    "v_exp_f32 v104, v104\n"
    "v_sub_f32 v105, v105, v248\n"
    "v_exp_f32 v105, v105\n"
    "v_sub_f32 v106, v106, v248\n"
    "v_exp_f32 v106, v106\n"
    "v_sub_f32 v107, v107, v248\n"
    "v_exp_f32 v107, v107\n"
    "v_sub_f32 v108, v108, v248\n"
    "v_exp_f32 v108, v108\n"
    "ds_write_b128 v242, v[128:131] offset:9216\n"
    "ds_write_b128 v242, v[132:135] offset:10240\n"
    "ds_write_b32 v243, v136 offset:9216\n"
    "v_mfma_f32_32x32x8_bf16 v[128:143], v[234:235], v[234:235], 0\n"
    "v_sub_f32 v109, v109, v248\n"
    "v_exp_f32 v109, v109\n"
    "v_sub_f32 v110, v110, v248\n"
    "v_exp_f32 v110, v110\n"
    "v_sub_f32 v111, v111, v248\n"
    "v_exp_f32 v111, v111\n"
    "v_smfmac_f32_32x32x32_bf16 v[128:143], v[32:35], v[96:103], v232\n"
    "v_sub_f32 v112, v112, v248\n"
    "v_exp_f32 v112, v112\n"
    "v_sub_f32 v113, v113, v248\n"
    "v_exp_f32 v113, v113\n"
    "v_smfmac_f32_32x32x32_bf16 v[128:143], v[36:39], v[104:111], v232\n"
    "v_sub_f32 v114, v114, v248\n"
    "v_exp_f32 v114, v114\n"
    "v_sub_f32 v115, v115, v248\n"
    "v_exp_f32 v115, v115\n"
    "v_sub_f32 v116, v116, v248\n"
    "v_exp_f32 v116, v116\n"
    "v_mfma_f32_32x32x16_bf16 v[96:111], v[8:11], v[144:147], 0\n"
    "v_sub_f32 v117, v117, v248\n"
    "v_exp_f32 v117, v117\n"
    "v_sub_f32 v118, v118, v248\n"
    "v_exp_f32 v118, v118\n"
    "v_sub_f32 v119, v119, v248\n"
    "v_exp_f32 v119, v119\n"
    "v_sub_f32 v120, v120, v248\n"
    "v_exp_f32 v120, v120\n"
    "v_sub_f32 v121, v121, v248\n"
    "v_exp_f32 v121, v121\n"
    "v_smfmac_f32_32x32x32_bf16 v[128:143], v[40:43], v[112:119], v232\n"
    "v_sub_f32 v122, v122, v248\n"
    "v_exp_f32 v122, v122\n"
    "v_sub_f32 v123, v123, v248\n"
    "v_exp_f32 v123, v123\n"
    "v_sub_f32 v124, v124, v248\n"
    "v_exp_f32 v124, v124\n"
    "v_sub_f32 v125, v125, v248\n"
    "v_exp_f32 v125, v125\n"
    "v_sub_f32 v126, v126, v248\n"
    "v_exp_f32 v126, v126\n"
    "v_sub_f32 v127, v127, v248\n"
    "v_exp_f32 v127, v127\n"
    "s_waitcnt lgkmcnt(0)\n"
    "s_barrier\n"
    "s_setprio 2\n"
    "v_sub_f32 v96, v96, v248\n"
    "v_exp_f32 v96, v96\n"
    "v_sub_f32 v97, v97, v248\n"
    "v_exp_f32 v97, v97\n"
    "v_smfmac_f32_32x32x32_bf16 v[128:143], v[44:47], v[120:127], v232\n"
    "ds_read_b128 v[250:253], v247 offset:1024\n"
    "v_sub_f32 v98, v98, v248\n"
    "v_exp_f32 v98, v98\n"
    "v_sub_f32 v99, v99, v248\n"
    "v_exp_f32 v99, v99\n"
    "v_sub_f32 v100, v100, v248\n"
    "v_exp_f32 v100, v100\n"
    "v_mfma_f32_32x32x16_bf16 v[112:127], v[12:15], v[144:147], 0\n"
    "ds_read_b128 v[208:211], v245 offset:0\n"
    "ds_read_b64 v[200:201], v244 offset:0\n"
    "ds_read_b64 v[202:203], v244 offset:2048\n"
    "ds_read_b64 v[204:205], v244 offset:4096\n"
    "ds_read_b64 v[206:207], v244 offset:6144\n"
    "v_sub_f32 v101, v101, v248\n"
    "v_exp_f32 v101, v101\n"
    "v_sub_f32 v102, v102, v248\n"
    "v_exp_f32 v102, v102\n"
    "v_sub_f32 v103, v103, v248\n"
    "v_exp_f32 v103, v103\n"
    "v_sub_f32 v104, v104, v248\n"
    "v_exp_f32 v104, v104\n"
    "v_sub_f32 v105, v105, v248\n"
    "v_exp_f32 v105, v105\n"
    "v_smfmac_f32_32x32x32_bf16 v[128:143], v[48:51], v[96:103], v232\n"
    "v_sub_f32 v106, v106, v248\n"
    "v_exp_f32 v106, v106\n"
    "v_sub_f32 v107, v107, v248\n"
    "v_exp_f32 v107, v107\n"
    "v_sub_f32 v108, v108, v248\n"
    "v_exp_f32 v108, v108\n"
    "v_sub_f32 v109, v109, v248\n"
    "v_exp_f32 v109, v109\n"
    "v_sub_f32 v110, v110, v248\n"
    "v_exp_f32 v110, v110\n"
    "v_sub_f32 v111, v111, v248\n"
    "v_exp_f32 v111, v111\n"
    "v_sub_f32 v112, v112, v248\n"
    "v_exp_f32 v112, v112\n"
    "v_sub_f32 v113, v113, v248\n"
    "v_exp_f32 v113, v113\n"
    "v_smfmac_f32_32x32x32_bf16 v[128:143], v[52:55], v[104:111], v232\n"
    "v_sub_f32 v114, v114, v248\n"
    "v_exp_f32 v114, v114\n"
    "v_sub_f32 v115, v115, v248\n"
    "v_exp_f32 v115, v115\n"
    "v_sub_f32 v116, v116, v248\n"
    "v_exp_f32 v116, v116\n"
    "v_mfma_f32_32x32x16_bf16 v[96:111], v[16:19], v[144:147], 0\n"
    "s_waitcnt lgkmcnt(4)\n"
    "v_add_f32 v208, v208, v209\n"
    "v_add_f32 v210, v210, v211\n"
    "s_waitcnt lgkmcnt(2)\n"
    "v_add_f32 v200, v200, v202\n"
    "v_add_f32 v208, v208, v210\n"
    "v_add_f32 v201, v201, v203\n"
    "v_rcp_f32 v209, v208\n"
    "v_sub_f32 v117, v117, v248\n"
    "v_exp_f32 v117, v117\n"
    "v_sub_f32 v118, v118, v248\n"
    "v_exp_f32 v118, v118\n"
    "v_sub_f32 v119, v119, v248\n"
    "v_exp_f32 v119, v119\n"
    "v_sub_f32 v120, v120, v248\n"
    "v_exp_f32 v120, v120\n"
    "v_sub_f32 v121, v121, v248\n"
    "v_exp_f32 v121, v121\n"
    "v_smfmac_f32_32x32x32_bf16 v[128:143], v[56:59], v[112:119], v232\n"
    "s_waitcnt lgkmcnt(0)\n"
    "v_add_f32 v204, v204, v206\n"
    "v_add_f32 v205, v205, v207\n"
    "v_add_f32 v200, v200, v204\n"
    "v_add_f32 v201, v201, v205\n"
    "v_mul_f32 v200, v200, v209\n"
    "v_mul_f32 v201, v201, v209\n"
    "s_xor_b32 s73, s75, 131072\n"
    "s_xor_b32 s74, s75, 135168\n"
    "v_sub_f32 v122, v122, v248\n"
    "v_exp_f32 v122, v122\n"
    "v_sub_f32 v123, v123, v248\n"
    "v_exp_f32 v123, v123\n"
    "v_sub_f32 v124, v124, v248\n"
    "v_exp_f32 v124, v124\n"
    "v_sub_f32 v125, v125, v248\n"
    "v_exp_f32 v125, v125\n"
    "v_sub_f32 v126, v126, v248\n"
    "v_exp_f32 v126, v126\n"
    "v_sub_f32 v127, v127, v248\n"
    "v_exp_f32 v127, v127\n"
    "s_waitcnt vmcnt(7)\n"
    "v_smfmac_f32_16x16x64_bf16 v[152:155], v[250:253], v[168:175], v233\n"
    "buffer_load_dwordx4 v[168:171], v241, s[12:15], s73 offen offset:0 sc1\n"
    "v_sub_f32 v96, v96, v248\n"
    "v_exp_f32 v96, v96\n"
    "v_sub_f32 v97, v97, v248\n"
    "v_exp_f32 v97, v97\n"
    "v_smfmac_f32_32x32x32_bf16 v[128:143], v[60:63], v[120:127], v232\n"
    "buffer_load_dwordx4 v[172:175], v241, s[12:15], s73 offen offset:1024 sc1\n"
    "v_sub_f32 v98, v98, v248\n"
    "v_exp_f32 v98, v98\n"
    "v_sub_f32 v99, v99, v248\n"
    "v_exp_f32 v99, v99\n"
    "v_sub_f32 v100, v100, v248\n"
    "v_exp_f32 v100, v100\n"
    "v_mfma_f32_32x32x16_bf16 v[112:127], v[20:23], v[144:147], 0\n"
    "v_max3_f32 v238, v238, v208, v209\n"
    "v_cvt_pk_bf16_f32 v202, v200, v201\n"
    "ds_write_b32 v246, v202 offset:0\n"
    "v_fmac_f32 v236, v200, v201\n"
    "v_sub_f32 v101, v101, v248\n"
    "v_exp_f32 v101, v101\n"
    "v_sub_f32 v102, v102, v248\n"
    "v_exp_f32 v102, v102\n"
    "v_sub_f32 v103, v103, v248\n"
    "v_exp_f32 v103, v103\n"
    "v_sub_f32 v104, v104, v248\n"
    "v_exp_f32 v104, v104\n"
    "v_sub_f32 v105, v105, v248\n"
    "v_exp_f32 v105, v105\n"
    "v_smfmac_f32_32x32x32_bf16 v[128:143], v[64:67], v[96:103], v232\n"
    "v_sub_f32 v106, v106, v248\n"
    "v_exp_f32 v106, v106\n"
    "v_sub_f32 v107, v107, v248\n"
    "v_exp_f32 v107, v107\n"
    "v_sub_f32 v108, v108, v248\n"
    "v_exp_f32 v108, v108\n"
    "v_sub_f32 v109, v109, v248\n"
    "v_exp_f32 v109, v109\n"
    "v_sub_f32 v110, v110, v248\n"
    "v_exp_f32 v110, v110\n"
    "v_sub_f32 v111, v111, v248\n"
    "v_exp_f32 v111, v111\n"
    "s_waitcnt vmcnt(7)\n"
    "v_smfmac_f32_16x16x64_bf16 v[156:159], v[250:253], v[176:183], v233\n"
    "buffer_load_dwordx4 v[176:179], v241, s[12:15], s73 offen offset:2048 sc1\n"
    "v_sub_f32 v112, v112, v248\n"
    "v_exp_f32 v112, v112\n"
    "v_sub_f32 v113, v113, v248\n"
    "v_exp_f32 v113, v113\n"
    "v_smfmac_f32_32x32x32_bf16 v[128:143], v[68:71], v[104:111], v232\n"
    "buffer_load_dwordx4 v[180:183], v241, s[12:15], s73 offen offset:3072 sc1\n"
    "v_sub_f32 v114, v114, v248\n"
    "v_exp_f32 v114, v114\n"
    "v_sub_f32 v115, v115, v248\n"
    "v_exp_f32 v115, v115\n"
    "v_sub_f32 v116, v116, v248\n"
    "v_exp_f32 v116, v116\n"
    "v_mfma_f32_32x32x16_bf16 v[96:111], v[24:27], v[144:147], 0\n"
    "v_sub_f32 v117, v117, v248\n"
    "v_exp_f32 v117, v117\n"
    "v_sub_f32 v118, v118, v248\n"
    "v_exp_f32 v118, v118\n"
    "v_sub_f32 v119, v119, v248\n"
    "v_exp_f32 v119, v119\n"
    "v_sub_f32 v120, v120, v248\n"
    "v_exp_f32 v120, v120\n"
    "v_sub_f32 v121, v121, v248\n"
    "v_exp_f32 v121, v121\n"
    "v_smfmac_f32_32x32x32_bf16 v[128:143], v[72:75], v[112:119], v232\n"
    "v_sub_f32 v122, v122, v248\n"
    "v_exp_f32 v122, v122\n"
    "v_sub_f32 v123, v123, v248\n"
    "v_exp_f32 v123, v123\n"
    "v_sub_f32 v124, v124, v248\n"
    "v_exp_f32 v124, v124\n"
    "v_sub_f32 v125, v125, v248\n"
    "v_exp_f32 v125, v125\n"
    "v_sub_f32 v126, v126, v248\n"
    "v_exp_f32 v126, v126\n"
    "v_sub_f32 v127, v127, v248\n"
    "v_exp_f32 v127, v127\n"
    "s_waitcnt vmcnt(7)\n"
    "v_smfmac_f32_16x16x64_bf16 v[160:163], v[250:253], v[184:191], v233\n"
    "buffer_load_dwordx4 v[184:187], v241, s[12:15], s74 offen offset:0 sc1\n"
    "v_sub_f32 v96, v96, v248\n"
    "v_exp_f32 v96, v96\n"
    "v_sub_f32 v97, v97, v248\n"
    "v_exp_f32 v97, v97\n"
    "v_smfmac_f32_32x32x32_bf16 v[128:143], v[76:79], v[120:127], v232\n"
    "buffer_load_dwordx4 v[188:191], v241, s[12:15], s74 offen offset:1024 sc1\n"
    "v_sub_f32 v98, v98, v248\n"
    "v_exp_f32 v98, v98\n"
    "v_sub_f32 v99, v99, v248\n"
    "v_exp_f32 v99, v99\n"
    "v_sub_f32 v100, v100, v248\n"
    "v_exp_f32 v100, v100\n"
    "v_mfma_f32_32x32x16_bf16 v[112:127], v[28:31], v[144:147], 0\n"
    "v_sub_f32 v101, v101, v248\n"
    "v_exp_f32 v101, v101\n"
    "v_sub_f32 v102, v102, v248\n"
    "v_exp_f32 v102, v102\n"
    "v_sub_f32 v103, v103, v248\n"
    "v_exp_f32 v103, v103\n"
    "v_sub_f32 v104, v104, v248\n"
    "v_exp_f32 v104, v104\n"
    "v_sub_f32 v105, v105, v248\n"
    "v_exp_f32 v105, v105\n"
    "v_smfmac_f32_32x32x32_bf16 v[128:143], v[80:83], v[96:103], v232\n"
    "v_sub_f32 v106, v106, v248\n"
    "v_exp_f32 v106, v106\n"
    "v_sub_f32 v107, v107, v248\n"
    "v_exp_f32 v107, v107\n"
    "v_sub_f32 v108, v108, v248\n"
    "v_exp_f32 v108, v108\n"
    "v_sub_f32 v109, v109, v248\n"
    "v_exp_f32 v109, v109\n"
    "v_sub_f32 v110, v110, v248\n"
    "v_exp_f32 v110, v110\n"
    "v_sub_f32 v111, v111, v248\n"
    "v_exp_f32 v111, v111\n"
    "s_waitcnt vmcnt(7)\n"
    "v_smfmac_f32_16x16x64_bf16 v[164:167], v[250:253], v[192:199], v233\n"
    "buffer_load_dwordx4 v[192:195], v241, s[12:15], s74 offen offset:2048 sc1\n"
    "v_sub_f32 v112, v112, v248\n"
    "v_exp_f32 v112, v112\n"
    "v_sub_f32 v113, v113, v248\n"
    "v_exp_f32 v113, v113\n"
    "v_smfmac_f32_32x32x32_bf16 v[128:143], v[84:87], v[104:111], v232\n"
    "buffer_load_dwordx4 v[196:199], v241, s[12:15], s74 offen offset:3072 sc1\n"
    "v_sub_f32 v114, v114, v248\n"
    "v_exp_f32 v114, v114\n"
    "v_sub_f32 v115, v115, v248\n"
    "v_exp_f32 v115, v115\n"
    "v_sub_f32 v116, v116, v248\n"
    "v_exp_f32 v116, v116\n"
    "s_waitcnt vmcnt(8)\n"
    "v_mfma_f32_32x32x16_bf16 v[96:111], v[0:3], v[148:151], 0\n"
    "v_sub_f32 v117, v117, v248\n"
    "v_exp_f32 v117, v117\n"
    "v_sub_f32 v118, v118, v248\n"
    "v_exp_f32 v118, v118\n"
    "v_sub_f32 v119, v119, v248\n"
    "v_exp_f32 v119, v119\n"
    "v_sub_f32 v120, v120, v248\n"
    "v_exp_f32 v120, v120\n"
    "v_sub_f32 v121, v121, v248\n"
    "v_exp_f32 v121, v121\n"
    "v_smfmac_f32_32x32x32_bf16 v[128:143], v[88:91], v[112:119], v232\n"
    "v_sub_f32 v122, v122, v248\n"
    "v_exp_f32 v122, v122\n"
    "v_sub_f32 v123, v123, v248\n"
    "v_exp_f32 v123, v123\n"
    "v_sub_f32 v124, v124, v248\n"
    "v_exp_f32 v124, v124\n"
    "v_sub_f32 v125, v125, v248\n"
    "v_exp_f32 v125, v125\n"
    "v_sub_f32 v126, v126, v248\n"
    "v_exp_f32 v126, v126\n"
    "v_sub_f32 v127, v127, v248\n"
    "v_exp_f32 v127, v127\n"
    "s_setprio 0\n"
    "v_and_b32 v248, 31, v240\n"
    "v_lshlrev_b32 v248, 5, v248\n"
    "v_add_u32 v248, 160788, v248\n"
    "ds_read_b32 v248, v248\n"
    "s_waitcnt lgkmcnt(0)\n"
    "v_sub_f32 v96, v96, v248\n"
    "v_exp_f32 v96, v96\n"
    "v_sub_f32 v97, v97, v248\n"
    "v_exp_f32 v97, v97\n"
    "v_smfmac_f32_32x32x32_bf16 v[128:143], v[92:95], v[120:127], v232\n"
    "s_xor_b32 s72, s71, 6144\n"
    "buffer_load_dwordx4 v[144:147], v241, s[12:15], s72 offen sc1\n"
    "v_sub_f32 v98, v98, v248\n"
    "v_exp_f32 v98, v98\n"
    "v_sub_f32 v99, v99, v248\n"
    "v_exp_f32 v99, v99\n"
    "v_sub_f32 v100, v100, v248\n"
    "v_exp_f32 v100, v100\n"
    "v_mfma_f32_32x32x16_bf16 v[112:127], v[4:7], v[148:151], 0\n"
    "v_sub_f32 v101, v101, v248\n"
    "v_exp_f32 v101, v101\n"
    "v_sub_f32 v102, v102, v248\n"
    "v_exp_f32 v102, v102\n"
    "v_sub_f32 v103, v103, v248\n"
    "v_exp_f32 v103, v103\n"
    "v_sub_f32 v104, v104, v248\n"
    "v_exp_f32 v104, v104\n"
    "v_sub_f32 v105, v105, v248\n"
    "v_exp_f32 v105, v105\n"
    "v_sub_f32 v106, v106, v248\n"
    "v_exp_f32 v106, v106\n"
    "v_sub_f32 v107, v107, v248\n"
    "v_exp_f32 v107, v107\n"
    "v_sub_f32 v108, v108, v248\n"
    "v_exp_f32 v108, v108\n"
    "ds_write_b128 v242, v[128:131] offset:0\n"
    "ds_write_b128 v242, v[132:135] offset:1024\n"
    "ds_write_b32 v243, v136 offset:0\n"
    "v_mfma_f32_32x32x8_bf16 v[128:143], v[234:235], v[234:235], 0\n"
    "v_sub_f32 v109, v109, v248\n"
    "v_exp_f32 v109, v109\n"
    "v_sub_f32 v110, v110, v248\n"
    "v_exp_f32 v110, v110\n"
    "v_sub_f32 v111, v111, v248\n"
    "v_exp_f32 v111, v111\n"
    "v_smfmac_f32_32x32x32_bf16 v[128:143], v[32:35], v[96:103], v232\n"
    "v_sub_f32 v112, v112, v248\n"
    "v_exp_f32 v112, v112\n"
    "v_sub_f32 v113, v113, v248\n"
    "v_exp_f32 v113, v113\n"
    "v_smfmac_f32_32x32x32_bf16 v[128:143], v[36:39], v[104:111], v232\n"
    "v_sub_f32 v114, v114, v248\n"
    "v_exp_f32 v114, v114\n"
    "v_sub_f32 v115, v115, v248\n"
    "v_exp_f32 v115, v115\n"
    "v_sub_f32 v116, v116, v248\n"
    "v_exp_f32 v116, v116\n"
    "v_mfma_f32_32x32x16_bf16 v[96:111], v[8:11], v[148:151], 0\n"
    "v_sub_f32 v117, v117, v248\n"
    "v_exp_f32 v117, v117\n"
    "v_sub_f32 v118, v118, v248\n"
    "v_exp_f32 v118, v118\n"
    "v_sub_f32 v119, v119, v248\n"
    "v_exp_f32 v119, v119\n"
    "v_sub_f32 v120, v120, v248\n"
    "v_exp_f32 v120, v120\n"
    "v_sub_f32 v121, v121, v248\n"
    "v_exp_f32 v121, v121\n"
    "v_smfmac_f32_32x32x32_bf16 v[128:143], v[40:43], v[112:119], v232\n"
    "v_sub_f32 v122, v122, v248\n"
    "v_exp_f32 v122, v122\n"
    "v_sub_f32 v123, v123, v248\n"
    "v_exp_f32 v123, v123\n"
    "v_sub_f32 v124, v124, v248\n"
    "v_exp_f32 v124, v124\n"
    "v_sub_f32 v125, v125, v248\n"
    "v_exp_f32 v125, v125\n"
    "v_sub_f32 v126, v126, v248\n"
    "v_exp_f32 v126, v126\n"
    "v_sub_f32 v127, v127, v248\n"
    "v_exp_f32 v127, v127\n"
    "s_waitcnt lgkmcnt(0)\n"
    "s_barrier\n"
    "s_setprio 2\n"
    "v_sub_f32 v96, v96, v248\n"
    "v_exp_f32 v96, v96\n"
    "v_sub_f32 v97, v97, v248\n"
    "v_exp_f32 v97, v97\n"
    "v_smfmac_f32_32x32x32_bf16 v[128:143], v[44:47], v[120:127], v232\n"
    "ds_read_b128 v[250:253], v247 offset:0\n"
    "v_sub_f32 v98, v98, v248\n"
    "v_exp_f32 v98, v98\n"
    "v_sub_f32 v99, v99, v248\n"
    "v_exp_f32 v99, v99\n"
    "v_sub_f32 v100, v100, v248\n"
    "v_exp_f32 v100, v100\n"
    "v_mfma_f32_32x32x16_bf16 v[112:127], v[12:15], v[148:151], 0\n"
    "ds_read_b128 v[208:211], v245 offset:512\n"
    "ds_read_b64 v[200:201], v244 offset:8192\n"
    "ds_read_b64 v[202:203], v244 offset:10240\n"
    "ds_read_b64 v[204:205], v244 offset:12288\n"
    "ds_read_b64 v[206:207], v244 offset:14336\n"
    "v_sub_f32 v101, v101, v248\n"
    "v_exp_f32 v101, v101\n"
    "v_sub_f32 v102, v102, v248\n"
    "v_exp_f32 v102, v102\n"
    "v_sub_f32 v103, v103, v248\n"
    "v_exp_f32 v103, v103\n"
    "v_sub_f32 v104, v104, v248\n"
    "v_exp_f32 v104, v104\n"
    "v_sub_f32 v105, v105, v248\n"
    "v_exp_f32 v105, v105\n"
    "v_smfmac_f32_32x32x32_bf16 v[128:143], v[48:51], v[96:103], v232\n"
    "v_sub_f32 v106, v106, v248\n"
    "v_exp_f32 v106, v106\n"
    "v_sub_f32 v107, v107, v248\n"
    "v_exp_f32 v107, v107\n"
    "v_sub_f32 v108, v108, v248\n"
    "v_exp_f32 v108, v108\n"
    "v_sub_f32 v109, v109, v248\n"
    "v_exp_f32 v109, v109\n"
    "v_sub_f32 v110, v110, v248\n"
    "v_exp_f32 v110, v110\n"
    "v_sub_f32 v111, v111, v248\n"
    "v_exp_f32 v111, v111\n"
    "v_sub_f32 v112, v112, v248\n"
    "v_exp_f32 v112, v112\n"
    "v_sub_f32 v113, v113, v248\n"
    "v_exp_f32 v113, v113\n"
    "v_smfmac_f32_32x32x32_bf16 v[128:143], v[52:55], v[104:111], v232\n"
    "v_sub_f32 v114, v114, v248\n"
    "v_exp_f32 v114, v114\n"
    "v_sub_f32 v115, v115, v248\n"
    "v_exp_f32 v115, v115\n"
    "v_sub_f32 v116, v116, v248\n"
    "v_exp_f32 v116, v116\n"
    "v_mfma_f32_32x32x16_bf16 v[96:111], v[16:19], v[148:151], 0\n"
    "s_waitcnt lgkmcnt(4)\n"
    "v_add_f32 v208, v208, v209\n"
    "v_add_f32 v210, v210, v211\n"
    "s_waitcnt lgkmcnt(2)\n"
    "v_add_f32 v200, v200, v202\n"
    "v_add_f32 v208, v208, v210\n"
    "v_add_f32 v201, v201, v203\n"
    "v_rcp_f32 v209, v208\n"
    "v_sub_f32 v117, v117, v248\n"
    "v_exp_f32 v117, v117\n"
    "v_sub_f32 v118, v118, v248\n"
    "v_exp_f32 v118, v118\n"
    "v_sub_f32 v119, v119, v248\n"
    "v_exp_f32 v119, v119\n"
    "v_sub_f32 v120, v120, v248\n"
    "v_exp_f32 v120, v120\n"
    "v_sub_f32 v121, v121, v248\n"
    "v_exp_f32 v121, v121\n"
    "v_smfmac_f32_32x32x32_bf16 v[128:143], v[56:59], v[112:119], v232\n"
    "s_waitcnt lgkmcnt(0)\n"
    "v_add_f32 v204, v204, v206\n"
    "v_add_f32 v205, v205, v207\n"
    "v_add_f32 v200, v200, v204\n"
    "v_add_f32 v201, v201, v205\n"
    "v_mul_f32 v200, v200, v209\n"
    "v_mul_f32 v201, v201, v209\n"
    "s_xor_b32 s73, s75, 196608\n"
    "s_xor_b32 s74, s75, 200704\n"
    "v_sub_f32 v122, v122, v248\n"
    "v_exp_f32 v122, v122\n"
    "v_sub_f32 v123, v123, v248\n"
    "v_exp_f32 v123, v123\n"
    "v_sub_f32 v124, v124, v248\n"
    "v_exp_f32 v124, v124\n"
    "v_sub_f32 v125, v125, v248\n"
    "v_exp_f32 v125, v125\n"
    "v_sub_f32 v126, v126, v248\n"
    "v_exp_f32 v126, v126\n"
    "v_sub_f32 v127, v127, v248\n"
    "v_exp_f32 v127, v127\n"
    "s_waitcnt vmcnt(7)\n"
    "v_smfmac_f32_16x16x64_bf16 v[152:155], v[250:253], v[168:175], v233\n"
    "buffer_load_dwordx4 v[168:171], v241, s[12:15], s73 offen offset:0 sc1\n"
    "v_sub_f32 v96, v96, v248\n"
    "v_exp_f32 v96, v96\n"
    "v_sub_f32 v97, v97, v248\n"
    "v_exp_f32 v97, v97\n"
    "v_smfmac_f32_32x32x32_bf16 v[128:143], v[60:63], v[120:127], v232\n"
    "buffer_load_dwordx4 v[172:175], v241, s[12:15], s73 offen offset:1024 sc1\n"
    "v_sub_f32 v98, v98, v248\n"
    "v_exp_f32 v98, v98\n"
    "v_sub_f32 v99, v99, v248\n"
    "v_exp_f32 v99, v99\n"
    "v_sub_f32 v100, v100, v248\n"
    "v_exp_f32 v100, v100\n"
    "v_mfma_f32_32x32x16_bf16 v[112:127], v[20:23], v[148:151], 0\n"
    "v_max3_f32 v238, v238, v208, v209\n"
    "v_cvt_pk_bf16_f32 v202, v200, v201\n"
    "ds_write_b32 v246, v202 offset:1024\n"
    "v_fmac_f32 v236, v200, v201\n"
    "v_sub_f32 v101, v101, v248\n"
    "v_exp_f32 v101, v101\n"
    "v_sub_f32 v102, v102, v248\n"
    "v_exp_f32 v102, v102\n"
    "v_sub_f32 v103, v103, v248\n"
    "v_exp_f32 v103, v103\n"
    "v_sub_f32 v104, v104, v248\n"
    "v_exp_f32 v104, v104\n"
    "v_sub_f32 v105, v105, v248\n"
    "v_exp_f32 v105, v105\n"
    "v_smfmac_f32_32x32x32_bf16 v[128:143], v[64:67], v[96:103], v232\n"
    "v_sub_f32 v106, v106, v248\n"
    "v_exp_f32 v106, v106\n"
    "v_sub_f32 v107, v107, v248\n"
    "v_exp_f32 v107, v107\n"
    "v_sub_f32 v108, v108, v248\n"
    "v_exp_f32 v108, v108\n"
    "v_sub_f32 v109, v109, v248\n"
    "v_exp_f32 v109, v109\n"
    "v_sub_f32 v110, v110, v248\n"
    "v_exp_f32 v110, v110\n"
    "v_sub_f32 v111, v111, v248\n"
    "v_exp_f32 v111, v111\n"
    "s_waitcnt vmcnt(7)\n"
    "v_smfmac_f32_16x16x64_bf16 v[156:159], v[250:253], v[176:183], v233\n"
    "buffer_load_dwordx4 v[176:179], v241, s[12:15], s73 offen offset:2048 sc1\n"
    "v_sub_f32 v112, v112, v248\n"
    "v_exp_f32 v112, v112\n"
    "v_sub_f32 v113, v113, v248\n"
    "v_exp_f32 v113, v113\n"
    "v_smfmac_f32_32x32x32_bf16 v[128:143], v[68:71], v[104:111], v232\n"
    "buffer_load_dwordx4 v[180:183], v241, s[12:15], s73 offen offset:3072 sc1\n"
    "v_sub_f32 v114, v114, v248\n"
    "v_exp_f32 v114, v114\n"
    "v_sub_f32 v115, v115, v248\n"
    "v_exp_f32 v115, v115\n"
    "v_sub_f32 v116, v116, v248\n"
    "v_exp_f32 v116, v116\n"
    "v_mfma_f32_32x32x16_bf16 v[96:111], v[24:27], v[148:151], 0\n"
    "v_sub_f32 v117, v117, v248\n"
    "v_exp_f32 v117, v117\n"
    "v_sub_f32 v118, v118, v248\n"
    "v_exp_f32 v118, v118\n"
    "v_sub_f32 v119, v119, v248\n"
    "v_exp_f32 v119, v119\n"
    "v_sub_f32 v120, v120, v248\n"
    "v_exp_f32 v120, v120\n"
    "v_sub_f32 v121, v121, v248\n"
    "v_exp_f32 v121, v121\n"
    "v_smfmac_f32_32x32x32_bf16 v[128:143], v[72:75], v[112:119], v232\n"
    "v_sub_f32 v122, v122, v248\n"
    "v_exp_f32 v122, v122\n"
    "v_sub_f32 v123, v123, v248\n"
    "v_exp_f32 v123, v123\n"
    "v_sub_f32 v124, v124, v248\n"
    "v_exp_f32 v124, v124\n"
    "v_sub_f32 v125, v125, v248\n"
    "v_exp_f32 v125, v125\n"
    "v_sub_f32 v126, v126, v248\n"
    "v_exp_f32 v126, v126\n"
    "v_sub_f32 v127, v127, v248\n"
    "v_exp_f32 v127, v127\n"
    "s_waitcnt vmcnt(7)\n"
    "v_smfmac_f32_16x16x64_bf16 v[160:163], v[250:253], v[184:191], v233\n"
    "buffer_load_dwordx4 v[184:187], v241, s[12:15], s74 offen offset:0 sc1\n"
    "v_sub_f32 v96, v96, v248\n"
    "v_exp_f32 v96, v96\n"
    "v_sub_f32 v97, v97, v248\n"
    "v_exp_f32 v97, v97\n"
    "v_smfmac_f32_32x32x32_bf16 v[128:143], v[76:79], v[120:127], v232\n"
    "buffer_load_dwordx4 v[188:191], v241, s[12:15], s74 offen offset:1024 sc1\n"
    "v_sub_f32 v98, v98, v248\n"
    "v_exp_f32 v98, v98\n"
    "v_sub_f32 v99, v99, v248\n"
    "v_exp_f32 v99, v99\n"
    "v_sub_f32 v100, v100, v248\n"
    "v_exp_f32 v100, v100\n"
    "v_mfma_f32_32x32x16_bf16 v[112:127], v[28:31], v[148:151], 0\n"
    "v_sub_f32 v101, v101, v248\n"
    "v_exp_f32 v101, v101\n"
    "v_sub_f32 v102, v102, v248\n"
    "v_exp_f32 v102, v102\n"
    "v_sub_f32 v103, v103, v248\n"
    "v_exp_f32 v103, v103\n"
    "v_sub_f32 v104, v104, v248\n"
    "v_exp_f32 v104, v104\n"
    "v_sub_f32 v105, v105, v248\n"
    "v_exp_f32 v105, v105\n"
    "v_smfmac_f32_32x32x32_bf16 v[128:143], v[80:83], v[96:103], v232\n"
    "v_sub_f32 v106, v106, v248\n"
    "v_exp_f32 v106, v106\n"
    "v_sub_f32 v107, v107, v248\n"
    "v_exp_f32 v107, v107\n"
    "v_sub_f32 v108, v108, v248\n"
    "v_exp_f32 v108, v108\n"
    "v_sub_f32 v109, v109, v248\n"
    "v_exp_f32 v109, v109\n"
    "v_sub_f32 v110, v110, v248\n"
    "v_exp_f32 v110, v110\n"
    "v_sub_f32 v111, v111, v248\n"
    "v_exp_f32 v111, v111\n"
    "s_waitcnt vmcnt(7)\n"
    "v_smfmac_f32_16x16x64_bf16 v[164:167], v[250:253], v[192:199], v233\n"
    "buffer_load_dwordx4 v[192:195], v241, s[12:15], s74 offen offset:2048 sc1\n"
    "v_sub_f32 v112, v112, v248\n"
    "v_exp_f32 v112, v112\n"
    "v_sub_f32 v113, v113, v248\n"
    "v_exp_f32 v113, v113\n"
    "v_smfmac_f32_32x32x32_bf16 v[128:143], v[84:87], v[104:111], v232\n"
    "buffer_load_dwordx4 v[196:199], v241, s[12:15], s74 offen offset:3072 sc1\n"
    "v_sub_f32 v114, v114, v248\n"
    "v_exp_f32 v114, v114\n"
    "v_sub_f32 v115, v115, v248\n"
    "v_exp_f32 v115, v115\n"
    "v_sub_f32 v116, v116, v248\n"
    "v_exp_f32 v116, v116\n"
    "s_waitcnt vmcnt(8)\n"
    "v_mfma_f32_32x32x16_bf16 v[96:111], v[0:3], v[144:147], 0\n"
    "v_sub_f32 v117, v117, v248\n"
    "v_exp_f32 v117, v117\n"
    "v_sub_f32 v118, v118, v248\n"
    "v_exp_f32 v118, v118\n"
    "v_sub_f32 v119, v119, v248\n"
    "v_exp_f32 v119, v119\n"
    "v_sub_f32 v120, v120, v248\n"
    "v_exp_f32 v120, v120\n"
    "v_sub_f32 v121, v121, v248\n"
    "v_exp_f32 v121, v121\n"
    "v_smfmac_f32_32x32x32_bf16 v[128:143], v[88:91], v[112:119], v232\n"
    "v_sub_f32 v122, v122, v248\n"
    "v_exp_f32 v122, v122\n"
    "v_sub_f32 v123, v123, v248\n"
    "v_exp_f32 v123, v123\n"
    "v_sub_f32 v124, v124, v248\n"
    "v_exp_f32 v124, v124\n"
    "v_sub_f32 v125, v125, v248\n"
    "v_exp_f32 v125, v125\n"
    "v_sub_f32 v126, v126, v248\n"
    "v_exp_f32 v126, v126\n"
    "v_sub_f32 v127, v127, v248\n"
    "v_exp_f32 v127, v127\n"
    "s_setprio 0\n"
    "v_and_b32 v248, 31, v240\n"
    "v_lshlrev_b32 v248, 5, v248\n"
    "v_add_u32 v248, 160792, v248\n"
    "ds_read_b32 v248, v248\n"
    "s_waitcnt lgkmcnt(0)\n"
    "v_sub_f32 v96, v96, v248\n"
    "v_exp_f32 v96, v96\n"
    "v_sub_f32 v97, v97, v248\n"
    "v_exp_f32 v97, v97\n"
    "v_smfmac_f32_32x32x32_bf16 v[128:143], v[92:95], v[120:127], v232\n"
    "s_xor_b32 s72, s71, 7168\n"
    "buffer_load_dwordx4 v[148:151], v241, s[12:15], s72 offen sc1\n"
    "v_sub_f32 v98, v98, v248\n"
    "v_exp_f32 v98, v98\n"
    "v_sub_f32 v99, v99, v248\n"
    "v_exp_f32 v99, v99\n"
    "v_sub_f32 v100, v100, v248\n"
    "v_exp_f32 v100, v100\n"
    "v_mfma_f32_32x32x16_bf16 v[112:127], v[4:7], v[144:147], 0\n"
    "v_sub_f32 v101, v101, v248\n"
    "v_exp_f32 v101, v101\n"
    "v_sub_f32 v102, v102, v248\n"
    "v_exp_f32 v102, v102\n"
    "v_sub_f32 v103, v103, v248\n"
    "v_exp_f32 v103, v103\n"
    "v_sub_f32 v104, v104, v248\n"
    "v_exp_f32 v104, v104\n"
    "v_sub_f32 v105, v105, v248\n"
    "v_exp_f32 v105, v105\n"
    "v_sub_f32 v106, v106, v248\n"
    "v_exp_f32 v106, v106\n"
    "v_sub_f32 v107, v107, v248\n"
    "v_exp_f32 v107, v107\n"
    "v_sub_f32 v108, v108, v248\n"
    "v_exp_f32 v108, v108\n"
    "ds_write_b128 v242, v[128:131] offset:9216\n"
    "ds_write_b128 v242, v[132:135] offset:10240\n"
    "ds_write_b32 v243, v136 offset:9216\n"
    "v_mfma_f32_32x32x8_bf16 v[128:143], v[234:235], v[234:235], 0\n"
    "v_sub_f32 v109, v109, v248\n"
    "v_exp_f32 v109, v109\n"
    "v_sub_f32 v110, v110, v248\n"
    "v_exp_f32 v110, v110\n"
    "v_sub_f32 v111, v111, v248\n"
    "v_exp_f32 v111, v111\n"
    "v_smfmac_f32_32x32x32_bf16 v[128:143], v[32:35], v[96:103], v232\n"
    "v_sub_f32 v112, v112, v248\n"
    "v_exp_f32 v112, v112\n"
    "v_sub_f32 v113, v113, v248\n"
    "v_exp_f32 v113, v113\n"
    "v_smfmac_f32_32x32x32_bf16 v[128:143], v[36:39], v[104:111], v232\n"
    "v_sub_f32 v114, v114, v248\n"
    "v_exp_f32 v114, v114\n"
    "v_sub_f32 v115, v115, v248\n"
    "v_exp_f32 v115, v115\n"
    "v_sub_f32 v116, v116, v248\n"
    "v_exp_f32 v116, v116\n"
    "v_mfma_f32_32x32x16_bf16 v[96:111], v[8:11], v[144:147], 0\n"
    "v_sub_f32 v117, v117, v248\n"
    "v_exp_f32 v117, v117\n"
    "v_sub_f32 v118, v118, v248\n"
    "v_exp_f32 v118, v118\n"
    "v_sub_f32 v119, v119, v248\n"
    "v_exp_f32 v119, v119\n"
    "v_sub_f32 v120, v120, v248\n"
    "v_exp_f32 v120, v120\n"
    "v_sub_f32 v121, v121, v248\n"
    "v_exp_f32 v121, v121\n"
    "v_smfmac_f32_32x32x32_bf16 v[128:143], v[40:43], v[112:119], v232\n"
    "v_sub_f32 v122, v122, v248\n"
    "v_exp_f32 v122, v122\n"
    "v_sub_f32 v123, v123, v248\n"
    "v_exp_f32 v123, v123\n"
    "v_sub_f32 v124, v124, v248\n"
    "v_exp_f32 v124, v124\n"
    "v_sub_f32 v125, v125, v248\n"
    "v_exp_f32 v125, v125\n"
    "v_sub_f32 v126, v126, v248\n"
    "v_exp_f32 v126, v126\n"
    "v_sub_f32 v127, v127, v248\n"
    "v_exp_f32 v127, v127\n"
    "s_waitcnt lgkmcnt(0)\n"
    "s_barrier\n"
    "s_setprio 2\n"
    "v_sub_f32 v96, v96, v248\n"
    "v_exp_f32 v96, v96\n"
    "v_sub_f32 v97, v97, v248\n"
    "v_exp_f32 v97, v97\n"
    "v_smfmac_f32_32x32x32_bf16 v[128:143], v[44:47], v[120:127], v232\n"
    "ds_read_b128 v[250:253], v247 offset:1024\n"
    "v_sub_f32 v98, v98, v248\n"
    "v_exp_f32 v98, v98\n"
    "v_sub_f32 v99, v99, v248\n"
    "v_exp_f32 v99, v99\n"
    "v_sub_f32 v100, v100, v248\n"
    "v_exp_f32 v100, v100\n"
    "v_mfma_f32_32x32x16_bf16 v[112:127], v[12:15], v[144:147], 0\n"
    "ds_read_b128 v[208:211], v245 offset:0\n"
    "ds_read_b64 v[200:201], v244 offset:0\n"
    "ds_read_b64 v[202:203], v244 offset:2048\n"
    "ds_read_b64 v[204:205], v244 offset:4096\n"
    "ds_read_b64 v[206:207], v244 offset:6144\n"
    "v_sub_f32 v101, v101, v248\n"
    "v_exp_f32 v101, v101\n"
    "v_sub_f32 v102, v102, v248\n"
    "v_exp_f32 v102, v102\n"
    "v_sub_f32 v103, v103, v248\n"
    "v_exp_f32 v103, v103\n"
    "v_sub_f32 v104, v104, v248\n"
    "v_exp_f32 v104, v104\n"
    "v_sub_f32 v105, v105, v248\n"
    "v_exp_f32 v105, v105\n"
    "v_smfmac_f32_32x32x32_bf16 v[128:143], v[48:51], v[96:103], v232\n"
    "v_sub_f32 v106, v106, v248\n"
    "v_exp_f32 v106, v106\n"
    "v_sub_f32 v107, v107, v248\n"
    "v_exp_f32 v107, v107\n"
    "v_sub_f32 v108, v108, v248\n"
    "v_exp_f32 v108, v108\n"
    "v_sub_f32 v109, v109, v248\n"
    "v_exp_f32 v109, v109\n"
    "v_sub_f32 v110, v110, v248\n"
    "v_exp_f32 v110, v110\n"
    "v_sub_f32 v111, v111, v248\n"
    "v_exp_f32 v111, v111\n"
    "v_sub_f32 v112, v112, v248\n"
    "v_exp_f32 v112, v112\n"
    "v_sub_f32 v113, v113, v248\n"
    "v_exp_f32 v113, v113\n"
    "v_smfmac_f32_32x32x32_bf16 v[128:143], v[52:55], v[104:111], v232\n"
    "v_sub_f32 v114, v114, v248\n"
    "v_exp_f32 v114, v114\n"
    "v_sub_f32 v115, v115, v248\n"
    "v_exp_f32 v115, v115\n"
    "v_sub_f32 v116, v116, v248\n"
    "v_exp_f32 v116, v116\n"
    "v_mfma_f32_32x32x16_bf16 v[96:111], v[16:19], v[144:147], 0\n"
    "s_waitcnt lgkmcnt(4)\n"
    "v_add_f32 v208, v208, v209\n"
    "v_add_f32 v210, v210, v211\n"
    "s_waitcnt lgkmcnt(2)\n"
    "v_add_f32 v200, v200, v202\n"
    "v_add_f32 v208, v208, v210\n"
    "v_add_f32 v201, v201, v203\n"
    "v_rcp_f32 v209, v208\n"
    "v_sub_f32 v117, v117, v248\n"
    "v_exp_f32 v117, v117\n"
    "v_sub_f32 v118, v118, v248\n"
    "v_exp_f32 v118, v118\n"
    "v_sub_f32 v119, v119, v248\n"
    "v_exp_f32 v119, v119\n"
    "v_sub_f32 v120, v120, v248\n"
    "v_exp_f32 v120, v120\n"
    "v_sub_f32 v121, v121, v248\n"
    "v_exp_f32 v121, v121\n"
    "v_smfmac_f32_32x32x32_bf16 v[128:143], v[56:59], v[112:119], v232\n"
    "s_waitcnt lgkmcnt(0)\n"
    "v_add_f32 v204, v204, v206\n"
    "v_add_f32 v205, v205, v207\n"
    "v_add_f32 v200, v200, v204\n"
    "v_add_f32 v201, v201, v205\n"
    "v_mul_f32 v200, v200, v209\n"
    "v_mul_f32 v201, v201, v209\n"
    "s_xor_b32 s73, s75, 262144\n"
    "s_xor_b32 s74, s75, 266240\n"
    "v_sub_f32 v122, v122, v248\n"
    "v_exp_f32 v122, v122\n"
    "v_sub_f32 v123, v123, v248\n"
    "v_exp_f32 v123, v123\n"
    "v_sub_f32 v124, v124, v248\n"
    "v_exp_f32 v124, v124\n"
    "v_sub_f32 v125, v125, v248\n"
    "v_exp_f32 v125, v125\n"
    "v_sub_f32 v126, v126, v248\n"
    "v_exp_f32 v126, v126\n"
    "v_sub_f32 v127, v127, v248\n"
    "v_exp_f32 v127, v127\n"
    "s_waitcnt vmcnt(7)\n"
    "v_smfmac_f32_16x16x64_bf16 v[152:155], v[250:253], v[168:175], v233\n"
    "buffer_load_dwordx4 v[168:171], v241, s[12:15], s73 offen offset:0 sc1\n"
    "v_sub_f32 v96, v96, v248\n"
    "v_exp_f32 v96, v96\n"
    "v_sub_f32 v97, v97, v248\n"
    "v_exp_f32 v97, v97\n"
    "v_smfmac_f32_32x32x32_bf16 v[128:143], v[60:63], v[120:127], v232\n"
    "buffer_load_dwordx4 v[172:175], v241, s[12:15], s73 offen offset:1024 sc1\n"
    "v_sub_f32 v98, v98, v248\n"
    "v_exp_f32 v98, v98\n"
    "v_sub_f32 v99, v99, v248\n"
    "v_exp_f32 v99, v99\n"
    "v_sub_f32 v100, v100, v248\n"
    "v_exp_f32 v100, v100\n"
    "v_mfma_f32_32x32x16_bf16 v[112:127], v[20:23], v[144:147], 0\n"
    "v_max3_f32 v238, v238, v208, v209\n"
    "v_cvt_pk_bf16_f32 v202, v200, v201\n"
    "ds_write_b32 v246, v202 offset:0\n"
    "v_fmac_f32 v236, v200, v201\n"
    "v_sub_f32 v101, v101, v248\n"
    "v_exp_f32 v101, v101\n"
    "v_sub_f32 v102, v102, v248\n"
    "v_exp_f32 v102, v102\n"
    "v_sub_f32 v103, v103, v248\n"
    "v_exp_f32 v103, v103\n"
    "v_sub_f32 v104, v104, v248\n"
    "v_exp_f32 v104, v104\n"
    "v_sub_f32 v105, v105, v248\n"
    "v_exp_f32 v105, v105\n"
    "v_smfmac_f32_32x32x32_bf16 v[128:143], v[64:67], v[96:103], v232\n"
    "v_sub_f32 v106, v106, v248\n"
    "v_exp_f32 v106, v106\n"
    "v_sub_f32 v107, v107, v248\n"
    "v_exp_f32 v107, v107\n"
    "v_sub_f32 v108, v108, v248\n"
    "v_exp_f32 v108, v108\n"
    "v_sub_f32 v109, v109, v248\n"
    "v_exp_f32 v109, v109\n"
    "v_sub_f32 v110, v110, v248\n"
    "v_exp_f32 v110, v110\n"
    "v_sub_f32 v111, v111, v248\n"
    "v_exp_f32 v111, v111\n"
    "s_waitcnt vmcnt(7)\n"
    "v_smfmac_f32_16x16x64_bf16 v[156:159], v[250:253], v[176:183], v233\n"
    "buffer_load_dwordx4 v[176:179], v241, s[12:15], s73 offen offset:2048 sc1\n"
    "v_sub_f32 v112, v112, v248\n"
    "v_exp_f32 v112, v112\n"
    "v_sub_f32 v113, v113, v248\n"
    "v_exp_f32 v113, v113\n"
    "v_smfmac_f32_32x32x32_bf16 v[128:143], v[68:71], v[104:111], v232\n"
    "buffer_load_dwordx4 v[180:183], v241, s[12:15], s73 offen offset:3072 sc1\n"
    "v_sub_f32 v114, v114, v248\n"
    "v_exp_f32 v114, v114\n"
    "v_sub_f32 v115, v115, v248\n"
    "v_exp_f32 v115, v115\n"
    "v_sub_f32 v116, v116, v248\n"
    "v_exp_f32 v116, v116\n"
    "v_mfma_f32_32x32x16_bf16 v[96:111], v[24:27], v[144:147], 0\n"
    "v_sub_f32 v117, v117, v248\n"
    "v_exp_f32 v117, v117\n"
    "v_sub_f32 v118, v118, v248\n"
    "v_exp_f32 v118, v118\n"
    "v_sub_f32 v119, v119, v248\n"
    "v_exp_f32 v119, v119\n"
    "v_sub_f32 v120, v120, v248\n"
    "v_exp_f32 v120, v120\n"
    "v_sub_f32 v121, v121, v248\n"
    "v_exp_f32 v121, v121\n"
    "v_smfmac_f32_32x32x32_bf16 v[128:143], v[72:75], v[112:119], v232\n"
    "v_sub_f32 v122, v122, v248\n"
    "v_exp_f32 v122, v122\n"
    "v_sub_f32 v123, v123, v248\n"
    "v_exp_f32 v123, v123\n"
    "v_sub_f32 v124, v124, v248\n"
    "v_exp_f32 v124, v124\n"
    "v_sub_f32 v125, v125, v248\n"
    "v_exp_f32 v125, v125\n"
    "v_sub_f32 v126, v126, v248\n"
    "v_exp_f32 v126, v126\n"
    "v_sub_f32 v127, v127, v248\n"
    "v_exp_f32 v127, v127\n"
    "s_waitcnt vmcnt(7)\n"
    "v_smfmac_f32_16x16x64_bf16 v[160:163], v[250:253], v[184:191], v233\n"
    "buffer_load_dwordx4 v[184:187], v241, s[12:15], s74 offen offset:0 sc1\n"
    "v_sub_f32 v96, v96, v248\n"
    "v_exp_f32 v96, v96\n"
    "v_sub_f32 v97, v97, v248\n"
    "v_exp_f32 v97, v97\n"
    "v_smfmac_f32_32x32x32_bf16 v[128:143], v[76:79], v[120:127], v232\n"
    "buffer_load_dwordx4 v[188:191], v241, s[12:15], s74 offen offset:1024 sc1\n"
    "v_sub_f32 v98, v98, v248\n"
    "v_exp_f32 v98, v98\n"
    "v_sub_f32 v99, v99, v248\n"
    "v_exp_f32 v99, v99\n"
    "v_sub_f32 v100, v100, v248\n"
    "v_exp_f32 v100, v100\n"
    "v_mfma_f32_32x32x16_bf16 v[112:127], v[28:31], v[144:147], 0\n"
    "v_sub_f32 v101, v101, v248\n"
    "v_exp_f32 v101, v101\n"
    "v_sub_f32 v102, v102, v248\n"
    "v_exp_f32 v102, v102\n"
    "v_sub_f32 v103, v103, v248\n"
    "v_exp_f32 v103, v103\n"
    "v_sub_f32 v104, v104, v248\n"
    "v_exp_f32 v104, v104\n"
    "v_sub_f32 v105, v105, v248\n"
    "v_exp_f32 v105, v105\n"
    "v_smfmac_f32_32x32x32_bf16 v[128:143], v[80:83], v[96:103], v232\n"
    "v_sub_f32 v106, v106, v248\n"
    "v_exp_f32 v106, v106\n"
    "v_sub_f32 v107, v107, v248\n"
    "v_exp_f32 v107, v107\n"
    "v_sub_f32 v108, v108, v248\n"
    "v_exp_f32 v108, v108\n"
    "v_sub_f32 v109, v109, v248\n"
    "v_exp_f32 v109, v109\n"
    "v_sub_f32 v110, v110, v248\n"
    "v_exp_f32 v110, v110\n"
    "v_sub_f32 v111, v111, v248\n"
    "v_exp_f32 v111, v111\n"
    "s_waitcnt vmcnt(7)\n"
    "v_smfmac_f32_16x16x64_bf16 v[164:167], v[250:253], v[192:199], v233\n"
    "buffer_load_dwordx4 v[192:195], v241, s[12:15], s74 offen offset:2048 sc1\n"
    "v_sub_f32 v112, v112, v248\n"
    "v_exp_f32 v112, v112\n"
    "v_sub_f32 v113, v113, v248\n"
    "v_exp_f32 v113, v113\n"
    "v_smfmac_f32_32x32x32_bf16 v[128:143], v[84:87], v[104:111], v232\n"
    "buffer_load_dwordx4 v[196:199], v241, s[12:15], s74 offen offset:3072 sc1\n"
    "v_sub_f32 v114, v114, v248\n"
    "v_exp_f32 v114, v114\n"
    "v_sub_f32 v115, v115, v248\n"
    "v_exp_f32 v115, v115\n"
    "v_sub_f32 v116, v116, v248\n"
    "v_exp_f32 v116, v116\n"
    "s_waitcnt vmcnt(8)\n"
    "v_mfma_f32_32x32x16_bf16 v[96:111], v[0:3], v[148:151], 0\n"
    "v_sub_f32 v117, v117, v248\n"
    "v_exp_f32 v117, v117\n"
    "v_sub_f32 v118, v118, v248\n"
    "v_exp_f32 v118, v118\n"
    "v_sub_f32 v119, v119, v248\n"
    "v_exp_f32 v119, v119\n"
    "v_sub_f32 v120, v120, v248\n"
    "v_exp_f32 v120, v120\n"
    "v_sub_f32 v121, v121, v248\n"
    "v_exp_f32 v121, v121\n"
    "v_smfmac_f32_32x32x32_bf16 v[128:143], v[88:91], v[112:119], v232\n"
    "v_sub_f32 v122, v122, v248\n"
    "v_exp_f32 v122, v122\n"
    "v_sub_f32 v123, v123, v248\n"
    "v_exp_f32 v123, v123\n"
    "v_sub_f32 v124, v124, v248\n"
    "v_exp_f32 v124, v124\n"
    "v_sub_f32 v125, v125, v248\n"
    "v_exp_f32 v125, v125\n"
    "v_sub_f32 v126, v126, v248\n"
    "v_exp_f32 v126, v126\n"
    "v_sub_f32 v127, v127, v248\n"
    "v_exp_f32 v127, v127\n"
    "s_setprio 0\n"
    "v_and_b32 v248, 31, v240\n"
    "v_lshlrev_b32 v248, 5, v248\n"
    "v_add_u32 v248, 160796, v248\n"
    "ds_read_b32 v248, v248\n"
    "s_waitcnt lgkmcnt(0)\n"
    "v_sub_f32 v96, v96, v248\n"
    "v_exp_f32 v96, v96\n"
    "v_sub_f32 v97, v97, v248\n"
    "v_exp_f32 v97, v97\n"
    "v_smfmac_f32_32x32x32_bf16 v[128:143], v[92:95], v[120:127], v232\n"
    "v_sub_f32 v98, v98, v248\n"
    "v_exp_f32 v98, v98\n"
    "v_sub_f32 v99, v99, v248\n"
    "v_exp_f32 v99, v99\n"
    "v_sub_f32 v100, v100, v248\n"
    "v_exp_f32 v100, v100\n"
    "v_mfma_f32_32x32x16_bf16 v[112:127], v[4:7], v[148:151], 0\n"
    "v_sub_f32 v101, v101, v248\n"
    "v_exp_f32 v101, v101\n"
    "v_sub_f32 v102, v102, v248\n"
    "v_exp_f32 v102, v102\n"
    "v_sub_f32 v103, v103, v248\n"
    "v_exp_f32 v103, v103\n"
    "v_sub_f32 v104, v104, v248\n"
    "v_exp_f32 v104, v104\n"
    "v_sub_f32 v105, v105, v248\n"
    "v_exp_f32 v105, v105\n"
    "v_sub_f32 v106, v106, v248\n"
    "v_exp_f32 v106, v106\n"
    "v_sub_f32 v107, v107, v248\n"
    "v_exp_f32 v107, v107\n"
    "v_sub_f32 v108, v108, v248\n"
    "v_exp_f32 v108, v108\n"
    "ds_write_b128 v242, v[128:131] offset:0\n"
    "ds_write_b128 v242, v[132:135] offset:1024\n"
    "ds_write_b32 v243, v136 offset:0\n"
    "v_mfma_f32_32x32x8_bf16 v[128:143], v[234:235], v[234:235], 0\n"
    "v_sub_f32 v109, v109, v248\n"
    "v_exp_f32 v109, v109\n"
    "v_sub_f32 v110, v110, v248\n"
    "v_exp_f32 v110, v110\n"
    "v_sub_f32 v111, v111, v248\n"
    "v_exp_f32 v111, v111\n"
    "v_smfmac_f32_32x32x32_bf16 v[128:143], v[32:35], v[96:103], v232\n"
    "v_sub_f32 v112, v112, v248\n"
    "v_exp_f32 v112, v112\n"
    "v_sub_f32 v113, v113, v248\n"
    "v_exp_f32 v113, v113\n"
    "v_smfmac_f32_32x32x32_bf16 v[128:143], v[36:39], v[104:111], v232\n"
    "v_sub_f32 v114, v114, v248\n"
    "v_exp_f32 v114, v114\n"
    "v_sub_f32 v115, v115, v248\n"
    "v_exp_f32 v115, v115\n"
    "v_sub_f32 v116, v116, v248\n"
    "v_exp_f32 v116, v116\n"
    "v_mfma_f32_32x32x16_bf16 v[96:111], v[8:11], v[148:151], 0\n"
    "v_sub_f32 v117, v117, v248\n"
    "v_exp_f32 v117, v117\n"
    "v_sub_f32 v118, v118, v248\n"
    "v_exp_f32 v118, v118\n"
    "v_sub_f32 v119, v119, v248\n"
    "v_exp_f32 v119, v119\n"
    "v_sub_f32 v120, v120, v248\n"
    "v_exp_f32 v120, v120\n"
    "v_sub_f32 v121, v121, v248\n"
    "v_exp_f32 v121, v121\n"
    "v_smfmac_f32_32x32x32_bf16 v[128:143], v[40:43], v[112:119], v232\n"
    "v_sub_f32 v122, v122, v248\n"
    "v_exp_f32 v122, v122\n"
    "v_sub_f32 v123, v123, v248\n"
    "v_exp_f32 v123, v123\n"
    "v_sub_f32 v124, v124, v248\n"
    "v_exp_f32 v124, v124\n"
    "v_sub_f32 v125, v125, v248\n"
    "v_exp_f32 v125, v125\n"
    "v_sub_f32 v126, v126, v248\n"
    "v_exp_f32 v126, v126\n"
    "v_sub_f32 v127, v127, v248\n"
    "v_exp_f32 v127, v127\n"
    "s_waitcnt lgkmcnt(0)\n"
    "s_barrier\n"
    "s_setprio 2\n"
    "v_sub_f32 v96, v96, v248\n"
    "v_exp_f32 v96, v96\n"
    "v_sub_f32 v97, v97, v248\n"
    "v_exp_f32 v97, v97\n"
    "v_smfmac_f32_32x32x32_bf16 v[128:143], v[44:47], v[120:127], v232\n"
    "ds_read_b128 v[250:253], v247 offset:0\n"
    "v_sub_f32 v98, v98, v248\n"
    "v_exp_f32 v98, v98\n"
    "v_sub_f32 v99, v99, v248\n"
    "v_exp_f32 v99, v99\n"
    "v_sub_f32 v100, v100, v248\n"
    "v_exp_f32 v100, v100\n"
    "v_mfma_f32_32x32x16_bf16 v[112:127], v[12:15], v[148:151], 0\n"
    "ds_read_b128 v[208:211], v245 offset:512\n"
    "ds_read_b64 v[200:201], v244 offset:8192\n"
    "ds_read_b64 v[202:203], v244 offset:10240\n"
    "ds_read_b64 v[204:205], v244 offset:12288\n"
    "ds_read_b64 v[206:207], v244 offset:14336\n"
    "v_sub_f32 v101, v101, v248\n"
    "v_exp_f32 v101, v101\n"
    "v_sub_f32 v102, v102, v248\n"
    "v_exp_f32 v102, v102\n"
    "v_sub_f32 v103, v103, v248\n"
    "v_exp_f32 v103, v103\n"
    "v_sub_f32 v104, v104, v248\n"
    "v_exp_f32 v104, v104\n"
    "v_sub_f32 v105, v105, v248\n"
    "v_exp_f32 v105, v105\n"
    "v_smfmac_f32_32x32x32_bf16 v[128:143], v[48:51], v[96:103], v232\n"
    "v_sub_f32 v106, v106, v248\n"
    "v_exp_f32 v106, v106\n"
    "v_sub_f32 v107, v107, v248\n"
    "v_exp_f32 v107, v107\n"
    "v_sub_f32 v108, v108, v248\n"
    "v_exp_f32 v108, v108\n"
    "v_sub_f32 v109, v109, v248\n"
    "v_exp_f32 v109, v109\n"
    "v_sub_f32 v110, v110, v248\n"
    "v_exp_f32 v110, v110\n"
    "v_sub_f32 v111, v111, v248\n"
    "v_exp_f32 v111, v111\n"
    "v_sub_f32 v112, v112, v248\n"
    "v_exp_f32 v112, v112\n"
    "v_sub_f32 v113, v113, v248\n"
    "v_exp_f32 v113, v113\n"
    "v_smfmac_f32_32x32x32_bf16 v[128:143], v[52:55], v[104:111], v232\n"
    "s_add_u32 s64, s56, s51\n"
    "s_lshl_b32 s64, s64, 15\n"
    "s_add_u32 s64, s64, 3158016\n"
    "v_sub_f32 v114, v114, v248\n"
    "v_exp_f32 v114, v114\n"
    "v_sub_f32 v115, v115, v248\n"
    "v_exp_f32 v115, v115\n"
    "v_sub_f32 v116, v116, v248\n"
    "v_exp_f32 v116, v116\n"
    "v_mfma_f32_32x32x16_bf16 v[96:111], v[16:19], v[148:151], 0\n"
    "s_waitcnt lgkmcnt(4)\n"
    "v_add_f32 v208, v208, v209\n"
    "v_add_f32 v210, v210, v211\n"
    "s_waitcnt lgkmcnt(2)\n"
    "v_add_f32 v200, v200, v202\n"
    "v_add_f32 v208, v208, v210\n"
    "v_add_f32 v201, v201, v203\n"
    "v_rcp_f32 v209, v208\n"
    "v_sub_f32 v117, v117, v248\n"
    "v_exp_f32 v117, v117\n"
    "v_sub_f32 v118, v118, v248\n"
    "v_exp_f32 v118, v118\n"
    "v_sub_f32 v119, v119, v248\n"
    "v_exp_f32 v119, v119\n"
    "v_sub_f32 v120, v120, v248\n"
    "v_exp_f32 v120, v120\n"
    "v_sub_f32 v121, v121, v248\n"
    "v_exp_f32 v121, v121\n"
    "v_smfmac_f32_32x32x32_bf16 v[128:143], v[56:59], v[112:119], v232\n"
    "s_waitcnt lgkmcnt(0)\n"
    "v_add_f32 v204, v204, v206\n"
    "v_add_f32 v205, v205, v207\n"
    "v_add_f32 v200, v200, v204\n"
    "v_add_f32 v201, v201, v205\n"
    "v_mul_f32 v200, v200, v209\n"
    "v_mul_f32 v201, v201, v209\n"
    "s_xor_b32 s73, s75, 327680\n"
    "s_xor_b32 s74, s75, 331776\n"
    "v_sub_f32 v122, v122, v248\n"
    "v_exp_f32 v122, v122\n"
    "v_sub_f32 v123, v123, v248\n"
    "v_exp_f32 v123, v123\n"
    "v_sub_f32 v124, v124, v248\n"
    "v_exp_f32 v124, v124\n"
    "v_sub_f32 v125, v125, v248\n"
    "v_exp_f32 v125, v125\n"
    "v_sub_f32 v126, v126, v248\n"
    "v_exp_f32 v126, v126\n"
    "v_sub_f32 v127, v127, v248\n"
    "v_exp_f32 v127, v127\n"
    "s_waitcnt vmcnt(6)\n"
    "v_smfmac_f32_16x16x64_bf16 v[152:155], v[250:253], v[168:175], v233\n"
    "buffer_load_dwordx4 v[168:171], v241, s[12:15], s73 offen offset:0 sc1\n"
    "v_sub_f32 v96, v96, v248\n"
    "v_exp_f32 v96, v96\n"
    "v_sub_f32 v97, v97, v248\n"
    "v_exp_f32 v97, v97\n"
    "v_smfmac_f32_32x32x32_bf16 v[128:143], v[60:63], v[120:127], v232\n"
    "buffer_load_dwordx4 v[172:175], v241, s[12:15], s73 offen offset:1024 sc1\n"
    "v_sub_f32 v98, v98, v248\n"
    "v_exp_f32 v98, v98\n"
    "v_sub_f32 v99, v99, v248\n"
    "v_exp_f32 v99, v99\n"
    "v_sub_f32 v100, v100, v248\n"
    "v_exp_f32 v100, v100\n"
    "v_mfma_f32_32x32x16_bf16 v[112:127], v[20:23], v[148:151], 0\n"
    "v_max3_f32 v238, v238, v208, v209\n"
    "v_cvt_pk_bf16_f32 v202, v200, v201\n"
    "ds_write_b32 v246, v202 offset:1024\n"
    "v_fmac_f32 v236, v200, v201\n"
    "v_sub_f32 v101, v101, v248\n"
    "v_exp_f32 v101, v101\n"
    "v_sub_f32 v102, v102, v248\n"
    "v_exp_f32 v102, v102\n"
    "v_sub_f32 v103, v103, v248\n"
    "v_exp_f32 v103, v103\n"
    "v_sub_f32 v104, v104, v248\n"
    "v_exp_f32 v104, v104\n"
    "v_sub_f32 v105, v105, v248\n"
    "v_exp_f32 v105, v105\n"
    "v_smfmac_f32_32x32x32_bf16 v[128:143], v[64:67], v[96:103], v232\n"
    "v_sub_f32 v106, v106, v248\n"
    "v_exp_f32 v106, v106\n"
    "v_sub_f32 v107, v107, v248\n"
    "v_exp_f32 v107, v107\n"
    "v_sub_f32 v108, v108, v248\n"
    "v_exp_f32 v108, v108\n"
    "v_sub_f32 v109, v109, v248\n"
    "v_exp_f32 v109, v109\n"
    "v_sub_f32 v110, v110, v248\n"
    "v_exp_f32 v110, v110\n"
    "v_sub_f32 v111, v111, v248\n"
    "v_exp_f32 v111, v111\n"
    "s_waitcnt vmcnt(6)\n"
    "v_smfmac_f32_16x16x64_bf16 v[156:159], v[250:253], v[176:183], v233\n"
    "buffer_load_dwordx4 v[176:179], v241, s[12:15], s73 offen offset:2048 sc1\n"
    "v_sub_f32 v112, v112, v248\n"
    "v_exp_f32 v112, v112\n"
    "v_sub_f32 v113, v113, v248\n"
    "v_exp_f32 v113, v113\n"
    "v_smfmac_f32_32x32x32_bf16 v[128:143], v[68:71], v[104:111], v232\n"
    "buffer_load_dwordx4 v[180:183], v241, s[12:15], s73 offen offset:3072 sc1\n"
    "v_sub_f32 v114, v114, v248\n"
    "v_exp_f32 v114, v114\n"
    "v_sub_f32 v115, v115, v248\n"
    "v_exp_f32 v115, v115\n"
    "v_sub_f32 v116, v116, v248\n"
    "v_exp_f32 v116, v116\n"
    "v_mfma_f32_32x32x16_bf16 v[96:111], v[24:27], v[148:151], 0\n"
    "v_sub_f32 v117, v117, v248\n"
    "v_exp_f32 v117, v117\n"
    "v_sub_f32 v118, v118, v248\n"
    "v_exp_f32 v118, v118\n"
    "v_sub_f32 v119, v119, v248\n"
    "v_exp_f32 v119, v119\n"
    "v_sub_f32 v120, v120, v248\n"
    "v_exp_f32 v120, v120\n"
    "v_sub_f32 v121, v121, v248\n"
    "v_exp_f32 v121, v121\n"
    "v_smfmac_f32_32x32x32_bf16 v[128:143], v[72:75], v[112:119], v232\n"
    "v_sub_f32 v122, v122, v248\n"
    "v_exp_f32 v122, v122\n"
    "v_sub_f32 v123, v123, v248\n"
    "v_exp_f32 v123, v123\n"
    "v_sub_f32 v124, v124, v248\n"
    "v_exp_f32 v124, v124\n"
    "v_sub_f32 v125, v125, v248\n"
    "v_exp_f32 v125, v125\n"
    "v_sub_f32 v126, v126, v248\n"
    "v_exp_f32 v126, v126\n"
    "v_sub_f32 v127, v127, v248\n"
    "v_exp_f32 v127, v127\n"
    "s_waitcnt vmcnt(6)\n"
    "v_smfmac_f32_16x16x64_bf16 v[160:163], v[250:253], v[184:191], v233\n"
    "buffer_load_dwordx4 v[184:187], v241, s[12:15], s74 offen offset:0 sc1\n"
    "v_sub_f32 v96, v96, v248\n"
    "v_exp_f32 v96, v96\n"
    "v_sub_f32 v97, v97, v248\n"
    "v_exp_f32 v97, v97\n"
    "v_smfmac_f32_32x32x32_bf16 v[128:143], v[76:79], v[120:127], v232\n"
    "buffer_load_dwordx4 v[188:191], v241, s[12:15], s74 offen offset:1024 sc1\n"
    "v_sub_f32 v98, v98, v248\n"
    "v_exp_f32 v98, v98\n"
    "v_sub_f32 v99, v99, v248\n"
    "v_exp_f32 v99, v99\n"
    "v_sub_f32 v100, v100, v248\n"
    "v_exp_f32 v100, v100\n"
    "v_mfma_f32_32x32x16_bf16 v[112:127], v[28:31], v[148:151], 0\n"
    "buffer_load_dwordx4 v[48:51], v241, s[12:15], s64 offen offset:0 sc1\n"
    "v_sub_f32 v101, v101, v248\n"
    "v_exp_f32 v101, v101\n"
    "v_sub_f32 v102, v102, v248\n"
    "v_exp_f32 v102, v102\n"
    "v_sub_f32 v103, v103, v248\n"
    "v_exp_f32 v103, v103\n"
    "v_sub_f32 v104, v104, v248\n"
    "v_exp_f32 v104, v104\n"
    "v_sub_f32 v105, v105, v248\n"
    "v_exp_f32 v105, v105\n"
    "v_smfmac_f32_32x32x32_bf16 v[128:143], v[80:83], v[96:103], v232\n"
    "buffer_load_dwordx4 v[52:55], v241, s[12:15], s64 offen offset:1024 sc1\n"
    "v_sub_f32 v106, v106, v248\n"
    "v_exp_f32 v106, v106\n"
    "v_sub_f32 v107, v107, v248\n"
    "v_exp_f32 v107, v107\n"
    "v_sub_f32 v108, v108, v248\n"
    "v_exp_f32 v108, v108\n"
    "v_sub_f32 v109, v109, v248\n"
    "v_exp_f32 v109, v109\n"
    "v_sub_f32 v110, v110, v248\n"
    "v_exp_f32 v110, v110\n"
    "v_sub_f32 v111, v111, v248\n"
    "v_exp_f32 v111, v111\n"
    "s_waitcnt vmcnt(8)\n"
    "v_smfmac_f32_16x16x64_bf16 v[164:167], v[250:253], v[192:199], v233\n"
    "buffer_load_dwordx4 v[192:195], v241, s[12:15], s74 offen offset:2048 sc1\n"
    "v_sub_f32 v112, v112, v248\n"
    "v_exp_f32 v112, v112\n"
    "v_sub_f32 v113, v113, v248\n"
    "v_exp_f32 v113, v113\n"
    "v_smfmac_f32_32x32x32_bf16 v[128:143], v[84:87], v[104:111], v232\n"
    "buffer_load_dwordx4 v[196:199], v241, s[12:15], s74 offen offset:3072 sc1\n"
    "v_sub_f32 v114, v114, v248\n"
    "v_exp_f32 v114, v114\n"
    "v_sub_f32 v115, v115, v248\n"
    "v_exp_f32 v115, v115\n"
    "v_sub_f32 v116, v116, v248\n"
    "v_exp_f32 v116, v116\n"
    "buffer_load_dwordx4 v[56:59], v241, s[12:15], s64 offen offset:2048 sc1\n"
    "v_sub_f32 v117, v117, v248\n"
    "v_exp_f32 v117, v117\n"
    "v_sub_f32 v118, v118, v248\n"
    "v_exp_f32 v118, v118\n"
    "v_sub_f32 v119, v119, v248\n"
    "v_exp_f32 v119, v119\n"
    "v_sub_f32 v120, v120, v248\n"
    "v_exp_f32 v120, v120\n"
    "v_sub_f32 v121, v121, v248\n"
    "v_exp_f32 v121, v121\n"
    "v_smfmac_f32_32x32x32_bf16 v[128:143], v[88:91], v[112:119], v232\n"
    "buffer_load_dwordx4 v[60:63], v241, s[12:15], s64 offen offset:3072 sc1\n"
    "v_sub_f32 v122, v122, v248\n"
    "v_exp_f32 v122, v122\n"
    "v_sub_f32 v123, v123, v248\n"
    "v_exp_f32 v123, v123\n"
    "v_sub_f32 v124, v124, v248\n"
    "v_exp_f32 v124, v124\n"
    "v_sub_f32 v125, v125, v248\n"
    "v_exp_f32 v125, v125\n"
    "v_sub_f32 v126, v126, v248\n"
    "v_exp_f32 v126, v126\n"
    "v_sub_f32 v127, v127, v248\n"
    "v_exp_f32 v127, v127\n"
    "s_nop 1\n"
    "v_smfmac_f32_32x32x32_bf16 v[128:143], v[92:95], v[120:127], v232\n"
    "s_xor_b32 s63, s75, 462848\n"
    "s_mul_i32 s62, s51, 12288\n"
    "s_add_u32 s62, s62, 65536\n"
    "v_add_u32 v249, s62, v241\n"
    "s_setprio 0\n"
    "s_add_u32 s64, s64, 4096\n"
    "buffer_load_dwordx4 v[64:67], v241, s[12:15], s64 offen offset:0 sc1\n"
    "buffer_load_dwordx4 v[68:71], v241, s[12:15], s64 offen offset:1024 sc1\n"
    "ds_read_b128 v[0:3], v249 offset:0\n"
    "ds_read_b128 v[4:7], v249 offset:1024\n"
    "ds_read_b128 v[8:11], v249 offset:2048\n"
    "ds_read_b128 v[12:15], v249 offset:3072\n"
    "ds_read_b128 v[16:19], v249 offset:4096\n"
    "ds_read_b128 v[20:23], v249 offset:5120\n"
    "ds_read_b128 v[24:27], v249 offset:6144\n"
    "ds_read_b128 v[28:31], v249 offset:7168\n"
    "ds_read_b128 v[32:35], v249 offset:8192\n"
    "ds_read_b128 v[36:39], v249 offset:9216\n"
    "ds_read_b128 v[40:43], v249 offset:10240\n"
    "ds_read_b128 v[44:47], v249 offset:11264\n"
    "ds_write_b128 v242, v[128:131] offset:9216\n"
    "ds_write_b128 v242, v[132:135] offset:10240\n"
    "ds_write_b32 v243, v136 offset:9216\n"
    "s_waitcnt vmcnt(3)\n"
    "buffer_load_dwordx4 v[128:131], v241, s[12:15], s63 offen offset:0 sc1\n"
    "s_waitcnt vmcnt(3)\n"
    "buffer_load_dwordx4 v[132:135], v241, s[12:15], s63 offen offset:1024 sc1\n"
    "s_waitcnt vmcnt(3)\n"
    "buffer_load_dwordx4 v[136:139], v241, s[12:15], s63 offen offset:2048 sc1\n"
    "s_waitcnt vmcnt(3)\n"
    "buffer_load_dwordx4 v[140:143], v241, s[12:15], s63 offen offset:3072 sc1\n"
    "s_waitcnt vmcnt(3)\n"
    "buffer_load_dwordx4 v[72:75], v241, s[12:15], s64 offen offset:2048 sc1\n"
    "s_waitcnt vmcnt(3)\n"
    "buffer_load_dwordx4 v[76:79], v241, s[12:15], s64 offen offset:3072 sc1\n"
    "s_waitcnt vmcnt(3)\n"
    "s_add_u32 s64, s64, 4096\n"
    "buffer_load_dwordx4 v[80:83], v241, s[12:15], s64 offen offset:0 sc1\n"
    "s_waitcnt vmcnt(3)\n"
    "buffer_load_dwordx4 v[84:87], v241, s[12:15], s64 offen offset:1024 sc1\n"
    "s_waitcnt lgkmcnt(0)\n"
    "s_barrier\n"
    "s_setprio 2\n"
    "ds_read_b128 v[250:253], v247 offset:1024\n"
    "ds_read_b128 v[208:211], v245 offset:0\n"
    "ds_read_b64 v[200:201], v244 offset:0\n"
    "ds_read_b64 v[202:203], v244 offset:2048\n"
    "ds_read_b64 v[204:205], v244 offset:4096\n"
    "ds_read_b64 v[206:207], v244 offset:6144\n"
    "buffer_load_dwordx4 v[88:91], v241, s[12:15], s64 offen offset:2048 sc1\n"
    "buffer_load_dwordx4 v[92:95], v241, s[12:15], s64 offen offset:3072 sc1\n"
    "s_add_u32 s64, s64, 4096\n"
    "buffer_load_dwordx4 v[96:99], v241, s[12:15], s64 offen offset:0 sc1\n"
    "buffer_load_dwordx4 v[100:103], v241, s[12:15], s64 offen offset:1024 sc1\n"
    "s_xor_b32 s73, s75, 393216\n"
    "s_xor_b32 s74, s75, 397312\n"
    "s_waitcnt lgkmcnt(5)\n"
    "v_smfmac_f32_16x16x64_bf16 v[152:155], v[250:253], v[168:175], v233\n"
    "s_waitcnt lgkmcnt(4)\n"
    "v_add_f32 v208, v208, v209\n"
    "v_add_f32 v210, v210, v211\n"
    "s_waitcnt lgkmcnt(2)\n"
    "v_add_f32 v200, v200, v202\n"
    "v_add_f32 v208, v208, v210\n"
    "v_smfmac_f32_16x16x64_bf16 v[156:159], v[250:253], v[176:183], v233\n"
    "buffer_load_dwordx4 v[168:171], v241, s[12:15], s73 offen offset:0 sc1\n"
    "v_add_f32 v201, v201, v203\n"
    "v_rcp_f32 v209, v208\n"
    "s_waitcnt lgkmcnt(0)\n"
    "v_add_f32 v204, v204, v206\n"
    "v_add_f32 v205, v205, v207\n"
    "v_smfmac_f32_16x16x64_bf16 v[160:163], v[250:253], v[184:191], v233\n"
    "buffer_load_dwordx4 v[172:175], v241, s[12:15], s73 offen offset:1024 sc1\n"
    "v_add_f32 v200, v200, v204\n"
    "v_add_f32 v201, v201, v205\n"
    "v_mul_f32 v200, v200, v209\n"
    "v_mul_f32 v201, v201, v209\n"
    "v_smfmac_f32_16x16x64_bf16 v[164:167], v[250:253], v[192:199], v233\n"
    "buffer_load_dwordx4 v[176:179], v241, s[12:15], s73 offen offset:2048 sc1\n"
    "v_max3_f32 v238, v238, v208, v209\n"
    "v_cvt_pk_bf16_f32 v202, v200, v201\n"
    "ds_write_b32 v246, v202 offset:0\n"
    "v_fmac_f32 v236, v200, v201\n"
    "buffer_load_dwordx4 v[180:183], v241, s[12:15], s73 offen offset:3072 sc1\n"
    "s_mul_i32 s62, s51, 2304\n"
    "v_subrev_u32 v243, s62, v243\n"
    "ds_read_b64 v[200:201], v244 offset:25600\n"
    "ds_read_b32 v208, v243 offset:9216\n"
    "ds_read_b64 v[202:203], v244 offset:27904\n"
    "ds_read_b32 v209, v243 offset:11520\n"
    "ds_read_b64 v[204:205], v244 offset:30208\n"
    "ds_read_b32 v210, v243 offset:13824\n"
    "ds_read_b64 v[206:207], v244 offset:32512\n"
    "ds_read_b32 v211, v243 offset:16128\n"
    "s_waitcnt lgkmcnt(5)\n"
    "v_add_f32 v200, v200, v202\n"
    "v_add_f32 v201, v201, v203\n"
    "s_waitcnt lgkmcnt(4)\n"
    "v_add_f32 v208, v208, v209\n"
    "s_waitcnt lgkmcnt(1)\n"
    "v_add_f32 v204, v204, v206\n"
    "v_add_f32 v205, v205, v207\n"
    "s_waitcnt lgkmcnt(0)\n"
    "v_add_f32 v210, v210, v211\n"
    "v_add_f32 v234, v200, v204\n"
    "v_add_f32 v235, v201, v205\n"
    "v_add_f32 v232, v208, v210\n"
    "buffer_load_dwordx4 v[184:187], v241, s[12:15], s74 offen offset:0 sc1\n"
    "s_and_b32 s61, s50, 4\n"
    "s_xor_b32 s61, s61, s48\n"
    "s_min_u32 s66, s61, 3\n"
    "s_mul_i32 s67, s61, 5\n"
    "s_add_u32 s66, s66, s67\n"
    "s_mul_i32 s67, s50, 43\n"
    "s_add_u32 s67, s67, s66\n"
    "s_lshl_b32 s67, s67, 12\n"
    "s_add_u32 s85, s67, 4718592\n"
    "v_bfe_u32 v242, v240, 4, 1\n"
    "v_lshrrev_b32 v243, 5, v240\n"
    "v_lshl_or_b32 v242, v242, 1, v243\n"
    "v_lshlrev_b32 v242, 12, v242\n"
    "v_and_b32 v243, 15, v240\n"
    "buffer_load_dwordx4 v[188:191], v241, s[12:15], s74 offen offset:1024 sc1\n"
    "v_lshl_or_b32 v242, v243, 4, v242\n"
    "s_lshl_b32 s62, s48, 8\n"
    "s_add_u32 s62, s62, 131072\n"
    "v_add_u32 v242, s62, v242\n"
    "s_lshl_b32 s62, s48, 11\n"
    "s_add_u32 s62, s62, 131072\n"
    "v_lshl_add_u32 v243, v240, 5, s62\n"
    "v_mov_b32 v249, 161792\n"
    "buffer_load_dwordx4 v[192:195], v241, s[12:15], s74 offen offset:2048 sc1\n"
    "v_lshlrev_b32 v144, 16, v228\n"
    "v_and_b32 v145, 0xffff0000, v228\n"
    "v_lshlrev_b32 v146, 16, v229\n"
    "v_and_b32 v147, 0xffff0000, v229\n"
    "v_lshlrev_b32 v148, 16, v230\n"
    "v_and_b32 v149, 0xffff0000, v230\n"
    "v_lshlrev_b32 v150, 16, v231\n"
    "v_and_b32 v151, 0xffff0000, v231\n"
    "buffer_load_dwordx4 v[196:199], v241, s[12:15], s74 offen offset:3072 sc1\n"
    "s_setprio 0\n"
    "s_waitcnt vmcnt(3)\n"
    "buffer_load_dwordx4 v[104:107], v241, s[12:15], s64 offen offset:2048 sc1\n"
    "s_waitcnt vmcnt(3)\n"
    "buffer_load_dwordx4 v[108:111], v241, s[12:15], s64 offen offset:3072 sc1\n"
    "s_waitcnt lgkmcnt(0)\n"
    "s_barrier\n"
    "s_setprio 2\n"
    "ds_read_b128 v[208:211], v245 offset:512\n"
    "ds_read_b64 v[200:201], v244 offset:8192\n"
    "ds_read_b64 v[202:203], v244 offset:10240\n"
    "ds_read_b64 v[204:205], v244 offset:12288\n"
    "ds_read_b64 v[206:207], v244 offset:14336\n"
    "ds_read_b128 v[250:253], v247 offset:0\n"
    "s_waitcnt lgkmcnt(5)\n"
    "v_add_f32 v208, v208, v209\n"
    "v_add_f32 v210, v210, v211\n"
    "v_add_f32 v208, v208, v232\n"
    "s_waitcnt lgkmcnt(3)\n"
    "v_add_f32 v200, v200, v202\n"
    "v_add_f32 v208, v208, v210\n"
    "v_add_f32 v201, v201, v203\n"
    "v_add_f32 v200, v200, v234\n"
    "v_add_f32 v201, v201, v235\n"
    "v_rcp_f32 v209, v208\n"
    "s_waitcnt lgkmcnt(1)\n"
    "v_add_f32 v204, v204, v206\n"
    "v_add_f32 v205, v205, v207\n"
    "v_add_f32 v200, v200, v204\n"
    "v_add_f32 v201, v201, v205\n"
    "v_mul_f32 v200, v200, v209\n"
    "v_mul_f32 v201, v201, v209\n"
    "v_cvt_pk_bf16_f32 v202, v200, v201\n"
    "ds_write_b32 v246, v202 offset:1024\n"
    "s_waitcnt lgkmcnt(1)\n"
    "v_smfmac_f32_16x16x64_bf16 v[152:155], v[250:253], v[168:175], v233\n"
    "v_smfmac_f32_16x16x64_bf16 v[156:159], v[250:253], v[176:183], v233\n"
    "v_smfmac_f32_16x16x64_bf16 v[160:163], v[250:253], v[184:191], v233\n"
    "s_waitcnt vmcnt(2)\n"
    "v_smfmac_f32_16x16x64_bf16 v[164:167], v[250:253], v[192:199], v233\n"
    "s_waitcnt lgkmcnt(0)\n"
    "s_barrier\n"
    "ds_read_b128 v[250:253], v247 offset:1024\n"
    "ds_read_b128 v[168:171], v247 offset:2048\n"
    "ds_read_b128 v[172:175], v247 offset:3072\n"
    "ds_read_b128 v[176:179], v247 offset:4096\n"
    "ds_read_b128 v[180:183], v247 offset:5120\n"
    "ds_read_b128 v[184:187], v247 offset:6144\n"
    "ds_read_b128 v[188:191], v247 offset:7168\n"
    "v_max3_f32 v238, v238, v208, v209\n"
    "v_fmac_f32 v236, v200, v201\n"
    "s_movk_i32 s62, 0x207\n"
    "v_cmp_lt_f32 vcc, 0x67800000, v238\n"
    "v_cmp_class_f32 s[76:77], v236, s62\n"
    "v_lshl_add_u32 v205, s51, 2, v249\n"
    "s_or_b64 s[76:77], s[76:77], vcc\n"
    "s_cselect_b32 s62, 0, 0\n"
    "v_mov_b32 v204, s62\n"
    "ds_write_b32 v205, v204\n"
    "s_waitcnt lgkmcnt(7)\n"
    "v_smfmac_f32_16x16x64_bf16 v[152:155], v[250:253], v[212:219], v233\n"
    "v_smfmac_f32_16x16x64_bf16 v[156:159], v[250:253], v[220:227], v233\n"
    "v_smfmac_f32_16x16x64_bf16 v[160:163], v[250:253], v[128:135], v233\n"
    "v_smfmac_f32_16x16x64_bf16 v[164:167], v[250:253], v[136:143], v233\n"
    "s_branch .LjoinA\n"
    ".LslowB:\n"
    "s_and_b32 s62, s60, 4\n"
    "s_xor_b32 s62, s62, s48\n"
    "s_lshl_b32 s62, s62, 2\n"
    "s_and_b32 s63, s60, 0\n"
    "s_or_b32 s62, s62, s63\n"
    "s_lshl_b32 s62, s62, 12\n"
    "s_add_u32 s70, s62, 131072\n"
    "s_xor_b32 s80, s70, 4096\n"
    "s_xor_b32 s81, s70, 8192\n"
    "s_xor_b32 s82, s70, 12288\n"
    "s_lshl_b32 s71, s55, 13\n"
    "s_lshl_b32 s62, s50, 10\n"
    "s_add_u32 s71, s71, s62\n"
    "s_add_u32 s71, s71, 1048576\n"
    "s_lshl_b32 s83, s48, 13\n"
    "v_add_u32 v249, s83, v241\n"
    "s_mov_b32 s88, 0xffff\n"
    "s_mov_b32 s89, 0xffff\n"
    "v_mov_b32 v232, 0xdddd\n"
    "v_bfe_u32 v200, v240, 3, 1\n"
    "v_mul_u32_u24 v200, 0xaaaa, v200\n"
    "v_add_u32 v233, 0x4444, v200\n"
    "v_add_u32 v247, 150528, v241\n"
    "s_mul_i32 s62, s51, 2304\n"
    "s_add_u32 s62, s62, 131072\n"
    "v_add_u32 v242, s62, v241\n"
    "s_add_u32 s62, s62, 2048\n"
    "v_lshl_add_u32 v243, v240, 2, s62\n"
    "v_and_b32 v210, 7, v240\n"
    "v_lshlrev_b32 v210, 7, v210\n"
    "s_lshl_b32 s84, s49, 10\n"
    "s_add_u32 s84, s84, 81984\n"
    "v_and_b32 v200, 31, v240\n"
    "s_lshl_b32 s62, s51, 11\n"
    "s_add_u32 s62, s62, 114688\n"
    "v_add_u32 v244, s62, v241\n"
    "s_lshl2_add_u32 s62, s51, 149504\n"
    "v_lshl_add_u32 v245, v200, 4, s62\n"
    "v_and_b32 v248, 31, v240\n"
    "v_lshlrev_b32 v248, 5, v248\n"
    "v_add_u32 v248, 160768, v248\n"
    "ds_read_b32 v248, v248\n"
    "s_waitcnt vmcnt(1)\n"
    "v_mfma_f32_32x32x16_bf16 v[96:111], v[0:3], v[144:147], 0\n"
    "s_waitcnt lgkmcnt(0)\n"
    "s_nop 10\n"
    "v_sub_f32 v96, v96, v248\n"
    "v_exp_f32 v96, v96\n"
    "v_sub_f32 v97, v97, v248\n"
    "v_exp_f32 v97, v97\n"
    "v_sub_f32 v98, v98, v248\n"
    "v_exp_f32 v98, v98\n"
    "v_sub_f32 v99, v99, v248\n"
    "v_exp_f32 v99, v99\n"
    "v_sub_f32 v100, v100, v248\n"
    "v_exp_f32 v100, v100\n"
    "v_mfma_f32_32x32x16_bf16 v[112:127], v[4:7], v[144:147], 0\n"
    "v_sub_f32 v101, v101, v248\n"
    "v_exp_f32 v101, v101\n"
    "v_sub_f32 v102, v102, v248\n"
    "v_exp_f32 v102, v102\n"
    "v_sub_f32 v103, v103, v248\n"
    "v_exp_f32 v103, v103\n"
    "v_sub_f32 v104, v104, v248\n"
    "v_exp_f32 v104, v104\n"
    "v_sub_f32 v105, v105, v248\n"
    "v_exp_f32 v105, v105\n"
    "v_smfmac_f32_32x32x32_bf16 v[128:143], v[32:35], v[96:103], v232\n"
    "v_sub_f32 v106, v106, v248\n"
    "v_exp_f32 v106, v106\n"
    "v_sub_f32 v107, v107, v248\n"
    "v_exp_f32 v107, v107\n"
    "v_sub_f32 v108, v108, v248\n"
    "v_exp_f32 v108, v108\n"
    "v_sub_f32 v109, v109, v248\n"
    "v_exp_f32 v109, v109\n"
    "v_sub_f32 v110, v110, v248\n"
    "v_exp_f32 v110, v110\n"
    "v_sub_f32 v111, v111, v248\n"
    "v_exp_f32 v111, v111\n"
    "v_sub_f32 v112, v112, v248\n"
    "v_exp_f32 v112, v112\n"
    "v_sub_f32 v113, v113, v248\n"
    "v_exp_f32 v113, v113\n"
    "v_smfmac_f32_32x32x32_bf16 v[128:143], v[36:39], v[104:111], v232\n"
    "v_sub_f32 v114, v114, v248\n"
    "v_exp_f32 v114, v114\n"
    "v_sub_f32 v115, v115, v248\n"
    "v_exp_f32 v115, v115\n"
    "v_sub_f32 v116, v116, v248\n"
    "v_exp_f32 v116, v116\n"
    "v_mfma_f32_32x32x16_bf16 v[96:111], v[8:11], v[144:147], 0\n"
    "v_sub_f32 v117, v117, v248\n"
    "v_exp_f32 v117, v117\n"
    "v_sub_f32 v118, v118, v248\n"
    "v_exp_f32 v118, v118\n"
    "v_sub_f32 v119, v119, v248\n"
    "v_exp_f32 v119, v119\n"
    "v_sub_f32 v120, v120, v248\n"
    "v_exp_f32 v120, v120\n"
    "v_sub_f32 v121, v121, v248\n"
    "v_exp_f32 v121, v121\n"
    "v_smfmac_f32_32x32x32_bf16 v[128:143], v[40:43], v[112:119], v232\n"
    "v_sub_f32 v122, v122, v248\n"
    "v_exp_f32 v122, v122\n"
    "v_sub_f32 v123, v123, v248\n"
    "v_exp_f32 v123, v123\n"
    "v_sub_f32 v124, v124, v248\n"
    "v_exp_f32 v124, v124\n"
    "v_sub_f32 v125, v125, v248\n"
    "v_exp_f32 v125, v125\n"
    "v_sub_f32 v126, v126, v248\n"
    "v_exp_f32 v126, v126\n"
    "v_sub_f32 v127, v127, v248\n"
    "v_exp_f32 v127, v127\n"
    "v_sub_f32 v96, v96, v248\n"
    "v_exp_f32 v96, v96\n"
    "v_sub_f32 v97, v97, v248\n"
    "v_exp_f32 v97, v97\n"
    "v_smfmac_f32_32x32x32_bf16 v[128:143], v[44:47], v[120:127], v232\n"
    "v_sub_f32 v98, v98, v248\n"
    "v_exp_f32 v98, v98\n"
    "v_sub_f32 v99, v99, v248\n"
    "v_exp_f32 v99, v99\n"
    "v_sub_f32 v100, v100, v248\n"
    "v_exp_f32 v100, v100\n"
    "v_mfma_f32_32x32x16_bf16 v[112:127], v[12:15], v[144:147], 0\n"
    "v_sub_f32 v101, v101, v248\n"
    "v_exp_f32 v101, v101\n"
    "v_sub_f32 v102, v102, v248\n"
    "v_exp_f32 v102, v102\n"
    "v_sub_f32 v103, v103, v248\n"
    "v_exp_f32 v103, v103\n"
    "v_sub_f32 v104, v104, v248\n"
    "v_exp_f32 v104, v104\n"
    "v_sub_f32 v105, v105, v248\n"
    "v_exp_f32 v105, v105\n"
    "v_smfmac_f32_32x32x32_bf16 v[128:143], v[48:51], v[96:103], v232\n"
    "v_sub_f32 v106, v106, v248\n"
    "v_exp_f32 v106, v106\n"
    "v_sub_f32 v107, v107, v248\n"
    "v_exp_f32 v107, v107\n"
    "v_sub_f32 v108, v108, v248\n"
    "v_exp_f32 v108, v108\n"
    "v_sub_f32 v109, v109, v248\n"
    "v_exp_f32 v109, v109\n"
    "v_sub_f32 v110, v110, v248\n"
    "v_exp_f32 v110, v110\n"
    "v_sub_f32 v111, v111, v248\n"
    "v_exp_f32 v111, v111\n"
    "s_barrier\n"
    "ds_read_b128 v[184:187], v242\n"
    "ds_read_b128 v[188:191], v242 offset:1024\n"
    "ds_read_b32 v192, v243\n"
    "v_sub_f32 v112, v112, v248\n"
    "v_exp_f32 v112, v112\n"
    "v_sub_f32 v113, v113, v248\n"
    "v_exp_f32 v113, v113\n"
    "v_smfmac_f32_32x32x32_bf16 v[128:143], v[52:55], v[104:111], v232\n"
    "v_sub_f32 v114, v114, v248\n"
    "v_exp_f32 v114, v114\n"
    "v_sub_f32 v115, v115, v248\n"
    "v_exp_f32 v115, v115\n"
    "v_sub_f32 v116, v116, v248\n"
    "v_exp_f32 v116, v116\n"
    "v_mfma_f32_32x32x16_bf16 v[96:111], v[16:19], v[144:147], 0\n"
    "v_sub_f32 v117, v117, v248\n"
    "v_exp_f32 v117, v117\n"
    "v_sub_f32 v118, v118, v248\n"
    "v_exp_f32 v118, v118\n"
    "v_sub_f32 v119, v119, v248\n"
    "v_exp_f32 v119, v119\n"
    "v_sub_f32 v120, v120, v248\n"
    "v_exp_f32 v120, v120\n"
    "v_sub_f32 v121, v121, v248\n"
    "v_exp_f32 v121, v121\n"
    "v_smfmac_f32_32x32x32_bf16 v[128:143], v[56:59], v[112:119], v232\n"
    "v_sub_f32 v122, v122, v248\n"
    "v_exp_f32 v122, v122\n"
    "v_sub_f32 v123, v123, v248\n"
    "v_exp_f32 v123, v123\n"
    "v_sub_f32 v124, v124, v248\n"
    "v_exp_f32 v124, v124\n"
    "v_sub_f32 v125, v125, v248\n"
    "v_exp_f32 v125, v125\n"
    "v_sub_f32 v126, v126, v248\n"
    "v_exp_f32 v126, v126\n"
    "v_sub_f32 v127, v127, v248\n"
    "v_exp_f32 v127, v127\n"
    "v_sub_f32 v96, v96, v248\n"
    "v_exp_f32 v96, v96\n"
    "v_sub_f32 v97, v97, v248\n"
    "v_exp_f32 v97, v97\n"
    "v_smfmac_f32_32x32x32_bf16 v[128:143], v[60:63], v[120:127], v232\n"
    "v_sub_f32 v98, v98, v248\n"
    "v_exp_f32 v98, v98\n"
    "v_sub_f32 v99, v99, v248\n"
    "v_exp_f32 v99, v99\n"
    "v_sub_f32 v100, v100, v248\n"
    "v_exp_f32 v100, v100\n"
    "v_mfma_f32_32x32x16_bf16 v[112:127], v[20:23], v[144:147], 0\n"
    "v_sub_f32 v101, v101, v248\n"
    "v_exp_f32 v101, v101\n"
    "v_sub_f32 v102, v102, v248\n"
    "v_exp_f32 v102, v102\n"
    "v_sub_f32 v103, v103, v248\n"
    "v_exp_f32 v103, v103\n"
    "v_sub_f32 v104, v104, v248\n"
    "v_exp_f32 v104, v104\n"
    "v_sub_f32 v105, v105, v248\n"
    "v_exp_f32 v105, v105\n"
    "v_smfmac_f32_32x32x32_bf16 v[128:143], v[64:67], v[96:103], v232\n"
    "v_sub_f32 v106, v106, v248\n"
    "v_exp_f32 v106, v106\n"
    "v_sub_f32 v107, v107, v248\n"
    "v_exp_f32 v107, v107\n"
    "v_sub_f32 v108, v108, v248\n"
    "v_exp_f32 v108, v108\n"
    "v_sub_f32 v109, v109, v248\n"
    "v_exp_f32 v109, v109\n"
    "v_sub_f32 v110, v110, v248\n"
    "v_exp_f32 v110, v110\n"
    "v_sub_f32 v111, v111, v248\n"
    "v_exp_f32 v111, v111\n"
    "v_sub_f32 v112, v112, v248\n"
    "v_exp_f32 v112, v112\n"
    "v_sub_f32 v113, v113, v248\n"
    "v_exp_f32 v113, v113\n"
    "v_smfmac_f32_32x32x32_bf16 v[128:143], v[68:71], v[104:111], v232\n"
    "v_sub_f32 v114, v114, v248\n"
    "v_exp_f32 v114, v114\n"
    "v_sub_f32 v115, v115, v248\n"
    "v_exp_f32 v115, v115\n"
    "v_sub_f32 v116, v116, v248\n"
    "v_exp_f32 v116, v116\n"
    "v_mfma_f32_32x32x16_bf16 v[96:111], v[24:27], v[144:147], 0\n"
    "v_sub_f32 v117, v117, v248\n"
    "v_exp_f32 v117, v117\n"
    "v_sub_f32 v118, v118, v248\n"
    "v_exp_f32 v118, v118\n"
    "v_sub_f32 v119, v119, v248\n"
    "v_exp_f32 v119, v119\n"
    "v_sub_f32 v120, v120, v248\n"
    "v_exp_f32 v120, v120\n"
    "v_sub_f32 v121, v121, v248\n"
    "v_exp_f32 v121, v121\n"
    "v_smfmac_f32_32x32x32_bf16 v[128:143], v[72:75], v[112:119], v232\n"
    "v_sub_f32 v122, v122, v248\n"
    "v_exp_f32 v122, v122\n"
    "v_sub_f32 v123, v123, v248\n"
    "v_exp_f32 v123, v123\n"
    "v_sub_f32 v124, v124, v248\n"
    "v_exp_f32 v124, v124\n"
    "v_sub_f32 v125, v125, v248\n"
    "v_exp_f32 v125, v125\n"
    "v_sub_f32 v126, v126, v248\n"
    "v_exp_f32 v126, v126\n"
    "v_sub_f32 v127, v127, v248\n"
    "v_exp_f32 v127, v127\n"
    "v_sub_f32 v96, v96, v248\n"
    "v_exp_f32 v96, v96\n"
    "v_sub_f32 v97, v97, v248\n"
    "v_exp_f32 v97, v97\n"
    "v_smfmac_f32_32x32x32_bf16 v[128:143], v[76:79], v[120:127], v232\n"
    "v_sub_f32 v98, v98, v248\n"
    "v_exp_f32 v98, v98\n"
    "v_sub_f32 v99, v99, v248\n"
    "v_exp_f32 v99, v99\n"
    "v_sub_f32 v100, v100, v248\n"
    "v_exp_f32 v100, v100\n"
    "v_mfma_f32_32x32x16_bf16 v[112:127], v[28:31], v[144:147], 0\n"
    "v_sub_f32 v101, v101, v248\n"
    "v_exp_f32 v101, v101\n"
    "v_sub_f32 v102, v102, v248\n"
    "v_exp_f32 v102, v102\n"
    "v_sub_f32 v103, v103, v248\n"
    "v_exp_f32 v103, v103\n"
    "v_sub_f32 v104, v104, v248\n"
    "v_exp_f32 v104, v104\n"
    "v_sub_f32 v105, v105, v248\n"
    "v_exp_f32 v105, v105\n"
    "v_smfmac_f32_32x32x32_bf16 v[128:143], v[80:83], v[96:103], v232\n"
    "v_sub_f32 v106, v106, v248\n"
    "v_exp_f32 v106, v106\n"
    "v_sub_f32 v107, v107, v248\n"
    "v_exp_f32 v107, v107\n"
    "v_sub_f32 v108, v108, v248\n"
    "v_exp_f32 v108, v108\n"
    "v_sub_f32 v109, v109, v248\n"
    "v_exp_f32 v109, v109\n"
    "v_sub_f32 v110, v110, v248\n"
    "v_exp_f32 v110, v110\n"
    "v_sub_f32 v111, v111, v248\n"
    "v_exp_f32 v111, v111\n"
    "v_sub_f32 v112, v112, v248\n"
    "v_exp_f32 v112, v112\n"
    "v_sub_f32 v113, v113, v248\n"
    "v_exp_f32 v113, v113\n"
    "v_smfmac_f32_32x32x32_bf16 v[128:143], v[84:87], v[104:111], v232\n"
    "v_sub_f32 v114, v114, v248\n"
    "v_exp_f32 v114, v114\n"
    "v_sub_f32 v115, v115, v248\n"
    "v_exp_f32 v115, v115\n"
    "v_sub_f32 v116, v116, v248\n"
    "v_exp_f32 v116, v116\n"
    "s_waitcnt vmcnt(0)\n"
    "v_mfma_f32_32x32x16_bf16 v[96:111], v[0:3], v[148:151], 0\n"
    "v_sub_f32 v117, v117, v248\n"
    "v_exp_f32 v117, v117\n"
    "v_sub_f32 v118, v118, v248\n"
    "v_exp_f32 v118, v118\n"
    "v_sub_f32 v119, v119, v248\n"
    "v_exp_f32 v119, v119\n"
    "v_sub_f32 v120, v120, v248\n"
    "v_exp_f32 v120, v120\n"
    "v_sub_f32 v121, v121, v248\n"
    "v_exp_f32 v121, v121\n"
    "v_smfmac_f32_32x32x32_bf16 v[128:143], v[88:91], v[112:119], v232\n"
    "v_sub_f32 v122, v122, v248\n"
    "v_exp_f32 v122, v122\n"
    "v_sub_f32 v123, v123, v248\n"
    "v_exp_f32 v123, v123\n"
    "v_sub_f32 v124, v124, v248\n"
    "v_exp_f32 v124, v124\n"
    "v_sub_f32 v125, v125, v248\n"
    "v_exp_f32 v125, v125\n"
    "v_sub_f32 v126, v126, v248\n"
    "v_exp_f32 v126, v126\n"
    "v_sub_f32 v127, v127, v248\n"
    "v_exp_f32 v127, v127\n"
    "v_and_b32 v248, 31, v240\n"
    "v_lshlrev_b32 v248, 5, v248\n"
    "v_add_u32 v248, 160772, v248\n"
    "ds_read_b32 v248, v248\n"
    "s_waitcnt lgkmcnt(0)\n"
    "v_sub_f32 v96, v96, v248\n"
    "v_exp_f32 v96, v96\n"
    "v_sub_f32 v97, v97, v248\n"
    "v_exp_f32 v97, v97\n"
    "v_smfmac_f32_32x32x32_bf16 v[128:143], v[92:95], v[120:127], v232\n"
    "v_sub_f32 v98, v98, v248\n"
    "v_exp_f32 v98, v98\n"
    "v_sub_f32 v99, v99, v248\n"
    "v_exp_f32 v99, v99\n"
    "v_sub_f32 v100, v100, v248\n"
    "v_exp_f32 v100, v100\n"
    "v_mfma_f32_32x32x16_bf16 v[112:127], v[4:7], v[148:151], 0\n"
    "v_sub_f32 v101, v101, v248\n"
    "v_exp_f32 v101, v101\n"
    "v_sub_f32 v102, v102, v248\n"
    "v_exp_f32 v102, v102\n"
    "v_sub_f32 v103, v103, v248\n"
    "v_exp_f32 v103, v103\n"
    "v_sub_f32 v104, v104, v248\n"
    "v_exp_f32 v104, v104\n"
    "v_sub_f32 v105, v105, v248\n"
    "v_exp_f32 v105, v105\n"
    "v_sub_f32 v106, v106, v248\n"
    "v_exp_f32 v106, v106\n"
    "v_sub_f32 v107, v107, v248\n"
    "v_exp_f32 v107, v107\n"
    "v_sub_f32 v108, v108, v248\n"
    "v_exp_f32 v108, v108\n"
    "v_add_f32 v128, v128, v184\n"
    "v_add_f32 v129, v129, v185\n"
    "v_add_f32 v130, v130, v186\n"
    "v_add_f32 v131, v131, v187\n"
    "v_add_f32 v132, v132, v188\n"
    "v_add_f32 v133, v133, v189\n"
    "v_add_f32 v134, v134, v190\n"
    "v_add_f32 v135, v135, v191\n"
    "v_add_f32 v136, v136, v192\n"
    "ds_write_b128 v244, v[128:131] offset:0\n"
    "ds_write_b128 v244, v[132:135] offset:1024\n"
    "ds_write_b32 v245, v136 offset:0\n"
    "v_sub_f32 v109, v109, v248\n"
    "v_exp_f32 v109, v109\n"
    "v_sub_f32 v110, v110, v248\n"
    "v_exp_f32 v110, v110\n"
    "v_sub_f32 v111, v111, v248\n"
    "v_exp_f32 v111, v111\n"
    "s_waitcnt lgkmcnt(0)\n"
    "s_barrier\n"
    "ds_read_b128 v[128:131], v242 offset:9216\n"
    "ds_read_b128 v[132:135], v242 offset:10240\n"
    "ds_read_b32 v136, v243 offset:9216\n"
    "s_xor_b32 s72, s71, 2048\n"
    "buffer_load_dwordx4 v[144:147], v241, s[12:15], s72 offen sc1\n"
    "s_xor_b32 s63, s75, 462848\n"
    "buffer_load_dwordx4 v[216:219], v241, s[12:15], s63 offen offset:0 sc1\n"
    "buffer_load_dwordx4 v[220:223], v241, s[12:15], s63 offen offset:1024 sc1\n"
    "buffer_load_dwordx4 v[224:227], v241, s[12:15], s63 offen offset:2048 sc1\n"
    "buffer_load_dwordx4 v[228:231], v241, s[12:15], s63 offen offset:3072 sc1\n"
    "s_waitcnt lgkmcnt(0)\n"
    "v_smfmac_f32_32x32x32_bf16 v[128:143], v[32:35], v[96:103], v232\n"
    "v_sub_f32 v112, v112, v248\n"
    "v_exp_f32 v112, v112\n"
    "v_sub_f32 v113, v113, v248\n"
    "v_exp_f32 v113, v113\n"
    "v_smfmac_f32_32x32x32_bf16 v[128:143], v[36:39], v[104:111], v232\n"
    "v_sub_f32 v114, v114, v248\n"
    "v_exp_f32 v114, v114\n"
    "v_sub_f32 v115, v115, v248\n"
    "v_exp_f32 v115, v115\n"
    "v_sub_f32 v116, v116, v248\n"
    "v_exp_f32 v116, v116\n"
    "v_mfma_f32_32x32x16_bf16 v[96:111], v[8:11], v[148:151], 0\n"
    "v_sub_f32 v117, v117, v248\n"
    "v_exp_f32 v117, v117\n"
    "v_sub_f32 v118, v118, v248\n"
    "v_exp_f32 v118, v118\n"
    "v_sub_f32 v119, v119, v248\n"
    "v_exp_f32 v119, v119\n"
    "v_sub_f32 v120, v120, v248\n"
    "v_exp_f32 v120, v120\n"
    "v_sub_f32 v121, v121, v248\n"
    "v_exp_f32 v121, v121\n"
    "v_smfmac_f32_32x32x32_bf16 v[128:143], v[40:43], v[112:119], v232\n"
    "v_sub_f32 v122, v122, v248\n"
    "v_exp_f32 v122, v122\n"
    "v_sub_f32 v123, v123, v248\n"
    "v_exp_f32 v123, v123\n"
    "v_sub_f32 v124, v124, v248\n"
    "v_exp_f32 v124, v124\n"
    "v_sub_f32 v125, v125, v248\n"
    "v_exp_f32 v125, v125\n"
    "v_sub_f32 v126, v126, v248\n"
    "v_exp_f32 v126, v126\n"
    "v_sub_f32 v127, v127, v248\n"
    "v_exp_f32 v127, v127\n"
    "v_sub_f32 v96, v96, v248\n"
    "v_exp_f32 v96, v96\n"
    "v_sub_f32 v97, v97, v248\n"
    "v_exp_f32 v97, v97\n"
    "v_smfmac_f32_32x32x32_bf16 v[128:143], v[44:47], v[120:127], v232\n"
    "v_sub_f32 v98, v98, v248\n"
    "v_exp_f32 v98, v98\n"
    "v_sub_f32 v99, v99, v248\n"
    "v_exp_f32 v99, v99\n"
    "v_sub_f32 v100, v100, v248\n"
    "v_exp_f32 v100, v100\n"
    "v_mfma_f32_32x32x16_bf16 v[112:127], v[12:15], v[148:151], 0\n"
    "v_sub_f32 v101, v101, v248\n"
    "v_exp_f32 v101, v101\n"
    "v_sub_f32 v102, v102, v248\n"
    "v_exp_f32 v102, v102\n"
    "v_sub_f32 v103, v103, v248\n"
    "v_exp_f32 v103, v103\n"
    "v_sub_f32 v104, v104, v248\n"
    "v_exp_f32 v104, v104\n"
    "v_sub_f32 v105, v105, v248\n"
    "v_exp_f32 v105, v105\n"
    "v_smfmac_f32_32x32x32_bf16 v[128:143], v[48:51], v[96:103], v232\n"
    "s_xor_b32 s73, s75, 0\n"
    "s_xor_b32 s74, s75, 4096\n"
    "v_sub_f32 v106, v106, v248\n"
    "v_exp_f32 v106, v106\n"
    "v_sub_f32 v107, v107, v248\n"
    "v_exp_f32 v107, v107\n"
    "v_sub_f32 v108, v108, v248\n"
    "v_exp_f32 v108, v108\n"
    "v_sub_f32 v109, v109, v248\n"
    "v_exp_f32 v109, v109\n"
    "v_sub_f32 v110, v110, v248\n"
    "v_exp_f32 v110, v110\n"
    "v_sub_f32 v111, v111, v248\n"
    "v_exp_f32 v111, v111\n"
    "buffer_load_dwordx4 v[168:171], v241, s[12:15], s73 offen offset:0 sc1\n"
    "v_sub_f32 v112, v112, v248\n"
    "v_exp_f32 v112, v112\n"
    "v_sub_f32 v113, v113, v248\n"
    "v_exp_f32 v113, v113\n"
    "v_smfmac_f32_32x32x32_bf16 v[128:143], v[52:55], v[104:111], v232\n"
    "buffer_load_dwordx4 v[172:175], v241, s[12:15], s73 offen offset:1024 sc1\n"
    "v_sub_f32 v114, v114, v248\n"
    "v_exp_f32 v114, v114\n"
    "v_sub_f32 v115, v115, v248\n"
    "v_exp_f32 v115, v115\n"
    "v_sub_f32 v116, v116, v248\n"
    "v_exp_f32 v116, v116\n"
    "v_mfma_f32_32x32x16_bf16 v[96:111], v[16:19], v[148:151], 0\n"
    "v_sub_f32 v117, v117, v248\n"
    "v_exp_f32 v117, v117\n"
    "v_sub_f32 v118, v118, v248\n"
    "v_exp_f32 v118, v118\n"
    "v_sub_f32 v119, v119, v248\n"
    "v_exp_f32 v119, v119\n"
    "v_sub_f32 v120, v120, v248\n"
    "v_exp_f32 v120, v120\n"
    "v_sub_f32 v121, v121, v248\n"
    "v_exp_f32 v121, v121\n"
    "v_smfmac_f32_32x32x32_bf16 v[128:143], v[56:59], v[112:119], v232\n"
    "v_sub_f32 v122, v122, v248\n"
    "v_exp_f32 v122, v122\n"
    "v_sub_f32 v123, v123, v248\n"
    "v_exp_f32 v123, v123\n"
    "v_sub_f32 v124, v124, v248\n"
    "v_exp_f32 v124, v124\n"
    "v_sub_f32 v125, v125, v248\n"
    "v_exp_f32 v125, v125\n"
    "v_sub_f32 v126, v126, v248\n"
    "v_exp_f32 v126, v126\n"
    "v_sub_f32 v127, v127, v248\n"
    "v_exp_f32 v127, v127\n"
    "buffer_load_dwordx4 v[176:179], v241, s[12:15], s73 offen offset:2048 sc1\n"
    "v_sub_f32 v96, v96, v248\n"
    "v_exp_f32 v96, v96\n"
    "v_sub_f32 v97, v97, v248\n"
    "v_exp_f32 v97, v97\n"
    "v_smfmac_f32_32x32x32_bf16 v[128:143], v[60:63], v[120:127], v232\n"
    "buffer_load_dwordx4 v[180:183], v241, s[12:15], s73 offen offset:3072 sc1\n"
    "v_sub_f32 v98, v98, v248\n"
    "v_exp_f32 v98, v98\n"
    "v_sub_f32 v99, v99, v248\n"
    "v_exp_f32 v99, v99\n"
    "v_sub_f32 v100, v100, v248\n"
    "v_exp_f32 v100, v100\n"
    "v_mfma_f32_32x32x16_bf16 v[112:127], v[20:23], v[148:151], 0\n"
    "v_sub_f32 v101, v101, v248\n"
    "v_exp_f32 v101, v101\n"
    "v_sub_f32 v102, v102, v248\n"
    "v_exp_f32 v102, v102\n"
    "v_sub_f32 v103, v103, v248\n"
    "v_exp_f32 v103, v103\n"
    "v_sub_f32 v104, v104, v248\n"
    "v_exp_f32 v104, v104\n"
    "v_sub_f32 v105, v105, v248\n"
    "v_exp_f32 v105, v105\n"
    "v_smfmac_f32_32x32x32_bf16 v[128:143], v[64:67], v[96:103], v232\n"
    "v_sub_f32 v106, v106, v248\n"
    "v_exp_f32 v106, v106\n"
    "v_sub_f32 v107, v107, v248\n"
    "v_exp_f32 v107, v107\n"
    "v_sub_f32 v108, v108, v248\n"
    "v_exp_f32 v108, v108\n"
    "v_sub_f32 v109, v109, v248\n"
    "v_exp_f32 v109, v109\n"
    "v_sub_f32 v110, v110, v248\n"
    "v_exp_f32 v110, v110\n"
    "v_sub_f32 v111, v111, v248\n"
    "v_exp_f32 v111, v111\n"
    "buffer_load_dwordx4 v[184:187], v241, s[12:15], s74 offen offset:0 sc1\n"
    "v_sub_f32 v112, v112, v248\n"
    "v_exp_f32 v112, v112\n"
    "v_sub_f32 v113, v113, v248\n"
    "v_exp_f32 v113, v113\n"
    "v_smfmac_f32_32x32x32_bf16 v[128:143], v[68:71], v[104:111], v232\n"
    "buffer_load_dwordx4 v[188:191], v241, s[12:15], s74 offen offset:1024 sc1\n"
    "v_sub_f32 v114, v114, v248\n"
    "v_exp_f32 v114, v114\n"
    "v_sub_f32 v115, v115, v248\n"
    "v_exp_f32 v115, v115\n"
    "v_sub_f32 v116, v116, v248\n"
    "v_exp_f32 v116, v116\n"
    "v_mfma_f32_32x32x16_bf16 v[96:111], v[24:27], v[148:151], 0\n"
    "v_sub_f32 v117, v117, v248\n"
    "v_exp_f32 v117, v117\n"
    "v_sub_f32 v118, v118, v248\n"
    "v_exp_f32 v118, v118\n"
    "v_sub_f32 v119, v119, v248\n"
    "v_exp_f32 v119, v119\n"
    "v_sub_f32 v120, v120, v248\n"
    "v_exp_f32 v120, v120\n"
    "v_sub_f32 v121, v121, v248\n"
    "v_exp_f32 v121, v121\n"
    "v_smfmac_f32_32x32x32_bf16 v[128:143], v[72:75], v[112:119], v232\n"
    "v_sub_f32 v122, v122, v248\n"
    "v_exp_f32 v122, v122\n"
    "v_sub_f32 v123, v123, v248\n"
    "v_exp_f32 v123, v123\n"
    "v_sub_f32 v124, v124, v248\n"
    "v_exp_f32 v124, v124\n"
    "v_sub_f32 v125, v125, v248\n"
    "v_exp_f32 v125, v125\n"
    "v_sub_f32 v126, v126, v248\n"
    "v_exp_f32 v126, v126\n"
    "v_sub_f32 v127, v127, v248\n"
    "v_exp_f32 v127, v127\n"
    "buffer_load_dwordx4 v[192:195], v241, s[12:15], s74 offen offset:2048 sc1\n"
    "v_sub_f32 v96, v96, v248\n"
    "v_exp_f32 v96, v96\n"
    "v_sub_f32 v97, v97, v248\n"
    "v_exp_f32 v97, v97\n"
    "v_smfmac_f32_32x32x32_bf16 v[128:143], v[76:79], v[120:127], v232\n"
    "buffer_load_dwordx4 v[196:199], v241, s[12:15], s74 offen offset:3072 sc1\n"
    "v_sub_f32 v98, v98, v248\n"
    "v_exp_f32 v98, v98\n"
    "v_sub_f32 v99, v99, v248\n"
    "v_exp_f32 v99, v99\n"
    "v_sub_f32 v100, v100, v248\n"
    "v_exp_f32 v100, v100\n"
    "v_mfma_f32_32x32x16_bf16 v[112:127], v[28:31], v[148:151], 0\n"
    "v_sub_f32 v101, v101, v248\n"
    "v_exp_f32 v101, v101\n"
    "v_sub_f32 v102, v102, v248\n"
    "v_exp_f32 v102, v102\n"
    "v_sub_f32 v103, v103, v248\n"
    "v_exp_f32 v103, v103\n"
    "v_sub_f32 v104, v104, v248\n"
    "v_exp_f32 v104, v104\n"
    "v_sub_f32 v105, v105, v248\n"
    "v_exp_f32 v105, v105\n"
    "v_smfmac_f32_32x32x32_bf16 v[128:143], v[80:83], v[96:103], v232\n"
    "v_sub_f32 v106, v106, v248\n"
    "v_exp_f32 v106, v106\n"
    "v_sub_f32 v107, v107, v248\n"
    "v_exp_f32 v107, v107\n"
    "v_sub_f32 v108, v108, v248\n"
    "v_exp_f32 v108, v108\n"
    "v_sub_f32 v109, v109, v248\n"
    "v_exp_f32 v109, v109\n"
    "v_sub_f32 v110, v110, v248\n"
    "v_exp_f32 v110, v110\n"
    "v_sub_f32 v111, v111, v248\n"
    "v_exp_f32 v111, v111\n"
    "v_sub_f32 v112, v112, v248\n"
    "v_exp_f32 v112, v112\n"
    "v_sub_f32 v113, v113, v248\n"
    "v_exp_f32 v113, v113\n"
    "v_smfmac_f32_32x32x32_bf16 v[128:143], v[84:87], v[104:111], v232\n"
    "v_sub_f32 v114, v114, v248\n"
    "v_exp_f32 v114, v114\n"
    "v_sub_f32 v115, v115, v248\n"
    "v_exp_f32 v115, v115\n"
    "v_sub_f32 v116, v116, v248\n"
    "v_exp_f32 v116, v116\n"
    "s_waitcnt vmcnt(12)\n"
    "v_mfma_f32_32x32x16_bf16 v[96:111], v[0:3], v[144:147], 0\n"
    "v_sub_f32 v117, v117, v248\n"
    "v_exp_f32 v117, v117\n"
    "v_sub_f32 v118, v118, v248\n"
    "v_exp_f32 v118, v118\n"
    "v_sub_f32 v119, v119, v248\n"
    "v_exp_f32 v119, v119\n"
    "v_sub_f32 v120, v120, v248\n"
    "v_exp_f32 v120, v120\n"
    "v_sub_f32 v121, v121, v248\n"
    "v_exp_f32 v121, v121\n"
    "v_smfmac_f32_32x32x32_bf16 v[128:143], v[88:91], v[112:119], v232\n"
    "v_sub_f32 v122, v122, v248\n"
    "v_exp_f32 v122, v122\n"
    "v_sub_f32 v123, v123, v248\n"
    "v_exp_f32 v123, v123\n"
    "v_sub_f32 v124, v124, v248\n"
    "v_exp_f32 v124, v124\n"
    "v_sub_f32 v125, v125, v248\n"
    "v_exp_f32 v125, v125\n"
    "v_sub_f32 v126, v126, v248\n"
    "v_exp_f32 v126, v126\n"
    "v_sub_f32 v127, v127, v248\n"
    "v_exp_f32 v127, v127\n"
    "v_and_b32 v248, 31, v240\n"
    "v_lshlrev_b32 v248, 5, v248\n"
    "v_add_u32 v248, 160776, v248\n"
    "ds_read_b32 v248, v248\n"
    "s_waitcnt lgkmcnt(0)\n"
    "v_sub_f32 v96, v96, v248\n"
    "v_exp_f32 v96, v96\n"
    "v_sub_f32 v97, v97, v248\n"
    "v_exp_f32 v97, v97\n"
    "v_smfmac_f32_32x32x32_bf16 v[128:143], v[92:95], v[120:127], v232\n"
    "v_sub_f32 v98, v98, v248\n"
    "v_exp_f32 v98, v98\n"
    "v_sub_f32 v99, v99, v248\n"
    "v_exp_f32 v99, v99\n"
    "v_sub_f32 v100, v100, v248\n"
    "v_exp_f32 v100, v100\n"
    "v_mfma_f32_32x32x16_bf16 v[112:127], v[4:7], v[144:147], 0\n"
    "v_sub_f32 v101, v101, v248\n"
    "v_exp_f32 v101, v101\n"
    "v_sub_f32 v102, v102, v248\n"
    "v_exp_f32 v102, v102\n"
    "v_sub_f32 v103, v103, v248\n"
    "v_exp_f32 v103, v103\n"
    "v_sub_f32 v104, v104, v248\n"
    "v_exp_f32 v104, v104\n"
    "v_sub_f32 v105, v105, v248\n"
    "v_exp_f32 v105, v105\n"
    "v_sub_f32 v106, v106, v248\n"
    "v_exp_f32 v106, v106\n"
    "v_sub_f32 v107, v107, v248\n"
    "v_exp_f32 v107, v107\n"
    "v_sub_f32 v108, v108, v248\n"
    "v_exp_f32 v108, v108\n"
    "ds_write_b128 v244, v[128:131] offset:8192\n"
    "ds_write_b128 v244, v[132:135] offset:9216\n"
    "ds_write_b32 v245, v136 offset:512\n"
    "v_sub_f32 v109, v109, v248\n"
    "v_exp_f32 v109, v109\n"
    "v_sub_f32 v110, v110, v248\n"
    "v_exp_f32 v110, v110\n"
    "v_sub_f32 v111, v111, v248\n"
    "v_exp_f32 v111, v111\n"
    "s_waitcnt lgkmcnt(0)\n"
    "s_barrier\n"
    "ds_read_b128 v[128:131], v242 offset:0\n"
    "ds_read_b128 v[132:135], v242 offset:1024\n"
    "ds_read_b32 v136, v243 offset:0\n"
    "ds_read_b128 v[250:253], v247 offset:0\n"
    "s_xor_b32 s72, s71, 3072\n"
    "buffer_load_dwordx4 v[148:151], v241, s[12:15], s72 offen sc1\n"
    "s_waitcnt lgkmcnt(1)\n"
    "v_smfmac_f32_32x32x32_bf16 v[128:143], v[32:35], v[96:103], v232\n"
    "v_sub_f32 v112, v112, v248\n"
    "v_exp_f32 v112, v112\n"
    "v_sub_f32 v113, v113, v248\n"
    "v_exp_f32 v113, v113\n"
    "v_smfmac_f32_32x32x32_bf16 v[128:143], v[36:39], v[104:111], v232\n"
    "v_sub_f32 v114, v114, v248\n"
    "v_exp_f32 v114, v114\n"
    "v_sub_f32 v115, v115, v248\n"
    "v_exp_f32 v115, v115\n"
    "v_sub_f32 v116, v116, v248\n"
    "v_exp_f32 v116, v116\n"
    "v_mfma_f32_32x32x16_bf16 v[96:111], v[8:11], v[144:147], 0\n"
    "v_sub_f32 v117, v117, v248\n"
    "v_exp_f32 v117, v117\n"
    "v_sub_f32 v118, v118, v248\n"
    "v_exp_f32 v118, v118\n"
    "v_sub_f32 v119, v119, v248\n"
    "v_exp_f32 v119, v119\n"
    "v_sub_f32 v120, v120, v248\n"
    "v_exp_f32 v120, v120\n"
    "v_sub_f32 v121, v121, v248\n"
    "v_exp_f32 v121, v121\n"
    "v_smfmac_f32_32x32x32_bf16 v[128:143], v[40:43], v[112:119], v232\n"
    "v_sub_f32 v122, v122, v248\n"
    "v_exp_f32 v122, v122\n"
    "v_sub_f32 v123, v123, v248\n"
    "v_exp_f32 v123, v123\n"
    "v_sub_f32 v124, v124, v248\n"
    "v_exp_f32 v124, v124\n"
    "v_sub_f32 v125, v125, v248\n"
    "v_exp_f32 v125, v125\n"
    "v_sub_f32 v126, v126, v248\n"
    "v_exp_f32 v126, v126\n"
    "v_sub_f32 v127, v127, v248\n"
    "v_exp_f32 v127, v127\n"
    "v_sub_f32 v96, v96, v248\n"
    "v_exp_f32 v96, v96\n"
    "v_sub_f32 v97, v97, v248\n"
    "v_exp_f32 v97, v97\n"
    "v_smfmac_f32_32x32x32_bf16 v[128:143], v[44:47], v[120:127], v232\n"
    "v_sub_f32 v98, v98, v248\n"
    "v_exp_f32 v98, v98\n"
    "v_sub_f32 v99, v99, v248\n"
    "v_exp_f32 v99, v99\n"
    "v_sub_f32 v100, v100, v248\n"
    "v_exp_f32 v100, v100\n"
    "v_mfma_f32_32x32x16_bf16 v[112:127], v[12:15], v[144:147], 0\n"
    "v_sub_f32 v101, v101, v248\n"
    "v_exp_f32 v101, v101\n"
    "v_sub_f32 v102, v102, v248\n"
    "v_exp_f32 v102, v102\n"
    "v_sub_f32 v103, v103, v248\n"
    "v_exp_f32 v103, v103\n"
    "v_sub_f32 v104, v104, v248\n"
    "v_exp_f32 v104, v104\n"
    "v_sub_f32 v105, v105, v248\n"
    "v_exp_f32 v105, v105\n"
    "v_smfmac_f32_32x32x32_bf16 v[128:143], v[48:51], v[96:103], v232\n"
    "s_xor_b32 s73, s75, 65536\n"
    "s_xor_b32 s74, s75, 69632\n"
    "v_sub_f32 v106, v106, v248\n"
    "v_exp_f32 v106, v106\n"
    "v_sub_f32 v107, v107, v248\n"
    "v_exp_f32 v107, v107\n"
    "v_sub_f32 v108, v108, v248\n"
    "v_exp_f32 v108, v108\n"
    "v_sub_f32 v109, v109, v248\n"
    "v_exp_f32 v109, v109\n"
    "v_sub_f32 v110, v110, v248\n"
    "v_exp_f32 v110, v110\n"
    "v_sub_f32 v111, v111, v248\n"
    "v_exp_f32 v111, v111\n"
    "s_waitcnt vmcnt(7) lgkmcnt(0)\n"
    "v_smfmac_f32_16x16x64_bf16 v[152:155], v[250:253], v[168:175], v233\n"
    "buffer_load_dwordx4 v[168:171], v241, s[12:15], s73 offen offset:0 sc1\n"
    "v_sub_f32 v112, v112, v248\n"
    "v_exp_f32 v112, v112\n"
    "v_sub_f32 v113, v113, v248\n"
    "v_exp_f32 v113, v113\n"
    "v_smfmac_f32_32x32x32_bf16 v[128:143], v[52:55], v[104:111], v232\n"
    "buffer_load_dwordx4 v[172:175], v241, s[12:15], s73 offen offset:1024 sc1\n"
    "v_sub_f32 v114, v114, v248\n"
    "v_exp_f32 v114, v114\n"
    "v_sub_f32 v115, v115, v248\n"
    "v_exp_f32 v115, v115\n"
    "v_sub_f32 v116, v116, v248\n"
    "v_exp_f32 v116, v116\n"
    "v_mfma_f32_32x32x16_bf16 v[96:111], v[16:19], v[144:147], 0\n"
    "v_sub_f32 v117, v117, v248\n"
    "v_exp_f32 v117, v117\n"
    "v_sub_f32 v118, v118, v248\n"
    "v_exp_f32 v118, v118\n"
    "v_sub_f32 v119, v119, v248\n"
    "v_exp_f32 v119, v119\n"
    "v_sub_f32 v120, v120, v248\n"
    "v_exp_f32 v120, v120\n"
    "v_sub_f32 v121, v121, v248\n"
    "v_exp_f32 v121, v121\n"
    "v_smfmac_f32_32x32x32_bf16 v[128:143], v[56:59], v[112:119], v232\n"
    "v_sub_f32 v122, v122, v248\n"
    "v_exp_f32 v122, v122\n"
    "v_sub_f32 v123, v123, v248\n"
    "v_exp_f32 v123, v123\n"
    "v_sub_f32 v124, v124, v248\n"
    "v_exp_f32 v124, v124\n"
    "v_sub_f32 v125, v125, v248\n"
    "v_exp_f32 v125, v125\n"
    "v_sub_f32 v126, v126, v248\n"
    "v_exp_f32 v126, v126\n"
    "v_sub_f32 v127, v127, v248\n"
    "v_exp_f32 v127, v127\n"
    "s_waitcnt vmcnt(7)\n"
    "v_smfmac_f32_16x16x64_bf16 v[156:159], v[250:253], v[176:183], v233\n"
    "buffer_load_dwordx4 v[176:179], v241, s[12:15], s73 offen offset:2048 sc1\n"
    "v_sub_f32 v96, v96, v248\n"
    "v_exp_f32 v96, v96\n"
    "v_sub_f32 v97, v97, v248\n"
    "v_exp_f32 v97, v97\n"
    "v_smfmac_f32_32x32x32_bf16 v[128:143], v[60:63], v[120:127], v232\n"
    "buffer_load_dwordx4 v[180:183], v241, s[12:15], s73 offen offset:3072 sc1\n"
    "v_sub_f32 v98, v98, v248\n"
    "v_exp_f32 v98, v98\n"
    "v_sub_f32 v99, v99, v248\n"
    "v_exp_f32 v99, v99\n"
    "v_sub_f32 v100, v100, v248\n"
    "v_exp_f32 v100, v100\n"
    "v_mfma_f32_32x32x16_bf16 v[112:127], v[20:23], v[144:147], 0\n"
    "v_sub_f32 v101, v101, v248\n"
    "v_exp_f32 v101, v101\n"
    "v_sub_f32 v102, v102, v248\n"
    "v_exp_f32 v102, v102\n"
    "v_sub_f32 v103, v103, v248\n"
    "v_exp_f32 v103, v103\n"
    "v_sub_f32 v104, v104, v248\n"
    "v_exp_f32 v104, v104\n"
    "v_sub_f32 v105, v105, v248\n"
    "v_exp_f32 v105, v105\n"
    "v_smfmac_f32_32x32x32_bf16 v[128:143], v[64:67], v[96:103], v232\n"
    "v_sub_f32 v106, v106, v248\n"
    "v_exp_f32 v106, v106\n"
    "v_sub_f32 v107, v107, v248\n"
    "v_exp_f32 v107, v107\n"
    "v_sub_f32 v108, v108, v248\n"
    "v_exp_f32 v108, v108\n"
    "v_sub_f32 v109, v109, v248\n"
    "v_exp_f32 v109, v109\n"
    "v_sub_f32 v110, v110, v248\n"
    "v_exp_f32 v110, v110\n"
    "v_sub_f32 v111, v111, v248\n"
    "v_exp_f32 v111, v111\n"
    "s_waitcnt vmcnt(7)\n"
    "v_smfmac_f32_16x16x64_bf16 v[160:163], v[250:253], v[184:191], v233\n"
    "buffer_load_dwordx4 v[184:187], v241, s[12:15], s74 offen offset:0 sc1\n"
    "v_sub_f32 v112, v112, v248\n"
    "v_exp_f32 v112, v112\n"
    "v_sub_f32 v113, v113, v248\n"
    "v_exp_f32 v113, v113\n"
    "v_smfmac_f32_32x32x32_bf16 v[128:143], v[68:71], v[104:111], v232\n"
    "buffer_load_dwordx4 v[188:191], v241, s[12:15], s74 offen offset:1024 sc1\n"
    "v_sub_f32 v114, v114, v248\n"
    "v_exp_f32 v114, v114\n"
    "v_sub_f32 v115, v115, v248\n"
    "v_exp_f32 v115, v115\n"
    "v_sub_f32 v116, v116, v248\n"
    "v_exp_f32 v116, v116\n"
    "v_mfma_f32_32x32x16_bf16 v[96:111], v[24:27], v[144:147], 0\n"
    "v_sub_f32 v117, v117, v248\n"
    "v_exp_f32 v117, v117\n"
    "v_sub_f32 v118, v118, v248\n"
    "v_exp_f32 v118, v118\n"
    "v_sub_f32 v119, v119, v248\n"
    "v_exp_f32 v119, v119\n"
    "v_sub_f32 v120, v120, v248\n"
    "v_exp_f32 v120, v120\n"
    "v_sub_f32 v121, v121, v248\n"
    "v_exp_f32 v121, v121\n"
    "v_smfmac_f32_32x32x32_bf16 v[128:143], v[72:75], v[112:119], v232\n"
    "v_sub_f32 v122, v122, v248\n"
    "v_exp_f32 v122, v122\n"
    "v_sub_f32 v123, v123, v248\n"
    "v_exp_f32 v123, v123\n"
    "v_sub_f32 v124, v124, v248\n"
    "v_exp_f32 v124, v124\n"
    "v_sub_f32 v125, v125, v248\n"
    "v_exp_f32 v125, v125\n"
    "v_sub_f32 v126, v126, v248\n"
    "v_exp_f32 v126, v126\n"
    "v_sub_f32 v127, v127, v248\n"
    "v_exp_f32 v127, v127\n"
    "s_waitcnt vmcnt(7)\n"
    "v_smfmac_f32_16x16x64_bf16 v[164:167], v[250:253], v[192:199], v233\n"
    "buffer_load_dwordx4 v[192:195], v241, s[12:15], s74 offen offset:2048 sc1\n"
    "v_sub_f32 v96, v96, v248\n"
    "v_exp_f32 v96, v96\n"
    "v_sub_f32 v97, v97, v248\n"
    "v_exp_f32 v97, v97\n"
    "v_smfmac_f32_32x32x32_bf16 v[128:143], v[76:79], v[120:127], v232\n"
    "buffer_load_dwordx4 v[196:199], v241, s[12:15], s74 offen offset:3072 sc1\n"
    "v_sub_f32 v98, v98, v248\n"
    "v_exp_f32 v98, v98\n"
    "v_sub_f32 v99, v99, v248\n"
    "v_exp_f32 v99, v99\n"
    "v_sub_f32 v100, v100, v248\n"
    "v_exp_f32 v100, v100\n"
    "v_mfma_f32_32x32x16_bf16 v[112:127], v[28:31], v[144:147], 0\n"
    "v_sub_f32 v101, v101, v248\n"
    "v_exp_f32 v101, v101\n"
    "v_sub_f32 v102, v102, v248\n"
    "v_exp_f32 v102, v102\n"
    "v_sub_f32 v103, v103, v248\n"
    "v_exp_f32 v103, v103\n"
    "v_sub_f32 v104, v104, v248\n"
    "v_exp_f32 v104, v104\n"
    "v_sub_f32 v105, v105, v248\n"
    "v_exp_f32 v105, v105\n"
    "v_smfmac_f32_32x32x32_bf16 v[128:143], v[80:83], v[96:103], v232\n"
    "v_sub_f32 v106, v106, v248\n"
    "v_exp_f32 v106, v106\n"
    "v_sub_f32 v107, v107, v248\n"
    "v_exp_f32 v107, v107\n"
    "v_sub_f32 v108, v108, v248\n"
    "v_exp_f32 v108, v108\n"
    "v_sub_f32 v109, v109, v248\n"
    "v_exp_f32 v109, v109\n"
    "v_sub_f32 v110, v110, v248\n"
    "v_exp_f32 v110, v110\n"
    "v_sub_f32 v111, v111, v248\n"
    "v_exp_f32 v111, v111\n"
    "v_sub_f32 v112, v112, v248\n"
    "v_exp_f32 v112, v112\n"
    "v_sub_f32 v113, v113, v248\n"
    "v_exp_f32 v113, v113\n"
    "v_smfmac_f32_32x32x32_bf16 v[128:143], v[84:87], v[104:111], v232\n"
    "v_sub_f32 v114, v114, v248\n"
    "v_exp_f32 v114, v114\n"
    "v_sub_f32 v115, v115, v248\n"
    "v_exp_f32 v115, v115\n"
    "v_sub_f32 v116, v116, v248\n"
    "v_exp_f32 v116, v116\n"
    "s_waitcnt vmcnt(8)\n"
    "v_mfma_f32_32x32x16_bf16 v[96:111], v[0:3], v[148:151], 0\n"
    "v_sub_f32 v117, v117, v248\n"
    "v_exp_f32 v117, v117\n"
    "v_sub_f32 v118, v118, v248\n"
    "v_exp_f32 v118, v118\n"
    "v_sub_f32 v119, v119, v248\n"
    "v_exp_f32 v119, v119\n"
    "v_sub_f32 v120, v120, v248\n"
    "v_exp_f32 v120, v120\n"
    "v_sub_f32 v121, v121, v248\n"
    "v_exp_f32 v121, v121\n"
    "v_smfmac_f32_32x32x32_bf16 v[128:143], v[88:91], v[112:119], v232\n"
    "v_sub_f32 v122, v122, v248\n"
    "v_exp_f32 v122, v122\n"
    "v_sub_f32 v123, v123, v248\n"
    "v_exp_f32 v123, v123\n"
    "v_sub_f32 v124, v124, v248\n"
    "v_exp_f32 v124, v124\n"
    "v_sub_f32 v125, v125, v248\n"
    "v_exp_f32 v125, v125\n"
    "v_sub_f32 v126, v126, v248\n"
    "v_exp_f32 v126, v126\n"
    "v_sub_f32 v127, v127, v248\n"
    "v_exp_f32 v127, v127\n"
    "v_and_b32 v248, 31, v240\n"
    "v_lshlrev_b32 v248, 5, v248\n"
    "v_add_u32 v248, 160780, v248\n"
    "ds_read_b32 v248, v248\n"
    "s_waitcnt lgkmcnt(0)\n"
    "v_sub_f32 v96, v96, v248\n"
    "v_exp_f32 v96, v96\n"
    "v_sub_f32 v97, v97, v248\n"
    "v_exp_f32 v97, v97\n"
    "v_smfmac_f32_32x32x32_bf16 v[128:143], v[92:95], v[120:127], v232\n"
    "v_sub_f32 v98, v98, v248\n"
    "v_exp_f32 v98, v98\n"
    "v_sub_f32 v99, v99, v248\n"
    "v_exp_f32 v99, v99\n"
    "v_sub_f32 v100, v100, v248\n"
    "v_exp_f32 v100, v100\n"
    "v_mfma_f32_32x32x16_bf16 v[112:127], v[4:7], v[148:151], 0\n"
    "v_sub_f32 v101, v101, v248\n"
    "v_exp_f32 v101, v101\n"
    "v_sub_f32 v102, v102, v248\n"
    "v_exp_f32 v102, v102\n"
    "v_sub_f32 v103, v103, v248\n"
    "v_exp_f32 v103, v103\n"
    "v_sub_f32 v104, v104, v248\n"
    "v_exp_f32 v104, v104\n"
    "v_sub_f32 v105, v105, v248\n"
    "v_exp_f32 v105, v105\n"
    "v_sub_f32 v106, v106, v248\n"
    "v_exp_f32 v106, v106\n"
    "v_sub_f32 v107, v107, v248\n"
    "v_exp_f32 v107, v107\n"
    "v_sub_f32 v108, v108, v248\n"
    "v_exp_f32 v108, v108\n"
    "ds_write_b128 v244, v[128:131] offset:0\n"
    "ds_write_b128 v244, v[132:135] offset:1024\n"
    "ds_write_b32 v245, v136 offset:0\n"
    "v_sub_f32 v109, v109, v248\n"
    "v_exp_f32 v109, v109\n"
    "v_sub_f32 v110, v110, v248\n"
    "v_exp_f32 v110, v110\n"
    "v_sub_f32 v111, v111, v248\n"
    "v_exp_f32 v111, v111\n"
    "s_waitcnt lgkmcnt(0)\n"
    "s_barrier\n"
    "ds_read_b128 v[128:131], v242 offset:9216\n"
    "ds_read_b128 v[132:135], v242 offset:10240\n"
    "ds_read_b32 v136, v243 offset:9216\n"
    "ds_read_b128 v[250:253], v247 offset:1024\n"
    "s_xor_b32 s72, s71, 4096\n"
    "buffer_load_dwordx4 v[144:147], v241, s[12:15], s72 offen sc1\n"
    "s_waitcnt lgkmcnt(1)\n"
    "v_smfmac_f32_32x32x32_bf16 v[128:143], v[32:35], v[96:103], v232\n"
    "v_sub_f32 v112, v112, v248\n"
    "v_exp_f32 v112, v112\n"
    "v_sub_f32 v113, v113, v248\n"
    "v_exp_f32 v113, v113\n"
    "v_smfmac_f32_32x32x32_bf16 v[128:143], v[36:39], v[104:111], v232\n"
    "v_sub_f32 v114, v114, v248\n"
    "v_exp_f32 v114, v114\n"
    "v_sub_f32 v115, v115, v248\n"
    "v_exp_f32 v115, v115\n"
    "v_sub_f32 v116, v116, v248\n"
    "v_exp_f32 v116, v116\n"
    "v_mfma_f32_32x32x16_bf16 v[96:111], v[8:11], v[148:151], 0\n"
    "v_sub_f32 v117, v117, v248\n"
    "v_exp_f32 v117, v117\n"
    "v_sub_f32 v118, v118, v248\n"
    "v_exp_f32 v118, v118\n"
    "v_sub_f32 v119, v119, v248\n"
    "v_exp_f32 v119, v119\n"
    "v_sub_f32 v120, v120, v248\n"
    "v_exp_f32 v120, v120\n"
    "v_sub_f32 v121, v121, v248\n"
    "v_exp_f32 v121, v121\n"
    "v_smfmac_f32_32x32x32_bf16 v[128:143], v[40:43], v[112:119], v232\n"
    "v_sub_f32 v122, v122, v248\n"
    "v_exp_f32 v122, v122\n"
    "v_sub_f32 v123, v123, v248\n"
    "v_exp_f32 v123, v123\n"
    "v_sub_f32 v124, v124, v248\n"
    "v_exp_f32 v124, v124\n"
    "v_sub_f32 v125, v125, v248\n"
    "v_exp_f32 v125, v125\n"
    "v_sub_f32 v126, v126, v248\n"
    "v_exp_f32 v126, v126\n"
    "v_sub_f32 v127, v127, v248\n"
    "v_exp_f32 v127, v127\n"
    "v_sub_f32 v96, v96, v248\n"
    "v_exp_f32 v96, v96\n"
    "v_sub_f32 v97, v97, v248\n"
    "v_exp_f32 v97, v97\n"
    "v_smfmac_f32_32x32x32_bf16 v[128:143], v[44:47], v[120:127], v232\n"
    "v_sub_f32 v98, v98, v248\n"
    "v_exp_f32 v98, v98\n"
    "v_sub_f32 v99, v99, v248\n"
    "v_exp_f32 v99, v99\n"
    "v_sub_f32 v100, v100, v248\n"
    "v_exp_f32 v100, v100\n"
    "v_mfma_f32_32x32x16_bf16 v[112:127], v[12:15], v[148:151], 0\n"
    "v_sub_f32 v101, v101, v248\n"
    "v_exp_f32 v101, v101\n"
    "v_sub_f32 v102, v102, v248\n"
    "v_exp_f32 v102, v102\n"
    "v_sub_f32 v103, v103, v248\n"
    "v_exp_f32 v103, v103\n"
    "v_sub_f32 v104, v104, v248\n"
    "v_exp_f32 v104, v104\n"
    "v_sub_f32 v105, v105, v248\n"
    "v_exp_f32 v105, v105\n"
    "v_smfmac_f32_32x32x32_bf16 v[128:143], v[48:51], v[96:103], v232\n"
    "s_xor_b32 s73, s75, 131072\n"
    "s_xor_b32 s74, s75, 135168\n"
    "v_sub_f32 v106, v106, v248\n"
    "v_exp_f32 v106, v106\n"
    "v_sub_f32 v107, v107, v248\n"
    "v_exp_f32 v107, v107\n"
    "v_sub_f32 v108, v108, v248\n"
    "v_exp_f32 v108, v108\n"
    "v_sub_f32 v109, v109, v248\n"
    "v_exp_f32 v109, v109\n"
    "v_sub_f32 v110, v110, v248\n"
    "v_exp_f32 v110, v110\n"
    "v_sub_f32 v111, v111, v248\n"
    "v_exp_f32 v111, v111\n"
    "s_waitcnt vmcnt(7) lgkmcnt(0)\n"
    "v_smfmac_f32_16x16x64_bf16 v[152:155], v[250:253], v[168:175], v233\n"
    "buffer_load_dwordx4 v[168:171], v241, s[12:15], s73 offen offset:0 sc1\n"
    "v_sub_f32 v112, v112, v248\n"
    "v_exp_f32 v112, v112\n"
    "v_sub_f32 v113, v113, v248\n"
    "v_exp_f32 v113, v113\n"
    "v_smfmac_f32_32x32x32_bf16 v[128:143], v[52:55], v[104:111], v232\n"
    "buffer_load_dwordx4 v[172:175], v241, s[12:15], s73 offen offset:1024 sc1\n"
    "v_sub_f32 v114, v114, v248\n"
    "v_exp_f32 v114, v114\n"
    "v_sub_f32 v115, v115, v248\n"
    "v_exp_f32 v115, v115\n"
    "v_sub_f32 v116, v116, v248\n"
    "v_exp_f32 v116, v116\n"
    "v_mfma_f32_32x32x16_bf16 v[96:111], v[16:19], v[148:151], 0\n"
    "v_sub_f32 v117, v117, v248\n"
    "v_exp_f32 v117, v117\n"
    "v_sub_f32 v118, v118, v248\n"
    "v_exp_f32 v118, v118\n"
    "v_sub_f32 v119, v119, v248\n"
    "v_exp_f32 v119, v119\n"
    "v_sub_f32 v120, v120, v248\n"
    "v_exp_f32 v120, v120\n"
    "v_sub_f32 v121, v121, v248\n"
    "v_exp_f32 v121, v121\n"
    "v_smfmac_f32_32x32x32_bf16 v[128:143], v[56:59], v[112:119], v232\n"
    "v_sub_f32 v122, v122, v248\n"
    "v_exp_f32 v122, v122\n"
    "v_sub_f32 v123, v123, v248\n"
    "v_exp_f32 v123, v123\n"
    "v_sub_f32 v124, v124, v248\n"
    "v_exp_f32 v124, v124\n"
    "v_sub_f32 v125, v125, v248\n"
    "v_exp_f32 v125, v125\n"
    "v_sub_f32 v126, v126, v248\n"
    "v_exp_f32 v126, v126\n"
    "v_sub_f32 v127, v127, v248\n"
    "v_exp_f32 v127, v127\n"
    "s_waitcnt vmcnt(7)\n"
    "v_smfmac_f32_16x16x64_bf16 v[156:159], v[250:253], v[176:183], v233\n"
    "buffer_load_dwordx4 v[176:179], v241, s[12:15], s73 offen offset:2048 sc1\n"
    "v_sub_f32 v96, v96, v248\n"
    "v_exp_f32 v96, v96\n"
    "v_sub_f32 v97, v97, v248\n"
    "v_exp_f32 v97, v97\n"
    "v_smfmac_f32_32x32x32_bf16 v[128:143], v[60:63], v[120:127], v232\n"
    "buffer_load_dwordx4 v[180:183], v241, s[12:15], s73 offen offset:3072 sc1\n"
    "v_sub_f32 v98, v98, v248\n"
    "v_exp_f32 v98, v98\n"
    "v_sub_f32 v99, v99, v248\n"
    "v_exp_f32 v99, v99\n"
    "v_sub_f32 v100, v100, v248\n"
    "v_exp_f32 v100, v100\n"
    "v_mfma_f32_32x32x16_bf16 v[112:127], v[20:23], v[148:151], 0\n"
    "v_sub_f32 v101, v101, v248\n"
    "v_exp_f32 v101, v101\n"
    "v_sub_f32 v102, v102, v248\n"
    "v_exp_f32 v102, v102\n"
    "v_sub_f32 v103, v103, v248\n"
    "v_exp_f32 v103, v103\n"
    "v_sub_f32 v104, v104, v248\n"
    "v_exp_f32 v104, v104\n"
    "v_sub_f32 v105, v105, v248\n"
    "v_exp_f32 v105, v105\n"
    "v_smfmac_f32_32x32x32_bf16 v[128:143], v[64:67], v[96:103], v232\n"
    "v_sub_f32 v106, v106, v248\n"
    "v_exp_f32 v106, v106\n"
    "v_sub_f32 v107, v107, v248\n"
    "v_exp_f32 v107, v107\n"
    "v_sub_f32 v108, v108, v248\n"
    "v_exp_f32 v108, v108\n"
    "v_sub_f32 v109, v109, v248\n"
    "v_exp_f32 v109, v109\n"
    "v_sub_f32 v110, v110, v248\n"
    "v_exp_f32 v110, v110\n"
    "v_sub_f32 v111, v111, v248\n"
    "v_exp_f32 v111, v111\n"
    "s_waitcnt vmcnt(7)\n"
    "v_smfmac_f32_16x16x64_bf16 v[160:163], v[250:253], v[184:191], v233\n"
    "buffer_load_dwordx4 v[184:187], v241, s[12:15], s74 offen offset:0 sc1\n"
    "v_sub_f32 v112, v112, v248\n"
    "v_exp_f32 v112, v112\n"
    "v_sub_f32 v113, v113, v248\n"
    "v_exp_f32 v113, v113\n"
    "v_smfmac_f32_32x32x32_bf16 v[128:143], v[68:71], v[104:111], v232\n"
    "buffer_load_dwordx4 v[188:191], v241, s[12:15], s74 offen offset:1024 sc1\n"
    "v_sub_f32 v114, v114, v248\n"
    "v_exp_f32 v114, v114\n"
    "v_sub_f32 v115, v115, v248\n"
    "v_exp_f32 v115, v115\n"
    "v_sub_f32 v116, v116, v248\n"
    "v_exp_f32 v116, v116\n"
    "v_mfma_f32_32x32x16_bf16 v[96:111], v[24:27], v[148:151], 0\n"
    "v_sub_f32 v117, v117, v248\n"
    "v_exp_f32 v117, v117\n"
    "v_sub_f32 v118, v118, v248\n"
    "v_exp_f32 v118, v118\n"
    "v_sub_f32 v119, v119, v248\n"
    "v_exp_f32 v119, v119\n"
    "v_sub_f32 v120, v120, v248\n"
    "v_exp_f32 v120, v120\n"
    "v_sub_f32 v121, v121, v248\n"
    "v_exp_f32 v121, v121\n"
    "v_smfmac_f32_32x32x32_bf16 v[128:143], v[72:75], v[112:119], v232\n"
    "v_sub_f32 v122, v122, v248\n"
    "v_exp_f32 v122, v122\n"
    "v_sub_f32 v123, v123, v248\n"
    "v_exp_f32 v123, v123\n"
    "v_sub_f32 v124, v124, v248\n"
    "v_exp_f32 v124, v124\n"
    "v_sub_f32 v125, v125, v248\n"
    "v_exp_f32 v125, v125\n"
    "v_sub_f32 v126, v126, v248\n"
    "v_exp_f32 v126, v126\n"
    "v_sub_f32 v127, v127, v248\n"
    "v_exp_f32 v127, v127\n"
    "s_waitcnt vmcnt(7)\n"
    "v_smfmac_f32_16x16x64_bf16 v[164:167], v[250:253], v[192:199], v233\n"
    "buffer_load_dwordx4 v[192:195], v241, s[12:15], s74 offen offset:2048 sc1\n"
    "v_sub_f32 v96, v96, v248\n"
    "v_exp_f32 v96, v96\n"
    "v_sub_f32 v97, v97, v248\n"
    "v_exp_f32 v97, v97\n"
    "v_smfmac_f32_32x32x32_bf16 v[128:143], v[76:79], v[120:127], v232\n"
    "buffer_load_dwordx4 v[196:199], v241, s[12:15], s74 offen offset:3072 sc1\n"
    "v_sub_f32 v98, v98, v248\n"
    "v_exp_f32 v98, v98\n"
    "v_sub_f32 v99, v99, v248\n"
    "v_exp_f32 v99, v99\n"
    "v_sub_f32 v100, v100, v248\n"
    "v_exp_f32 v100, v100\n"
    "v_mfma_f32_32x32x16_bf16 v[112:127], v[28:31], v[148:151], 0\n"
    "v_sub_f32 v101, v101, v248\n"
    "v_exp_f32 v101, v101\n"
    "v_sub_f32 v102, v102, v248\n"
    "v_exp_f32 v102, v102\n"
    "v_sub_f32 v103, v103, v248\n"
    "v_exp_f32 v103, v103\n"
    "v_sub_f32 v104, v104, v248\n"
    "v_exp_f32 v104, v104\n"
    "v_sub_f32 v105, v105, v248\n"
    "v_exp_f32 v105, v105\n"
    "v_smfmac_f32_32x32x32_bf16 v[128:143], v[80:83], v[96:103], v232\n"
    "v_sub_f32 v106, v106, v248\n"
    "v_exp_f32 v106, v106\n"
    "v_sub_f32 v107, v107, v248\n"
    "v_exp_f32 v107, v107\n"
    "v_sub_f32 v108, v108, v248\n"
    "v_exp_f32 v108, v108\n"
    "v_sub_f32 v109, v109, v248\n"
    "v_exp_f32 v109, v109\n"
    "v_sub_f32 v110, v110, v248\n"
    "v_exp_f32 v110, v110\n"
    "v_sub_f32 v111, v111, v248\n"
    "v_exp_f32 v111, v111\n"
    "v_sub_f32 v112, v112, v248\n"
    "v_exp_f32 v112, v112\n"
    "v_sub_f32 v113, v113, v248\n"
    "v_exp_f32 v113, v113\n"
    "v_smfmac_f32_32x32x32_bf16 v[128:143], v[84:87], v[104:111], v232\n"
    "v_sub_f32 v114, v114, v248\n"
    "v_exp_f32 v114, v114\n"
    "v_sub_f32 v115, v115, v248\n"
    "v_exp_f32 v115, v115\n"
    "v_sub_f32 v116, v116, v248\n"
    "v_exp_f32 v116, v116\n"
    "s_waitcnt vmcnt(8)\n"
    "v_mfma_f32_32x32x16_bf16 v[96:111], v[0:3], v[144:147], 0\n"
    "v_sub_f32 v117, v117, v248\n"
    "v_exp_f32 v117, v117\n"
    "v_sub_f32 v118, v118, v248\n"
    "v_exp_f32 v118, v118\n"
    "v_sub_f32 v119, v119, v248\n"
    "v_exp_f32 v119, v119\n"
    "v_sub_f32 v120, v120, v248\n"
    "v_exp_f32 v120, v120\n"
    "v_sub_f32 v121, v121, v248\n"
    "v_exp_f32 v121, v121\n"
    "v_smfmac_f32_32x32x32_bf16 v[128:143], v[88:91], v[112:119], v232\n"
    "v_sub_f32 v122, v122, v248\n"
    "v_exp_f32 v122, v122\n"
    "v_sub_f32 v123, v123, v248\n"
    "v_exp_f32 v123, v123\n"
    "v_sub_f32 v124, v124, v248\n"
    "v_exp_f32 v124, v124\n"
    "v_sub_f32 v125, v125, v248\n"
    "v_exp_f32 v125, v125\n"
    "v_sub_f32 v126, v126, v248\n"
    "v_exp_f32 v126, v126\n"
    "v_sub_f32 v127, v127, v248\n"
    "v_exp_f32 v127, v127\n"
    "v_and_b32 v248, 31, v240\n"
    "v_lshlrev_b32 v248, 5, v248\n"
    "v_add_u32 v248, 160784, v248\n"
    "ds_read_b32 v248, v248\n"
    "s_waitcnt lgkmcnt(0)\n"
    "v_sub_f32 v96, v96, v248\n"
    "v_exp_f32 v96, v96\n"
    "v_sub_f32 v97, v97, v248\n"
    "v_exp_f32 v97, v97\n"
    "v_smfmac_f32_32x32x32_bf16 v[128:143], v[92:95], v[120:127], v232\n"
    "v_sub_f32 v98, v98, v248\n"
    "v_exp_f32 v98, v98\n"
    "v_sub_f32 v99, v99, v248\n"
    "v_exp_f32 v99, v99\n"
    "v_sub_f32 v100, v100, v248\n"
    "v_exp_f32 v100, v100\n"
    "v_mfma_f32_32x32x16_bf16 v[112:127], v[4:7], v[144:147], 0\n"
    "v_sub_f32 v101, v101, v248\n"
    "v_exp_f32 v101, v101\n"
    "v_sub_f32 v102, v102, v248\n"
    "v_exp_f32 v102, v102\n"
    "v_sub_f32 v103, v103, v248\n"
    "v_exp_f32 v103, v103\n"
    "v_sub_f32 v104, v104, v248\n"
    "v_exp_f32 v104, v104\n"
    "v_sub_f32 v105, v105, v248\n"
    "v_exp_f32 v105, v105\n"
    "v_sub_f32 v106, v106, v248\n"
    "v_exp_f32 v106, v106\n"
    "v_sub_f32 v107, v107, v248\n"
    "v_exp_f32 v107, v107\n"
    "v_sub_f32 v108, v108, v248\n"
    "v_exp_f32 v108, v108\n"
    "ds_write_b128 v244, v[128:131] offset:8192\n"
    "ds_write_b128 v244, v[132:135] offset:9216\n"
    "ds_write_b32 v245, v136 offset:512\n"
    "v_sub_f32 v109, v109, v248\n"
    "v_exp_f32 v109, v109\n"
    "v_sub_f32 v110, v110, v248\n"
    "v_exp_f32 v110, v110\n"
    "v_sub_f32 v111, v111, v248\n"
    "v_exp_f32 v111, v111\n"
    "s_waitcnt lgkmcnt(0)\n"
    "s_barrier\n"
    "ds_read_b128 v[128:131], v242 offset:0\n"
    "ds_read_b128 v[132:135], v242 offset:1024\n"
    "ds_read_b32 v136, v243 offset:0\n"
    "ds_read_b128 v[250:253], v247 offset:0\n"
    "s_xor_b32 s72, s71, 5120\n"
    "buffer_load_dwordx4 v[148:151], v241, s[12:15], s72 offen sc1\n"
    "s_waitcnt lgkmcnt(1)\n"
    "v_smfmac_f32_32x32x32_bf16 v[128:143], v[32:35], v[96:103], v232\n"
    "v_sub_f32 v112, v112, v248\n"
    "v_exp_f32 v112, v112\n"
    "v_sub_f32 v113, v113, v248\n"
    "v_exp_f32 v113, v113\n"
    "v_smfmac_f32_32x32x32_bf16 v[128:143], v[36:39], v[104:111], v232\n"
    "v_sub_f32 v114, v114, v248\n"
    "v_exp_f32 v114, v114\n"
    "v_sub_f32 v115, v115, v248\n"
    "v_exp_f32 v115, v115\n"
    "v_sub_f32 v116, v116, v248\n"
    "v_exp_f32 v116, v116\n"
    "v_mfma_f32_32x32x16_bf16 v[96:111], v[8:11], v[144:147], 0\n"
    "v_sub_f32 v117, v117, v248\n"
    "v_exp_f32 v117, v117\n"
    "v_sub_f32 v118, v118, v248\n"
    "v_exp_f32 v118, v118\n"
    "v_sub_f32 v119, v119, v248\n"
    "v_exp_f32 v119, v119\n"
    "v_sub_f32 v120, v120, v248\n"
    "v_exp_f32 v120, v120\n"
    "v_sub_f32 v121, v121, v248\n"
    "v_exp_f32 v121, v121\n"
    "v_smfmac_f32_32x32x32_bf16 v[128:143], v[40:43], v[112:119], v232\n"
    "v_sub_f32 v122, v122, v248\n"
    "v_exp_f32 v122, v122\n"
    "v_sub_f32 v123, v123, v248\n"
    "v_exp_f32 v123, v123\n"
    "v_sub_f32 v124, v124, v248\n"
    "v_exp_f32 v124, v124\n"
    "v_sub_f32 v125, v125, v248\n"
    "v_exp_f32 v125, v125\n"
    "v_sub_f32 v126, v126, v248\n"
    "v_exp_f32 v126, v126\n"
    "v_sub_f32 v127, v127, v248\n"
    "v_exp_f32 v127, v127\n"
    "v_sub_f32 v96, v96, v248\n"
    "v_exp_f32 v96, v96\n"
    "v_sub_f32 v97, v97, v248\n"
    "v_exp_f32 v97, v97\n"
    "v_smfmac_f32_32x32x32_bf16 v[128:143], v[44:47], v[120:127], v232\n"
    "v_sub_f32 v98, v98, v248\n"
    "v_exp_f32 v98, v98\n"
    "v_sub_f32 v99, v99, v248\n"
    "v_exp_f32 v99, v99\n"
    "v_sub_f32 v100, v100, v248\n"
    "v_exp_f32 v100, v100\n"
    "v_mfma_f32_32x32x16_bf16 v[112:127], v[12:15], v[144:147], 0\n"
    "v_sub_f32 v101, v101, v248\n"
    "v_exp_f32 v101, v101\n"
    "v_sub_f32 v102, v102, v248\n"
    "v_exp_f32 v102, v102\n"
    "v_sub_f32 v103, v103, v248\n"
    "v_exp_f32 v103, v103\n"
    "v_sub_f32 v104, v104, v248\n"
    "v_exp_f32 v104, v104\n"
    "v_sub_f32 v105, v105, v248\n"
    "v_exp_f32 v105, v105\n"
    "v_smfmac_f32_32x32x32_bf16 v[128:143], v[48:51], v[96:103], v232\n"
    "s_xor_b32 s73, s75, 196608\n"
    "s_xor_b32 s74, s75, 200704\n"
    "v_sub_f32 v106, v106, v248\n"
    "v_exp_f32 v106, v106\n"
    "v_sub_f32 v107, v107, v248\n"
    "v_exp_f32 v107, v107\n"
    "v_sub_f32 v108, v108, v248\n"
    "v_exp_f32 v108, v108\n"
    "v_sub_f32 v109, v109, v248\n"
    "v_exp_f32 v109, v109\n"
    "v_sub_f32 v110, v110, v248\n"
    "v_exp_f32 v110, v110\n"
    "v_sub_f32 v111, v111, v248\n"
    "v_exp_f32 v111, v111\n"
    "s_waitcnt vmcnt(7) lgkmcnt(0)\n"
    "v_smfmac_f32_16x16x64_bf16 v[152:155], v[250:253], v[168:175], v233\n"
    "buffer_load_dwordx4 v[168:171], v241, s[12:15], s73 offen offset:0 sc1\n"
    "v_sub_f32 v112, v112, v248\n"
    "v_exp_f32 v112, v112\n"
    "v_sub_f32 v113, v113, v248\n"
    "v_exp_f32 v113, v113\n"
    "v_smfmac_f32_32x32x32_bf16 v[128:143], v[52:55], v[104:111], v232\n"
    "buffer_load_dwordx4 v[172:175], v241, s[12:15], s73 offen offset:1024 sc1\n"
    "v_sub_f32 v114, v114, v248\n"
    "v_exp_f32 v114, v114\n"
    "v_sub_f32 v115, v115, v248\n"
    "v_exp_f32 v115, v115\n"
    "v_sub_f32 v116, v116, v248\n"
    "v_exp_f32 v116, v116\n"
    "v_mfma_f32_32x32x16_bf16 v[96:111], v[16:19], v[144:147], 0\n"
    "v_sub_f32 v117, v117, v248\n"
    "v_exp_f32 v117, v117\n"
    "v_sub_f32 v118, v118, v248\n"
    "v_exp_f32 v118, v118\n"
    "v_sub_f32 v119, v119, v248\n"
    "v_exp_f32 v119, v119\n"
    "v_sub_f32 v120, v120, v248\n"
    "v_exp_f32 v120, v120\n"
    "v_sub_f32 v121, v121, v248\n"
    "v_exp_f32 v121, v121\n"
    "v_smfmac_f32_32x32x32_bf16 v[128:143], v[56:59], v[112:119], v232\n"
    "v_sub_f32 v122, v122, v248\n"
    "v_exp_f32 v122, v122\n"
    "v_sub_f32 v123, v123, v248\n"
    "v_exp_f32 v123, v123\n"
    "v_sub_f32 v124, v124, v248\n"
    "v_exp_f32 v124, v124\n"
    "v_sub_f32 v125, v125, v248\n"
    "v_exp_f32 v125, v125\n"
    "v_sub_f32 v126, v126, v248\n"
    "v_exp_f32 v126, v126\n"
    "v_sub_f32 v127, v127, v248\n"
    "v_exp_f32 v127, v127\n"
    "s_waitcnt vmcnt(7)\n"
    "v_smfmac_f32_16x16x64_bf16 v[156:159], v[250:253], v[176:183], v233\n"
    "buffer_load_dwordx4 v[176:179], v241, s[12:15], s73 offen offset:2048 sc1\n"
    "v_sub_f32 v96, v96, v248\n"
    "v_exp_f32 v96, v96\n"
    "v_sub_f32 v97, v97, v248\n"
    "v_exp_f32 v97, v97\n"
    "v_smfmac_f32_32x32x32_bf16 v[128:143], v[60:63], v[120:127], v232\n"
    "buffer_load_dwordx4 v[180:183], v241, s[12:15], s73 offen offset:3072 sc1\n"
    "v_sub_f32 v98, v98, v248\n"
    "v_exp_f32 v98, v98\n"
    "v_sub_f32 v99, v99, v248\n"
    "v_exp_f32 v99, v99\n"
    "v_sub_f32 v100, v100, v248\n"
    "v_exp_f32 v100, v100\n"
    "v_mfma_f32_32x32x16_bf16 v[112:127], v[20:23], v[144:147], 0\n"
    "v_sub_f32 v101, v101, v248\n"
    "v_exp_f32 v101, v101\n"
    "v_sub_f32 v102, v102, v248\n"
    "v_exp_f32 v102, v102\n"
    "v_sub_f32 v103, v103, v248\n"
    "v_exp_f32 v103, v103\n"
    "v_sub_f32 v104, v104, v248\n"
    "v_exp_f32 v104, v104\n"
    "v_sub_f32 v105, v105, v248\n"
    "v_exp_f32 v105, v105\n"
    "v_smfmac_f32_32x32x32_bf16 v[128:143], v[64:67], v[96:103], v232\n"
    "v_sub_f32 v106, v106, v248\n"
    "v_exp_f32 v106, v106\n"
    "v_sub_f32 v107, v107, v248\n"
    "v_exp_f32 v107, v107\n"
    "v_sub_f32 v108, v108, v248\n"
    "v_exp_f32 v108, v108\n"
    "v_sub_f32 v109, v109, v248\n"
    "v_exp_f32 v109, v109\n"
    "v_sub_f32 v110, v110, v248\n"
    "v_exp_f32 v110, v110\n"
    "v_sub_f32 v111, v111, v248\n"
    "v_exp_f32 v111, v111\n"
    "s_waitcnt vmcnt(7)\n"
    "v_smfmac_f32_16x16x64_bf16 v[160:163], v[250:253], v[184:191], v233\n"
    "buffer_load_dwordx4 v[184:187], v241, s[12:15], s74 offen offset:0 sc1\n"
    "v_sub_f32 v112, v112, v248\n"
    "v_exp_f32 v112, v112\n"
    "v_sub_f32 v113, v113, v248\n"
    "v_exp_f32 v113, v113\n"
    "v_smfmac_f32_32x32x32_bf16 v[128:143], v[68:71], v[104:111], v232\n"
    "buffer_load_dwordx4 v[188:191], v241, s[12:15], s74 offen offset:1024 sc1\n"
    "v_sub_f32 v114, v114, v248\n"
    "v_exp_f32 v114, v114\n"
    "v_sub_f32 v115, v115, v248\n"
    "v_exp_f32 v115, v115\n"
    "v_sub_f32 v116, v116, v248\n"
    "v_exp_f32 v116, v116\n"
    "v_mfma_f32_32x32x16_bf16 v[96:111], v[24:27], v[144:147], 0\n"
    "v_sub_f32 v117, v117, v248\n"
    "v_exp_f32 v117, v117\n"
    "v_sub_f32 v118, v118, v248\n"
    "v_exp_f32 v118, v118\n"
    "v_sub_f32 v119, v119, v248\n"
    "v_exp_f32 v119, v119\n"
    "v_sub_f32 v120, v120, v248\n"
    "v_exp_f32 v120, v120\n"
    "v_sub_f32 v121, v121, v248\n"
    "v_exp_f32 v121, v121\n"
    "v_smfmac_f32_32x32x32_bf16 v[128:143], v[72:75], v[112:119], v232\n"
    "v_sub_f32 v122, v122, v248\n"
    "v_exp_f32 v122, v122\n"
    "v_sub_f32 v123, v123, v248\n"
    "v_exp_f32 v123, v123\n"
    "v_sub_f32 v124, v124, v248\n"
    "v_exp_f32 v124, v124\n"
    "v_sub_f32 v125, v125, v248\n"
    "v_exp_f32 v125, v125\n"
    "v_sub_f32 v126, v126, v248\n"
    "v_exp_f32 v126, v126\n"
    "v_sub_f32 v127, v127, v248\n"
    "v_exp_f32 v127, v127\n"
    "s_waitcnt vmcnt(7)\n"
    "v_smfmac_f32_16x16x64_bf16 v[164:167], v[250:253], v[192:199], v233\n"
    "buffer_load_dwordx4 v[192:195], v241, s[12:15], s74 offen offset:2048 sc1\n"
    "v_sub_f32 v96, v96, v248\n"
    "v_exp_f32 v96, v96\n"
    "v_sub_f32 v97, v97, v248\n"
    "v_exp_f32 v97, v97\n"
    "v_smfmac_f32_32x32x32_bf16 v[128:143], v[76:79], v[120:127], v232\n"
    "buffer_load_dwordx4 v[196:199], v241, s[12:15], s74 offen offset:3072 sc1\n"
    "v_sub_f32 v98, v98, v248\n"
    "v_exp_f32 v98, v98\n"
    "v_sub_f32 v99, v99, v248\n"
    "v_exp_f32 v99, v99\n"
    "v_sub_f32 v100, v100, v248\n"
    "v_exp_f32 v100, v100\n"
    "v_mfma_f32_32x32x16_bf16 v[112:127], v[28:31], v[144:147], 0\n"
    "v_sub_f32 v101, v101, v248\n"
    "v_exp_f32 v101, v101\n"
    "v_sub_f32 v102, v102, v248\n"
    "v_exp_f32 v102, v102\n"
    "v_sub_f32 v103, v103, v248\n"
    "v_exp_f32 v103, v103\n"
    "v_sub_f32 v104, v104, v248\n"
    "v_exp_f32 v104, v104\n"
    "v_sub_f32 v105, v105, v248\n"
    "v_exp_f32 v105, v105\n"
    "v_smfmac_f32_32x32x32_bf16 v[128:143], v[80:83], v[96:103], v232\n"
    "v_sub_f32 v106, v106, v248\n"
    "v_exp_f32 v106, v106\n"
    "v_sub_f32 v107, v107, v248\n"
    "v_exp_f32 v107, v107\n"
    "v_sub_f32 v108, v108, v248\n"
    "v_exp_f32 v108, v108\n"
    "v_sub_f32 v109, v109, v248\n"
    "v_exp_f32 v109, v109\n"
    "v_sub_f32 v110, v110, v248\n"
    "v_exp_f32 v110, v110\n"
    "v_sub_f32 v111, v111, v248\n"
    "v_exp_f32 v111, v111\n"
    "v_sub_f32 v112, v112, v248\n"
    "v_exp_f32 v112, v112\n"
    "v_sub_f32 v113, v113, v248\n"
    "v_exp_f32 v113, v113\n"
    "v_smfmac_f32_32x32x32_bf16 v[128:143], v[84:87], v[104:111], v232\n"
    "v_sub_f32 v114, v114, v248\n"
    "v_exp_f32 v114, v114\n"
    "v_sub_f32 v115, v115, v248\n"
    "v_exp_f32 v115, v115\n"
    "v_sub_f32 v116, v116, v248\n"
    "v_exp_f32 v116, v116\n"
    "s_waitcnt vmcnt(8)\n"
    "v_mfma_f32_32x32x16_bf16 v[96:111], v[0:3], v[148:151], 0\n"
    "v_sub_f32 v117, v117, v248\n"
    "v_exp_f32 v117, v117\n"
    "v_sub_f32 v118, v118, v248\n"
    "v_exp_f32 v118, v118\n"
    "v_sub_f32 v119, v119, v248\n"
    "v_exp_f32 v119, v119\n"
    "v_sub_f32 v120, v120, v248\n"
    "v_exp_f32 v120, v120\n"
    "v_sub_f32 v121, v121, v248\n"
    "v_exp_f32 v121, v121\n"
    "v_smfmac_f32_32x32x32_bf16 v[128:143], v[88:91], v[112:119], v232\n"
    "v_sub_f32 v122, v122, v248\n"
    "v_exp_f32 v122, v122\n"
    "v_sub_f32 v123, v123, v248\n"
    "v_exp_f32 v123, v123\n"
    "v_sub_f32 v124, v124, v248\n"
    "v_exp_f32 v124, v124\n"
    "v_sub_f32 v125, v125, v248\n"
    "v_exp_f32 v125, v125\n"
    "v_sub_f32 v126, v126, v248\n"
    "v_exp_f32 v126, v126\n"
    "v_sub_f32 v127, v127, v248\n"
    "v_exp_f32 v127, v127\n"
    "v_and_b32 v248, 31, v240\n"
    "v_lshlrev_b32 v248, 5, v248\n"
    "v_add_u32 v248, 160788, v248\n"
    "ds_read_b32 v248, v248\n"
    "s_waitcnt lgkmcnt(0)\n"
    "v_sub_f32 v96, v96, v248\n"
    "v_exp_f32 v96, v96\n"
    "v_sub_f32 v97, v97, v248\n"
    "v_exp_f32 v97, v97\n"
    "v_smfmac_f32_32x32x32_bf16 v[128:143], v[92:95], v[120:127], v232\n"
    "v_sub_f32 v98, v98, v248\n"
    "v_exp_f32 v98, v98\n"
    "v_sub_f32 v99, v99, v248\n"
    "v_exp_f32 v99, v99\n"
    "v_sub_f32 v100, v100, v248\n"
    "v_exp_f32 v100, v100\n"
    "v_mfma_f32_32x32x16_bf16 v[112:127], v[4:7], v[148:151], 0\n"
    "v_sub_f32 v101, v101, v248\n"
    "v_exp_f32 v101, v101\n"
    "v_sub_f32 v102, v102, v248\n"
    "v_exp_f32 v102, v102\n"
    "v_sub_f32 v103, v103, v248\n"
    "v_exp_f32 v103, v103\n"
    "v_sub_f32 v104, v104, v248\n"
    "v_exp_f32 v104, v104\n"
    "v_sub_f32 v105, v105, v248\n"
    "v_exp_f32 v105, v105\n"
    "v_sub_f32 v106, v106, v248\n"
    "v_exp_f32 v106, v106\n"
    "v_sub_f32 v107, v107, v248\n"
    "v_exp_f32 v107, v107\n"
    "v_sub_f32 v108, v108, v248\n"
    "v_exp_f32 v108, v108\n"
    "ds_write_b128 v244, v[128:131] offset:0\n"
    "ds_write_b128 v244, v[132:135] offset:1024\n"
    "ds_write_b32 v245, v136 offset:0\n"
    "v_sub_f32 v109, v109, v248\n"
    "v_exp_f32 v109, v109\n"
    "v_sub_f32 v110, v110, v248\n"
    "v_exp_f32 v110, v110\n"
    "v_sub_f32 v111, v111, v248\n"
    "v_exp_f32 v111, v111\n"
    "s_waitcnt lgkmcnt(0)\n"
    "s_barrier\n"
    "ds_read_b128 v[128:131], v242 offset:9216\n"
    "ds_read_b128 v[132:135], v242 offset:10240\n"
    "ds_read_b32 v136, v243 offset:9216\n"
    "ds_read_b128 v[250:253], v247 offset:1024\n"
    "s_xor_b32 s72, s71, 6144\n"
    "buffer_load_dwordx4 v[144:147], v241, s[12:15], s72 offen sc1\n"
    "s_waitcnt lgkmcnt(1)\n"
    "v_smfmac_f32_32x32x32_bf16 v[128:143], v[32:35], v[96:103], v232\n"
    "v_sub_f32 v112, v112, v248\n"
    "v_exp_f32 v112, v112\n"
    "v_sub_f32 v113, v113, v248\n"
    "v_exp_f32 v113, v113\n"
    "v_smfmac_f32_32x32x32_bf16 v[128:143], v[36:39], v[104:111], v232\n"
    "v_sub_f32 v114, v114, v248\n"
    "v_exp_f32 v114, v114\n"
    "v_sub_f32 v115, v115, v248\n"
    "v_exp_f32 v115, v115\n"
    "v_sub_f32 v116, v116, v248\n"
    "v_exp_f32 v116, v116\n"
    "v_mfma_f32_32x32x16_bf16 v[96:111], v[8:11], v[148:151], 0\n"
    "v_sub_f32 v117, v117, v248\n"
    "v_exp_f32 v117, v117\n"
    "v_sub_f32 v118, v118, v248\n"
    "v_exp_f32 v118, v118\n"
    "v_sub_f32 v119, v119, v248\n"
    "v_exp_f32 v119, v119\n"
    "v_sub_f32 v120, v120, v248\n"
    "v_exp_f32 v120, v120\n"
    "v_sub_f32 v121, v121, v248\n"
    "v_exp_f32 v121, v121\n"
    "v_smfmac_f32_32x32x32_bf16 v[128:143], v[40:43], v[112:119], v232\n"
    "v_sub_f32 v122, v122, v248\n"
    "v_exp_f32 v122, v122\n"
    "v_sub_f32 v123, v123, v248\n"
    "v_exp_f32 v123, v123\n"
    "v_sub_f32 v124, v124, v248\n"
    "v_exp_f32 v124, v124\n"
    "v_sub_f32 v125, v125, v248\n"
    "v_exp_f32 v125, v125\n"
    "v_sub_f32 v126, v126, v248\n"
    "v_exp_f32 v126, v126\n"
    "v_sub_f32 v127, v127, v248\n"
    "v_exp_f32 v127, v127\n"
    "v_sub_f32 v96, v96, v248\n"
    "v_exp_f32 v96, v96\n"
    "v_sub_f32 v97, v97, v248\n"
    "v_exp_f32 v97, v97\n"
    "v_smfmac_f32_32x32x32_bf16 v[128:143], v[44:47], v[120:127], v232\n"
    "v_sub_f32 v98, v98, v248\n"
    "v_exp_f32 v98, v98\n"
    "v_sub_f32 v99, v99, v248\n"
    "v_exp_f32 v99, v99\n"
    "v_sub_f32 v100, v100, v248\n"
    "v_exp_f32 v100, v100\n"
    "v_mfma_f32_32x32x16_bf16 v[112:127], v[12:15], v[148:151], 0\n"
    "v_sub_f32 v101, v101, v248\n"
    "v_exp_f32 v101, v101\n"
    "v_sub_f32 v102, v102, v248\n"
    "v_exp_f32 v102, v102\n"
    "v_sub_f32 v103, v103, v248\n"
    "v_exp_f32 v103, v103\n"
    "v_sub_f32 v104, v104, v248\n"
    "v_exp_f32 v104, v104\n"
    "v_sub_f32 v105, v105, v248\n"
    "v_exp_f32 v105, v105\n"
    "v_smfmac_f32_32x32x32_bf16 v[128:143], v[48:51], v[96:103], v232\n"
    "s_xor_b32 s73, s75, 262144\n"
    "s_xor_b32 s74, s75, 266240\n"
    "v_sub_f32 v106, v106, v248\n"
    "v_exp_f32 v106, v106\n"
    "v_sub_f32 v107, v107, v248\n"
    "v_exp_f32 v107, v107\n"
    "v_sub_f32 v108, v108, v248\n"
    "v_exp_f32 v108, v108\n"
    "v_sub_f32 v109, v109, v248\n"
    "v_exp_f32 v109, v109\n"
    "v_sub_f32 v110, v110, v248\n"
    "v_exp_f32 v110, v110\n"
    "v_sub_f32 v111, v111, v248\n"
    "v_exp_f32 v111, v111\n"
    "s_waitcnt vmcnt(7) lgkmcnt(0)\n"
    "v_smfmac_f32_16x16x64_bf16 v[152:155], v[250:253], v[168:175], v233\n"
    "buffer_load_dwordx4 v[168:171], v241, s[12:15], s73 offen offset:0 sc1\n"
    "v_sub_f32 v112, v112, v248\n"
    "v_exp_f32 v112, v112\n"
    "v_sub_f32 v113, v113, v248\n"
    "v_exp_f32 v113, v113\n"
    "v_smfmac_f32_32x32x32_bf16 v[128:143], v[52:55], v[104:111], v232\n"
    "buffer_load_dwordx4 v[172:175], v241, s[12:15], s73 offen offset:1024 sc1\n"
    "v_sub_f32 v114, v114, v248\n"
    "v_exp_f32 v114, v114\n"
    "v_sub_f32 v115, v115, v248\n"
    "v_exp_f32 v115, v115\n"
    "v_sub_f32 v116, v116, v248\n"
    "v_exp_f32 v116, v116\n"
    "v_mfma_f32_32x32x16_bf16 v[96:111], v[16:19], v[148:151], 0\n"
    "v_sub_f32 v117, v117, v248\n"
    "v_exp_f32 v117, v117\n"
    "v_sub_f32 v118, v118, v248\n"
    "v_exp_f32 v118, v118\n"
    "v_sub_f32 v119, v119, v248\n"
    "v_exp_f32 v119, v119\n"
    "v_sub_f32 v120, v120, v248\n"
    "v_exp_f32 v120, v120\n"
    "v_sub_f32 v121, v121, v248\n"
    "v_exp_f32 v121, v121\n"
    "v_smfmac_f32_32x32x32_bf16 v[128:143], v[56:59], v[112:119], v232\n"
    "v_sub_f32 v122, v122, v248\n"
    "v_exp_f32 v122, v122\n"
    "v_sub_f32 v123, v123, v248\n"
    "v_exp_f32 v123, v123\n"
    "v_sub_f32 v124, v124, v248\n"
    "v_exp_f32 v124, v124\n"
    "v_sub_f32 v125, v125, v248\n"
    "v_exp_f32 v125, v125\n"
    "v_sub_f32 v126, v126, v248\n"
    "v_exp_f32 v126, v126\n"
    "v_sub_f32 v127, v127, v248\n"
    "v_exp_f32 v127, v127\n"
    "s_waitcnt vmcnt(7)\n"
    "v_smfmac_f32_16x16x64_bf16 v[156:159], v[250:253], v[176:183], v233\n"
    "buffer_load_dwordx4 v[176:179], v241, s[12:15], s73 offen offset:2048 sc1\n"
    "v_sub_f32 v96, v96, v248\n"
    "v_exp_f32 v96, v96\n"
    "v_sub_f32 v97, v97, v248\n"
    "v_exp_f32 v97, v97\n"
    "v_smfmac_f32_32x32x32_bf16 v[128:143], v[60:63], v[120:127], v232\n"
    "buffer_load_dwordx4 v[180:183], v241, s[12:15], s73 offen offset:3072 sc1\n"
    "v_sub_f32 v98, v98, v248\n"
    "v_exp_f32 v98, v98\n"
    "v_sub_f32 v99, v99, v248\n"
    "v_exp_f32 v99, v99\n"
    "v_sub_f32 v100, v100, v248\n"
    "v_exp_f32 v100, v100\n"
    "v_mfma_f32_32x32x16_bf16 v[112:127], v[20:23], v[148:151], 0\n"
    "v_sub_f32 v101, v101, v248\n"
    "v_exp_f32 v101, v101\n"
    "v_sub_f32 v102, v102, v248\n"
    "v_exp_f32 v102, v102\n"
    "v_sub_f32 v103, v103, v248\n"
    "v_exp_f32 v103, v103\n"
    "v_sub_f32 v104, v104, v248\n"
    "v_exp_f32 v104, v104\n"
    "v_sub_f32 v105, v105, v248\n"
    "v_exp_f32 v105, v105\n"
    "v_smfmac_f32_32x32x32_bf16 v[128:143], v[64:67], v[96:103], v232\n"
    "v_sub_f32 v106, v106, v248\n"
    "v_exp_f32 v106, v106\n"
    "v_sub_f32 v107, v107, v248\n"
    "v_exp_f32 v107, v107\n"
    "v_sub_f32 v108, v108, v248\n"
    "v_exp_f32 v108, v108\n"
    "v_sub_f32 v109, v109, v248\n"
    "v_exp_f32 v109, v109\n"
    "v_sub_f32 v110, v110, v248\n"
    "v_exp_f32 v110, v110\n"
    "v_sub_f32 v111, v111, v248\n"
    "v_exp_f32 v111, v111\n"
    "s_waitcnt vmcnt(7)\n"
    "v_smfmac_f32_16x16x64_bf16 v[160:163], v[250:253], v[184:191], v233\n"
    "buffer_load_dwordx4 v[184:187], v241, s[12:15], s74 offen offset:0 sc1\n"
    "v_sub_f32 v112, v112, v248\n"
    "v_exp_f32 v112, v112\n"
    "v_sub_f32 v113, v113, v248\n"
    "v_exp_f32 v113, v113\n"
    "v_smfmac_f32_32x32x32_bf16 v[128:143], v[68:71], v[104:111], v232\n"
    "buffer_load_dwordx4 v[188:191], v241, s[12:15], s74 offen offset:1024 sc1\n"
    "v_sub_f32 v114, v114, v248\n"
    "v_exp_f32 v114, v114\n"
    "v_sub_f32 v115, v115, v248\n"
    "v_exp_f32 v115, v115\n"
    "v_sub_f32 v116, v116, v248\n"
    "v_exp_f32 v116, v116\n"
    "v_mfma_f32_32x32x16_bf16 v[96:111], v[24:27], v[148:151], 0\n"
    "v_sub_f32 v117, v117, v248\n"
    "v_exp_f32 v117, v117\n"
    "v_sub_f32 v118, v118, v248\n"
    "v_exp_f32 v118, v118\n"
    "v_sub_f32 v119, v119, v248\n"
    "v_exp_f32 v119, v119\n"
    "v_sub_f32 v120, v120, v248\n"
    "v_exp_f32 v120, v120\n"
    "v_sub_f32 v121, v121, v248\n"
    "v_exp_f32 v121, v121\n"
    "v_smfmac_f32_32x32x32_bf16 v[128:143], v[72:75], v[112:119], v232\n"
    "v_sub_f32 v122, v122, v248\n"
    "v_exp_f32 v122, v122\n"
    "v_sub_f32 v123, v123, v248\n"
    "v_exp_f32 v123, v123\n"
    "v_sub_f32 v124, v124, v248\n"
    "v_exp_f32 v124, v124\n"
    "v_sub_f32 v125, v125, v248\n"
    "v_exp_f32 v125, v125\n"
    "v_sub_f32 v126, v126, v248\n"
    "v_exp_f32 v126, v126\n"
    "v_sub_f32 v127, v127, v248\n"
    "v_exp_f32 v127, v127\n"
    "s_waitcnt vmcnt(7)\n"
    "v_smfmac_f32_16x16x64_bf16 v[164:167], v[250:253], v[192:199], v233\n"
    "buffer_load_dwordx4 v[192:195], v241, s[12:15], s74 offen offset:2048 sc1\n"
    "v_sub_f32 v96, v96, v248\n"
    "v_exp_f32 v96, v96\n"
    "v_sub_f32 v97, v97, v248\n"
    "v_exp_f32 v97, v97\n"
    "v_smfmac_f32_32x32x32_bf16 v[128:143], v[76:79], v[120:127], v232\n"
    "buffer_load_dwordx4 v[196:199], v241, s[12:15], s74 offen offset:3072 sc1\n"
    "v_sub_f32 v98, v98, v248\n"
    "v_exp_f32 v98, v98\n"
    "v_sub_f32 v99, v99, v248\n"
    "v_exp_f32 v99, v99\n"
    "v_sub_f32 v100, v100, v248\n"
    "v_exp_f32 v100, v100\n"
    "v_mfma_f32_32x32x16_bf16 v[112:127], v[28:31], v[148:151], 0\n"
    "v_sub_f32 v101, v101, v248\n"
    "v_exp_f32 v101, v101\n"
    "v_sub_f32 v102, v102, v248\n"
    "v_exp_f32 v102, v102\n"
    "v_sub_f32 v103, v103, v248\n"
    "v_exp_f32 v103, v103\n"
    "v_sub_f32 v104, v104, v248\n"
    "v_exp_f32 v104, v104\n"
    "v_sub_f32 v105, v105, v248\n"
    "v_exp_f32 v105, v105\n"
    "v_smfmac_f32_32x32x32_bf16 v[128:143], v[80:83], v[96:103], v232\n"
    "v_sub_f32 v106, v106, v248\n"
    "v_exp_f32 v106, v106\n"
    "v_sub_f32 v107, v107, v248\n"
    "v_exp_f32 v107, v107\n"
    "v_sub_f32 v108, v108, v248\n"
    "v_exp_f32 v108, v108\n"
    "v_sub_f32 v109, v109, v248\n"
    "v_exp_f32 v109, v109\n"
    "v_sub_f32 v110, v110, v248\n"
    "v_exp_f32 v110, v110\n"
    "v_sub_f32 v111, v111, v248\n"
    "v_exp_f32 v111, v111\n"
    "v_sub_f32 v112, v112, v248\n"
    "v_exp_f32 v112, v112\n"
    "v_sub_f32 v113, v113, v248\n"
    "v_exp_f32 v113, v113\n"
    "v_smfmac_f32_32x32x32_bf16 v[128:143], v[84:87], v[104:111], v232\n"
    "v_sub_f32 v114, v114, v248\n"
    "v_exp_f32 v114, v114\n"
    "v_sub_f32 v115, v115, v248\n"
    "v_exp_f32 v115, v115\n"
    "v_sub_f32 v116, v116, v248\n"
    "v_exp_f32 v116, v116\n"
    "s_waitcnt vmcnt(8)\n"
    "v_mfma_f32_32x32x16_bf16 v[96:111], v[0:3], v[144:147], 0\n"
    "v_sub_f32 v117, v117, v248\n"
    "v_exp_f32 v117, v117\n"
    "v_sub_f32 v118, v118, v248\n"
    "v_exp_f32 v118, v118\n"
    "v_sub_f32 v119, v119, v248\n"
    "v_exp_f32 v119, v119\n"
    "v_sub_f32 v120, v120, v248\n"
    "v_exp_f32 v120, v120\n"
    "v_sub_f32 v121, v121, v248\n"
    "v_exp_f32 v121, v121\n"
    "v_smfmac_f32_32x32x32_bf16 v[128:143], v[88:91], v[112:119], v232\n"
    "v_sub_f32 v122, v122, v248\n"
    "v_exp_f32 v122, v122\n"
    "v_sub_f32 v123, v123, v248\n"
    "v_exp_f32 v123, v123\n"
    "v_sub_f32 v124, v124, v248\n"
    "v_exp_f32 v124, v124\n"
    "v_sub_f32 v125, v125, v248\n"
    "v_exp_f32 v125, v125\n"
    "v_sub_f32 v126, v126, v248\n"
    "v_exp_f32 v126, v126\n"
    "v_sub_f32 v127, v127, v248\n"
    "v_exp_f32 v127, v127\n"
    "v_and_b32 v248, 31, v240\n"
    "v_lshlrev_b32 v248, 5, v248\n"
    "v_add_u32 v248, 160792, v248\n"
    "ds_read_b32 v248, v248\n"
    "s_waitcnt lgkmcnt(0)\n"
    "v_sub_f32 v96, v96, v248\n"
    "v_exp_f32 v96, v96\n"
    "v_sub_f32 v97, v97, v248\n"
    "v_exp_f32 v97, v97\n"
    "v_smfmac_f32_32x32x32_bf16 v[128:143], v[92:95], v[120:127], v232\n"
    "v_sub_f32 v98, v98, v248\n"
    "v_exp_f32 v98, v98\n"
    "v_sub_f32 v99, v99, v248\n"
    "v_exp_f32 v99, v99\n"
    "v_sub_f32 v100, v100, v248\n"
    "v_exp_f32 v100, v100\n"
    "v_mfma_f32_32x32x16_bf16 v[112:127], v[4:7], v[144:147], 0\n"
    "v_sub_f32 v101, v101, v248\n"
    "v_exp_f32 v101, v101\n"
    "v_sub_f32 v102, v102, v248\n"
    "v_exp_f32 v102, v102\n"
    "v_sub_f32 v103, v103, v248\n"
    "v_exp_f32 v103, v103\n"
    "v_sub_f32 v104, v104, v248\n"
    "v_exp_f32 v104, v104\n"
    "v_sub_f32 v105, v105, v248\n"
    "v_exp_f32 v105, v105\n"
    "v_sub_f32 v106, v106, v248\n"
    "v_exp_f32 v106, v106\n"
    "v_sub_f32 v107, v107, v248\n"
    "v_exp_f32 v107, v107\n"
    "v_sub_f32 v108, v108, v248\n"
    "v_exp_f32 v108, v108\n"
    "ds_write_b128 v244, v[128:131] offset:8192\n"
    "ds_write_b128 v244, v[132:135] offset:9216\n"
    "ds_write_b32 v245, v136 offset:512\n"
    "v_sub_f32 v109, v109, v248\n"
    "v_exp_f32 v109, v109\n"
    "v_sub_f32 v110, v110, v248\n"
    "v_exp_f32 v110, v110\n"
    "v_sub_f32 v111, v111, v248\n"
    "v_exp_f32 v111, v111\n"
    "s_waitcnt lgkmcnt(0)\n"
    "s_barrier\n"
    "ds_read_b128 v[128:131], v242 offset:0\n"
    "ds_read_b128 v[132:135], v242 offset:1024\n"
    "ds_read_b32 v136, v243 offset:0\n"
    "ds_read_b128 v[250:253], v247 offset:0\n"
    "s_xor_b32 s72, s71, 7168\n"
    "buffer_load_dwordx4 v[148:151], v241, s[12:15], s72 offen sc1\n"
    "s_xor_b32 s62, s75, 458752\n"
    "buffer_load_dwordx4 v[200:203], v241, s[12:15], s62 offen offset:0 sc1\n"
    "buffer_load_dwordx4 v[204:207], v241, s[12:15], s62 offen offset:1024 sc1\n"
    "buffer_load_dwordx4 v[208:211], v241, s[12:15], s62 offen offset:2048 sc1\n"
    "buffer_load_dwordx4 v[212:215], v241, s[12:15], s62 offen offset:3072 sc1\n"
    "s_waitcnt lgkmcnt(1)\n"
    "v_smfmac_f32_32x32x32_bf16 v[128:143], v[32:35], v[96:103], v232\n"
    "v_sub_f32 v112, v112, v248\n"
    "v_exp_f32 v112, v112\n"
    "v_sub_f32 v113, v113, v248\n"
    "v_exp_f32 v113, v113\n"
    "v_smfmac_f32_32x32x32_bf16 v[128:143], v[36:39], v[104:111], v232\n"
    "v_sub_f32 v114, v114, v248\n"
    "v_exp_f32 v114, v114\n"
    "v_sub_f32 v115, v115, v248\n"
    "v_exp_f32 v115, v115\n"
    "v_sub_f32 v116, v116, v248\n"
    "v_exp_f32 v116, v116\n"
    "v_mfma_f32_32x32x16_bf16 v[96:111], v[8:11], v[144:147], 0\n"
    "v_sub_f32 v117, v117, v248\n"
    "v_exp_f32 v117, v117\n"
    "v_sub_f32 v118, v118, v248\n"
    "v_exp_f32 v118, v118\n"
    "v_sub_f32 v119, v119, v248\n"
    "v_exp_f32 v119, v119\n"
    "v_sub_f32 v120, v120, v248\n"
    "v_exp_f32 v120, v120\n"
    "v_sub_f32 v121, v121, v248\n"
    "v_exp_f32 v121, v121\n"
    "v_smfmac_f32_32x32x32_bf16 v[128:143], v[40:43], v[112:119], v232\n"
    "v_sub_f32 v122, v122, v248\n"
    "v_exp_f32 v122, v122\n"
    "v_sub_f32 v123, v123, v248\n"
    "v_exp_f32 v123, v123\n"
    "v_sub_f32 v124, v124, v248\n"
    "v_exp_f32 v124, v124\n"
    "v_sub_f32 v125, v125, v248\n"
    "v_exp_f32 v125, v125\n"
    "v_sub_f32 v126, v126, v248\n"
    "v_exp_f32 v126, v126\n"
    "v_sub_f32 v127, v127, v248\n"
    "v_exp_f32 v127, v127\n"
    "v_sub_f32 v96, v96, v248\n"
    "v_exp_f32 v96, v96\n"
    "v_sub_f32 v97, v97, v248\n"
    "v_exp_f32 v97, v97\n"
    "v_smfmac_f32_32x32x32_bf16 v[128:143], v[44:47], v[120:127], v232\n"
    "v_sub_f32 v98, v98, v248\n"
    "v_exp_f32 v98, v98\n"
    "v_sub_f32 v99, v99, v248\n"
    "v_exp_f32 v99, v99\n"
    "v_sub_f32 v100, v100, v248\n"
    "v_exp_f32 v100, v100\n"
    "v_mfma_f32_32x32x16_bf16 v[112:127], v[12:15], v[144:147], 0\n"
    "v_sub_f32 v101, v101, v248\n"
    "v_exp_f32 v101, v101\n"
    "v_sub_f32 v102, v102, v248\n"
    "v_exp_f32 v102, v102\n"
    "v_sub_f32 v103, v103, v248\n"
    "v_exp_f32 v103, v103\n"
    "v_sub_f32 v104, v104, v248\n"
    "v_exp_f32 v104, v104\n"
    "v_sub_f32 v105, v105, v248\n"
    "v_exp_f32 v105, v105\n"
    "v_smfmac_f32_32x32x32_bf16 v[128:143], v[48:51], v[96:103], v232\n"
    "s_xor_b32 s73, s75, 327680\n"
    "s_xor_b32 s74, s75, 331776\n"
    "v_sub_f32 v106, v106, v248\n"
    "v_exp_f32 v106, v106\n"
    "v_sub_f32 v107, v107, v248\n"
    "v_exp_f32 v107, v107\n"
    "v_sub_f32 v108, v108, v248\n"
    "v_exp_f32 v108, v108\n"
    "v_sub_f32 v109, v109, v248\n"
    "v_exp_f32 v109, v109\n"
    "v_sub_f32 v110, v110, v248\n"
    "v_exp_f32 v110, v110\n"
    "v_sub_f32 v111, v111, v248\n"
    "v_exp_f32 v111, v111\n"
    "s_waitcnt vmcnt(11) lgkmcnt(0)\n"
    "v_smfmac_f32_16x16x64_bf16 v[152:155], v[250:253], v[168:175], v233\n"
    "buffer_load_dwordx4 v[168:171], v241, s[12:15], s73 offen offset:0 sc1\n"
    "v_sub_f32 v112, v112, v248\n"
    "v_exp_f32 v112, v112\n"
    "v_sub_f32 v113, v113, v248\n"
    "v_exp_f32 v113, v113\n"
    "v_smfmac_f32_32x32x32_bf16 v[128:143], v[52:55], v[104:111], v232\n"
    "buffer_load_dwordx4 v[172:175], v241, s[12:15], s73 offen offset:1024 sc1\n"
    "v_sub_f32 v114, v114, v248\n"
    "v_exp_f32 v114, v114\n"
    "v_sub_f32 v115, v115, v248\n"
    "v_exp_f32 v115, v115\n"
    "v_sub_f32 v116, v116, v248\n"
    "v_exp_f32 v116, v116\n"
    "v_mfma_f32_32x32x16_bf16 v[96:111], v[16:19], v[144:147], 0\n"
    "v_sub_f32 v117, v117, v248\n"
    "v_exp_f32 v117, v117\n"
    "v_sub_f32 v118, v118, v248\n"
    "v_exp_f32 v118, v118\n"
    "v_sub_f32 v119, v119, v248\n"
    "v_exp_f32 v119, v119\n"
    "v_sub_f32 v120, v120, v248\n"
    "v_exp_f32 v120, v120\n"
    "v_sub_f32 v121, v121, v248\n"
    "v_exp_f32 v121, v121\n"
    "v_smfmac_f32_32x32x32_bf16 v[128:143], v[56:59], v[112:119], v232\n"
    "v_sub_f32 v122, v122, v248\n"
    "v_exp_f32 v122, v122\n"
    "v_sub_f32 v123, v123, v248\n"
    "v_exp_f32 v123, v123\n"
    "v_sub_f32 v124, v124, v248\n"
    "v_exp_f32 v124, v124\n"
    "v_sub_f32 v125, v125, v248\n"
    "v_exp_f32 v125, v125\n"
    "v_sub_f32 v126, v126, v248\n"
    "v_exp_f32 v126, v126\n"
    "v_sub_f32 v127, v127, v248\n"
    "v_exp_f32 v127, v127\n"
    "s_waitcnt vmcnt(11)\n"
    "v_smfmac_f32_16x16x64_bf16 v[156:159], v[250:253], v[176:183], v233\n"
    "buffer_load_dwordx4 v[176:179], v241, s[12:15], s73 offen offset:2048 sc1\n"
    "v_sub_f32 v96, v96, v248\n"
    "v_exp_f32 v96, v96\n"
    "v_sub_f32 v97, v97, v248\n"
    "v_exp_f32 v97, v97\n"
    "v_smfmac_f32_32x32x32_bf16 v[128:143], v[60:63], v[120:127], v232\n"
    "buffer_load_dwordx4 v[180:183], v241, s[12:15], s73 offen offset:3072 sc1\n"
    "v_sub_f32 v98, v98, v248\n"
    "v_exp_f32 v98, v98\n"
    "v_sub_f32 v99, v99, v248\n"
    "v_exp_f32 v99, v99\n"
    "v_sub_f32 v100, v100, v248\n"
    "v_exp_f32 v100, v100\n"
    "v_mfma_f32_32x32x16_bf16 v[112:127], v[20:23], v[144:147], 0\n"
    "v_sub_f32 v101, v101, v248\n"
    "v_exp_f32 v101, v101\n"
    "v_sub_f32 v102, v102, v248\n"
    "v_exp_f32 v102, v102\n"
    "v_sub_f32 v103, v103, v248\n"
    "v_exp_f32 v103, v103\n"
    "v_sub_f32 v104, v104, v248\n"
    "v_exp_f32 v104, v104\n"
    "v_sub_f32 v105, v105, v248\n"
    "v_exp_f32 v105, v105\n"
    "v_smfmac_f32_32x32x32_bf16 v[128:143], v[64:67], v[96:103], v232\n"
    "v_sub_f32 v106, v106, v248\n"
    "v_exp_f32 v106, v106\n"
    "v_sub_f32 v107, v107, v248\n"
    "v_exp_f32 v107, v107\n"
    "v_sub_f32 v108, v108, v248\n"
    "v_exp_f32 v108, v108\n"
    "v_sub_f32 v109, v109, v248\n"
    "v_exp_f32 v109, v109\n"
    "v_sub_f32 v110, v110, v248\n"
    "v_exp_f32 v110, v110\n"
    "v_sub_f32 v111, v111, v248\n"
    "v_exp_f32 v111, v111\n"
    "s_waitcnt vmcnt(11)\n"
    "v_smfmac_f32_16x16x64_bf16 v[160:163], v[250:253], v[184:191], v233\n"
    "buffer_load_dwordx4 v[184:187], v241, s[12:15], s74 offen offset:0 sc1\n"
    "v_sub_f32 v112, v112, v248\n"
    "v_exp_f32 v112, v112\n"
    "v_sub_f32 v113, v113, v248\n"
    "v_exp_f32 v113, v113\n"
    "v_smfmac_f32_32x32x32_bf16 v[128:143], v[68:71], v[104:111], v232\n"
    "buffer_load_dwordx4 v[188:191], v241, s[12:15], s74 offen offset:1024 sc1\n"
    "v_sub_f32 v114, v114, v248\n"
    "v_exp_f32 v114, v114\n"
    "v_sub_f32 v115, v115, v248\n"
    "v_exp_f32 v115, v115\n"
    "v_sub_f32 v116, v116, v248\n"
    "v_exp_f32 v116, v116\n"
    "v_mfma_f32_32x32x16_bf16 v[96:111], v[24:27], v[144:147], 0\n"
    "v_sub_f32 v117, v117, v248\n"
    "v_exp_f32 v117, v117\n"
    "v_sub_f32 v118, v118, v248\n"
    "v_exp_f32 v118, v118\n"
    "v_sub_f32 v119, v119, v248\n"
    "v_exp_f32 v119, v119\n"
    "v_sub_f32 v120, v120, v248\n"
    "v_exp_f32 v120, v120\n"
    "v_sub_f32 v121, v121, v248\n"
    "v_exp_f32 v121, v121\n"
    "v_smfmac_f32_32x32x32_bf16 v[128:143], v[72:75], v[112:119], v232\n"
    "v_sub_f32 v122, v122, v248\n"
    "v_exp_f32 v122, v122\n"
    "v_sub_f32 v123, v123, v248\n"
    "v_exp_f32 v123, v123\n"
    "v_sub_f32 v124, v124, v248\n"
    "v_exp_f32 v124, v124\n"
    "v_sub_f32 v125, v125, v248\n"
    "v_exp_f32 v125, v125\n"
    "v_sub_f32 v126, v126, v248\n"
    "v_exp_f32 v126, v126\n"
    "v_sub_f32 v127, v127, v248\n"
    "v_exp_f32 v127, v127\n"
    "s_waitcnt vmcnt(11)\n"
    "v_smfmac_f32_16x16x64_bf16 v[164:167], v[250:253], v[192:199], v233\n"
    "buffer_load_dwordx4 v[192:195], v241, s[12:15], s74 offen offset:2048 sc1\n"
    "v_sub_f32 v96, v96, v248\n"
    "v_exp_f32 v96, v96\n"
    "v_sub_f32 v97, v97, v248\n"
    "v_exp_f32 v97, v97\n"
    "v_smfmac_f32_32x32x32_bf16 v[128:143], v[76:79], v[120:127], v232\n"
    "buffer_load_dwordx4 v[196:199], v241, s[12:15], s74 offen offset:3072 sc1\n"
    "v_sub_f32 v98, v98, v248\n"
    "v_exp_f32 v98, v98\n"
    "v_sub_f32 v99, v99, v248\n"
    "v_exp_f32 v99, v99\n"
    "v_sub_f32 v100, v100, v248\n"
    "v_exp_f32 v100, v100\n"
    "v_mfma_f32_32x32x16_bf16 v[112:127], v[28:31], v[144:147], 0\n"
    "v_sub_f32 v101, v101, v248\n"
    "v_exp_f32 v101, v101\n"
    "v_sub_f32 v102, v102, v248\n"
    "v_exp_f32 v102, v102\n"
    "v_sub_f32 v103, v103, v248\n"
    "v_exp_f32 v103, v103\n"
    "v_sub_f32 v104, v104, v248\n"
    "v_exp_f32 v104, v104\n"
    "v_sub_f32 v105, v105, v248\n"
    "v_exp_f32 v105, v105\n"
    "v_smfmac_f32_32x32x32_bf16 v[128:143], v[80:83], v[96:103], v232\n"
    "v_sub_f32 v106, v106, v248\n"
    "v_exp_f32 v106, v106\n"
    "v_sub_f32 v107, v107, v248\n"
    "v_exp_f32 v107, v107\n"
    "v_sub_f32 v108, v108, v248\n"
    "v_exp_f32 v108, v108\n"
    "v_sub_f32 v109, v109, v248\n"
    "v_exp_f32 v109, v109\n"
    "v_sub_f32 v110, v110, v248\n"
    "v_exp_f32 v110, v110\n"
    "v_sub_f32 v111, v111, v248\n"
    "v_exp_f32 v111, v111\n"
    "v_sub_f32 v112, v112, v248\n"
    "v_exp_f32 v112, v112\n"
    "v_sub_f32 v113, v113, v248\n"
    "v_exp_f32 v113, v113\n"
    "v_smfmac_f32_32x32x32_bf16 v[128:143], v[84:87], v[104:111], v232\n"
    "v_sub_f32 v114, v114, v248\n"
    "v_exp_f32 v114, v114\n"
    "v_sub_f32 v115, v115, v248\n"
    "v_exp_f32 v115, v115\n"
    "v_sub_f32 v116, v116, v248\n"
    "v_exp_f32 v116, v116\n"
    "s_waitcnt vmcnt(12)\n"
    "v_mfma_f32_32x32x16_bf16 v[96:111], v[0:3], v[148:151], 0\n"
    "v_sub_f32 v117, v117, v248\n"
    "v_exp_f32 v117, v117\n"
    "v_sub_f32 v118, v118, v248\n"
    "v_exp_f32 v118, v118\n"
    "v_sub_f32 v119, v119, v248\n"
    "v_exp_f32 v119, v119\n"
    "v_sub_f32 v120, v120, v248\n"
    "v_exp_f32 v120, v120\n"
    "v_sub_f32 v121, v121, v248\n"
    "v_exp_f32 v121, v121\n"
    "v_smfmac_f32_32x32x32_bf16 v[128:143], v[88:91], v[112:119], v232\n"
    "v_sub_f32 v122, v122, v248\n"
    "v_exp_f32 v122, v122\n"
    "v_sub_f32 v123, v123, v248\n"
    "v_exp_f32 v123, v123\n"
    "v_sub_f32 v124, v124, v248\n"
    "v_exp_f32 v124, v124\n"
    "v_sub_f32 v125, v125, v248\n"
    "v_exp_f32 v125, v125\n"
    "v_sub_f32 v126, v126, v248\n"
    "v_exp_f32 v126, v126\n"
    "v_sub_f32 v127, v127, v248\n"
    "v_exp_f32 v127, v127\n"
    "v_and_b32 v248, 31, v240\n"
    "v_lshlrev_b32 v248, 5, v248\n"
    "v_add_u32 v248, 160796, v248\n"
    "ds_read_b32 v248, v248\n"
    "s_waitcnt lgkmcnt(0)\n"
    "v_sub_f32 v96, v96, v248\n"
    "v_exp_f32 v96, v96\n"
    "v_sub_f32 v97, v97, v248\n"
    "v_exp_f32 v97, v97\n"
    "v_smfmac_f32_32x32x32_bf16 v[128:143], v[92:95], v[120:127], v232\n"
    "v_sub_f32 v98, v98, v248\n"
    "v_exp_f32 v98, v98\n"
    "v_sub_f32 v99, v99, v248\n"
    "v_exp_f32 v99, v99\n"
    "v_sub_f32 v100, v100, v248\n"
    "v_exp_f32 v100, v100\n"
    "v_mfma_f32_32x32x16_bf16 v[112:127], v[4:7], v[148:151], 0\n"
    "v_sub_f32 v101, v101, v248\n"
    "v_exp_f32 v101, v101\n"
    "v_sub_f32 v102, v102, v248\n"
    "v_exp_f32 v102, v102\n"
    "v_sub_f32 v103, v103, v248\n"
    "v_exp_f32 v103, v103\n"
    "v_sub_f32 v104, v104, v248\n"
    "v_exp_f32 v104, v104\n"
    "v_sub_f32 v105, v105, v248\n"
    "v_exp_f32 v105, v105\n"
    "v_sub_f32 v106, v106, v248\n"
    "v_exp_f32 v106, v106\n"
    "v_sub_f32 v107, v107, v248\n"
    "v_exp_f32 v107, v107\n"
    "v_sub_f32 v108, v108, v248\n"
    "v_exp_f32 v108, v108\n"
    "ds_write_b128 v244, v[128:131] offset:0\n"
    "ds_write_b128 v244, v[132:135] offset:1024\n"
    "ds_write_b32 v245, v136 offset:0\n"
    "v_mfma_f32_32x32x8_bf16 v[128:143], v[234:235], v[234:235], 0\n"
    "v_sub_f32 v109, v109, v248\n"
    "v_exp_f32 v109, v109\n"
    "v_sub_f32 v110, v110, v248\n"
    "v_exp_f32 v110, v110\n"
    "v_sub_f32 v111, v111, v248\n"
    "v_exp_f32 v111, v111\n"
    "v_smfmac_f32_32x32x32_bf16 v[128:143], v[32:35], v[96:103], v232\n"
    "v_sub_f32 v112, v112, v248\n"
    "v_exp_f32 v112, v112\n"
    "v_sub_f32 v113, v113, v248\n"
    "v_exp_f32 v113, v113\n"
    "v_smfmac_f32_32x32x32_bf16 v[128:143], v[36:39], v[104:111], v232\n"
    "v_sub_f32 v114, v114, v248\n"
    "v_exp_f32 v114, v114\n"
    "v_sub_f32 v115, v115, v248\n"
    "v_exp_f32 v115, v115\n"
    "v_sub_f32 v116, v116, v248\n"
    "v_exp_f32 v116, v116\n"
    "v_mfma_f32_32x32x16_bf16 v[96:111], v[8:11], v[148:151], 0\n"
    "v_sub_f32 v117, v117, v248\n"
    "v_exp_f32 v117, v117\n"
    "v_sub_f32 v118, v118, v248\n"
    "v_exp_f32 v118, v118\n"
    "v_sub_f32 v119, v119, v248\n"
    "v_exp_f32 v119, v119\n"
    "v_sub_f32 v120, v120, v248\n"
    "v_exp_f32 v120, v120\n"
    "v_sub_f32 v121, v121, v248\n"
    "v_exp_f32 v121, v121\n"
    "v_smfmac_f32_32x32x32_bf16 v[128:143], v[40:43], v[112:119], v232\n"
    "s_waitcnt lgkmcnt(0)\n"
    "s_barrier\n"
    "ds_read_b128 v[250:253], v247 offset:1024\n"
    "v_sub_f32 v122, v122, v248\n"
    "v_exp_f32 v122, v122\n"
    "v_sub_f32 v123, v123, v248\n"
    "v_exp_f32 v123, v123\n"
    "v_sub_f32 v124, v124, v248\n"
    "v_exp_f32 v124, v124\n"
    "v_sub_f32 v125, v125, v248\n"
    "v_exp_f32 v125, v125\n"
    "v_sub_f32 v126, v126, v248\n"
    "v_exp_f32 v126, v126\n"
    "v_sub_f32 v127, v127, v248\n"
    "v_exp_f32 v127, v127\n"
    "v_sub_f32 v96, v96, v248\n"
    "v_exp_f32 v96, v96\n"
    "v_sub_f32 v97, v97, v248\n"
    "v_exp_f32 v97, v97\n"
    "v_smfmac_f32_32x32x32_bf16 v[128:143], v[44:47], v[120:127], v232\n"
    "v_sub_f32 v98, v98, v248\n"
    "v_exp_f32 v98, v98\n"
    "v_sub_f32 v99, v99, v248\n"
    "v_exp_f32 v99, v99\n"
    "v_sub_f32 v100, v100, v248\n"
    "v_exp_f32 v100, v100\n"
    "v_mfma_f32_32x32x16_bf16 v[112:127], v[12:15], v[148:151], 0\n"
    "v_sub_f32 v101, v101, v248\n"
    "v_exp_f32 v101, v101\n"
    "v_sub_f32 v102, v102, v248\n"
    "v_exp_f32 v102, v102\n"
    "v_sub_f32 v103, v103, v248\n"
    "v_exp_f32 v103, v103\n"
    "v_sub_f32 v104, v104, v248\n"
    "v_exp_f32 v104, v104\n"
    "v_sub_f32 v105, v105, v248\n"
    "v_exp_f32 v105, v105\n"
    "v_smfmac_f32_32x32x32_bf16 v[128:143], v[48:51], v[96:103], v232\n"
    "s_xor_b32 s73, s75, 393216\n"
    "s_xor_b32 s74, s75, 397312\n"
    "v_sub_f32 v106, v106, v248\n"
    "v_exp_f32 v106, v106\n"
    "v_sub_f32 v107, v107, v248\n"
    "v_exp_f32 v107, v107\n"
    "v_sub_f32 v108, v108, v248\n"
    "v_exp_f32 v108, v108\n"
    "v_sub_f32 v109, v109, v248\n"
    "v_exp_f32 v109, v109\n"
    "v_sub_f32 v110, v110, v248\n"
    "v_exp_f32 v110, v110\n"
    "v_sub_f32 v111, v111, v248\n"
    "v_exp_f32 v111, v111\n"
    "s_waitcnt vmcnt(6) lgkmcnt(0)\n"
    "v_smfmac_f32_16x16x64_bf16 v[152:155], v[250:253], v[168:175], v233\n"
    "buffer_load_dwordx4 v[168:171], v241, s[12:15], s73 offen offset:0 sc1\n"
    "v_sub_f32 v112, v112, v248\n"
    "v_exp_f32 v112, v112\n"
    "v_sub_f32 v113, v113, v248\n"
    "v_exp_f32 v113, v113\n"
    "v_smfmac_f32_32x32x32_bf16 v[128:143], v[52:55], v[104:111], v232\n"
    "buffer_load_dwordx4 v[172:175], v241, s[12:15], s73 offen offset:1024 sc1\n"
    "v_sub_f32 v114, v114, v248\n"
    "v_exp_f32 v114, v114\n"
    "v_sub_f32 v115, v115, v248\n"
    "v_exp_f32 v115, v115\n"
    "v_sub_f32 v116, v116, v248\n"
    "v_exp_f32 v116, v116\n"
    "v_mfma_f32_32x32x16_bf16 v[96:111], v[16:19], v[148:151], 0\n"
    "v_sub_f32 v117, v117, v248\n"
    "v_exp_f32 v117, v117\n"
    "v_sub_f32 v118, v118, v248\n"
    "v_exp_f32 v118, v118\n"
    "v_sub_f32 v119, v119, v248\n"
    "v_exp_f32 v119, v119\n"
    "v_sub_f32 v120, v120, v248\n"
    "v_exp_f32 v120, v120\n"
    "v_sub_f32 v121, v121, v248\n"
    "v_exp_f32 v121, v121\n"
    "v_smfmac_f32_32x32x32_bf16 v[128:143], v[56:59], v[112:119], v232\n"
    "v_sub_f32 v122, v122, v248\n"
    "v_exp_f32 v122, v122\n"
    "v_sub_f32 v123, v123, v248\n"
    "v_exp_f32 v123, v123\n"
    "v_sub_f32 v124, v124, v248\n"
    "v_exp_f32 v124, v124\n"
    "v_sub_f32 v125, v125, v248\n"
    "v_exp_f32 v125, v125\n"
    "v_sub_f32 v126, v126, v248\n"
    "v_exp_f32 v126, v126\n"
    "v_sub_f32 v127, v127, v248\n"
    "v_exp_f32 v127, v127\n"
    "s_waitcnt vmcnt(6)\n"
    "v_smfmac_f32_16x16x64_bf16 v[156:159], v[250:253], v[176:183], v233\n"
    "buffer_load_dwordx4 v[176:179], v241, s[12:15], s73 offen offset:2048 sc1\n"
    "v_sub_f32 v96, v96, v248\n"
    "v_exp_f32 v96, v96\n"
    "v_sub_f32 v97, v97, v248\n"
    "v_exp_f32 v97, v97\n"
    "v_smfmac_f32_32x32x32_bf16 v[128:143], v[60:63], v[120:127], v232\n"
    "buffer_load_dwordx4 v[180:183], v241, s[12:15], s73 offen offset:3072 sc1\n"
    "v_sub_f32 v98, v98, v248\n"
    "v_exp_f32 v98, v98\n"
    "v_sub_f32 v99, v99, v248\n"
    "v_exp_f32 v99, v99\n"
    "v_sub_f32 v100, v100, v248\n"
    "v_exp_f32 v100, v100\n"
    "v_mfma_f32_32x32x16_bf16 v[112:127], v[20:23], v[148:151], 0\n"
    "v_sub_f32 v101, v101, v248\n"
    "v_exp_f32 v101, v101\n"
    "v_sub_f32 v102, v102, v248\n"
    "v_exp_f32 v102, v102\n"
    "v_sub_f32 v103, v103, v248\n"
    "v_exp_f32 v103, v103\n"
    "v_sub_f32 v104, v104, v248\n"
    "v_exp_f32 v104, v104\n"
    "v_sub_f32 v105, v105, v248\n"
    "v_exp_f32 v105, v105\n"
    "v_smfmac_f32_32x32x32_bf16 v[128:143], v[64:67], v[96:103], v232\n"
    "v_sub_f32 v106, v106, v248\n"
    "v_exp_f32 v106, v106\n"
    "v_sub_f32 v107, v107, v248\n"
    "v_exp_f32 v107, v107\n"
    "v_sub_f32 v108, v108, v248\n"
    "v_exp_f32 v108, v108\n"
    "v_sub_f32 v109, v109, v248\n"
    "v_exp_f32 v109, v109\n"
    "v_sub_f32 v110, v110, v248\n"
    "v_exp_f32 v110, v110\n"
    "v_sub_f32 v111, v111, v248\n"
    "v_exp_f32 v111, v111\n"
    "s_waitcnt vmcnt(6)\n"
    "v_smfmac_f32_16x16x64_bf16 v[160:163], v[250:253], v[184:191], v233\n"
    "buffer_load_dwordx4 v[184:187], v241, s[12:15], s74 offen offset:0 sc1\n"
    "v_sub_f32 v112, v112, v248\n"
    "v_exp_f32 v112, v112\n"
    "v_sub_f32 v113, v113, v248\n"
    "v_exp_f32 v113, v113\n"
    "v_smfmac_f32_32x32x32_bf16 v[128:143], v[68:71], v[104:111], v232\n"
    "buffer_load_dwordx4 v[188:191], v241, s[12:15], s74 offen offset:1024 sc1\n"
    "v_sub_f32 v114, v114, v248\n"
    "v_exp_f32 v114, v114\n"
    "v_sub_f32 v115, v115, v248\n"
    "v_exp_f32 v115, v115\n"
    "v_sub_f32 v116, v116, v248\n"
    "v_exp_f32 v116, v116\n"
    "v_mfma_f32_32x32x16_bf16 v[96:111], v[24:27], v[148:151], 0\n"
    "v_sub_f32 v117, v117, v248\n"
    "v_exp_f32 v117, v117\n"
    "v_sub_f32 v118, v118, v248\n"
    "v_exp_f32 v118, v118\n"
    "v_sub_f32 v119, v119, v248\n"
    "v_exp_f32 v119, v119\n"
    "v_sub_f32 v120, v120, v248\n"
    "v_exp_f32 v120, v120\n"
    "v_sub_f32 v121, v121, v248\n"
    "v_exp_f32 v121, v121\n"
    "v_smfmac_f32_32x32x32_bf16 v[128:143], v[72:75], v[112:119], v232\n"
    "v_sub_f32 v122, v122, v248\n"
    "v_exp_f32 v122, v122\n"
    "v_sub_f32 v123, v123, v248\n"
    "v_exp_f32 v123, v123\n"
    "v_sub_f32 v124, v124, v248\n"
    "v_exp_f32 v124, v124\n"
    "v_sub_f32 v125, v125, v248\n"
    "v_exp_f32 v125, v125\n"
    "v_sub_f32 v126, v126, v248\n"
    "v_exp_f32 v126, v126\n"
    "v_sub_f32 v127, v127, v248\n"
    "v_exp_f32 v127, v127\n"
    "s_waitcnt vmcnt(6)\n"
    "v_smfmac_f32_16x16x64_bf16 v[164:167], v[250:253], v[192:199], v233\n"
    "buffer_load_dwordx4 v[192:195], v241, s[12:15], s74 offen offset:2048 sc1\n"
    "v_sub_f32 v96, v96, v248\n"
    "v_exp_f32 v96, v96\n"
    "v_sub_f32 v97, v97, v248\n"
    "v_exp_f32 v97, v97\n"
    "v_smfmac_f32_32x32x32_bf16 v[128:143], v[76:79], v[120:127], v232\n"
    "buffer_load_dwordx4 v[196:199], v241, s[12:15], s74 offen offset:3072 sc1\n"
    "v_sub_f32 v98, v98, v248\n"
    "v_exp_f32 v98, v98\n"
    "v_sub_f32 v99, v99, v248\n"
    "v_exp_f32 v99, v99\n"
    "v_sub_f32 v100, v100, v248\n"
    "v_exp_f32 v100, v100\n"
    "v_mfma_f32_32x32x16_bf16 v[112:127], v[28:31], v[148:151], 0\n"
    "v_sub_f32 v101, v101, v248\n"
    "v_exp_f32 v101, v101\n"
    "v_sub_f32 v102, v102, v248\n"
    "v_exp_f32 v102, v102\n"
    "v_sub_f32 v103, v103, v248\n"
    "v_exp_f32 v103, v103\n"
    "v_sub_f32 v104, v104, v248\n"
    "v_exp_f32 v104, v104\n"
    "v_sub_f32 v105, v105, v248\n"
    "v_exp_f32 v105, v105\n"
    "v_smfmac_f32_32x32x32_bf16 v[128:143], v[80:83], v[96:103], v232\n"
    "s_lshl3_add_u32 s62, s55, s48\n"
    "s_lshl_b32 s62, s62, 10\n"
    "buffer_load_dwordx4 v[0:3], v241, s[16:19], s62 offen\n"
    "v_sub_f32 v106, v106, v248\n"
    "v_exp_f32 v106, v106\n"
    "v_sub_f32 v107, v107, v248\n"
    "v_exp_f32 v107, v107\n"
    "v_sub_f32 v108, v108, v248\n"
    "v_exp_f32 v108, v108\n"
    "v_sub_f32 v109, v109, v248\n"
    "v_exp_f32 v109, v109\n"
    "v_sub_f32 v110, v110, v248\n"
    "v_exp_f32 v110, v110\n"
    "v_sub_f32 v111, v111, v248\n"
    "v_exp_f32 v111, v111\n"
    "v_sub_f32 v112, v112, v248\n"
    "v_exp_f32 v112, v112\n"
    "v_sub_f32 v113, v113, v248\n"
    "v_exp_f32 v113, v113\n"
    "v_smfmac_f32_32x32x32_bf16 v[128:143], v[84:87], v[104:111], v232\n"
    "v_sub_f32 v114, v114, v248\n"
    "v_exp_f32 v114, v114\n"
    "v_sub_f32 v115, v115, v248\n"
    "v_exp_f32 v115, v115\n"
    "v_sub_f32 v116, v116, v248\n"
    "v_exp_f32 v116, v116\n"
    "v_sub_f32 v117, v117, v248\n"
    "v_exp_f32 v117, v117\n"
    "v_sub_f32 v118, v118, v248\n"
    "v_exp_f32 v118, v118\n"
    "v_sub_f32 v119, v119, v248\n"
    "v_exp_f32 v119, v119\n"
    "v_sub_f32 v120, v120, v248\n"
    "v_exp_f32 v120, v120\n"
    "v_sub_f32 v121, v121, v248\n"
    "v_exp_f32 v121, v121\n"
    "v_smfmac_f32_32x32x32_bf16 v[128:143], v[88:91], v[112:119], v232\n"
    "v_sub_f32 v122, v122, v248\n"
    "v_exp_f32 v122, v122\n"
    "v_sub_f32 v123, v123, v248\n"
    "v_exp_f32 v123, v123\n"
    "v_sub_f32 v124, v124, v248\n"
    "v_exp_f32 v124, v124\n"
    "v_sub_f32 v125, v125, v248\n"
    "v_exp_f32 v125, v125\n"
    "v_sub_f32 v126, v126, v248\n"
    "v_exp_f32 v126, v126\n"
    "v_sub_f32 v127, v127, v248\n"
    "v_exp_f32 v127, v127\n"
    "s_nop 1\n"
    "v_smfmac_f32_32x32x32_bf16 v[128:143], v[92:95], v[120:127], v232\n"
    "s_nop 11\n"
    "ds_write_b128 v244, v[128:131] offset:8192\n"
    "ds_write_b128 v244, v[132:135] offset:9216\n"
    "ds_write_b32 v245, v136 offset:512\n"
    "s_waitcnt lgkmcnt(0)\n"
    "s_barrier\n"
    "s_setprio 1\n"
    "ds_read_b128 v[250:253], v247 offset:0\n"
    "s_and_b32 s61, s50, 4\n"
    "s_xor_b32 s61, s61, s48\n"
    "s_min_u32 s66, s61, 3\n"
    "s_mul_i32 s67, s61, 5\n"
    "s_add_u32 s66, s66, s67\n"
    "s_mul_i32 s67, s50, 43\n"
    "s_add_u32 s67, s67, s66\n"
    "s_lshl_b32 s67, s67, 12\n"
    "s_add_u32 s85, s67, 4718592\n"
    "v_bfe_u32 v242, v240, 4, 1\n"
    "v_lshrrev_b32 v243, 5, v240\n"
    "v_lshl_or_b32 v242, v242, 1, v243\n"
    "v_lshlrev_b32 v242, 12, v242\n"
    "v_and_b32 v243, 15, v240\n"
    "v_lshl_or_b32 v242, v243, 4, v242\n"
    "s_lshl_b32 s62, s48, 8\n"
    "s_add_u32 s62, s62, 131072\n"
    "v_add_u32 v242, s62, v242\n"
    "s_lshl_b32 s62, s48, 11\n"
    "s_add_u32 s62, s62, 131072\n"
    "v_lshl_add_u32 v243, v240, 5, s62\n"
    "v_mov_b32 v249, 161792\n"
    "s_waitcnt vmcnt(0)\n"
    "v_lshlrev_b32 v144, 16, v0\n"
    "v_and_b32 v145, 0xffff0000, v0\n"
    "v_lshlrev_b32 v146, 16, v1\n"
    "v_and_b32 v147, 0xffff0000, v1\n"
    "v_lshlrev_b32 v148, 16, v2\n"
    "v_and_b32 v149, 0xffff0000, v2\n"
    "v_lshlrev_b32 v150, 16, v3\n"
    "v_and_b32 v151, 0xffff0000, v3\n"
    "s_waitcnt lgkmcnt(0)\n"
    "v_smfmac_f32_16x16x64_bf16 v[152:155], v[250:253], v[168:175], v233\n"
    "v_smfmac_f32_16x16x64_bf16 v[156:159], v[250:253], v[176:183], v233\n"
    "v_smfmac_f32_16x16x64_bf16 v[160:163], v[250:253], v[184:191], v233\n"
    "v_smfmac_f32_16x16x64_bf16 v[164:167], v[250:253], v[192:199], v233\n"
    "ds_read_b128 v[168:171], v247 offset:2048\n"
    "ds_read_b128 v[172:175], v247 offset:3072\n"
    "ds_read_b128 v[176:179], v247 offset:4096\n"
    "ds_read_b128 v[180:183], v247 offset:5120\n"
    "ds_read_b128 v[184:187], v247 offset:6144\n"
    "ds_read_b128 v[188:191], v247 offset:7168\n"
    "buffer_load_dwordx4 v[16:19], v241, s[12:15], s85 offen offset:0 sc1\n"
    "buffer_load_dwordx4 v[20:23], v241, s[12:15], s85 offen offset:1024 sc1\n"
    "buffer_load_dwordx4 v[24:27], v241, s[12:15], s85 offen offset:2048 sc1\n"
    "buffer_load_dwordx4 v[28:31], v241, s[12:15], s85 offen offset:3072 sc1\n"
    "s_barrier\n"
    "ds_read_b128 v[250:253], v247 offset:1024\n"
    "s_waitcnt lgkmcnt(0)\n"
    "v_smfmac_f32_16x16x64_bf16 v[152:155], v[250:253], v[200:207], v233\n"
    "v_smfmac_f32_16x16x64_bf16 v[156:159], v[250:253], v[208:215], v233\n"
    "v_smfmac_f32_16x16x64_bf16 v[160:163], v[250:253], v[216:223], v233\n"
    "v_smfmac_f32_16x16x64_bf16 v[164:167], v[250:253], v[224:231], v233\n"
    "s_branch .LjoinB\n"
    :
    : "{s[0:1]}"(__builtin_amdgcn_kernarg_segment_ptr()), "{s2}"(blockIdx.x), "{v0}"(threadIdx.x), "i"(0), "s"(lds_base)
    : "v1", "v2", "v3", "v4", "v5", "v6", "v7", "v8", "v9", "v10", "v11", "v12", "v13", "v14", "v15", "v16", "v17", "v18", "v19", "v20", "v21", "v22", "v23", "v24", "v25", "v26", "v27", "v28", "v29", "v30", "v31", "v32", "v33", "v34", "v35", "v36", "v37", "v38", "v39", "v40", "v41", "v42", "v43", "v44", "v45", "v46", "v47", "v48", "v49", "v50", "v51", "v52", "v53", "v54", "v55", "v56", "v57", "v58", "v59", "v60", "v61", "v62", "v63", "v64", "v65", "v66", "v67", "v68", "v69", "v70", "v71", "v72", "v73", "v74", "v75", "v76", "v77", "v78", "v79", "v80", "v81", "v82", "v83", "v84", "v85", "v86", "v87", "v88", "v89", "v90", "v91", "v92", "v93", "v94", "v95", "v96", "v97", "v98", "v99", "v100", "v101", "v102", "v103", "v104", "v105", "v106", "v107", "v108", "v109", "v110", "v111", "v112", "v113", "v114", "v115", "v116", "v117", "v118", "v119", "v120", "v121", "v122", "v123", "v124", "v125", "v126", "v127", "v128", "v129", "v130", "v131", "v132", "v133", "v134", "v135", "v136", "v137", "v138", "v139", "v140", "v141", "v142", "v143", "v144", "v145", "v146", "v147", "v148", "v149", "v150", "v151", "v152", "v153", "v154", "v155", "v156", "v157", "v158", "v159", "v160", "v161", "v162", "v163", "v164", "v165", "v166", "v167", "v168", "v169", "v170", "v171", "v172", "v173", "v174", "v175", "v176", "v177", "v178", "v179", "v180", "v181", "v182", "v183", "v184", "v185", "v186", "v187", "v188", "v189", "v190", "v191", "v192", "v193", "v194", "v195", "v196", "v197", "v198", "v199", "v200", "v201", "v202", "v203", "v204", "v205", "v206", "v207", "v208", "v209", "v210", "v211", "v212", "v213", "v214", "v215", "v216", "v217", "v218", "v219", "v220", "v221", "v222", "v223", "v224", "v225", "v226", "v227", "v228", "v229", "v230", "v231", "v232", "v233", "v234", "v235", "v236", "v237", "v238", "v239", "v240", "v241", "v242", "v243", "v244", "v245", "v246", "v247", "v248", "v249", "v250", "v251", "v252", "v253", "v254", "v255", "s3", "s4", "s5", "s6", "s7", "s8", "s9", "s10", "s11", "s12", "s13", "s14", "s15", "s16", "s17", "s18", "s19", "s20", "s21", "s22", "s23", "s24", "s25", "s26", "s27", "s28", "s29", "s30", "s31", "s34", "s35", "s36", "s37", "s38", "s39", "s40", "s41", "s42", "s43", "s44", "s45", "s46", "s47", "s48", "s49", "s50", "s51", "s52", "s53", "s54", "s55", "s56", "s57", "s58", "s59", "s60", "s61", "s62", "s63", "s64", "s65", "s66", "s67", "s68", "s69", "s70", "s71", "s72", "s73", "s74", "s75", "s76", "s77", "s78", "s79", "s80", "s81", "s82", "s83", "s84", "s85", "s86", "s87", "s88", "s89", "s90", "s91", "s92", "s93", "s94", "s95", "s96", "s97", "s98", "s99", "vcc", "scc", "memory");
}

extern "C" void kernel_launch(void* const* d_in, const int* in_sizes, int n_in, void* d_out, int out_size, void* d_ws, size_t ws_size,
                              hipStream_t stream) {
  const unsigned F = 0x00020000u;
  adaln_mqa_ffn_kernel<<<256, 512, 0, stream>>>(
      d_in[9], 512u * 3072u * 2u, F, d_in[8], 512u * 2u, F, d_ws, 17825792u, F, d_in[0], 2048u * 512u * 2u, F,
      d_in[1], 512u * 512u * 2u, F, d_in[2], 512u * 16u * 2u, F, d_in[3], 512u * 16u * 2u, F, d_in[4], 512u * 512u * 2u, F,
      d_in[6], 512u * 1376u * 2u, F, d_in[7], 1376u * 512u * 2u, F, d_out, 2048u * 512u * 2u, F);
}
```
